```python
import torch

def _softmax(x, axis=-1):
    return torch.softmax(x, dim=axis)
def _gelu(x):
    return torch.nn.functional.gelu(x, approximate="tanh")
def _silu(x):
    return torch.nn.functional.silu(x)
def _sigmoid(x):
    return torch.sigmoid(x)
def _layernorm(x, axis=-1, eps=1e-5):
    m = x.mean(dim=axis, keepdim=True)
    v = x.var(dim=axis, keepdim=True, unbiased=False)
    return (x - m) / torch.sqrt(v + eps)
def _scatter_add(tgt, idx, src, axis=0):
    return tgt.clone().index_add_(axis, idx, src)
def _segment_sum(src, seg, n):
    out = torch.zeros((n,) + src.shape[1:], dtype=src.dtype)
    return out.index_add_(0, seg, src)
def _pad(x, pw):
    flat = tuple(v for pair in reversed(pw) for v in pair)
    return torch.nn.functional.pad(x, flat)

kernel_name = 'recurrent_scan__mha_block_cached__glu_gated_glu__prompt'

def setup_inputs():

    _w = (lambda a: (lambda t: t.to(torch.bfloat16) if t.is_floating_point() else t)(torch.as_tensor(a)))
    def _A_setup():
        B, Sq, D = 1, 2048, 448
        H, Sk, Dh = 16, 2048, 256
        return {
            "x": _w((lambda *s: torch.randn(*s, dtype=torch.bfloat16) * 0.05)(B, Sq, D)),
            "k_cache": _w((lambda *s: torch.randn(*s, dtype=torch.bfloat16) * 1.6)(B, H, Sk, Dh)),
            "v_cache": _w((lambda *s: torch.randn(*s, dtype=torch.bfloat16) * 0.2)(B, H, Sk, Dh)),
            "wq": _w((lambda *s: torch.randn(*s, dtype=torch.bfloat16) * 1.6)(D, H * Dh) * (D ** -0.5)),
            "wo": _w((lambda *s: torch.randn(*s, dtype=torch.bfloat16) * 0.2)(H * Dh, D) * ((H * Dh) ** -0.5)),
        }
    _A_in = _A_setup()

    def _B_setup():
        B, S, D, F = 1, 2048, 448, 1792
        return {
            "x": _w((lambda *s: torch.randn(*s, dtype=torch.bfloat16) * 0.05)(B, S, D)),
            "w_gate": _w((lambda *s: torch.randn(*s, dtype=torch.bfloat16) * 0.2)(D, F)),
            "w_up": _w((lambda *s: torch.randn(*s, dtype=torch.bfloat16) * 0.1)(D, F)),
            "w_down": _w((lambda *s: torch.randn(*s, dtype=torch.bfloat16) * 0.1)(F, D)),
        }
    _B_in = _B_setup()

    return {
        "A_x": _A_in["x"],
        "A_k_cache": _A_in["k_cache"],
        "A_v_cache": _A_in["v_cache"],
        "A_wq": _A_in["wq"],
        "A_wo": _A_in["wo"],
        "B_x": _B_in["x"],
        "B_w_gate": _B_in["w_gate"],
        "B_w_up": _B_in["w_up"],
        "B_w_down": _B_in["w_down"],
    }

def reference(A_x, A_k_cache, A_v_cache, A_wq, A_wo, B_x, B_w_gate, B_w_up, B_w_down):
    def _A_ref(x, k_cache, v_cache, wq, wo):
        B, Sq, D = x.shape
        H, Dh = 16, 256
        q = (lambda x, *d: x.permute(*d))((x @ wq).reshape(B, Sq, H, Dh), 0, 2, 1, 3)
        scores = torch.einsum("bhqd,bhkd->bhqk", q, k_cache) * (Dh ** -0.5)
        p = _softmax(scores, axis=-1)
        o = torch.einsum("bhqk,bhkd->bhqd", p, v_cache)
        return (lambda x, *d: x.permute(*d))(o, 0, 2, 1, 3).reshape(B, Sq, H * Dh) @ wo

    def _B_ref(x, w_gate, w_up, w_down):
        g = torch.einsum("bsd,df->bsf", x, w_gate)
        u = torch.einsum("bsd,df->bsf", x, w_up)
        h = _sigmoid(g) * u
        return torch.einsum("bsf,fd->bsd", h, w_down)

    x = A_x
    xn = _layernorm(x, axis=-1)
    s = _A_ref(xn, A_k_cache, A_v_cache, A_wq, A_wo)
    g = _silu(_B_ref(xn, B_w_gate, B_w_up, B_w_down))
    return x + s * g
```

```cpp
#include <hip/hip_runtime.h>
#include <hip/hip_bf16.h>
typedef unsigned short u16;
__device__ __forceinline__ float b2f(u16 v){ return __uint_as_float(((unsigned)v)<<16); }
__device__ __forceinline__ u16 f2b(float x){ unsigned u=__float_as_uint(x); u += 0x7fffu + ((u>>16)&1u); return (u16)(u>>16); }
__device__ __forceinline__ float rb(float x){ return b2f(f2b(x)); }

__global__ void ln_k(const u16* x, u16* xn){
  int r = blockIdx.x; int l = threadIdx.x;
  float v[7]; float s=0;
  for(int i=0;i<7;i++){ v[i]=b2f(x[r*448+l+64*i]); s+=v[i]; }
  for(int o=32;o;o>>=1) s+=__shfl_xor(s,o);
  float m = s/448.f; float q=0;
  for(int i=0;i<7;i++){ float d=v[i]-m; q+=d*d; }
  for(int o=32;o;o>>=1) q+=__shfl_xor(q,o);
  float var = rb(q/448.f); float mb = rb(m);
  float den = rb(sqrtf(rb(var+1e-5f)));
  for(int i=0;i<7;i++){ xn[r*448+l+64*i] = f2b(rb(v[i]-mb)/den); }
}
__global__ void gemm_k(const u16* A, const u16* B, u16* C, int M,int N,int K,int lda){
  int n = blockIdx.x*64+threadIdx.x; int m0 = blockIdx.y*8;
  if(n>=N) return;
  float acc[8]={0,0,0,0,0,0,0,0};
  for(int k=0;k<K;k++){ float b=b2f(B[(size_t)k*N+n]);
    for(int i=0;i<8;i++) acc[i]+=b2f(A[(size_t)(m0+i)*lda+k])*b; }
  for(int i=0;i<8;i++) C[(size_t)(m0+i)*N+n]=f2b(acc[i]);
}
__global__ void attn_k(const u16* Q, const u16* K, const u16* V, u16* O){
  __shared__ float sq[256]; __shared__ float sp[2048]; __shared__ float red[8];
  int qi=blockIdx.x, h=blockIdx.y, t=threadIdx.x;
  sq[t]=b2f(Q[(size_t)qi*4096+h*256+t]); __syncthreads();
  float mx=-1e30f;
  for(int j=t;j<2048;j+=256){ const u16* kr=K+((size_t)h*2048+j)*256; float s=0;
    for(int d=0;d<256;d++) s+=sq[d]*b2f(kr[d]);
    s=rb(s)*0.0625f; sp[j]=s; mx=fmaxf(mx,s);}
  for(int o=32;o;o>>=1) mx=fmaxf(mx,__shfl_xor(mx,o));
  if((t&63)==0) red[t>>6]=mx; __syncthreads();
  mx=fmaxf(fmaxf(red[0],red[1]),fmaxf(red[2],red[3])); __syncthreads();
  float sum=0;
  for(int j=t;j<2048;j+=256){ float e=__expf(sp[j]-mx); sp[j]=e; sum+=e; }
  for(int o=32;o;o>>=1) sum+=__shfl_xor(sum,o);
  if((t&63)==0) red[t>>6]=sum; __syncthreads();
  sum=red[0]+red[1]+red[2]+red[3];
  float inv=1.f/sum; float acc=0;
  for(int j=0;j<2048;j++) acc+=rb(sp[j]*inv)*b2f(V[((size_t)h*2048+j)*256+t]);
  O[(size_t)qi*4096+h*256+t]=f2b(acc);
}
__global__ void glu_k(const u16* g,const u16* u,u16* h,int n){ int i=blockIdx.x*256+threadIdx.x; if(i<n){ float gg=b2f(g[i]); float s=rb(1.f/(1.f+__expf(-gg))); h[i]=f2b(s*b2f(u[i])); } }
__global__ void fin_k(const u16* x,const u16* s,const u16* d,u16* o,int n){ int i=blockIdx.x*256+threadIdx.x; if(i<n){ float dd=b2f(d[i]); float g=rb(dd/(1.f+__expf(-dd))); o[i]=f2b(b2f(x[i])+rb(b2f(s[i])*g)); } }

extern "C" void kernel_launch(void* const* d_in, const int* in_sizes, int n_in, void* d_out, int out_size, void* d_ws, size_t ws_size, hipStream_t stream){
  const u16* x=(const u16*)d_in[0]; const u16* K=(const u16*)d_in[1]; const u16* V=(const u16*)d_in[2];
  const u16* wq=(const u16*)d_in[3]; const u16* wo=(const u16*)d_in[4];
  const u16* wg=(const u16*)d_in[6]; const u16* wu=(const u16*)d_in[7]; const u16* wd=(const u16*)d_in[8];
  u16* ws=(u16*)d_ws;
  u16* xn=ws; u16* Q=xn+2048*448; u16* O=Q+2048*4096; u16* g=O+2048*4096; u16* u=g+2048*1792; u16* h=u+2048*1792; u16* s=h+2048*1792; u16* d=s+2048*448;
  ln_k<<<2048,64,0,stream>>>(x,xn);
  gemm_k<<<dim3(64,256),64,0,stream>>>(xn,wq,Q,2048,4096,448,448);
  attn_k<<<dim3(2048,16),256,0,stream>>>(Q,K,V,O);
  gemm_k<<<dim3(7,256),64,0,stream>>>(O,wo,s,2048,448,4096,4096);
  gemm_k<<<dim3(28,256),64,0,stream>>>(xn,wg,g,2048,1792,448,448);
  gemm_k<<<dim3(28,256),64,0,stream>>>(xn,wu,u,2048,1792,448,448);
  glu_k<<<(2048*1792+255)/256,256,0,stream>>>(g,u,h,2048*1792);
  gemm_k<<<dim3(7,256),64,0,stream>>>(h,wd,d,2048,448,1792,1792);
  fin_k<<<(2048*448+255)/256,256,0,stream>>>(x,s,d,(u16*)d_out,2048*448);
}
```

```cpp
#include <hip/hip_runtime.h>
#include <hip/hip_bf16.h>

__global__ __launch_bounds__(256, 1) void fused_main(const void* x, const void* kc, const void* vc, const void* wq, const void* wo,
                                                     const void* wg, const void* wu, const void* wd, void* ws, void* out) {
  int bid = blockIdx.x;
  auto karg = __builtin_amdgcn_kernarg_segment_ptr();
  asm volatile(
    "s_load_dwordx16 s[4:19], %0, 0\n"
    "s_load_dwordx4 s[20:23], %0, 0x40\n"
    "s_getpc_b64 s[64:65]\n"
    "Lpc1%=:\n"
    "s_mov_b32 s66, Lend%=-Lpc1%=\n"
    "s_mov_b32 s67, 0x20000\n"
    "s_lshr_b32 s84, %1, 3\n"
    "s_mul_i32 s84, s84, 24\n"
    "v_cmp_gt_u32 vcc, 32, v0\n"
    "v_add_u32 v6, s84, v0\n"
    "v_mov_b32 v8, 0x80000000\n"
    "v_cndmask_b32 v6, v6, v0, vcc\n"
    "v_cmp_gt_u32 vcc, 56, v0\n"
    "v_lshlrev_b32 v6, 7, v6\n"
    "v_lshrrev_b32 v1, 6, v0\n"
    "v_cndmask_b32 v6, v8, v6, vcc\n"
    "buffer_load_dword v127, v6, s[64:67], 0 offen\n"
    "v_lshrrev_b32 v1, 6, v0\n"
    "v_and_b32 v2, 63, v0\n"
    "v_readfirstlane_b32 s81, v1\n"
    "v_bfe_u32 v10, v2, 2, 3\n"
    "v_and_b32 v7, 3, v2\n"
    "v_lshrrev_b32 v11, 5, v2\n"
    "v_lshlrev_b32 v11, 6, v11\n"
    "v_lshl_add_u32 v11, v7, 4, v11\n"
    "s_lshr_b32 s85, %1, 4\n"
    "s_lshr_b32 s83, s81, 1\n"
    "v_mul_u32_u24 v5, 896, v10\n"
    "s_mul_i32 s86, s81, 28672\n"
    "v_add3_u32 v5, v5, v11, s86\n"
    "s_mul_i32 s87, s81, 29568\n"
    "s_mul_i32 s86, s83, 45312\n"
    "s_add_u32 s87, s87, s86\n"
    "s_mov_b32 s92, 0\n"
    "s_mov_b32 s93, 7168\n"
    "s_mov_b32 s94, 14336\n"
    "s_mov_b32 s95, 21504\n"
    "s_and_b32 s84, %1, 7\n"
    "s_lshl_b32 s84, s84, 1\n"
    "s_bfe_u32 s86, %1, 0x10003\n"
    "s_add_u32 s84, s84, s86\n"
    "s_mul_i32 s86, s84, 56\n"
    "v_add_u32 v19, s86, v0\n"
    "v_cmp_gt_u32 vcc, 56, v0\n"
    "v_lshlrev_b32 v19, 7, v19\n"
    "v_mov_b32 v8, 0x80000000\n"
    "v_cndmask_b32 v19, v8, v19, vcc\n"
    "v_and_b32 v16, 127, v0\n"
    "v_cmp_gt_u32 vcc, 84, v16\n"
    "s_mul_i32 s86, s85, 84\n"
    "v_add_u32 v16, s86, v16\n"
    "v_mul_u32_u24 v17, 21846, v16\n"
    "v_lshrrev_b32 v17, 16, v17\n"
    "v_mul_u32_u24 v18, 3, v17\n"
    "v_sub_u32 v16, v16, v18\n"
    "v_mul_u32_u24 v17, 3584, v17\n"
    "v_mul_u32_u24 v16, 104, v16\n"
    "v_add_u32 v16, v16, v17\n"
    "v_cndmask_b32 v16, v8, v16, vcc\n"
    "s_mul_i32 s86, s85, 114688\n"
    "s_waitcnt lgkmcnt(0)\n"
    "s_mov_b64 s[40:41], s[4:5]\n"
    "s_add_u32 s40, s40, s86\n"
    "s_addc_u32 s41, s41, 0\n"
    "s_mov_b32 s42, 114688\n"
    "s_mov_b32 s43, 0x20000\n"
    "buffer_load_dword v127, v19, s[40:43], 0 offen\n"
    "s_cmp_eq_u32 s83, 0\n"
    "s_cselect_b64 s[44:45], s[14:15], s[16:17]\n"
    "s_mul_i32 s86, s84, 224\n"
    "s_add_u32 s44, s44, s86\n"
    "s_addc_u32 s45, s45, 0\n"
    "s_sub_u32 s46, 1605632, s86\n"
    "s_mov_b32 s47, 0x20000\n"
    "buffer_load_dword v127, v16, s[44:47], 0 offen\n"
    "s_add_u32 m0, s87, 0\n"
    "s_nop 0\n"
    "buffer_load_dwordx4 v5, s[40:43], s92 offen offset:0 lds\n"
    "s_add_u32 m0, s87, 1056\n"
    "s_nop 0\n"
    "buffer_load_dwordx4 v5, s[40:43], s93 offen offset:0 lds\n"
    "s_add_u32 m0, s87, 1984\n"
    "s_nop 0\n"
    "buffer_load_dwordx4 v5, s[40:43], s92 offen offset:128 lds\n"
    "s_add_u32 m0, s87, 3040\n"
    "s_nop 0\n"
    "buffer_load_dwordx4 v5, s[40:43], s93 offen offset:128 lds\n"
    "s_add_u32 m0, s87, 3968\n"
    "s_nop 0\n"
    "buffer_load_dwordx4 v5, s[40:43], s92 offen offset:256 lds\n"
    "s_add_u32 m0, s87, 5024\n"
    "s_nop 0\n"
    "buffer_load_dwordx4 v5, s[40:43], s93 offen offset:256 lds\n"
    "s_add_u32 m0, s87, 5952\n"
    "s_nop 0\n"
    "buffer_load_dwordx4 v5, s[40:43], s92 offen offset:384 lds\n"
    "s_add_u32 m0, s87, 7008\n"
    "s_nop 0\n"
    "buffer_load_dwordx4 v5, s[40:43], s93 offen offset:384 lds\n"
    "s_add_u32 m0, s87, 7936\n"
    "s_nop 0\n"
    "buffer_load_dwordx4 v5, s[40:43], s92 offen offset:512 lds\n"
    "s_add_u32 m0, s87, 8992\n"
    "s_nop 0\n"
    "buffer_load_dwordx4 v5, s[40:43], s93 offen offset:512 lds\n"
    "s_add_u32 m0, s87, 9920\n"
    "s_nop 0\n"
    "buffer_load_dwordx4 v5, s[40:43], s92 offen offset:640 lds\n"
    "s_add_u32 m0, s87, 10976\n"
    "s_nop 0\n"
    "buffer_load_dwordx4 v5, s[40:43], s93 offen offset:640 lds\n"
    "s_add_u32 m0, s87, 11904\n"
    "s_nop 0\n"
    "buffer_load_dwordx4 v5, s[40:43], s92 offen offset:768 lds\n"
    "s_add_u32 m0, s87, 12960\n"
    "s_nop 0\n"
    "buffer_load_dwordx4 v5, s[40:43], s93 offen offset:768 lds\n"
    "s_add_u32 m0, s87, 14784\n"
    "s_nop 0\n"
    "buffer_load_dwordx4 v5, s[40:43], s94 offen offset:0 lds\n"
    "s_add_u32 m0, s87, 15840\n"
    "s_nop 0\n"
    "buffer_load_dwordx4 v5, s[40:43], s95 offen offset:0 lds\n"
    "s_add_u32 m0, s87, 16768\n"
    "s_nop 0\n"
    "buffer_load_dwordx4 v5, s[40:43], s94 offen offset:128 lds\n"
    "s_add_u32 m0, s87, 17824\n"
    "s_nop 0\n"
    "buffer_load_dwordx4 v5, s[40:43], s95 offen offset:128 lds\n"
    "s_add_u32 m0, s87, 18752\n"
    "s_nop 0\n"
    "buffer_load_dwordx4 v5, s[40:43], s94 offen offset:256 lds\n"
    "s_add_u32 m0, s87, 19808\n"
    "s_nop 0\n"
    "buffer_load_dwordx4 v5, s[40:43], s95 offen offset:256 lds\n"
    "s_add_u32 m0, s87, 20736\n"
    "s_nop 0\n"
    "buffer_load_dwordx4 v5, s[40:43], s94 offen offset:384 lds\n"
    "s_add_u32 m0, s87, 21792\n"
    "s_nop 0\n"
    "buffer_load_dwordx4 v5, s[40:43], s95 offen offset:384 lds\n"
    "s_add_u32 m0, s87, 22720\n"
    "s_nop 0\n"
    "buffer_load_dwordx4 v5, s[40:43], s94 offen offset:512 lds\n"
    "s_add_u32 m0, s87, 23776\n"
    "s_nop 0\n"
    "buffer_load_dwordx4 v5, s[40:43], s95 offen offset:512 lds\n"
    "s_add_u32 m0, s87, 24704\n"
    "s_nop 0\n"
    "buffer_load_dwordx4 v5, s[40:43], s94 offen offset:640 lds\n"
    "s_add_u32 m0, s87, 25760\n"
    "s_nop 0\n"
    "buffer_load_dwordx4 v5, s[40:43], s95 offen offset:640 lds\n"
    "s_add_u32 m0, s87, 26688\n"
    "s_nop 0\n"
    "buffer_load_dwordx4 v5, s[40:43], s94 offen offset:768 lds\n"
    "s_add_u32 m0, s87, 27744\n"
    "s_nop 0\n"
    "buffer_load_dwordx4 v5, s[40:43], s95 offen offset:768 lds\n"
    "s_mov_b64 s[30:31], s[22:23]\n"
    "s_and_b32 s84, %1, 7\n"
    "s_lshl_b32 s84, s84, 1\n"
    "s_bfe_u32 s86, %1, 0x10003\n"
    "s_add_u32 s84, s84, s86\n"
    "s_and_b32 s82, s81, 1\n"
    "v_and_b32 v3, 15, v2\n"
    "v_lshrrev_b32 v4, 4, v2\n"
    "v_lshrrev_b32 v12, 3, v3\n"
    "v_mul_u32_u24 v12, 1056, v12\n"
    "v_and_b32 v13, 7, v3\n"
    "v_lshl_add_u32 v12, v13, 6, v12\n"
    "v_lshl_add_u32 v9, v4, 4, v12\n"
    "v_add_u32 v14, s87, v9\n"
    "v_mul_u32_u24 v6, 37, v2\n"
    "v_lshrrev_b32 v6, 9, v6\n"
    "v_mul_u32_u24 v8, 14, v6\n"
    "v_sub_u32 v8, v2, v8\n"
    "v_mul_u32_u24 v6, 3584, v6\n"
    "v_lshl_add_u32 v6, v8, 4, v6\n"
    "v_cmp_gt_u32 vcc, 56, v2\n"
    "v_mov_b32 v8, 0x80000000\n"
    "s_nop 0\n"
    "v_cndmask_b32 v243, v8, v6, vcc\n"
    "s_mul_i32 s99, s83, 17408\n"
    "s_mul_i32 s86, s82, 8704\n"
    "s_add_u32 s99, s99, s86\n"
    "s_mul_i32 s88, s82, 114688\n"
    "s_add_u32 m0, s99, 69632\n"
    "s_nop 0\n"
    "buffer_load_dwordx4 v243, s[44:47], s88 offen lds\n"
    "s_add_u32 s88, s88, 14336\n"
    "s_add_u32 m0, s99, 70720\n"
    "s_nop 0\n"
    "buffer_load_dwordx4 v243, s[44:47], s88 offen lds\n"
    "s_add_u32 s88, s88, 14336\n"
    "s_add_u32 m0, s99, 71808\n"
    "s_nop 0\n"
    "buffer_load_dwordx4 v243, s[44:47], s88 offen lds\n"
    "s_add_u32 s88, s88, 14336\n"
    "s_add_u32 m0, s99, 72896\n"
    "s_nop 0\n"
    "buffer_load_dwordx4 v243, s[44:47], s88 offen lds\n"
    "s_add_u32 s88, s88, 14336\n"
    "s_add_u32 m0, s99, 73984\n"
    "s_nop 0\n"
    "buffer_load_dwordx4 v243, s[44:47], s88 offen lds\n"
    "s_add_u32 s88, s88, 14336\n"
    "s_add_u32 m0, s99, 75072\n"
    "s_nop 0\n"
    "buffer_load_dwordx4 v243, s[44:47], s88 offen lds\n"
    "s_add_u32 s88, s88, 14336\n"
    "s_add_u32 m0, s99, 76160\n"
    "s_nop 0\n"
    "buffer_load_dwordx4 v243, s[44:47], s88 offen lds\n"
    "s_add_u32 s88, s88, 14336\n"
    "s_add_u32 m0, s99, 77248\n"
    "s_nop 0\n"
    "buffer_load_dwordx4 v243, s[44:47], s88 offen lds\n"
    "s_add_u32 s88, s88, 129024\n"
    "s_lshl_b32 s86, s85, 9\n"
    "s_add_u32 s86, s86, 100663296\n"
    "s_mov_b64 s[64:65], s[20:21]\n"
    "s_add_u32 s64, s64, s86\n"
    "s_addc_u32 s65, s65, 0\n"
    "s_mov_b32 s66, 512\n"
    "s_mov_b32 s67, 0x20000\n"
    "s_lshl_b32 s86, s84, 9\n"
    "s_mov_b64 s[52:53], s[10:11]\n"
    "s_add_u32 s52, s52, s86\n"
    "s_addc_u32 s53, s53, 0\n"
    "s_sub_u32 s54, 3670016, s86\n"
    "s_mov_b32 s55, 0x20000\n"
    "s_mul_i32 s86, s84, 229376\n"
    "s_mov_b64 s[56:57], s[12:13]\n"
    "s_add_u32 s56, s56, s86\n"
    "s_addc_u32 s57, s57, 0\n"
    "s_mov_b32 s58, 229376\n"
    "s_mov_b32 s59, 0x20000\n"
    "s_mov_b64 s[48:49], s[18:19]\n"
    "s_mov_b32 s50, 1605632\n"
    "s_mov_b32 s51, 0x20000\n"
    "s_mul_i32 s86, s85, 112\n"
    "v_add_u32 v16, s86, v0\n"
    "v_cmp_gt_u32 vcc, 112, v0\n"
    "v_lshrrev_b32 v17, 2, v16\n"
    "v_lshlrev_b32 v17, 13, v17\n"
    "v_and_b32 v18, 3, v16\n"
    "v_lshl_add_u32 v17, v18, 7, v17\n"
    "v_cndmask_b32 v124, v8, v17, vcc\n"
    "buffer_load_dword v127, v124, s[52:55], 0 offen\n"
    "v_lshlrev_b32 v17, 7, v16\n"
    "v_cndmask_b32 v125, v8, v17, vcc\n"
    "s_mul_i32 s86, %1, 49\n"
    "v_add_u32 v16, s86, v0\n"
    "v_lshlrev_b32 v17, 7, v16\n"
    "v_cmp_gt_u32 vcc, 49, v0\n"
    "s_nop 1\n"
    "v_cndmask_b32 v126, v8, v17, vcc\n"
    "s_lshl_b32 s86, s84, 20\n"
    "s_mov_b64 s[24:25], s[6:7]\n"
    "s_add_u32 s24, s24, s86\n"
    "s_addc_u32 s25, s25, 0\n"
    "s_mov_b32 s26, 1048576\n"
    "s_mov_b32 s27, 0x20000\n"
    "s_mov_b64 s[36:37], s[8:9]\n"
    "s_add_u32 s36, s36, s86\n"
    "s_addc_u32 s37, s37, 0\n"
    "s_mov_b32 s38, 1048576\n"
    "s_mov_b32 s39, 0x20000\n"
    "s_lshl_b32 s86, s85, 4\n"
    "s_add_u32 s86, s86, s84\n"
    "s_mul_i32 s86, s86, 143360\n"
    "s_mov_b64 s[60:61], s[20:21]\n"
    "s_add_u32 s60, s60, s86\n"
    "s_addc_u32 s61, s61, 0\n"
    "s_mov_b32 s62, 143360\n"
    "s_mov_b32 s63, 0x20000\n"
    "s_lshl_b32 s86, %1, 16\n"
    "s_add_u32 s86, s86, 0x4000000\n"
    "s_mov_b64 s[68:69], s[20:21]\n"
    "s_add_u32 s68, s68, s86\n"
    "s_addc_u32 s69, s69, 0\n"
    "s_mov_b32 s70, 65536\n"
    "s_mov_b32 s71, 0x20000\n"
    "s_mov_b32 s92, 0\n"
    "s_mov_b32 s93, 4096\n"
    "s_mov_b32 s94, 8192\n"
    "s_mov_b32 s95, 12288\n"
    "s_mov_b32 s96, 16384\n"
    "s_mov_b32 s97, 20480\n"
    "s_mov_b32 s98, 24576\n"
    "v_lshrrev_b32 v6, 2, v3\n"
    "v_mul_u32_u24 v12, 2176, v4\n"
    "v_mul_u32_u24 v13, 224, v6\n"
    "v_add_u32 v12, v12, v13\n"
    "v_lshl_add_u32 v240, v7, 3, v12\n"
    "v_mul_u32_u24 v12, 1032, v4\n"
    "v_lshl_add_u32 v12, v6, 6, v12\n"
    "v_lshl_add_u32 v241, v7, 4, v12\n"
    "v_mul_u32_u24 v12, 1056, v4\n"
    "v_lshl_add_u32 v12, v6, 6, v12\n"
    "v_lshl_add_u32 v12, v7, 3, v12\n"
    "s_mul_i32 s86, s82, 8448\n"
    "s_add_u32 s87, s86, 67584\n"
    "v_add_u32 v121, s87, v12\n"
    "v_add_u32 v120, s86, v9\n"
    "s_mul_i32 s87, s83, 1056\n"
    "s_add_u32 s77, s86, s87\n"
    "s_mul_i32 s87, s83, 4224\n"
    "s_add_u32 s78, s86, s87\n"
    "s_add_u32 s78, s78, 67584\n"
    "s_mul_i32 s89, s81, 1056\n"
    "v_lshl_add_u32 v12, v10, 13, v11\n"
    "s_lshl_b32 s86, s81, 16\n"
    "v_add_u32 v244, s86, v12\n"
    "s_lshl_b32 s86, s82, 19\n"
    "v_lshl_add_u32 v12, v10, 9, v11\n"
    "v_add_u32 v122, s86, v12\n"
    "v_lshrrev_b32 v12, 2, v10\n"
    "v_and_b32 v13, 3, v10\n"
    "v_lshl_add_u32 v12, v12, 3, v13\n"
    "v_lshl_add_u32 v12, v12, 9, v11\n"
    "s_lshl_b32 s87, s83, 13\n"
    "s_add_u32 s86, s86, s87\n"
    "v_add_u32 v123, s86, v12\n"
    "s_lshl_b32 s86, s83, 14\n"
    "s_add_u32 s73, s86, 0\n"
    "s_add_u32 s74, s86, 4096\n"
    "s_add_u32 s75, s86, 8192\n"
    "s_add_u32 s76, s86, 12288\n"
    "s_mov_b32 s72, 0\n"
    "s_mov_b32 s29, 0\n"
    "s_mov_b32 s28, 0\n"
    "s_mov_b32 s79, 14\n"
    "v_lshlrev_b32 v12, 4, v2\n"
    "s_mul_i32 s86, s81, 7168\n"
    "s_add_u32 s86, s86, 114688\n"
    "v_add_u32 v246, s86, v12\n"
    "s_lshl_b32 s86, s81, 11\n"
    "s_add_u32 s86, s86, 98304\n"
    "v_add_u32 v254, s86, v12\n"
    "v_xor_b32 v255, 2048, v254\n"
    "s_mul_i32 s87, s81, 1032\n"
    "s_mov_b32 s80, 0x58800000\n"
    "s_getpc_b64 s[90:91]\n"
    "Lpc0%=:\n"
    "s_add_u32 s90, s90, LbackF%=-Lpc0%=\n"
    "s_addc_u32 s91, s91, 0\n"
    "v_mov_b32 v16, 0x3f803f80\n"
    "v_mov_b32 v17, 0x3f803f80\n"
    "v_mov_b32 v18, 0x3f803f80\n"
    "v_mov_b32 v19, 0x3f803f80\n"
    "v_lshrrev_b32 v12, 2, v3\n"
    "v_lshl_add_u32 v12, v12, 4, v3\n"
    "v_lshlrev_b32 v12, 2, v12\n"
    "v_and_b32 v13, 3, v3\n"
    "s_waitcnt vmcnt(35)\n"
    "ds_read_b128 v[128:131], v14 offset:0\n"
    "ds_read_b128 v[136:139], v14 offset:512\n"
    "s_waitcnt vmcnt(33)\n"
    "ds_read_b128 v[144:147], v14 offset:2112\n"
    "ds_read_b128 v[152:155], v14 offset:2624\n"
    "s_waitcnt lgkmcnt(2)\n"
    "v_mfma_f32_16x16x32_bf16 v[20:23], v[16:19], v[128:131], 0\n"
    "v_mfma_f32_16x16x32_bf16 v[28:31], v[128:131], v[128:131], 0\n"
    "v_mfma_f32_16x16x32_bf16 v[20:23], v[16:19], v[136:139], v[20:23]\n"
    "v_mfma_f32_16x16x32_bf16 v[28:31], v[136:139], v[136:139], v[28:31]\n"
    "s_waitcnt vmcnt(31)\n"
    "ds_read_b128 v[160:163], v14 offset:4224\n"
    "ds_read_b128 v[168:171], v14 offset:4736\n"
    "s_waitcnt lgkmcnt(2)\n"
    "v_mfma_f32_16x16x32_bf16 v[20:23], v[16:19], v[144:147], v[20:23]\n"
    "v_mfma_f32_16x16x32_bf16 v[28:31], v[144:147], v[144:147], v[28:31]\n"
    "v_mfma_f32_16x16x32_bf16 v[20:23], v[16:19], v[152:155], v[20:23]\n"
    "v_mfma_f32_16x16x32_bf16 v[28:31], v[152:155], v[152:155], v[28:31]\n"
    "s_waitcnt vmcnt(29)\n"
    "ds_read_b128 v[176:179], v14 offset:6336\n"
    "ds_read_b128 v[184:187], v14 offset:6848\n"
    "s_waitcnt lgkmcnt(2)\n"
    "v_mfma_f32_16x16x32_bf16 v[20:23], v[16:19], v[160:163], v[20:23]\n"
    "v_mfma_f32_16x16x32_bf16 v[28:31], v[160:163], v[160:163], v[28:31]\n"
    "v_mfma_f32_16x16x32_bf16 v[20:23], v[16:19], v[168:171], v[20:23]\n"
    "v_mfma_f32_16x16x32_bf16 v[28:31], v[168:171], v[168:171], v[28:31]\n"
    "s_waitcnt vmcnt(27)\n"
    "ds_read_b128 v[192:195], v14 offset:8448\n"
    "ds_read_b128 v[200:203], v14 offset:8960\n"
    "s_waitcnt lgkmcnt(2)\n"
    "v_mfma_f32_16x16x32_bf16 v[20:23], v[16:19], v[176:179], v[20:23]\n"
    "v_mfma_f32_16x16x32_bf16 v[28:31], v[176:179], v[176:179], v[28:31]\n"
    "v_mfma_f32_16x16x32_bf16 v[20:23], v[16:19], v[184:187], v[20:23]\n"
    "v_mfma_f32_16x16x32_bf16 v[28:31], v[184:187], v[184:187], v[28:31]\n"
    "s_waitcnt vmcnt(25)\n"
    "ds_read_b128 v[208:211], v14 offset:10560\n"
    "ds_read_b128 v[216:219], v14 offset:11072\n"
    "s_waitcnt lgkmcnt(2)\n"
    "v_mfma_f32_16x16x32_bf16 v[20:23], v[16:19], v[192:195], v[20:23]\n"
    "v_mfma_f32_16x16x32_bf16 v[28:31], v[192:195], v[192:195], v[28:31]\n"
    "v_mfma_f32_16x16x32_bf16 v[20:23], v[16:19], v[200:203], v[20:23]\n"
    "v_mfma_f32_16x16x32_bf16 v[28:31], v[200:203], v[200:203], v[28:31]\n"
    "s_waitcnt vmcnt(23)\n"
    "ds_read_b128 v[224:227], v14 offset:12672\n"
    "ds_read_b128 v[232:235], v14 offset:13184\n"
    "s_waitcnt lgkmcnt(2)\n"
    "v_mfma_f32_16x16x32_bf16 v[20:23], v[16:19], v[208:211], v[20:23]\n"
    "v_mfma_f32_16x16x32_bf16 v[28:31], v[208:211], v[208:211], v[28:31]\n"
    "v_mfma_f32_16x16x32_bf16 v[20:23], v[16:19], v[216:219], v[20:23]\n"
    "v_mfma_f32_16x16x32_bf16 v[28:31], v[216:219], v[216:219], v[28:31]\n"
    "s_waitcnt lgkmcnt(0)\n"
    "v_mfma_f32_16x16x32_bf16 v[20:23], v[16:19], v[224:227], v[20:23]\n"
    "v_mfma_f32_16x16x32_bf16 v[28:31], v[224:227], v[224:227], v[28:31]\n"
    "v_mfma_f32_16x16x32_bf16 v[20:23], v[16:19], v[232:235], v[20:23]\n"
    "v_mfma_f32_16x16x32_bf16 v[28:31], v[232:235], v[232:235], v[28:31]\n"
    "s_nop 6\n"
    "v_mul_f32 v118, 0x3b124925, v20\n"
    "v_cvt_pk_bf16_f32 v242, v118, v118\n"
    "v_and_b32 v242, 0xffff0000, v242\n"
    "v_sub_f32 v40, 0, v242\n"
    "v_cmp_eq_u32 vcc, 1, v13\n"
    "s_nop 1\n"
    "v_cndmask_b32 v28, v28, v29, vcc\n"
    "v_cmp_eq_u32 vcc, 2, v13\n"
    "s_nop 1\n"
    "v_cndmask_b32 v28, v28, v30, vcc\n"
    "v_cmp_eq_u32 vcc, 3, v13\n"
    "s_nop 1\n"
    "v_cndmask_b32 v28, v28, v31, vcc\n"
    "ds_bpermute_b32 v116, v12, v28\n"
    "v_lshlrev_b32 v48, 16, v128\n"
    "v_lshlrev_b32 v50, 16, v129\n"
    "v_lshlrev_b32 v52, 16, v130\n"
    "v_lshlrev_b32 v54, 16, v131\n"
    "v_and_b32 v49, 0xffff0000, v128\n"
    "v_and_b32 v51, 0xffff0000, v129\n"
    "v_and_b32 v53, 0xffff0000, v130\n"
    "v_and_b32 v55, 0xffff0000, v131\n"
    "v_pk_add_f32 v[48:49], v[48:49], v[40:41] op_sel_hi:[1,0]\n"
    "v_pk_add_f32 v[50:51], v[50:51], v[40:41] op_sel_hi:[1,0]\n"
    "v_pk_add_f32 v[52:53], v[52:53], v[40:41] op_sel_hi:[1,0]\n"
    "v_pk_add_f32 v[54:55], v[54:55], v[40:41] op_sel_hi:[1,0]\n"
    "v_cvt_pk_bf16_f32 v128, v48, v49\n"
    "v_cvt_pk_bf16_f32 v129, v50, v51\n"
    "v_cvt_pk_bf16_f32 v130, v52, v53\n"
    "v_cvt_pk_bf16_f32 v131, v54, v55\n"
    "v_lshlrev_b32 v48, 16, v136\n"
    "v_lshlrev_b32 v50, 16, v137\n"
    "v_lshlrev_b32 v52, 16, v138\n"
    "v_lshlrev_b32 v54, 16, v139\n"
    "v_and_b32 v49, 0xffff0000, v136\n"
    "v_and_b32 v51, 0xffff0000, v137\n"
    "v_and_b32 v53, 0xffff0000, v138\n"
    "v_and_b32 v55, 0xffff0000, v139\n"
    "v_pk_add_f32 v[48:49], v[48:49], v[40:41] op_sel_hi:[1,0]\n"
    "v_pk_add_f32 v[50:51], v[50:51], v[40:41] op_sel_hi:[1,0]\n"
    "v_pk_add_f32 v[52:53], v[52:53], v[40:41] op_sel_hi:[1,0]\n"
    "v_pk_add_f32 v[54:55], v[54:55], v[40:41] op_sel_hi:[1,0]\n"
    "v_cvt_pk_bf16_f32 v136, v48, v49\n"
    "v_cvt_pk_bf16_f32 v137, v50, v51\n"
    "v_cvt_pk_bf16_f32 v138, v52, v53\n"
    "v_cvt_pk_bf16_f32 v139, v54, v55\n"
    "v_lshlrev_b32 v48, 16, v144\n"
    "v_lshlrev_b32 v50, 16, v145\n"
    "v_lshlrev_b32 v52, 16, v146\n"
    "v_lshlrev_b32 v54, 16, v147\n"
    "v_and_b32 v49, 0xffff0000, v144\n"
    "v_and_b32 v51, 0xffff0000, v145\n"
    "v_and_b32 v53, 0xffff0000, v146\n"
    "v_and_b32 v55, 0xffff0000, v147\n"
    "v_pk_add_f32 v[48:49], v[48:49], v[40:41] op_sel_hi:[1,0]\n"
    "v_pk_add_f32 v[50:51], v[50:51], v[40:41] op_sel_hi:[1,0]\n"
    "v_pk_add_f32 v[52:53], v[52:53], v[40:41] op_sel_hi:[1,0]\n"
    "v_pk_add_f32 v[54:55], v[54:55], v[40:41] op_sel_hi:[1,0]\n"
    "v_cvt_pk_bf16_f32 v144, v48, v49\n"
    "v_cvt_pk_bf16_f32 v145, v50, v51\n"
    "v_cvt_pk_bf16_f32 v146, v52, v53\n"
    "v_cvt_pk_bf16_f32 v147, v54, v55\n"
    "v_lshlrev_b32 v48, 16, v152\n"
    "v_lshlrev_b32 v50, 16, v153\n"
    "v_lshlrev_b32 v52, 16, v154\n"
    "v_lshlrev_b32 v54, 16, v155\n"
    "v_and_b32 v49, 0xffff0000, v152\n"
    "v_and_b32 v51, 0xffff0000, v153\n"
    "v_and_b32 v53, 0xffff0000, v154\n"
    "v_and_b32 v55, 0xffff0000, v155\n"
    "v_pk_add_f32 v[48:49], v[48:49], v[40:41] op_sel_hi:[1,0]\n"
    "v_pk_add_f32 v[50:51], v[50:51], v[40:41] op_sel_hi:[1,0]\n"
    "v_pk_add_f32 v[52:53], v[52:53], v[40:41] op_sel_hi:[1,0]\n"
    "v_pk_add_f32 v[54:55], v[54:55], v[40:41] op_sel_hi:[1,0]\n"
    "v_cvt_pk_bf16_f32 v152, v48, v49\n"
    "v_cvt_pk_bf16_f32 v153, v50, v51\n"
    "v_cvt_pk_bf16_f32 v154, v52, v53\n"
    "v_cvt_pk_bf16_f32 v155, v54, v55\n"
    "v_lshlrev_b32 v48, 16, v160\n"
    "v_lshlrev_b32 v50, 16, v161\n"
    "v_lshlrev_b32 v52, 16, v162\n"
    "v_lshlrev_b32 v54, 16, v163\n"
    "v_and_b32 v49, 0xffff0000, v160\n"
    "v_and_b32 v51, 0xffff0000, v161\n"
    "v_and_b32 v53, 0xffff0000, v162\n"
    "v_and_b32 v55, 0xffff0000, v163\n"
    "v_pk_add_f32 v[48:49], v[48:49], v[40:41] op_sel_hi:[1,0]\n"
    "v_pk_add_f32 v[50:51], v[50:51], v[40:41] op_sel_hi:[1,0]\n"
    "v_pk_add_f32 v[52:53], v[52:53], v[40:41] op_sel_hi:[1,0]\n"
    "v_pk_add_f32 v[54:55], v[54:55], v[40:41] op_sel_hi:[1,0]\n"
    "v_cvt_pk_bf16_f32 v160, v48, v49\n"
    "v_cvt_pk_bf16_f32 v161, v50, v51\n"
    "v_cvt_pk_bf16_f32 v162, v52, v53\n"
    "v_cvt_pk_bf16_f32 v163, v54, v55\n"
    "v_lshlrev_b32 v48, 16, v168\n"
    "v_lshlrev_b32 v50, 16, v169\n"
    "v_lshlrev_b32 v52, 16, v170\n"
    "v_lshlrev_b32 v54, 16, v171\n"
    "v_and_b32 v49, 0xffff0000, v168\n"
    "v_and_b32 v51, 0xffff0000, v169\n"
    "v_and_b32 v53, 0xffff0000, v170\n"
    "v_and_b32 v55, 0xffff0000, v171\n"
    "v_pk_add_f32 v[48:49], v[48:49], v[40:41] op_sel_hi:[1,0]\n"
    "v_pk_add_f32 v[50:51], v[50:51], v[40:41] op_sel_hi:[1,0]\n"
    "v_pk_add_f32 v[52:53], v[52:53], v[40:41] op_sel_hi:[1,0]\n"
    "v_pk_add_f32 v[54:55], v[54:55], v[40:41] op_sel_hi:[1,0]\n"
    "v_cvt_pk_bf16_f32 v168, v48, v49\n"
    "v_cvt_pk_bf16_f32 v169, v50, v51\n"
    "v_cvt_pk_bf16_f32 v170, v52, v53\n"
    "v_cvt_pk_bf16_f32 v171, v54, v55\n"
    "v_lshlrev_b32 v48, 16, v176\n"
    "v_lshlrev_b32 v50, 16, v177\n"
    "v_lshlrev_b32 v52, 16, v178\n"
    "v_lshlrev_b32 v54, 16, v179\n"
    "v_and_b32 v49, 0xffff0000, v176\n"
    "v_and_b32 v51, 0xffff0000, v177\n"
    "v_and_b32 v53, 0xffff0000, v178\n"
    "v_and_b32 v55, 0xffff0000, v179\n"
    "v_pk_add_f32 v[48:49], v[48:49], v[40:41] op_sel_hi:[1,0]\n"
    "v_pk_add_f32 v[50:51], v[50:51], v[40:41] op_sel_hi:[1,0]\n"
    "v_pk_add_f32 v[52:53], v[52:53], v[40:41] op_sel_hi:[1,0]\n"
    "v_pk_add_f32 v[54:55], v[54:55], v[40:41] op_sel_hi:[1,0]\n"
    "v_cvt_pk_bf16_f32 v176, v48, v49\n"
    "v_cvt_pk_bf16_f32 v177, v50, v51\n"
    "v_cvt_pk_bf16_f32 v178, v52, v53\n"
    "v_cvt_pk_bf16_f32 v179, v54, v55\n"
    "v_lshlrev_b32 v48, 16, v184\n"
    "v_lshlrev_b32 v50, 16, v185\n"
    "v_lshlrev_b32 v52, 16, v186\n"
    "v_lshlrev_b32 v54, 16, v187\n"
    "v_and_b32 v49, 0xffff0000, v184\n"
    "v_and_b32 v51, 0xffff0000, v185\n"
    "v_and_b32 v53, 0xffff0000, v186\n"
    "v_and_b32 v55, 0xffff0000, v187\n"
    "v_pk_add_f32 v[48:49], v[48:49], v[40:41] op_sel_hi:[1,0]\n"
    "v_pk_add_f32 v[50:51], v[50:51], v[40:41] op_sel_hi:[1,0]\n"
    "v_pk_add_f32 v[52:53], v[52:53], v[40:41] op_sel_hi:[1,0]\n"
    "v_pk_add_f32 v[54:55], v[54:55], v[40:41] op_sel_hi:[1,0]\n"
    "v_cvt_pk_bf16_f32 v184, v48, v49\n"
    "v_cvt_pk_bf16_f32 v185, v50, v51\n"
    "v_cvt_pk_bf16_f32 v186, v52, v53\n"
    "v_cvt_pk_bf16_f32 v187, v54, v55\n"
    "v_lshlrev_b32 v48, 16, v192\n"
    "v_lshlrev_b32 v50, 16, v193\n"
    "v_lshlrev_b32 v52, 16, v194\n"
    "v_lshlrev_b32 v54, 16, v195\n"
    "v_and_b32 v49, 0xffff0000, v192\n"
    "v_and_b32 v51, 0xffff0000, v193\n"
    "v_and_b32 v53, 0xffff0000, v194\n"
    "v_and_b32 v55, 0xffff0000, v195\n"
    "v_pk_add_f32 v[48:49], v[48:49], v[40:41] op_sel_hi:[1,0]\n"
    "v_pk_add_f32 v[50:51], v[50:51], v[40:41] op_sel_hi:[1,0]\n"
    "v_pk_add_f32 v[52:53], v[52:53], v[40:41] op_sel_hi:[1,0]\n"
    "v_pk_add_f32 v[54:55], v[54:55], v[40:41] op_sel_hi:[1,0]\n"
    "v_cvt_pk_bf16_f32 v192, v48, v49\n"
    "v_cvt_pk_bf16_f32 v193, v50, v51\n"
    "v_cvt_pk_bf16_f32 v194, v52, v53\n"
    "v_cvt_pk_bf16_f32 v195, v54, v55\n"
    "v_lshlrev_b32 v48, 16, v200\n"
    "v_lshlrev_b32 v50, 16, v201\n"
    "v_lshlrev_b32 v52, 16, v202\n"
    "v_lshlrev_b32 v54, 16, v203\n"
    "v_and_b32 v49, 0xffff0000, v200\n"
    "v_and_b32 v51, 0xffff0000, v201\n"
    "v_and_b32 v53, 0xffff0000, v202\n"
    "v_and_b32 v55, 0xffff0000, v203\n"
    "v_pk_add_f32 v[48:49], v[48:49], v[40:41] op_sel_hi:[1,0]\n"
    "v_pk_add_f32 v[50:51], v[50:51], v[40:41] op_sel_hi:[1,0]\n"
    "v_pk_add_f32 v[52:53], v[52:53], v[40:41] op_sel_hi:[1,0]\n"
    "v_pk_add_f32 v[54:55], v[54:55], v[40:41] op_sel_hi:[1,0]\n"
    "v_cvt_pk_bf16_f32 v200, v48, v49\n"
    "v_cvt_pk_bf16_f32 v201, v50, v51\n"
    "v_cvt_pk_bf16_f32 v202, v52, v53\n"
    "v_cvt_pk_bf16_f32 v203, v54, v55\n"
    "v_lshlrev_b32 v48, 16, v208\n"
    "v_lshlrev_b32 v50, 16, v209\n"
    "v_lshlrev_b32 v52, 16, v210\n"
    "v_lshlrev_b32 v54, 16, v211\n"
    "v_and_b32 v49, 0xffff0000, v208\n"
    "v_and_b32 v51, 0xffff0000, v209\n"
    "v_and_b32 v53, 0xffff0000, v210\n"
    "v_and_b32 v55, 0xffff0000, v211\n"
    "v_pk_add_f32 v[48:49], v[48:49], v[40:41] op_sel_hi:[1,0]\n"
    "v_pk_add_f32 v[50:51], v[50:51], v[40:41] op_sel_hi:[1,0]\n"
    "v_pk_add_f32 v[52:53], v[52:53], v[40:41] op_sel_hi:[1,0]\n"
    "v_pk_add_f32 v[54:55], v[54:55], v[40:41] op_sel_hi:[1,0]\n"
    "v_cvt_pk_bf16_f32 v208, v48, v49\n"
    "v_cvt_pk_bf16_f32 v209, v50, v51\n"
    "v_cvt_pk_bf16_f32 v210, v52, v53\n"
    "v_cvt_pk_bf16_f32 v211, v54, v55\n"
    "v_lshlrev_b32 v48, 16, v216\n"
    "v_lshlrev_b32 v50, 16, v217\n"
    "v_lshlrev_b32 v52, 16, v218\n"
    "v_lshlrev_b32 v54, 16, v219\n"
    "v_and_b32 v49, 0xffff0000, v216\n"
    "v_and_b32 v51, 0xffff0000, v217\n"
    "v_and_b32 v53, 0xffff0000, v218\n"
    "v_and_b32 v55, 0xffff0000, v219\n"
    "v_pk_add_f32 v[48:49], v[48:49], v[40:41] op_sel_hi:[1,0]\n"
    "v_pk_add_f32 v[50:51], v[50:51], v[40:41] op_sel_hi:[1,0]\n"
    "v_pk_add_f32 v[52:53], v[52:53], v[40:41] op_sel_hi:[1,0]\n"
    "v_pk_add_f32 v[54:55], v[54:55], v[40:41] op_sel_hi:[1,0]\n"
    "v_cvt_pk_bf16_f32 v216, v48, v49\n"
    "v_cvt_pk_bf16_f32 v217, v50, v51\n"
    "v_cvt_pk_bf16_f32 v218, v52, v53\n"
    "v_cvt_pk_bf16_f32 v219, v54, v55\n"
    "v_lshlrev_b32 v48, 16, v224\n"
    "v_lshlrev_b32 v50, 16, v225\n"
    "v_lshlrev_b32 v52, 16, v226\n"
    "v_lshlrev_b32 v54, 16, v227\n"
    "v_and_b32 v49, 0xffff0000, v224\n"
    "v_and_b32 v51, 0xffff0000, v225\n"
    "v_and_b32 v53, 0xffff0000, v226\n"
    "v_and_b32 v55, 0xffff0000, v227\n"
    "v_pk_add_f32 v[48:49], v[48:49], v[40:41] op_sel_hi:[1,0]\n"
    "v_pk_add_f32 v[50:51], v[50:51], v[40:41] op_sel_hi:[1,0]\n"
    "v_pk_add_f32 v[52:53], v[52:53], v[40:41] op_sel_hi:[1,0]\n"
    "v_pk_add_f32 v[54:55], v[54:55], v[40:41] op_sel_hi:[1,0]\n"
    "v_cvt_pk_bf16_f32 v224, v48, v49\n"
    "v_cvt_pk_bf16_f32 v225, v50, v51\n"
    "v_cvt_pk_bf16_f32 v226, v52, v53\n"
    "v_cvt_pk_bf16_f32 v227, v54, v55\n"
    "v_lshlrev_b32 v48, 16, v232\n"
    "v_lshlrev_b32 v50, 16, v233\n"
    "v_lshlrev_b32 v52, 16, v234\n"
    "v_lshlrev_b32 v54, 16, v235\n"
    "v_and_b32 v49, 0xffff0000, v232\n"
    "v_and_b32 v51, 0xffff0000, v233\n"
    "v_and_b32 v53, 0xffff0000, v234\n"
    "v_and_b32 v55, 0xffff0000, v235\n"
    "v_pk_add_f32 v[48:49], v[48:49], v[40:41] op_sel_hi:[1,0]\n"
    "v_pk_add_f32 v[50:51], v[50:51], v[40:41] op_sel_hi:[1,0]\n"
    "v_pk_add_f32 v[52:53], v[52:53], v[40:41] op_sel_hi:[1,0]\n"
    "v_pk_add_f32 v[54:55], v[54:55], v[40:41] op_sel_hi:[1,0]\n"
    "v_cvt_pk_bf16_f32 v232, v48, v49\n"
    "v_cvt_pk_bf16_f32 v233, v50, v51\n"
    "v_cvt_pk_bf16_f32 v234, v52, v53\n"
    "v_cvt_pk_bf16_f32 v235, v54, v55\n"
    "s_waitcnt lgkmcnt(0)\n"
    "s_waitcnt vmcnt(21)\n"
    "ds_read_b128 v[132:135], v14 offset:14784\n"
    "ds_read_b128 v[140:143], v14 offset:15296\n"
    "s_waitcnt vmcnt(19)\n"
    "ds_read_b128 v[148:151], v14 offset:16896\n"
    "ds_read_b128 v[156:159], v14 offset:17408\n"
    "s_waitcnt lgkmcnt(2)\n"
    "v_mfma_f32_16x16x32_bf16 v[20:23], v[16:19], v[132:135], 0\n"
    "v_mfma_f32_16x16x32_bf16 v[28:31], v[132:135], v[132:135], 0\n"
    "v_mfma_f32_16x16x32_bf16 v[20:23], v[16:19], v[140:143], v[20:23]\n"
    "v_mfma_f32_16x16x32_bf16 v[28:31], v[140:143], v[140:143], v[28:31]\n"
    "s_waitcnt vmcnt(17)\n"
    "ds_read_b128 v[164:167], v14 offset:19008\n"
    "ds_read_b128 v[172:175], v14 offset:19520\n"
    "s_waitcnt lgkmcnt(2)\n"
    "v_mfma_f32_16x16x32_bf16 v[20:23], v[16:19], v[148:151], v[20:23]\n"
    "v_mfma_f32_16x16x32_bf16 v[28:31], v[148:151], v[148:151], v[28:31]\n"
    "v_mfma_f32_16x16x32_bf16 v[20:23], v[16:19], v[156:159], v[20:23]\n"
    "v_mfma_f32_16x16x32_bf16 v[28:31], v[156:159], v[156:159], v[28:31]\n"
    "s_waitcnt vmcnt(15)\n"
    "ds_read_b128 v[180:183], v14 offset:21120\n"
    "ds_read_b128 v[188:191], v14 offset:21632\n"
    "s_waitcnt lgkmcnt(2)\n"
    "v_mfma_f32_16x16x32_bf16 v[20:23], v[16:19], v[164:167], v[20:23]\n"
    "v_mfma_f32_16x16x32_bf16 v[28:31], v[164:167], v[164:167], v[28:31]\n"
    "v_mfma_f32_16x16x32_bf16 v[20:23], v[16:19], v[172:175], v[20:23]\n"
    "v_mfma_f32_16x16x32_bf16 v[28:31], v[172:175], v[172:175], v[28:31]\n"
    "s_waitcnt vmcnt(13)\n"
    "ds_read_b128 v[196:199], v14 offset:23232\n"
    "ds_read_b128 v[204:207], v14 offset:23744\n"
    "s_waitcnt lgkmcnt(2)\n"
    "v_mfma_f32_16x16x32_bf16 v[20:23], v[16:19], v[180:183], v[20:23]\n"
    "v_mfma_f32_16x16x32_bf16 v[28:31], v[180:183], v[180:183], v[28:31]\n"
    "v_mfma_f32_16x16x32_bf16 v[20:23], v[16:19], v[188:191], v[20:23]\n"
    "v_mfma_f32_16x16x32_bf16 v[28:31], v[188:191], v[188:191], v[28:31]\n"
    "s_waitcnt vmcnt(11)\n"
    "ds_read_b128 v[212:215], v14 offset:25344\n"
    "ds_read_b128 v[220:223], v14 offset:25856\n"
    "s_waitcnt lgkmcnt(2)\n"
    "v_mfma_f32_16x16x32_bf16 v[20:23], v[16:19], v[196:199], v[20:23]\n"
    "v_mfma_f32_16x16x32_bf16 v[28:31], v[196:199], v[196:199], v[28:31]\n"
    "v_mfma_f32_16x16x32_bf16 v[20:23], v[16:19], v[204:207], v[20:23]\n"
    "v_mfma_f32_16x16x32_bf16 v[28:31], v[204:207], v[204:207], v[28:31]\n"
    "s_waitcnt vmcnt(9)\n"
    "ds_read_b128 v[228:231], v14 offset:27456\n"
    "ds_read_b128 v[236:239], v14 offset:27968\n"
    "s_waitcnt lgkmcnt(2)\n"
    "v_mfma_f32_16x16x32_bf16 v[20:23], v[16:19], v[212:215], v[20:23]\n"
    "v_mfma_f32_16x16x32_bf16 v[28:31], v[212:215], v[212:215], v[28:31]\n"
    "v_mfma_f32_16x16x32_bf16 v[20:23], v[16:19], v[220:223], v[20:23]\n"
    "v_mfma_f32_16x16x32_bf16 v[28:31], v[220:223], v[220:223], v[28:31]\n"
    "s_waitcnt lgkmcnt(0)\n"
    "v_mfma_f32_16x16x32_bf16 v[20:23], v[16:19], v[228:231], v[20:23]\n"
    "v_mfma_f32_16x16x32_bf16 v[28:31], v[228:231], v[228:231], v[28:31]\n"
    "v_mfma_f32_16x16x32_bf16 v[20:23], v[16:19], v[236:239], v[20:23]\n"
    "v_mfma_f32_16x16x32_bf16 v[28:31], v[236:239], v[236:239], v[28:31]\n"
    "s_nop 6\n"
    "v_mul_f32 v119, 0x3b124925, v20\n"
    "v_cvt_pk_bf16_f32 v245, v119, v119\n"
    "v_and_b32 v245, 0xffff0000, v245\n"
    "v_sub_f32 v40, 0, v245\n"
    "v_cmp_eq_u32 vcc, 1, v13\n"
    "s_nop 1\n"
    "v_cndmask_b32 v28, v28, v29, vcc\n"
    "v_cmp_eq_u32 vcc, 2, v13\n"
    "s_nop 1\n"
    "v_cndmask_b32 v28, v28, v30, vcc\n"
    "v_cmp_eq_u32 vcc, 3, v13\n"
    "s_nop 1\n"
    "v_cndmask_b32 v28, v28, v31, vcc\n"
    "ds_bpermute_b32 v117, v12, v28\n"
    "v_lshlrev_b32 v48, 16, v132\n"
    "v_lshlrev_b32 v50, 16, v133\n"
    "v_lshlrev_b32 v52, 16, v134\n"
    "v_lshlrev_b32 v54, 16, v135\n"
    "v_and_b32 v49, 0xffff0000, v132\n"
    "v_and_b32 v51, 0xffff0000, v133\n"
    "v_and_b32 v53, 0xffff0000, v134\n"
    "v_and_b32 v55, 0xffff0000, v135\n"
    "v_pk_add_f32 v[48:49], v[48:49], v[40:41] op_sel_hi:[1,0]\n"
    "v_pk_add_f32 v[50:51], v[50:51], v[40:41] op_sel_hi:[1,0]\n"
    "v_pk_add_f32 v[52:53], v[52:53], v[40:41] op_sel_hi:[1,0]\n"
    "v_pk_add_f32 v[54:55], v[54:55], v[40:41] op_sel_hi:[1,0]\n"
    "v_cvt_pk_bf16_f32 v132, v48, v49\n"
    "v_cvt_pk_bf16_f32 v133, v50, v51\n"
    "v_cvt_pk_bf16_f32 v134, v52, v53\n"
    "v_cvt_pk_bf16_f32 v135, v54, v55\n"
    "v_lshlrev_b32 v48, 16, v140\n"
    "v_lshlrev_b32 v50, 16, v141\n"
    "v_lshlrev_b32 v52, 16, v142\n"
    "v_lshlrev_b32 v54, 16, v143\n"
    "v_and_b32 v49, 0xffff0000, v140\n"
    "v_and_b32 v51, 0xffff0000, v141\n"
    "v_and_b32 v53, 0xffff0000, v142\n"
    "v_and_b32 v55, 0xffff0000, v143\n"
    "v_pk_add_f32 v[48:49], v[48:49], v[40:41] op_sel_hi:[1,0]\n"
    "v_pk_add_f32 v[50:51], v[50:51], v[40:41] op_sel_hi:[1,0]\n"
    "v_pk_add_f32 v[52:53], v[52:53], v[40:41] op_sel_hi:[1,0]\n"
    "v_pk_add_f32 v[54:55], v[54:55], v[40:41] op_sel_hi:[1,0]\n"
    "v_cvt_pk_bf16_f32 v140, v48, v49\n"
    "v_cvt_pk_bf16_f32 v141, v50, v51\n"
    "v_cvt_pk_bf16_f32 v142, v52, v53\n"
    "v_cvt_pk_bf16_f32 v143, v54, v55\n"
    "s_waitcnt vmcnt(1)\n"
    "s_barrier\n"
    "v_add_u32 v247, 69632, v240\n"
    "ds_read_b64_tr_b16 a[0:1], v247 offset:0\n"
    "ds_read_b64_tr_b16 a[2:3], v247 offset:1088\n"
    "ds_read_b64_tr_b16 a[4:5], v247 offset:8704\n"
    "ds_read_b64_tr_b16 a[6:7], v247 offset:9792\n"
    "s_waitcnt lgkmcnt(0)\n"
    "v_mfma_f32_16x16x32_bf16 v[0:3], a[0:3], v[128:131], 0\n"
    "s_add_u32 m0, s99, 104448\n"
    "ds_read_b64_tr_b16 a[8:9], v247 offset:32\n"
    "buffer_load_dwordx4 v243, s[44:47], s88 offen lds\n"
    "v_mfma_f32_16x16x32_bf16 v[0:3], a[4:7], v[136:139], v[0:3]\n"
    "s_add_u32 s88, s88, 14336\n"
    "s_add_u32 m0, s99, 105536\n"
    "ds_read_b64_tr_b16 a[10:11], v247 offset:1120\n"
    "v_mfma_f32_16x16x32_bf16 v[4:7], a[0:3], v[132:135], 0\n"
    "ds_read_b64_tr_b16 a[12:13], v247 offset:8736\n"
    "ds_read_b64_tr_b16 a[14:15], v247 offset:9824\n"
    "v_mfma_f32_16x16x32_bf16 v[4:7], a[4:7], v[140:143], v[4:7]\n"
    "s_waitcnt lgkmcnt(0)\n"
    "buffer_load_dwordx4 v243, s[44:47], s88 offen lds\n"
    "s_add_u32 s88, s88, 14336\n"
    "s_add_u32 m0, s99, 106624\n"
    "v_mfma_f32_16x16x32_bf16 v[8:11], a[8:11], v[128:131], 0\n"
    "buffer_load_dwordx4 v243, s[44:47], s88 offen lds\n"
    "s_add_u32 s88, s88, 14336\n"
    "s_add_u32 m0, s99, 107712\n"
    "v_mfma_f32_16x16x32_bf16 v[8:11], a[12:15], v[136:139], v[8:11]\n"
    "ds_read_b64_tr_b16 a[16:17], v247 offset:64\n"
    "ds_read_b64_tr_b16 a[18:19], v247 offset:1152\n"
    "ds_read_b64_tr_b16 a[20:21], v247 offset:8768\n"
    "ds_read_b64_tr_b16 a[22:23], v247 offset:9856\n"
    "ds_read_b64_tr_b16 a[24:25], v247 offset:96\n"
    "ds_read_b64_tr_b16 a[26:27], v247 offset:1184\n"
    "v_mfma_f32_16x16x32_bf16 v[12:15], a[8:11], v[132:135], 0\n"
    "ds_read_b64_tr_b16 a[28:29], v247 offset:8800\n"
    "buffer_load_dwordx4 v243, s[44:47], s88 offen lds\n"
    "s_add_u32 s88, s88, 14336\n"
    "s_add_u32 m0, s99, 108800\n"
    "v_mfma_f32_16x16x32_bf16 v[12:15], a[12:15], v[140:143], v[12:15]\n"
    "s_waitcnt lgkmcnt(3)\n"
    "ds_read_b64_tr_b16 a[30:31], v247 offset:9888\n"
    "buffer_load_dwordx4 v243, s[44:47], s88 offen lds\n"
    "s_add_u32 s88, s88, 14336\n"
    "s_add_u32 m0, s99, 109888\n"
    "v_mfma_f32_16x16x32_bf16 v[16:19], a[16:19], v[128:131], 0\n"
    "ds_read_b64_tr_b16 a[32:33], v247 offset:128\n"
    "ds_read_b64_tr_b16 a[34:35], v247 offset:1216\n"
    "v_mfma_f32_16x16x32_bf16 v[16:19], a[20:23], v[136:139], v[16:19]\n"
    "buffer_load_dwordx4 v243, s[44:47], s88 offen lds\n"
    "s_add_u32 s88, s88, 14336\n"
    "s_add_u32 m0, s99, 110976\n"
    "v_mfma_f32_16x16x32_bf16 v[20:23], a[16:19], v[132:135], 0\n"
    "ds_read_b64_tr_b16 a[36:37], v247 offset:8832\n"
    "buffer_load_dwordx4 v243, s[44:47], s88 offen lds\n"
    "s_add_u32 s88, s88, 14336\n"
    "s_add_u32 m0, s99, 112064\n"
    "v_mfma_f32_16x16x32_bf16 v[20:23], a[20:23], v[140:143], v[20:23]\n"
    "s_waitcnt lgkmcnt(3)\n"
    "ds_read_b64_tr_b16 a[38:39], v247 offset:9920\n"
    "ds_read_b64_tr_b16 a[40:41], v247 offset:160\n"
    "v_mfma_f32_16x16x32_bf16 v[24:27], a[24:27], v[128:131], 0\n"
    "buffer_load_dwordx4 v243, s[44:47], s88 offen lds\n"
    "ds_read_b64_tr_b16 a[42:43], v247 offset:1248\n"
    "v_mfma_f32_16x16x32_bf16 v[24:27], a[28:31], v[136:139], v[24:27]\n"
    "s_add_u32 s88, s88, 129024\n"
    "ds_read_b64_tr_b16 a[44:45], v247 offset:8864\n"
    "ds_read_b64_tr_b16 a[46:47], v247 offset:9952\n"
    "v_mfma_f32_16x16x32_bf16 v[28:31], a[24:27], v[132:135], 0\n"
    "s_add_u32 m0, s99, 0\n"
    "ds_read_b64_tr_b16 a[48:49], v247 offset:192\n"
    "ds_read_b64_tr_b16 a[50:51], v247 offset:1280\n"
    "v_mfma_f32_16x16x32_bf16 v[28:31], a[28:31], v[140:143], v[28:31]\n"
    "s_waitcnt lgkmcnt(6)\n"
    "buffer_load_dwordx4 v243, s[44:47], s88 offen lds\n"
    "ds_read_b64_tr_b16 a[52:53], v247 offset:8896\n"
    "v_mfma_f32_16x16x32_bf16 v[32:35], a[32:35], v[128:131], 0\n"
    "s_add_u32 s88, s88, 14336\n"
    "ds_read_b64_tr_b16 a[54:55], v247 offset:9984\n"
    "ds_read_b64_tr_b16 a[56:57], v247 offset:17408\n"
    "v_mfma_f32_16x16x32_bf16 v[32:35], a[36:39], v[136:139], v[32:35]\n"
    "s_add_u32 m0, s99, 1088\n"
    "ds_read_b64_tr_b16 a[58:59], v247 offset:18496\n"
    "buffer_load_dwordx4 v243, s[44:47], s88 offen lds\n"
    "v_mfma_f32_16x16x32_bf16 v[36:39], a[32:35], v[132:135], 0\n"
    "s_add_u32 s88, s88, 14336\n"
    "ds_read_b64_tr_b16 a[60:61], v247 offset:26112\n"
    "ds_read_b64_tr_b16 a[62:63], v247 offset:27200\n"
    "v_mfma_f32_16x16x32_bf16 v[36:39], a[36:39], v[140:143], v[36:39]\n"
    "s_waitcnt lgkmcnt(8)\n"
    "ds_read_b64_tr_b16 a[0:1], v247 offset:17440\n"
    "ds_read_b64_tr_b16 a[2:3], v247 offset:18528\n"
    "v_mfma_f32_16x16x32_bf16 v[40:43], a[40:43], v[128:131], 0\n"
    "s_add_u32 m0, s99, 2176\n"
    "ds_read_b64_tr_b16 a[4:5], v247 offset:26144\n"
    "buffer_load_dwordx4 v243, s[44:47], s88 offen lds\n"
    "v_mfma_f32_16x16x32_bf16 v[40:43], a[44:47], v[136:139], v[40:43]\n"
    "s_add_u32 s88, s88, 14336\n"
    "ds_read_b64_tr_b16 a[6:7], v247 offset:27232\n"
    "v_lshlrev_b32 v112, 16, v148\n"
    "v_mfma_f32_16x16x32_bf16 v[44:47], a[40:43], v[132:135], 0\n"
    "s_add_u32 m0, s99, 3264\n"
    "ds_read_b64_tr_b16 a[8:9], v247 offset:17472\n"
    "buffer_load_dwordx4 v243, s[44:47], s88 offen lds\n"
    "v_mfma_f32_16x16x32_bf16 v[44:47], a[44:47], v[140:143], v[44:47]\n"
    "s_waitcnt lgkmcnt(9)\n"
    "ds_read_b64_tr_b16 a[10:11], v247 offset:18560\n"
    "v_and_b32 v113, 0xffff0000, v148\n"
    "v_mfma_f32_16x16x32_bf16 v[48:51], a[48:51], v[128:131], 0\n"
    "s_add_u32 s88, s88, 14336\n"
    "ds_read_b64_tr_b16 a[12:13], v247 offset:26176\n"
    "v_sub_f32 v112, v112, v245\n"
    "v_mfma_f32_16x16x32_bf16 v[48:51], a[52:55], v[136:139], v[48:51]\n"
    "ds_read_b64_tr_b16 a[14:15], v247 offset:27264\n"
    "v_sub_f32 v113, v113, v245\n"
    "v_mfma_f32_16x16x32_bf16 v[52:55], a[48:51], v[132:135], 0\n"
    "s_add_u32 m0, s99, 4352\n"
    "ds_read_b64_tr_b16 a[16:17], v247 offset:17504\n"
    "buffer_load_dwordx4 v243, s[44:47], s88 offen lds\n"
    "v_mfma_f32_16x16x32_bf16 v[52:55], a[52:55], v[140:143], v[52:55]\n"
    "s_waitcnt lgkmcnt(9)\n"
    "ds_read_b64_tr_b16 a[18:19], v247 offset:18592\n"
    "v_cvt_pk_bf16_f32 v148, v112, v113\n"
    "v_mfma_f32_16x16x32_bf16 v[56:59], a[56:59], v[128:131], 0\n"
    "s_add_u32 s88, s88, 14336\n"
    "ds_read_b64_tr_b16 a[20:21], v247 offset:26208\n"
    "v_lshlrev_b32 v114, 16, v149\n"
    "v_mfma_f32_16x16x32_bf16 v[56:59], a[60:63], v[136:139], v[56:59]\n"
    "ds_read_b64_tr_b16 a[22:23], v247 offset:27296\n"
    "v_and_b32 v115, 0xffff0000, v149\n"
    "v_mfma_f32_16x16x32_bf16 v[60:63], a[56:59], v[132:135], 0\n"
    "s_add_u32 m0, s99, 5440\n"
    "ds_read_b64_tr_b16 a[24:25], v247 offset:17536\n"
    "buffer_load_dwordx4 v243, s[44:47], s88 offen lds\n"
    "v_mfma_f32_16x16x32_bf16 v[60:63], a[60:63], v[140:143], v[60:63]\n"
    "s_waitcnt lgkmcnt(9)\n"
    "ds_read_b64_tr_b16 a[26:27], v247 offset:18624\n"
    "v_sub_f32 v114, v114, v245\n"
    "v_mfma_f32_16x16x32_bf16 v[64:67], a[0:3], v[128:131], 0\n"
    "s_add_u32 s88, s88, 14336\n"
    "ds_read_b64_tr_b16 a[28:29], v247 offset:26240\n"
    "v_sub_f32 v115, v115, v245\n"
    "v_mfma_f32_16x16x32_bf16 v[64:67], a[4:7], v[136:139], v[64:67]\n"
    "s_add_u32 m0, s99, 6528\n"
    "ds_read_b64_tr_b16 a[30:31], v247 offset:27328\n"
    "buffer_load_dwordx4 v243, s[44:47], s88 offen lds\n"
    "v_mfma_f32_16x16x32_bf16 v[68:71], a[0:3], v[132:135], 0\n"
    "s_add_u32 s88, s88, 14336\n"
    "ds_read_b64_tr_b16 a[32:33], v247 offset:17568\n"
    "v_cvt_pk_bf16_f32 v149, v114, v115\n"
    "v_mfma_f32_16x16x32_bf16 v[68:71], a[4:7], v[140:143], v[68:71]\n"
    "s_waitcnt lgkmcnt(9)\n"
    "ds_read_b64_tr_b16 a[34:35], v247 offset:18656\n"
    "v_lshlrev_b32 v112, 16, v150\n"
    "v_mfma_f32_16x16x32_bf16 v[72:75], a[8:11], v[128:131], 0\n"
    "ds_read_b64_tr_b16 a[36:37], v247 offset:26272\n"
    "v_and_b32 v113, 0xffff0000, v150\n"
    "v_mfma_f32_16x16x32_bf16 v[72:75], a[12:15], v[136:139], v[72:75]\n"
    "s_add_u32 m0, s99, 7616\n"
    "ds_read_b64_tr_b16 a[38:39], v247 offset:27360\n"
    "buffer_load_dwordx4 v243, s[44:47], s88 offen lds\n"
    "v_mfma_f32_16x16x32_bf16 v[76:79], a[8:11], v[132:135], 0\n"
    "s_add_u32 s88, s88, 129024\n"
    "ds_read_b64_tr_b16 a[40:41], v247 offset:17600\n"
    "v_sub_f32 v112, v112, v245\n"
    "v_mfma_f32_16x16x32_bf16 v[76:79], a[12:15], v[140:143], v[76:79]\n"
    "s_waitcnt lgkmcnt(9)\n"
    "ds_read_b64_tr_b16 a[42:43], v247 offset:18688\n"
    "v_sub_f32 v113, v113, v245\n"
    "v_mfma_f32_16x16x32_bf16 v[80:83], a[16:19], v[128:131], 0\n"
    "ds_read_b64_tr_b16 a[44:45], v247 offset:26304\n"
    "v_cvt_pk_bf16_f32 v150, v112, v113\n"
    "v_mfma_f32_16x16x32_bf16 v[80:83], a[20:23], v[136:139], v[80:83]\n"
    "s_add_u32 m0, s99, 34816\n"
    "ds_read_b64_tr_b16 a[46:47], v247 offset:27392\n"
    "buffer_load_dwordx4 v243, s[44:47], s88 offen lds\n"
    "v_mfma_f32_16x16x32_bf16 v[84:87], a[16:19], v[132:135], 0\n"
    "s_add_u32 s88, s88, 14336\n"
    "v_lshlrev_b32 v114, 16, v151\n"
    "v_and_b32 v115, 0xffff0000, v151\n"
    "v_mfma_f32_16x16x32_bf16 v[84:87], a[20:23], v[140:143], v[84:87]\n"
    "s_waitcnt lgkmcnt(8)\n"
    "v_sub_f32 v114, v114, v245\n"
    "v_sub_f32 v115, v115, v245\n"
    "v_mfma_f32_16x16x32_bf16 v[88:91], a[24:27], v[128:131], 0\n"
    "s_add_u32 m0, s99, 35904\n"
    "v_cvt_pk_bf16_f32 v151, v114, v115\n"
    "buffer_load_dwordx4 v243, s[44:47], s88 offen lds\n"
    "v_mfma_f32_16x16x32_bf16 v[88:91], a[28:31], v[136:139], v[88:91]\n"
    "s_waitcnt vmcnt(10)\n"
    "v_lshlrev_b32 v112, 16, v156\n"
    "v_and_b32 v113, 0xffff0000, v156\n"
    "v_mfma_f32_16x16x32_bf16 v[92:95], a[24:27], v[132:135], 0\n"
    "s_barrier\n"
    "v_add_u32 v247, 104448, v240\n"
    "ds_read_b64_tr_b16 a[48:49], v247 offset:0\n"
    "v_mfma_f32_16x16x32_bf16 v[92:95], a[28:31], v[140:143], v[92:95]\n"
    "s_waitcnt lgkmcnt(5)\n"
    "ds_read_b64_tr_b16 a[50:51], v247 offset:1088\n"
    "ds_read_b64_tr_b16 a[52:53], v247 offset:8704\n"
    "v_mfma_f32_16x16x32_bf16 v[96:99], a[32:35], v[128:131], 0\n"
    "s_add_u32 s88, s88, 14336\n"
    "ds_read_b64_tr_b16 a[54:55], v247 offset:9792\n"
    "ds_read_b64_tr_b16 a[56:57], v247 offset:32\n"
    "v_mfma_f32_16x16x32_bf16 v[96:99], a[36:39], v[136:139], v[96:99]\n"
    "s_add_u32 m0, s99, 36992\n"
    "ds_read_b64_tr_b16 a[58:59], v247 offset:1120\n"
    "buffer_load_dwordx4 v243, s[44:47], s88 offen lds\n"
    "v_mfma_f32_16x16x32_bf16 v[100:103], a[32:35], v[132:135], 0\n"
    "s_add_u32 s88, s88, 14336\n"
    "ds_read_b64_tr_b16 a[60:61], v247 offset:8736\n"
    "v_sub_f32 v112, v112, v245\n"
    "v_mfma_f32_16x16x32_bf16 v[100:103], a[36:39], v[140:143], v[100:103]\n"
    "s_waitcnt lgkmcnt(7)\n"
    "v_sub_f32 v113, v113, v245\n"
    "v_cvt_pk_bf16_f32 v156, v112, v113\n"
    "v_mfma_f32_16x16x32_bf16 v[104:107], a[40:43], v[128:131], 0\n"
    "s_add_u32 m0, s99, 38080\n"
    "v_lshlrev_b32 v114, 16, v157\n"
    "v_and_b32 v115, 0xffff0000, v157\n"
    "v_mfma_f32_16x16x32_bf16 v[104:107], a[44:47], v[136:139], v[104:107]\n"
    "v_sub_f32 v114, v114, v245\n"
    "v_sub_f32 v115, v115, v245\n"
    "v_mfma_f32_16x16x32_bf16 v[108:111], a[40:43], v[132:135], 0\n"
    "v_cvt_pk_bf16_f32 v157, v114, v115\n"
    "v_lshlrev_b32 v112, 16, v158\n"
    "v_and_b32 v113, 0xffff0000, v158\n"
    "v_sub_f32 v112, v112, v245\n"
    "v_sub_f32 v113, v113, v245\n"
    "v_cvt_pk_bf16_f32 v158, v112, v113\n"
    "v_lshlrev_b32 v114, 16, v159\n"
    "v_and_b32 v115, 0xffff0000, v159\n"
    "v_sub_f32 v114, v114, v245\n"
    "v_sub_f32 v115, v115, v245\n"
    "v_cvt_pk_bf16_f32 v159, v114, v115\n"
    "v_mfma_f32_16x16x32_bf16 v[108:111], a[44:47], v[140:143], v[108:111]\n"
    "s_waitcnt lgkmcnt(3)\n"
    "ds_read_b64_tr_b16 a[62:63], v247 offset:9824\n"
    "ds_read_b64_tr_b16 a[0:1], v247 offset:64\n"
    "v_mfma_f32_16x16x32_bf16 v[0:3], a[48:51], v[144:147], v[0:3]\n"
    "buffer_load_dwordx4 v243, s[44:47], s88 offen lds\n"
    "ds_read_b64_tr_b16 a[2:3], v247 offset:1152\n"
    "v_mfma_f32_16x16x32_bf16 v[0:3], a[52:55], v[152:155], v[0:3]\n"
    "s_add_u32 s88, s88, 14336\n"
    "ds_read_b64_tr_b16 a[4:5], v247 offset:8768\n"
    "ds_read_b64_tr_b16 a[6:7], v247 offset:9856\n"
    "v_mfma_f32_16x16x32_bf16 v[4:7], a[48:51], v[148:151], v[4:7]\n"
    "s_add_u32 m0, s99, 39168\n"
    "ds_read_b64_tr_b16 a[8:9], v247 offset:96\n"
    "buffer_load_dwordx4 v243, s[44:47], s88 offen lds\n"
    "v_mfma_f32_16x16x32_bf16 v[4:7], a[52:55], v[156:159], v[4:7]\n"
    "s_waitcnt lgkmcnt(5)\n"
    "ds_read_b64_tr_b16 a[10:11], v247 offset:1184\n"
    "ds_read_b64_tr_b16 a[12:13], v247 offset:8800\n"
    "v_mfma_f32_16x16x32_bf16 v[8:11], a[56:59], v[144:147], v[8:11]\n"
    "s_add_u32 s88, s88, 14336\n"
    "ds_read_b64_tr_b16 a[14:15], v247 offset:9888\n"
    "ds_read_b64_tr_b16 a[16:17], v247 offset:128\n"
    "v_mfma_f32_16x16x32_bf16 v[8:11], a[60:63], v[152:155], v[8:11]\n"
    "s_add_u32 m0, s99, 40256\n"
    "ds_read_b64_tr_b16 a[18:19], v247 offset:1216\n"
    "buffer_load_dwordx4 v243, s[44:47], s88 offen lds\n"
    "v_mfma_f32_16x16x32_bf16 v[12:15], a[56:59], v[148:151], v[12:15]\n"
    "s_add_u32 s88, s88, 14336\n"
    "ds_read_b64_tr_b16 a[20:21], v247 offset:8832\n"
    "ds_read_b64_tr_b16 a[22:23], v247 offset:9920\n"
    "v_mfma_f32_16x16x32_bf16 v[12:15], a[60:63], v[156:159], v[12:15]\n"
    "s_waitcnt lgkmcnt(8)\n"
    "ds_read_b64_tr_b16 a[24:25], v247 offset:160\n"
    "ds_read_b64_tr_b16 a[26:27], v247 offset:1248\n"
    "v_mfma_f32_16x16x32_bf16 v[16:19], a[0:3], v[144:147], v[16:19]\n"
    "s_add_u32 m0, s99, 41344\n"
    "ds_read_b64_tr_b16 a[28:29], v247 offset:8864\n"
    "buffer_load_dwordx4 v243, s[44:47], s88 offen lds\n"
    "v_mfma_f32_16x16x32_bf16 v[16:19], a[4:7], v[152:155], v[16:19]\n"
    "s_add_u32 s88, s88, 14336\n"
    "ds_read_b64_tr_b16 a[30:31], v247 offset:9952\n"
    "v_lshlrev_b32 v112, 16, v164\n"
    "v_mfma_f32_16x16x32_bf16 v[20:23], a[0:3], v[148:151], v[20:23]\n"
    "s_add_u32 m0, s99, 42432\n"
    "ds_read_b64_tr_b16 a[32:33], v247 offset:192\n"
    "buffer_load_dwordx4 v243, s[44:47], s88 offen lds\n"
    "v_mfma_f32_16x16x32_bf16 v[20:23], a[4:7], v[156:159], v[20:23]\n"
    "s_waitcnt lgkmcnt(9)\n"
    "ds_read_b64_tr_b16 a[34:35], v247 offset:1280\n"
    "v_and_b32 v113, 0xffff0000, v164\n"
    "v_mfma_f32_16x16x32_bf16 v[24:27], a[8:11], v[144:147], v[24:27]\n"
    "s_add_u32 s88, s88, 129024\n"
    "ds_read_b64_tr_b16 a[36:37], v247 offset:8896\n"
    "v_sub_f32 v112, v112, v245\n"
    "v_mfma_f32_16x16x32_bf16 v[24:27], a[12:15], v[152:155], v[24:27]\n"
    "ds_read_b64_tr_b16 a[38:39], v247 offset:9984\n"
    "v_sub_f32 v113, v113, v245\n"
    "v_mfma_f32_16x16x32_bf16 v[28:31], a[8:11], v[148:151], v[28:31]\n"
    "ds_read_b64_tr_b16 a[40:41], v247 offset:17408\n"
    "v_cvt_pk_bf16_f32 v164, v112, v113\n"
    "v_mfma_f32_16x16x32_bf16 v[28:31], a[12:15], v[156:159], v[28:31]\n"
    "s_waitcnt lgkmcnt(9)\n"
    "ds_read_b64_tr_b16 a[42:43], v247 offset:18496\n"
    "v_lshlrev_b32 v114, 16, v165\n"
    "v_mfma_f32_16x16x32_bf16 v[32:35], a[16:19], v[144:147], v[32:35]\n"
    "ds_read_b64_tr_b16 a[44:45], v247 offset:26112\n"
    "v_and_b32 v115, 0xffff0000, v165\n"
    "v_mfma_f32_16x16x32_bf16 v[32:35], a[20:23], v[152:155], v[32:35]\n"
    "ds_read_b64_tr_b16 a[46:47], v247 offset:27200\n"
    "v_sub_f32 v114, v114, v245\n"
    "v_mfma_f32_16x16x32_bf16 v[36:39], a[16:19], v[148:151], v[36:39]\n"
    "ds_read_b64_tr_b16 a[48:49], v247 offset:17440\n"
    "buffer_load_dword v124, off, s[64:67], 0 offset:64 sc1\n"
    "v_mfma_f32_16x16x32_bf16 v[36:39], a[20:23], v[156:159], v[36:39]\n"
    "s_waitcnt lgkmcnt(9)\n"
    "ds_read_b64_tr_b16 a[50:51], v247 offset:18528\n"
    "v_sub_f32 v115, v115, v245\n"
    "v_mfma_f32_16x16x32_bf16 v[40:43], a[24:27], v[144:147], v[40:43]\n"
    "ds_read_b64_tr_b16 a[52:53], v247 offset:26144\n"
    "buffer_load_dword v127, off, s[64:67], 0 sc1\n"
    "v_mfma_f32_16x16x32_bf16 v[40:43], a[28:31], v[152:155], v[40:43]\n"
    "ds_read_b64_tr_b16 a[54:55], v247 offset:27232\n"
    "v_cvt_pk_bf16_f32 v165, v114, v115\n"
    "v_mfma_f32_16x16x32_bf16 v[44:47], a[24:27], v[148:151], v[44:47]\n"
    "ds_read_b64_tr_b16 a[56:57], v247 offset:17472\n"
    "v_lshlrev_b32 v112, 16, v166\n"
    "v_mfma_f32_16x16x32_bf16 v[44:47], a[28:31], v[156:159], v[44:47]\n"
    "s_waitcnt lgkmcnt(9)\n"
    "ds_read_b64_tr_b16 a[58:59], v247 offset:18560\n"
    "v_and_b32 v113, 0xffff0000, v166\n"
    "v_mfma_f32_16x16x32_bf16 v[48:51], a[32:35], v[144:147], v[48:51]\n"
    "ds_read_b64_tr_b16 a[60:61], v247 offset:26176\n"
    "v_sub_f32 v112, v112, v245\n"
    "v_mfma_f32_16x16x32_bf16 v[48:51], a[36:39], v[152:155], v[48:51]\n"
    "ds_read_b64_tr_b16 a[62:63], v247 offset:27264\n"
    "v_sub_f32 v113, v113, v245\n"
    "v_mfma_f32_16x16x32_bf16 v[52:55], a[32:35], v[148:151], v[52:55]\n"
    "ds_read_b64_tr_b16 a[0:1], v247 offset:17504\n"
    "v_cvt_pk_bf16_f32 v166, v112, v113\n"
    "v_mfma_f32_16x16x32_bf16 v[52:55], a[36:39], v[156:159], v[52:55]\n"
    "s_waitcnt lgkmcnt(9)\n"
    "ds_read_b64_tr_b16 a[2:3], v247 offset:18592\n"
    "v_lshlrev_b32 v114, 16, v167\n"
    "v_mfma_f32_16x16x32_bf16 v[56:59], a[40:43], v[144:147], v[56:59]\n"
    "ds_read_b64_tr_b16 a[4:5], v247 offset:26208\n"
    "v_and_b32 v115, 0xffff0000, v167\n"
    "v_mfma_f32_16x16x32_bf16 v[56:59], a[44:47], v[152:155], v[56:59]\n"
    "ds_read_b64_tr_b16 a[6:7], v247 offset:27296\n"
    "v_sub_f32 v114, v114, v245\n"
    "v_mfma_f32_16x16x32_bf16 v[60:63], a[40:43], v[148:151], v[60:63]\n"
    "ds_read_b64_tr_b16 a[8:9], v247 offset:17536\n"
    "v_sub_f32 v115, v115, v245\n"
    "v_mfma_f32_16x16x32_bf16 v[60:63], a[44:47], v[156:159], v[60:63]\n"
    "s_waitcnt lgkmcnt(9)\n"
    "ds_read_b64_tr_b16 a[10:11], v247 offset:18624\n"
    "v_cvt_pk_bf16_f32 v167, v114, v115\n"
    "v_mfma_f32_16x16x32_bf16 v[64:67], a[48:51], v[144:147], v[64:67]\n"
    "ds_read_b64_tr_b16 a[12:13], v247 offset:26240\n"
    "v_lshlrev_b32 v112, 16, v172\n"
    "v_mfma_f32_16x16x32_bf16 v[64:67], a[52:55], v[152:155], v[64:67]\n"
    "ds_read_b64_tr_b16 a[14:15], v247 offset:27328\n"
    "v_and_b32 v113, 0xffff0000, v172\n"
    "v_mfma_f32_16x16x32_bf16 v[68:71], a[48:51], v[148:151], v[68:71]\n"
    "ds_read_b64_tr_b16 a[16:17], v247 offset:17568\n"
    "v_sub_f32 v112, v112, v245\n"
    "v_mfma_f32_16x16x32_bf16 v[68:71], a[52:55], v[156:159], v[68:71]\n"
    "s_waitcnt lgkmcnt(9)\n"
    "ds_read_b64_tr_b16 a[18:19], v247 offset:18656\n"
    "v_sub_f32 v113, v113, v245\n"
    "v_mfma_f32_16x16x32_bf16 v[72:75], a[56:59], v[144:147], v[72:75]\n"
    "ds_read_b64_tr_b16 a[20:21], v247 offset:26272\n"
    "v_cvt_pk_bf16_f32 v172, v112, v113\n"
    "v_mfma_f32_16x16x32_bf16 v[72:75], a[60:63], v[152:155], v[72:75]\n"
    "ds_read_b64_tr_b16 a[22:23], v247 offset:27360\n"
    "v_lshlrev_b32 v114, 16, v173\n"
    "v_mfma_f32_16x16x32_bf16 v[76:79], a[56:59], v[148:151], v[76:79]\n"
    "ds_read_b64_tr_b16 a[24:25], v247 offset:17600\n"
    "v_and_b32 v115, 0xffff0000, v173\n"
    "v_mfma_f32_16x16x32_bf16 v[76:79], a[60:63], v[156:159], v[76:79]\n"
    "s_waitcnt lgkmcnt(9)\n"
    "ds_read_b64_tr_b16 a[26:27], v247 offset:18688\n"
    "v_sub_f32 v114, v114, v245\n"
    "v_mfma_f32_16x16x32_bf16 v[80:83], a[0:3], v[144:147], v[80:83]\n"
    "ds_read_b64_tr_b16 a[28:29], v247 offset:26304\n"
    "v_sub_f32 v115, v115, v245\n"
    "v_mfma_f32_16x16x32_bf16 v[80:83], a[4:7], v[152:155], v[80:83]\n"
    "ds_read_b64_tr_b16 a[30:31], v247 offset:27392\n"
    "v_cvt_pk_bf16_f32 v173, v114, v115\n"
    "v_mfma_f32_16x16x32_bf16 v[84:87], a[0:3], v[148:151], v[84:87]\n"
    "s_waitcnt vmcnt(10)\n"
    "v_lshlrev_b32 v112, 16, v174\n"
    "v_and_b32 v113, 0xffff0000, v174\n"
    "v_mfma_f32_16x16x32_bf16 v[84:87], a[4:7], v[156:159], v[84:87]\n"
    "s_waitcnt lgkmcnt(8)\n"
    "v_sub_f32 v112, v112, v245\n"
    "v_sub_f32 v113, v113, v245\n"
    "v_mfma_f32_16x16x32_bf16 v[88:91], a[8:11], v[144:147], v[88:91]\n"
    "s_barrier\n"
    "v_add_u32 v247, 0, v240\n"
    "ds_read_b64_tr_b16 a[32:33], v247 offset:0\n"
    "v_mfma_f32_16x16x32_bf16 v[88:91], a[12:15], v[152:155], v[88:91]\n"
    "ds_read_b64_tr_b16 a[34:35], v247 offset:1088\n"
    "ds_read_b64_tr_b16 a[36:37], v247 offset:8704\n"
    "v_mfma_f32_16x16x32_bf16 v[92:95], a[8:11], v[148:151], v[92:95]\n"
    "s_add_u32 m0, s99, 69632\n"
    "ds_read_b64_tr_b16 a[38:39], v247 offset:9792\n"
    "ds_read_b64_tr_b16 a[40:41], v247 offset:32\n"
    "v_mfma_f32_16x16x32_bf16 v[92:95], a[12:15], v[156:159], v[92:95]\n"
    "s_waitcnt lgkmcnt(9)\n"
    "buffer_load_dwordx4 v243, s[44:47], s88 offen lds\n"
    "ds_read_b64_tr_b16 a[42:43], v247 offset:1120\n"
    "v_mfma_f32_16x16x32_bf16 v[96:99], a[16:19], v[144:147], v[96:99]\n"
    "s_add_u32 s88, s88, 14336\n"
    "ds_read_b64_tr_b16 a[44:45], v247 offset:8736\n"
    "v_cvt_pk_bf16_f32 v174, v112, v113\n"
    "v_mfma_f32_16x16x32_bf16 v[96:99], a[20:23], v[152:155], v[96:99]\n"
    "v_lshlrev_b32 v114, 16, v175\n"
    "v_and_b32 v115, 0xffff0000, v175\n"
    "v_mfma_f32_16x16x32_bf16 v[100:103], a[16:19], v[148:151], v[100:103]\n"
    "v_sub_f32 v114, v114, v245\n"
    "v_sub_f32 v115, v115, v245\n"
    "v_mfma_f32_16x16x32_bf16 v[100:103], a[20:23], v[156:159], v[100:103]\n"
    "s_waitcnt lgkmcnt(7)\n"
    "v_cvt_pk_bf16_f32 v175, v114, v115\n"
    "ds_read_b64_tr_b16 a[46:47], v247 offset:9824\n"
    "v_mfma_f32_16x16x32_bf16 v[104:107], a[24:27], v[144:147], v[104:107]\n"
    "s_add_u32 m0, s99, 70720\n"
    "ds_read_b64_tr_b16 a[48:49], v247 offset:64\n"
    "ds_read_b64_tr_b16 a[50:51], v247 offset:1152\n"
    "v_mfma_f32_16x16x32_bf16 v[104:107], a[28:31], v[152:155], v[104:107]\n"
    "buffer_load_dwordx4 v243, s[44:47], s88 offen lds\n"
    "ds_read_b64_tr_b16 a[52:53], v247 offset:8768\n"
    "v_mfma_f32_16x16x32_bf16 v[108:111], a[24:27], v[148:151], v[108:111]\n"
    "s_add_u32 s88, s88, 14336\n"
    "ds_read_b64_tr_b16 a[54:55], v247 offset:9856\n"
    "ds_read_b64_tr_b16 a[56:57], v247 offset:96\n"
    "v_mfma_f32_16x16x32_bf16 v[108:111], a[28:31], v[156:159], v[108:111]\n"
    "s_waitcnt lgkmcnt(9)\n"
    "ds_read_b64_tr_b16 a[58:59], v247 offset:1184\n"
    "v_mfma_f32_16x16x32_bf16 v[0:3], a[32:35], v[160:163], v[0:3]\n"
    "ds_read_b64_tr_b16 a[60:61], v247 offset:8800\n"
    "v_mfma_f32_16x16x32_bf16 v[0:3], a[36:39], v[168:171], v[0:3]\n"
    "s_add_u32 m0, s99, 71808\n"
    "ds_read_b64_tr_b16 a[62:63], v247 offset:9888\n"
    "buffer_load_dwordx4 v243, s[44:47], s88 offen lds\n"
    "v_mfma_f32_16x16x32_bf16 v[4:7], a[32:35], v[164:167], v[4:7]\n"
    "s_add_u32 s88, s88, 14336\n"
    "ds_read_b64_tr_b16 a[0:1], v247 offset:128\n"
    "v_mfma_f32_16x16x32_bf16 v[4:7], a[36:39], v[172:175], v[4:7]\n"
    "s_waitcnt lgkmcnt(9)\n"
    "ds_read_b64_tr_b16 a[2:3], v247 offset:1216\n"
    "v_mfma_f32_16x16x32_bf16 v[8:11], a[40:43], v[160:163], v[8:11]\n"
    "ds_read_b64_tr_b16 a[4:5], v247 offset:8832\n"
    "v_mfma_f32_16x16x32_bf16 v[8:11], a[44:47], v[168:171], v[8:11]\n"
    "ds_read_b64_tr_b16 a[6:7], v247 offset:9920\n"
    "v_mfma_f32_16x16x32_bf16 v[12:15], a[40:43], v[164:167], v[12:15]\n"
    "ds_read_b64_tr_b16 a[8:9], v247 offset:160\n"
    "v_mfma_f32_16x16x32_bf16 v[12:15], a[44:47], v[172:175], v[12:15]\n"
    "s_waitcnt lgkmcnt(9)\n"
    "ds_read_b64_tr_b16 a[10:11], v247 offset:1248\n"
    "v_mfma_f32_16x16x32_bf16 v[16:19], a[48:51], v[160:163], v[16:19]\n"
    "s_add_u32 m0, s99, 72896\n"
    "ds_read_b64_tr_b16 a[12:13], v247 offset:8864\n"
    "buffer_load_dwordx4 v243, s[44:47], s88 offen lds\n"
    "v_mfma_f32_16x16x32_bf16 v[16:19], a[52:55], v[168:171], v[16:19]\n"
    "s_add_u32 s88, s88, 14336\n"
    "ds_read_b64_tr_b16 a[14:15], v247 offset:9952\n"
    "v_lshlrev_b32 v112, 16, v180\n"
    "v_mfma_f32_16x16x32_bf16 v[20:23], a[48:51], v[164:167], v[20:23]\n"
    "ds_read_b64_tr_b16 a[16:17], v247 offset:192\n"
    "v_and_b32 v113, 0xffff0000, v180\n"
    "v_mfma_f32_16x16x32_bf16 v[20:23], a[52:55], v[172:175], v[20:23]\n"
    "s_waitcnt lgkmcnt(9)\n"
    "ds_read_b64_tr_b16 a[18:19], v247 offset:1280\n"
    "v_sub_f32 v112, v112, v245\n"
    "v_mfma_f32_16x16x32_bf16 v[24:27], a[56:59], v[160:163], v[24:27]\n"
    "ds_read_b64_tr_b16 a[20:21], v247 offset:8896\n"
    "v_sub_f32 v113, v113, v245\n"
    "v_mfma_f32_16x16x32_bf16 v[24:27], a[60:63], v[168:171], v[24:27]\n"
    "s_add_u32 m0, s99, 73984\n"
    "ds_read_b64_tr_b16 a[22:23], v247 offset:9984\n"
    "buffer_load_dwordx4 v243, s[44:47], s88 offen lds\n"
    "v_mfma_f32_16x16x32_bf16 v[28:31], a[56:59], v[164:167], v[28:31]\n"
    "s_add_u32 s88, s88, 14336\n"
    "ds_read_b64_tr_b16 a[24:25], v247 offset:17408\n"
    "v_cvt_pk_bf16_f32 v180, v112, v113\n"
    "v_mfma_f32_16x16x32_bf16 v[28:31], a[60:63], v[172:175], v[28:31]\n"
    "s_waitcnt lgkmcnt(9)\n"
    "ds_read_b64_tr_b16 a[26:27], v247 offset:18496\n"
    "v_lshlrev_b32 v114, 16, v181\n"
    "v_mfma_f32_16x16x32_bf16 v[32:35], a[0:3], v[160:163], v[32:35]\n"
    "ds_read_b64_tr_b16 a[28:29], v247 offset:26112\n"
    "v_and_b32 v115, 0xffff0000, v181\n"
    "v_mfma_f32_16x16x32_bf16 v[32:35], a[4:7], v[168:171], v[32:35]\n"
    "ds_read_b64_tr_b16 a[30:31], v247 offset:27200\n"
    "v_sub_f32 v114, v114, v245\n"
    "v_mfma_f32_16x16x32_bf16 v[36:39], a[0:3], v[164:167], v[36:39]\n"
    "s_add_u32 m0, s99, 75072\n"
    "ds_read_b64_tr_b16 a[32:33], v247 offset:17440\n"
    "buffer_load_dwordx4 v243, s[44:47], s88 offen lds\n"
    "v_mfma_f32_16x16x32_bf16 v[36:39], a[4:7], v[172:175], v[36:39]\n"
    "s_waitcnt lgkmcnt(9)\n"
    "ds_read_b64_tr_b16 a[34:35], v247 offset:18528\n"
    "v_sub_f32 v115, v115, v245\n"
    "v_mfma_f32_16x16x32_bf16 v[40:43], a[8:11], v[160:163], v[40:43]\n"
    "s_add_u32 s88, s88, 14336\n"
    "ds_read_b64_tr_b16 a[36:37], v247 offset:26144\n"
    "v_cvt_pk_bf16_f32 v181, v114, v115\n"
    "v_mfma_f32_16x16x32_bf16 v[40:43], a[12:15], v[168:171], v[40:43]\n"
    "ds_read_b64_tr_b16 a[38:39], v247 offset:27232\n"
    "v_lshlrev_b32 v112, 16, v182\n"
    "v_mfma_f32_16x16x32_bf16 v[44:47], a[8:11], v[164:167], v[44:47]\n"
    "ds_read_b64_tr_b16 a[40:41], v247 offset:17472\n"
    "v_and_b32 v113, 0xffff0000, v182\n"
    "v_mfma_f32_16x16x32_bf16 v[44:47], a[12:15], v[172:175], v[44:47]\n"
    "s_waitcnt lgkmcnt(9)\n"
    "ds_read_b64_tr_b16 a[42:43], v247 offset:18560\n"
    "v_sub_f32 v112, v112, v245\n"
    "v_mfma_f32_16x16x32_bf16 v[48:51], a[16:19], v[160:163], v[48:51]\n"
    "s_add_u32 m0, s99, 76160\n"
    "ds_read_b64_tr_b16 a[44:45], v247 offset:26176\n"
    "buffer_load_dwordx4 v243, s[44:47], s88 offen lds\n"
    "v_mfma_f32_16x16x32_bf16 v[48:51], a[20:23], v[168:171], v[48:51]\n"
    "s_add_u32 s88, s88, 14336\n"
    "ds_read_b64_tr_b16 a[46:47], v247 offset:27264\n"
    "v_sub_f32 v113, v113, v245\n"
    "v_mfma_f32_16x16x32_bf16 v[52:55], a[16:19], v[164:167], v[52:55]\n"
    "ds_read_b64_tr_b16 a[48:49], v247 offset:17504\n"
    "v_cvt_pk_bf16_f32 v182, v112, v113\n"
    "v_mfma_f32_16x16x32_bf16 v[52:55], a[20:23], v[172:175], v[52:55]\n"
    "s_waitcnt lgkmcnt(9)\n"
    "ds_read_b64_tr_b16 a[50:51], v247 offset:18592\n"
    "v_lshlrev_b32 v114, 16, v183\n"
    "v_mfma_f32_16x16x32_bf16 v[56:59], a[24:27], v[160:163], v[56:59]\n"
    "ds_read_b64_tr_b16 a[52:53], v247 offset:26208\n"
    "v_and_b32 v115, 0xffff0000, v183\n"
    "v_mfma_f32_16x16x32_bf16 v[56:59], a[28:31], v[168:171], v[56:59]\n"
    "ds_read_b64_tr_b16 a[54:55], v247 offset:27296\n"
    "v_sub_f32 v114, v114, v245\n"
    "v_mfma_f32_16x16x32_bf16 v[60:63], a[24:27], v[164:167], v[60:63]\n"
    "s_add_u32 m0, s99, 77248\n"
    "ds_read_b64_tr_b16 a[56:57], v247 offset:17536\n"
    "buffer_load_dwordx4 v243, s[44:47], s88 offen lds\n"
    "v_mfma_f32_16x16x32_bf16 v[60:63], a[28:31], v[172:175], v[60:63]\n"
    "s_waitcnt lgkmcnt(9)\n"
    "ds_read_b64_tr_b16 a[58:59], v247 offset:18624\n"
    "v_sub_f32 v115, v115, v245\n"
    "v_mfma_f32_16x16x32_bf16 v[64:67], a[32:35], v[160:163], v[64:67]\n"
    "s_add_u32 s88, s88, 129024\n"
    "ds_read_b64_tr_b16 a[60:61], v247 offset:26240\n"
    "v_cvt_pk_bf16_f32 v183, v114, v115\n"
    "v_mfma_f32_16x16x32_bf16 v[64:67], a[36:39], v[168:171], v[64:67]\n"
    "ds_read_b64_tr_b16 a[62:63], v247 offset:27328\n"
    "v_lshlrev_b32 v112, 16, v188\n"
    "v_mfma_f32_16x16x32_bf16 v[68:71], a[32:35], v[164:167], v[68:71]\n"
    "ds_read_b64_tr_b16 a[0:1], v247 offset:17568\n"
    "v_and_b32 v113, 0xffff0000, v188\n"
    "v_mfma_f32_16x16x32_bf16 v[68:71], a[36:39], v[172:175], v[68:71]\n"
    "s_waitcnt lgkmcnt(9)\n"
    "ds_read_b64_tr_b16 a[2:3], v247 offset:18656\n"
    "v_sub_f32 v112, v112, v245\n"
    "v_mfma_f32_16x16x32_bf16 v[72:75], a[40:43], v[160:163], v[72:75]\n"
    "ds_read_b64_tr_b16 a[4:5], v247 offset:26272\n"
    "v_sub_f32 v113, v113, v245\n"
    "v_mfma_f32_16x16x32_bf16 v[72:75], a[44:47], v[168:171], v[72:75]\n"
    "ds_read_b64_tr_b16 a[6:7], v247 offset:27360\n"
    "v_cvt_pk_bf16_f32 v188, v112, v113\n"
    "v_mfma_f32_16x16x32_bf16 v[76:79], a[40:43], v[164:167], v[76:79]\n"
    "ds_read_b64_tr_b16 a[8:9], v247 offset:17600\n"
    "v_lshlrev_b32 v114, 16, v189\n"
    "v_mfma_f32_16x16x32_bf16 v[76:79], a[44:47], v[172:175], v[76:79]\n"
    "s_waitcnt lgkmcnt(9)\n"
    "ds_read_b64_tr_b16 a[10:11], v247 offset:18688\n"
    "v_and_b32 v115, 0xffff0000, v189\n"
    "v_mfma_f32_16x16x32_bf16 v[80:83], a[48:51], v[160:163], v[80:83]\n"
    "ds_read_b64_tr_b16 a[12:13], v247 offset:26304\n"
    "v_sub_f32 v114, v114, v245\n"
    "v_mfma_f32_16x16x32_bf16 v[80:83], a[52:55], v[168:171], v[80:83]\n"
    "ds_read_b64_tr_b16 a[14:15], v247 offset:27392\n"
    "v_sub_f32 v115, v115, v245\n"
    "v_mfma_f32_16x16x32_bf16 v[84:87], a[48:51], v[164:167], v[84:87]\n"
    "s_waitcnt vmcnt(10)\n"
    "v_cvt_pk_bf16_f32 v189, v114, v115\n"
    "v_lshlrev_b32 v112, 16, v190\n"
    "v_mfma_f32_16x16x32_bf16 v[84:87], a[52:55], v[172:175], v[84:87]\n"
    "s_waitcnt lgkmcnt(8)\n"
    "v_and_b32 v113, 0xffff0000, v190\n"
    "v_sub_f32 v112, v112, v245\n"
    "v_mfma_f32_16x16x32_bf16 v[88:91], a[56:59], v[160:163], v[88:91]\n"
    "s_barrier\n"
    "v_add_u32 v247, 34816, v240\n"
    "ds_read_b64_tr_b16 a[16:17], v247 offset:0\n"
    "v_mfma_f32_16x16x32_bf16 v[88:91], a[60:63], v[168:171], v[88:91]\n"
    "ds_read_b64_tr_b16 a[18:19], v247 offset:1088\n"
    "ds_read_b64_tr_b16 a[20:21], v247 offset:8704\n"
    "v_mfma_f32_16x16x32_bf16 v[92:95], a[56:59], v[164:167], v[92:95]\n"
    "s_add_u32 m0, s99, 104448\n"
    "ds_read_b64_tr_b16 a[22:23], v247 offset:9792\n"
    "ds_read_b64_tr_b16 a[24:25], v247 offset:32\n"
    "v_mfma_f32_16x16x32_bf16 v[92:95], a[60:63], v[172:175], v[92:95]\n"
    "s_waitcnt lgkmcnt(9)\n"
    "buffer_load_dwordx4 v243, s[44:47], s88 offen lds\n"
    "ds_read_b64_tr_b16 a[26:27], v247 offset:1120\n"
    "v_mfma_f32_16x16x32_bf16 v[96:99], a[0:3], v[160:163], v[96:99]\n"
    "s_add_u32 s88, s88, 14336\n"
    "ds_read_b64_tr_b16 a[28:29], v247 offset:8736\n"
    "v_sub_f32 v113, v113, v245\n"
    "v_mfma_f32_16x16x32_bf16 v[96:99], a[4:7], v[168:171], v[96:99]\n"
    "v_cvt_pk_bf16_f32 v190, v112, v113\n"
    "v_lshlrev_b32 v114, 16, v191\n"
    "v_mfma_f32_16x16x32_bf16 v[100:103], a[0:3], v[164:167], v[100:103]\n"
    "v_and_b32 v115, 0xffff0000, v191\n"
    "v_sub_f32 v114, v114, v245\n"
    "v_mfma_f32_16x16x32_bf16 v[100:103], a[4:7], v[172:175], v[100:103]\n"
    "s_waitcnt lgkmcnt(7)\n"
    "v_sub_f32 v115, v115, v245\n"
    "v_cvt_pk_bf16_f32 v191, v114, v115\n"
    "v_mfma_f32_16x16x32_bf16 v[104:107], a[8:11], v[160:163], v[104:107]\n"
    "s_add_u32 m0, s99, 105536\n"
    "ds_read_b64_tr_b16 a[30:31], v247 offset:9824\n"
    "ds_read_b64_tr_b16 a[32:33], v247 offset:64\n"
    "v_mfma_f32_16x16x32_bf16 v[104:107], a[12:15], v[168:171], v[104:107]\n"
    "ds_read_b64_tr_b16 a[34:35], v247 offset:1152\n"
    "buffer_load_dwordx4 v243, s[44:47], s88 offen lds\n"
    "v_mfma_f32_16x16x32_bf16 v[108:111], a[8:11], v[164:167], v[108:111]\n"
    "s_add_u32 s88, s88, 14336\n"
    "ds_read_b64_tr_b16 a[36:37], v247 offset:8768\n"
    "ds_read_b64_tr_b16 a[38:39], v247 offset:9856\n"
    "v_mfma_f32_16x16x32_bf16 v[108:111], a[12:15], v[172:175], v[108:111]\n"
    "s_waitcnt lgkmcnt(8)\n"
    "ds_read_b64_tr_b16 a[40:41], v247 offset:96\n"
    "ds_read_b64_tr_b16 a[42:43], v247 offset:1184\n"
    "v_mfma_f32_16x16x32_bf16 v[0:3], a[16:19], v[176:179], v[0:3]\n"
    "ds_read_b64_tr_b16 a[44:45], v247 offset:8800\n"
    "v_mfma_f32_16x16x32_bf16 v[0:3], a[20:23], v[184:187], v[0:3]\n"
    "s_add_u32 m0, s99, 106624\n"
    "ds_read_b64_tr_b16 a[46:47], v247 offset:9888\n"
    "buffer_load_dwordx4 v243, s[44:47], s88 offen lds\n"
    "v_mfma_f32_16x16x32_bf16 v[4:7], a[16:19], v[180:183], v[4:7]\n"
    "s_add_u32 s88, s88, 14336\n"
    "ds_read_b64_tr_b16 a[48:49], v247 offset:128\n"
    "v_mfma_f32_16x16x32_bf16 v[4:7], a[20:23], v[188:191], v[4:7]\n"
    "s_waitcnt lgkmcnt(9)\n"
    "ds_read_b64_tr_b16 a[50:51], v247 offset:1216\n"
    "v_mfma_f32_16x16x32_bf16 v[8:11], a[24:27], v[176:179], v[8:11]\n"
    "ds_read_b64_tr_b16 a[52:53], v247 offset:8832\n"
    "v_mfma_f32_16x16x32_bf16 v[8:11], a[28:31], v[184:187], v[8:11]\n"
    "ds_read_b64_tr_b16 a[54:55], v247 offset:9920\n"
    "v_mfma_f32_16x16x32_bf16 v[12:15], a[24:27], v[180:183], v[12:15]\n"
    "ds_read_b64_tr_b16 a[56:57], v247 offset:160\n"
    "v_mfma_f32_16x16x32_bf16 v[12:15], a[28:31], v[188:191], v[12:15]\n"
    "s_waitcnt lgkmcnt(9)\n"
    "ds_read_b64_tr_b16 a[58:59], v247 offset:1248\n"
    "v_mfma_f32_16x16x32_bf16 v[16:19], a[32:35], v[176:179], v[16:19]\n"
    "s_add_u32 m0, s99, 107712\n"
    "ds_read_b64_tr_b16 a[60:61], v247 offset:8864\n"
    "buffer_load_dwordx4 v243, s[44:47], s88 offen lds\n"
    "v_mfma_f32_16x16x32_bf16 v[16:19], a[36:39], v[184:187], v[16:19]\n"
    "s_add_u32 s88, s88, 14336\n"
    "ds_read_b64_tr_b16 a[62:63], v247 offset:9952\n"
    "v_lshlrev_b32 v112, 16, v196\n"
    "v_mfma_f32_16x16x32_bf16 v[20:23], a[32:35], v[180:183], v[20:23]\n"
    "ds_read_b64_tr_b16 a[0:1], v247 offset:192\n"
    "v_and_b32 v113, 0xffff0000, v196\n"
    "v_mfma_f32_16x16x32_bf16 v[20:23], a[36:39], v[188:191], v[20:23]\n"
    "s_waitcnt lgkmcnt(9)\n"
    "ds_read_b64_tr_b16 a[2:3], v247 offset:1280\n"
    "v_sub_f32 v112, v112, v245\n"
    "v_mfma_f32_16x16x32_bf16 v[24:27], a[40:43], v[176:179], v[24:27]\n"
    "ds_read_b64_tr_b16 a[4:5], v247 offset:8896\n"
    "v_sub_f32 v113, v113, v245\n"
    "v_mfma_f32_16x16x32_bf16 v[24:27], a[44:47], v[184:187], v[24:27]\n"
    "s_add_u32 m0, s99, 108800\n"
    "ds_read_b64_tr_b16 a[6:7], v247 offset:9984\n"
    "buffer_load_dwordx4 v243, s[44:47], s88 offen lds\n"
    "v_mfma_f32_16x16x32_bf16 v[28:31], a[40:43], v[180:183], v[28:31]\n"
    "s_add_u32 s88, s88, 14336\n"
    "ds_read_b64_tr_b16 a[8:9], v247 offset:17408\n"
    "v_cvt_pk_bf16_f32 v196, v112, v113\n"
    "v_mfma_f32_16x16x32_bf16 v[28:31], a[44:47], v[188:191], v[28:31]\n"
    "s_waitcnt lgkmcnt(9)\n"
    "ds_read_b64_tr_b16 a[10:11], v247 offset:18496\n"
    "v_lshlrev_b32 v114, 16, v197\n"
    "v_mfma_f32_16x16x32_bf16 v[32:35], a[48:51], v[176:179], v[32:35]\n"
    "ds_read_b64_tr_b16 a[12:13], v247 offset:26112\n"
    "v_and_b32 v115, 0xffff0000, v197\n"
    "v_mfma_f32_16x16x32_bf16 v[32:35], a[52:55], v[184:187], v[32:35]\n"
    "ds_read_b64_tr_b16 a[14:15], v247 offset:27200\n"
    "v_sub_f32 v114, v114, v245\n"
    "v_mfma_f32_16x16x32_bf16 v[36:39], a[48:51], v[180:183], v[36:39]\n"
    "s_add_u32 m0, s99, 109888\n"
    "ds_read_b64_tr_b16 a[16:17], v247 offset:17440\n"
    "buffer_load_dwordx4 v243, s[44:47], s88 offen lds\n"
    "v_mfma_f32_16x16x32_bf16 v[36:39], a[52:55], v[188:191], v[36:39]\n"
    "s_waitcnt lgkmcnt(9)\n"
    "ds_read_b64_tr_b16 a[18:19], v247 offset:18528\n"
    "v_sub_f32 v115, v115, v245\n"
    "v_mfma_f32_16x16x32_bf16 v[40:43], a[56:59], v[176:179], v[40:43]\n"
    "s_add_u32 s88, s88, 14336\n"
    "ds_read_b64_tr_b16 a[20:21], v247 offset:26144\n"
    "v_cvt_pk_bf16_f32 v197, v114, v115\n"
    "v_mfma_f32_16x16x32_bf16 v[40:43], a[60:63], v[184:187], v[40:43]\n"
    "ds_read_b64_tr_b16 a[22:23], v247 offset:27232\n"
    "v_lshlrev_b32 v112, 16, v198\n"
    "v_mfma_f32_16x16x32_bf16 v[44:47], a[56:59], v[180:183], v[44:47]\n"
    "ds_read_b64_tr_b16 a[24:25], v247 offset:17472\n"
    "v_and_b32 v113, 0xffff0000, v198\n"
    "v_mfma_f32_16x16x32_bf16 v[44:47], a[60:63], v[188:191], v[44:47]\n"
    "s_waitcnt lgkmcnt(9)\n"
    "ds_read_b64_tr_b16 a[26:27], v247 offset:18560\n"
    "v_sub_f32 v112, v112, v245\n"
    "v_mfma_f32_16x16x32_bf16 v[48:51], a[0:3], v[176:179], v[48:51]\n"
    "s_add_u32 m0, s99, 110976\n"
    "ds_read_b64_tr_b16 a[28:29], v247 offset:26176\n"
    "buffer_load_dwordx4 v243, s[44:47], s88 offen lds\n"
    "v_mfma_f32_16x16x32_bf16 v[48:51], a[4:7], v[184:187], v[48:51]\n"
    "s_add_u32 s88, s88, 14336\n"
    "ds_read_b64_tr_b16 a[30:31], v247 offset:27264\n"
    "v_sub_f32 v113, v113, v245\n"
    "v_mfma_f32_16x16x32_bf16 v[52:55], a[0:3], v[180:183], v[52:55]\n"
    "ds_read_b64_tr_b16 a[32:33], v247 offset:17504\n"
    "v_cvt_pk_bf16_f32 v198, v112, v113\n"
    "v_mfma_f32_16x16x32_bf16 v[52:55], a[4:7], v[188:191], v[52:55]\n"
    "s_waitcnt lgkmcnt(9)\n"
    "ds_read_b64_tr_b16 a[34:35], v247 offset:18592\n"
    "v_lshlrev_b32 v114, 16, v199\n"
    "v_mfma_f32_16x16x32_bf16 v[56:59], a[8:11], v[176:179], v[56:59]\n"
    "ds_read_b64_tr_b16 a[36:37], v247 offset:26208\n"
    "v_and_b32 v115, 0xffff0000, v199\n"
    "v_mfma_f32_16x16x32_bf16 v[56:59], a[12:15], v[184:187], v[56:59]\n"
    "ds_read_b64_tr_b16 a[38:39], v247 offset:27296\n"
    "v_sub_f32 v114, v114, v245\n"
    "v_mfma_f32_16x16x32_bf16 v[60:63], a[8:11], v[180:183], v[60:63]\n"
    "s_add_u32 m0, s99, 112064\n"
    "ds_read_b64_tr_b16 a[40:41], v247 offset:17536\n"
    "buffer_load_dwordx4 v243, s[44:47], s88 offen lds\n"
    "v_mfma_f32_16x16x32_bf16 v[60:63], a[12:15], v[188:191], v[60:63]\n"
    "s_waitcnt lgkmcnt(9)\n"
    "ds_read_b64_tr_b16 a[42:43], v247 offset:18624\n"
    "v_sub_f32 v115, v115, v245\n"
    "v_mfma_f32_16x16x32_bf16 v[64:67], a[16:19], v[176:179], v[64:67]\n"
    "s_add_u32 s88, s88, 129024\n"
    "ds_read_b64_tr_b16 a[44:45], v247 offset:26240\n"
    "v_cvt_pk_bf16_f32 v199, v114, v115\n"
    "v_mfma_f32_16x16x32_bf16 v[64:67], a[20:23], v[184:187], v[64:67]\n"
    "ds_read_b64_tr_b16 a[46:47], v247 offset:27328\n"
    "v_lshlrev_b32 v112, 16, v204\n"
    "v_mfma_f32_16x16x32_bf16 v[68:71], a[16:19], v[180:183], v[68:71]\n"
    "ds_read_b64_tr_b16 a[48:49], v247 offset:17568\n"
    "v_and_b32 v113, 0xffff0000, v204\n"
    "v_mfma_f32_16x16x32_bf16 v[68:71], a[20:23], v[188:191], v[68:71]\n"
    "s_waitcnt lgkmcnt(9)\n"
    "ds_read_b64_tr_b16 a[50:51], v247 offset:18656\n"
    "v_sub_f32 v112, v112, v245\n"
    "v_mfma_f32_16x16x32_bf16 v[72:75], a[24:27], v[176:179], v[72:75]\n"
    "ds_read_b64_tr_b16 a[52:53], v247 offset:26272\n"
    "v_sub_f32 v113, v113, v245\n"
    "v_mfma_f32_16x16x32_bf16 v[72:75], a[28:31], v[184:187], v[72:75]\n"
    "ds_read_b64_tr_b16 a[54:55], v247 offset:27360\n"
    "v_cvt_pk_bf16_f32 v204, v112, v113\n"
    "v_mfma_f32_16x16x32_bf16 v[76:79], a[24:27], v[180:183], v[76:79]\n"
    "ds_read_b64_tr_b16 a[56:57], v247 offset:17600\n"
    "v_lshlrev_b32 v114, 16, v205\n"
    "v_mfma_f32_16x16x32_bf16 v[76:79], a[28:31], v[188:191], v[76:79]\n"
    "s_waitcnt lgkmcnt(9)\n"
    "ds_read_b64_tr_b16 a[58:59], v247 offset:18688\n"
    "v_and_b32 v115, 0xffff0000, v205\n"
    "v_mfma_f32_16x16x32_bf16 v[80:83], a[32:35], v[176:179], v[80:83]\n"
    "ds_read_b64_tr_b16 a[60:61], v247 offset:26304\n"
    "v_sub_f32 v114, v114, v245\n"
    "v_mfma_f32_16x16x32_bf16 v[80:83], a[36:39], v[184:187], v[80:83]\n"
    "ds_read_b64_tr_b16 a[62:63], v247 offset:27392\n"
    "v_sub_f32 v115, v115, v245\n"
    "v_mfma_f32_16x16x32_bf16 v[84:87], a[32:35], v[180:183], v[84:87]\n"
    "s_waitcnt vmcnt(8)\n"
    "v_cvt_pk_bf16_f32 v205, v114, v115\n"
    "v_lshlrev_b32 v112, 16, v206\n"
    "v_mfma_f32_16x16x32_bf16 v[84:87], a[36:39], v[188:191], v[84:87]\n"
    "s_waitcnt lgkmcnt(8)\n"
    "v_and_b32 v113, 0xffff0000, v206\n"
    "v_sub_f32 v112, v112, v245\n"
    "v_mfma_f32_16x16x32_bf16 v[88:91], a[40:43], v[176:179], v[88:91]\n"
    "s_barrier\n"
    "v_add_u32 v247, 69632, v240\n"
    "ds_read_b64_tr_b16 a[0:1], v247 offset:0\n"
    "v_mfma_f32_16x16x32_bf16 v[88:91], a[44:47], v[184:187], v[88:91]\n"
    "ds_read_b64_tr_b16 a[2:3], v247 offset:1088\n"
    "ds_read_b64_tr_b16 a[4:5], v247 offset:8704\n"
    "v_mfma_f32_16x16x32_bf16 v[92:95], a[40:43], v[180:183], v[92:95]\n"
    "s_add_u32 m0, s99, 0\n"
    "ds_read_b64_tr_b16 a[6:7], v247 offset:9792\n"
    "ds_read_b64_tr_b16 a[8:9], v247 offset:32\n"
    "v_mfma_f32_16x16x32_bf16 v[92:95], a[44:47], v[188:191], v[92:95]\n"
    "s_waitcnt lgkmcnt(9)\n"
    "buffer_load_dwordx4 v243, s[44:47], s88 offen lds\n"
    "ds_read_b64_tr_b16 a[10:11], v247 offset:1120\n"
    "v_mfma_f32_16x16x32_bf16 v[96:99], a[48:51], v[176:179], v[96:99]\n"
    "s_add_u32 s88, s88, 14336\n"
    "ds_read_b64_tr_b16 a[12:13], v247 offset:8736\n"
    "v_sub_f32 v113, v113, v245\n"
    "v_mfma_f32_16x16x32_bf16 v[96:99], a[52:55], v[184:187], v[96:99]\n"
    "v_cvt_pk_bf16_f32 v206, v112, v113\n"
    "v_lshlrev_b32 v114, 16, v207\n"
    "v_mfma_f32_16x16x32_bf16 v[100:103], a[48:51], v[180:183], v[100:103]\n"
    "v_and_b32 v115, 0xffff0000, v207\n"
    "v_sub_f32 v114, v114, v245\n"
    "v_mfma_f32_16x16x32_bf16 v[100:103], a[52:55], v[188:191], v[100:103]\n"
    "s_waitcnt lgkmcnt(7)\n"
    "v_sub_f32 v115, v115, v245\n"
    "v_cvt_pk_bf16_f32 v207, v114, v115\n"
    "v_mfma_f32_16x16x32_bf16 v[104:107], a[56:59], v[176:179], v[104:107]\n"
    "s_add_u32 m0, s99, 1088\n"
    "ds_read_b64_tr_b16 a[14:15], v247 offset:9824\n"
    "ds_read_b64_tr_b16 a[16:17], v247 offset:64\n"
    "v_mfma_f32_16x16x32_bf16 v[104:107], a[60:63], v[184:187], v[104:107]\n"
    "ds_read_b64_tr_b16 a[18:19], v247 offset:1152\n"
    "buffer_load_dwordx4 v243, s[44:47], s88 offen lds\n"
    "v_mfma_f32_16x16x32_bf16 v[108:111], a[56:59], v[180:183], v[108:111]\n"
    "s_add_u32 s88, s88, 14336\n"
    "ds_read_b64_tr_b16 a[20:21], v247 offset:8768\n"
    "ds_read_b64_tr_b16 a[22:23], v247 offset:9856\n"
    "v_mfma_f32_16x16x32_bf16 v[108:111], a[60:63], v[188:191], v[108:111]\n"
    "s_waitcnt lgkmcnt(8)\n"
    "ds_read_b64_tr_b16 a[24:25], v247 offset:96\n"
    "ds_read_b64_tr_b16 a[26:27], v247 offset:1184\n"
    "v_mfma_f32_16x16x32_bf16 v[0:3], a[0:3], v[192:195], v[0:3]\n"
    "ds_read_b64_tr_b16 a[28:29], v247 offset:8800\n"
    "v_mfma_f32_16x16x32_bf16 v[0:3], a[4:7], v[200:203], v[0:3]\n"
    "s_add_u32 m0, s99, 2176\n"
    "ds_read_b64_tr_b16 a[30:31], v247 offset:9888\n"
    "buffer_load_dwordx4 v243, s[44:47], s88 offen lds\n"
    "v_mfma_f32_16x16x32_bf16 v[4:7], a[0:3], v[196:199], v[4:7]\n"
    "s_add_u32 s88, s88, 14336\n"
    "ds_read_b64_tr_b16 a[32:33], v247 offset:128\n"
    "v_mfma_f32_16x16x32_bf16 v[4:7], a[4:7], v[204:207], v[4:7]\n"
    "s_waitcnt lgkmcnt(9)\n"
    "ds_read_b64_tr_b16 a[34:35], v247 offset:1216\n"
    "v_mfma_f32_16x16x32_bf16 v[8:11], a[8:11], v[192:195], v[8:11]\n"
    "ds_read_b64_tr_b16 a[36:37], v247 offset:8832\n"
    "v_mfma_f32_16x16x32_bf16 v[8:11], a[12:15], v[200:203], v[8:11]\n"
    "ds_read_b64_tr_b16 a[38:39], v247 offset:9920\n"
    "v_mfma_f32_16x16x32_bf16 v[12:15], a[8:11], v[196:199], v[12:15]\n"
    "ds_read_b64_tr_b16 a[40:41], v247 offset:160\n"
    "v_mfma_f32_16x16x32_bf16 v[12:15], a[12:15], v[204:207], v[12:15]\n"
    "s_waitcnt lgkmcnt(9)\n"
    "ds_read_b64_tr_b16 a[42:43], v247 offset:1248\n"
    "v_mfma_f32_16x16x32_bf16 v[16:19], a[16:19], v[192:195], v[16:19]\n"
    "s_add_u32 m0, s99, 3264\n"
    "ds_read_b64_tr_b16 a[44:45], v247 offset:8864\n"
    "buffer_load_dwordx4 v243, s[44:47], s88 offen lds\n"
    "v_mfma_f32_16x16x32_bf16 v[16:19], a[20:23], v[200:203], v[16:19]\n"
    "s_add_u32 s88, s88, 14336\n"
    "ds_read_b64_tr_b16 a[46:47], v247 offset:9952\n"
    "v_lshlrev_b32 v112, 16, v212\n"
    "v_mfma_f32_16x16x32_bf16 v[20:23], a[16:19], v[196:199], v[20:23]\n"
    "ds_read_b64_tr_b16 a[48:49], v247 offset:192\n"
    "v_and_b32 v113, 0xffff0000, v212\n"
    "v_mfma_f32_16x16x32_bf16 v[20:23], a[20:23], v[204:207], v[20:23]\n"
    "s_waitcnt lgkmcnt(9)\n"
    "ds_read_b64_tr_b16 a[50:51], v247 offset:1280\n"
    "v_sub_f32 v112, v112, v245\n"
    "v_mfma_f32_16x16x32_bf16 v[24:27], a[24:27], v[192:195], v[24:27]\n"
    "ds_read_b64_tr_b16 a[52:53], v247 offset:8896\n"
    "v_sub_f32 v113, v113, v245\n"
    "v_mfma_f32_16x16x32_bf16 v[24:27], a[28:31], v[200:203], v[24:27]\n"
    "s_add_u32 m0, s99, 4352\n"
    "ds_read_b64_tr_b16 a[54:55], v247 offset:9984\n"
    "buffer_load_dwordx4 v243, s[44:47], s88 offen lds\n"
    "v_mfma_f32_16x16x32_bf16 v[28:31], a[24:27], v[196:199], v[28:31]\n"
    "s_add_u32 s88, s88, 14336\n"
    "ds_read_b64_tr_b16 a[56:57], v247 offset:17408\n"
    "v_cvt_pk_bf16_f32 v212, v112, v113\n"
    "v_mfma_f32_16x16x32_bf16 v[28:31], a[28:31], v[204:207], v[28:31]\n"
    "s_waitcnt lgkmcnt(9)\n"
    "ds_read_b64_tr_b16 a[58:59], v247 offset:18496\n"
    "v_lshlrev_b32 v114, 16, v213\n"
    "v_mfma_f32_16x16x32_bf16 v[32:35], a[32:35], v[192:195], v[32:35]\n"
    "ds_read_b64_tr_b16 a[60:61], v247 offset:26112\n"
    "v_and_b32 v115, 0xffff0000, v213\n"
    "v_mfma_f32_16x16x32_bf16 v[32:35], a[36:39], v[200:203], v[32:35]\n"
    "ds_read_b64_tr_b16 a[62:63], v247 offset:27200\n"
    "v_sub_f32 v114, v114, v245\n"
    "v_mfma_f32_16x16x32_bf16 v[36:39], a[32:35], v[196:199], v[36:39]\n"
    "s_add_u32 m0, s99, 5440\n"
    "ds_read_b64_tr_b16 a[0:1], v247 offset:17440\n"
    "buffer_load_dwordx4 v243, s[44:47], s88 offen lds\n"
    "v_mfma_f32_16x16x32_bf16 v[36:39], a[36:39], v[204:207], v[36:39]\n"
    "s_waitcnt lgkmcnt(9)\n"
    "ds_read_b64_tr_b16 a[2:3], v247 offset:18528\n"
    "v_sub_f32 v115, v115, v245\n"
    "v_mfma_f32_16x16x32_bf16 v[40:43], a[40:43], v[192:195], v[40:43]\n"
    "s_add_u32 s88, s88, 14336\n"
    "ds_read_b64_tr_b16 a[4:5], v247 offset:26144\n"
    "v_cvt_pk_bf16_f32 v213, v114, v115\n"
    "v_mfma_f32_16x16x32_bf16 v[40:43], a[44:47], v[200:203], v[40:43]\n"
    "ds_read_b64_tr_b16 a[6:7], v247 offset:27232\n"
    "v_lshlrev_b32 v112, 16, v214\n"
    "v_mfma_f32_16x16x32_bf16 v[44:47], a[40:43], v[196:199], v[44:47]\n"
    "ds_read_b64_tr_b16 a[8:9], v247 offset:17472\n"
    "v_and_b32 v113, 0xffff0000, v214\n"
    "v_mfma_f32_16x16x32_bf16 v[44:47], a[44:47], v[204:207], v[44:47]\n"
    "s_waitcnt lgkmcnt(9)\n"
    "ds_read_b64_tr_b16 a[10:11], v247 offset:18560\n"
    "v_sub_f32 v112, v112, v245\n"
    "v_mfma_f32_16x16x32_bf16 v[48:51], a[48:51], v[192:195], v[48:51]\n"
    "s_add_u32 m0, s99, 6528\n"
    "ds_read_b64_tr_b16 a[12:13], v247 offset:26176\n"
    "buffer_load_dwordx4 v243, s[44:47], s88 offen lds\n"
    "v_mfma_f32_16x16x32_bf16 v[48:51], a[52:55], v[200:203], v[48:51]\n"
    "s_add_u32 s88, s88, 14336\n"
    "ds_read_b64_tr_b16 a[14:15], v247 offset:27264\n"
    "v_sub_f32 v113, v113, v245\n"
    "v_mfma_f32_16x16x32_bf16 v[52:55], a[48:51], v[196:199], v[52:55]\n"
    "ds_read_b64_tr_b16 a[16:17], v247 offset:17504\n"
    "v_cvt_pk_bf16_f32 v214, v112, v113\n"
    "v_mfma_f32_16x16x32_bf16 v[52:55], a[52:55], v[204:207], v[52:55]\n"
    "s_waitcnt lgkmcnt(9)\n"
    "ds_read_b64_tr_b16 a[18:19], v247 offset:18592\n"
    "v_lshlrev_b32 v114, 16, v215\n"
    "v_mfma_f32_16x16x32_bf16 v[56:59], a[56:59], v[192:195], v[56:59]\n"
    "ds_read_b64_tr_b16 a[20:21], v247 offset:26208\n"
    "v_and_b32 v115, 0xffff0000, v215\n"
    "v_mfma_f32_16x16x32_bf16 v[56:59], a[60:63], v[200:203], v[56:59]\n"
    "ds_read_b64_tr_b16 a[22:23], v247 offset:27296\n"
    "v_sub_f32 v114, v114, v245\n"
    "v_mfma_f32_16x16x32_bf16 v[60:63], a[56:59], v[196:199], v[60:63]\n"
    "s_add_u32 m0, s99, 7616\n"
    "ds_read_b64_tr_b16 a[24:25], v247 offset:17536\n"
    "buffer_load_dwordx4 v243, s[44:47], s88 offen lds\n"
    "v_mfma_f32_16x16x32_bf16 v[60:63], a[60:63], v[204:207], v[60:63]\n"
    "s_waitcnt lgkmcnt(9)\n"
    "ds_read_b64_tr_b16 a[26:27], v247 offset:18624\n"
    "v_sub_f32 v115, v115, v245\n"
    "v_mfma_f32_16x16x32_bf16 v[64:67], a[0:3], v[192:195], v[64:67]\n"
    "s_add_u32 s88, s88, 129024\n"
    "ds_read_b64_tr_b16 a[28:29], v247 offset:26240\n"
    "v_cvt_pk_bf16_f32 v215, v114, v115\n"
    "v_mfma_f32_16x16x32_bf16 v[64:67], a[4:7], v[200:203], v[64:67]\n"
    "ds_read_b64_tr_b16 a[30:31], v247 offset:27328\n"
    "v_lshlrev_b32 v112, 16, v220\n"
    "v_mfma_f32_16x16x32_bf16 v[68:71], a[0:3], v[196:199], v[68:71]\n"
    "ds_read_b64_tr_b16 a[32:33], v247 offset:17568\n"
    "v_and_b32 v113, 0xffff0000, v220\n"
    "v_mfma_f32_16x16x32_bf16 v[68:71], a[4:7], v[204:207], v[68:71]\n"
    "s_waitcnt lgkmcnt(9)\n"
    "ds_read_b64_tr_b16 a[34:35], v247 offset:18656\n"
    "v_sub_f32 v112, v112, v245\n"
    "v_mfma_f32_16x16x32_bf16 v[72:75], a[8:11], v[192:195], v[72:75]\n"
    "ds_read_b64_tr_b16 a[36:37], v247 offset:26272\n"
    "v_sub_f32 v113, v113, v245\n"
    "v_mfma_f32_16x16x32_bf16 v[72:75], a[12:15], v[200:203], v[72:75]\n"
    "ds_read_b64_tr_b16 a[38:39], v247 offset:27360\n"
    "v_cvt_pk_bf16_f32 v220, v112, v113\n"
    "v_mfma_f32_16x16x32_bf16 v[76:79], a[8:11], v[196:199], v[76:79]\n"
    "ds_read_b64_tr_b16 a[40:41], v247 offset:17600\n"
    "v_lshlrev_b32 v114, 16, v221\n"
    "v_mfma_f32_16x16x32_bf16 v[76:79], a[12:15], v[204:207], v[76:79]\n"
    "s_waitcnt lgkmcnt(9)\n"
    "ds_read_b64_tr_b16 a[42:43], v247 offset:18688\n"
    "v_and_b32 v115, 0xffff0000, v221\n"
    "v_mfma_f32_16x16x32_bf16 v[80:83], a[16:19], v[192:195], v[80:83]\n"
    "ds_read_b64_tr_b16 a[44:45], v247 offset:26304\n"
    "v_sub_f32 v114, v114, v245\n"
    "v_mfma_f32_16x16x32_bf16 v[80:83], a[20:23], v[200:203], v[80:83]\n"
    "ds_read_b64_tr_b16 a[46:47], v247 offset:27392\n"
    "v_sub_f32 v115, v115, v245\n"
    "v_mfma_f32_16x16x32_bf16 v[84:87], a[16:19], v[196:199], v[84:87]\n"
    "s_waitcnt vmcnt(8)\n"
    "v_cvt_pk_bf16_f32 v221, v114, v115\n"
    "v_lshlrev_b32 v112, 16, v222\n"
    "v_mfma_f32_16x16x32_bf16 v[84:87], a[20:23], v[204:207], v[84:87]\n"
    "s_waitcnt lgkmcnt(8)\n"
    "v_and_b32 v113, 0xffff0000, v222\n"
    "v_sub_f32 v112, v112, v245\n"
    "v_mfma_f32_16x16x32_bf16 v[88:91], a[24:27], v[192:195], v[88:91]\n"
    "s_barrier\n"
    "v_add_u32 v247, 104448, v240\n"
    "ds_read_b64_tr_b16 a[48:49], v247 offset:0\n"
    "v_mfma_f32_16x16x32_bf16 v[88:91], a[28:31], v[200:203], v[88:91]\n"
    "ds_read_b64_tr_b16 a[50:51], v247 offset:1088\n"
    "ds_read_b64_tr_b16 a[52:53], v247 offset:8704\n"
    "v_mfma_f32_16x16x32_bf16 v[92:95], a[24:27], v[196:199], v[92:95]\n"
    "s_add_u32 m0, s87, 34816\n"
    "ds_read_b64_tr_b16 a[54:55], v247 offset:9792\n"
    "ds_read_b64_tr_b16 a[56:57], v247 offset:32\n"
    "v_mfma_f32_16x16x32_bf16 v[92:95], a[28:31], v[204:207], v[92:95]\n"
    "s_waitcnt lgkmcnt(9)\n"
    "buffer_load_dwordx4 v244, s[52:55], s29 offen offset:0 lds\n"
    "ds_read_b64_tr_b16 a[58:59], v247 offset:1120\n"
    "v_mfma_f32_16x16x32_bf16 v[96:99], a[32:35], v[192:195], v[96:99]\n"
    "ds_read_b64_tr_b16 a[60:61], v247 offset:8736\n"
    "v_sub_f32 v113, v113, v245\n"
    "v_mfma_f32_16x16x32_bf16 v[96:99], a[36:39], v[200:203], v[96:99]\n"
    "v_cvt_pk_bf16_f32 v222, v112, v113\n"
    "v_lshlrev_b32 v114, 16, v223\n"
    "v_mfma_f32_16x16x32_bf16 v[100:103], a[32:35], v[196:199], v[100:103]\n"
    "v_and_b32 v115, 0xffff0000, v223\n"
    "v_sub_f32 v114, v114, v245\n"
    "v_mfma_f32_16x16x32_bf16 v[100:103], a[36:39], v[204:207], v[100:103]\n"
    "s_waitcnt lgkmcnt(7)\n"
    "v_sub_f32 v115, v115, v245\n"
    "v_cvt_pk_bf16_f32 v223, v114, v115\n"
    "v_mfma_f32_16x16x32_bf16 v[104:107], a[40:43], v[192:195], v[104:107]\n"
    "s_add_u32 m0, s87, 38816\n"
    "ds_read_b64_tr_b16 a[62:63], v247 offset:9824\n"
    "ds_read_b64_tr_b16 a[0:1], v247 offset:64\n"
    "v_mfma_f32_16x16x32_bf16 v[104:107], a[44:47], v[200:203], v[104:107]\n"
    "ds_read_b64_tr_b16 a[2:3], v247 offset:1152\n"
    "buffer_load_dwordx4 v244, s[52:55], s29 offen offset:128 lds\n"
    "v_mfma_f32_16x16x32_bf16 v[108:111], a[40:43], v[196:199], v[108:111]\n"
    "ds_read_b64_tr_b16 a[4:5], v247 offset:8768\n"
    "ds_read_b64_tr_b16 a[6:7], v247 offset:9856\n"
    "v_mfma_f32_16x16x32_bf16 v[108:111], a[44:47], v[204:207], v[108:111]\n"
    "s_waitcnt lgkmcnt(8)\n"
    "ds_read_b64_tr_b16 a[8:9], v247 offset:96\n"
    "ds_read_b64_tr_b16 a[10:11], v247 offset:1184\n"
    "v_mfma_f32_16x16x32_bf16 v[0:3], a[48:51], v[208:211], v[0:3]\n"
    "ds_read_b64_tr_b16 a[12:13], v247 offset:8800\n"
    "v_mfma_f32_16x16x32_bf16 v[0:3], a[52:55], v[216:219], v[0:3]\n"
    "s_add_u32 m0, s87, 42816\n"
    "ds_read_b64_tr_b16 a[14:15], v247 offset:9888\n"
    "buffer_load_dwordx4 v244, s[52:55], s29 offen offset:256 lds\n"
    "v_mfma_f32_16x16x32_bf16 v[4:7], a[48:51], v[212:215], v[4:7]\n"
    "ds_read_b64_tr_b16 a[16:17], v247 offset:128\n"
    "v_mfma_f32_16x16x32_bf16 v[4:7], a[52:55], v[220:223], v[4:7]\n"
    "s_waitcnt lgkmcnt(9)\n"
    "ds_read_b64_tr_b16 a[18:19], v247 offset:1216\n"
    "v_mfma_f32_16x16x32_bf16 v[8:11], a[56:59], v[208:211], v[8:11]\n"
    "ds_read_b64_tr_b16 a[20:21], v247 offset:8832\n"
    "v_mfma_f32_16x16x32_bf16 v[8:11], a[60:63], v[216:219], v[8:11]\n"
    "ds_read_b64_tr_b16 a[22:23], v247 offset:9920\n"
    "v_mfma_f32_16x16x32_bf16 v[12:15], a[56:59], v[212:215], v[12:15]\n"
    "ds_read_b64_tr_b16 a[24:25], v247 offset:160\n"
    "v_mfma_f32_16x16x32_bf16 v[12:15], a[60:63], v[220:223], v[12:15]\n"
    "s_waitcnt lgkmcnt(9)\n"
    "ds_read_b64_tr_b16 a[26:27], v247 offset:1248\n"
    "v_mfma_f32_16x16x32_bf16 v[16:19], a[0:3], v[208:211], v[16:19]\n"
    "s_add_u32 m0, s87, 46816\n"
    "ds_read_b64_tr_b16 a[28:29], v247 offset:8864\n"
    "buffer_load_dwordx4 v244, s[52:55], s29 offen offset:384 lds\n"
    "v_mfma_f32_16x16x32_bf16 v[16:19], a[4:7], v[216:219], v[16:19]\n"
    "s_add_u32 s29, s29, 262144\n"
    "ds_read_b64_tr_b16 a[30:31], v247 offset:9952\n"
    "v_lshlrev_b32 v112, 16, v228\n"
    "v_mfma_f32_16x16x32_bf16 v[20:23], a[0:3], v[212:215], v[20:23]\n"
    "ds_read_b64_tr_b16 a[32:33], v247 offset:192\n"
    "v_and_b32 v113, 0xffff0000, v228\n"
    "v_mfma_f32_16x16x32_bf16 v[20:23], a[4:7], v[220:223], v[20:23]\n"
    "s_waitcnt lgkmcnt(9)\n"
    "ds_read_b64_tr_b16 a[34:35], v247 offset:1280\n"
    "v_sub_f32 v112, v112, v245\n"
    "v_mfma_f32_16x16x32_bf16 v[24:27], a[8:11], v[208:211], v[24:27]\n"
    "ds_read_b64_tr_b16 a[36:37], v247 offset:8896\n"
    "v_sub_f32 v113, v113, v245\n"
    "v_mfma_f32_16x16x32_bf16 v[24:27], a[12:15], v[216:219], v[24:27]\n"
    "s_add_u32 m0, s87, 51328\n"
    "ds_read_b64_tr_b16 a[38:39], v247 offset:9984\n"
    "buffer_load_dwordx4 v244, s[52:55], s29 offen offset:0 lds\n"
    "v_mfma_f32_16x16x32_bf16 v[28:31], a[8:11], v[212:215], v[28:31]\n"
    "ds_read_b64_tr_b16 a[40:41], v247 offset:17408\n"
    "v_cvt_pk_bf16_f32 v228, v112, v113\n"
    "v_mfma_f32_16x16x32_bf16 v[28:31], a[12:15], v[220:223], v[28:31]\n"
    "s_waitcnt lgkmcnt(9)\n"
    "ds_read_b64_tr_b16 a[42:43], v247 offset:18496\n"
    "v_lshlrev_b32 v114, 16, v229\n"
    "v_mfma_f32_16x16x32_bf16 v[32:35], a[16:19], v[208:211], v[32:35]\n"
    "ds_read_b64_tr_b16 a[44:45], v247 offset:26112\n"
    "v_and_b32 v115, 0xffff0000, v229\n"
    "v_mfma_f32_16x16x32_bf16 v[32:35], a[20:23], v[216:219], v[32:35]\n"
    "ds_read_b64_tr_b16 a[46:47], v247 offset:27200\n"
    "v_sub_f32 v114, v114, v245\n"
    "v_mfma_f32_16x16x32_bf16 v[36:39], a[16:19], v[212:215], v[36:39]\n"
    "s_add_u32 m0, s87, 55328\n"
    "ds_read_b64_tr_b16 a[48:49], v247 offset:17440\n"
    "buffer_load_dwordx4 v244, s[52:55], s29 offen offset:128 lds\n"
    "v_mfma_f32_16x16x32_bf16 v[36:39], a[20:23], v[220:223], v[36:39]\n"
    "s_waitcnt lgkmcnt(9)\n"
    "ds_read_b64_tr_b16 a[50:51], v247 offset:18528\n"
    "v_sub_f32 v115, v115, v245\n"
    "v_mfma_f32_16x16x32_bf16 v[40:43], a[24:27], v[208:211], v[40:43]\n"
    "ds_read_b64_tr_b16 a[52:53], v247 offset:26144\n"
    "v_cvt_pk_bf16_f32 v229, v114, v115\n"
    "v_mfma_f32_16x16x32_bf16 v[40:43], a[28:31], v[216:219], v[40:43]\n"
    "ds_read_b64_tr_b16 a[54:55], v247 offset:27232\n"
    "v_lshlrev_b32 v112, 16, v230\n"
    "v_mfma_f32_16x16x32_bf16 v[44:47], a[24:27], v[212:215], v[44:47]\n"
    "ds_read_b64_tr_b16 a[56:57], v247 offset:17472\n"
    "v_and_b32 v113, 0xffff0000, v230\n"
    "v_mfma_f32_16x16x32_bf16 v[44:47], a[28:31], v[220:223], v[44:47]\n"
    "s_waitcnt lgkmcnt(9)\n"
    "ds_read_b64_tr_b16 a[58:59], v247 offset:18560\n"
    "v_sub_f32 v112, v112, v245\n"
    "v_mfma_f32_16x16x32_bf16 v[48:51], a[32:35], v[208:211], v[48:51]\n"
    "s_add_u32 m0, s87, 59328\n"
    "ds_read_b64_tr_b16 a[60:61], v247 offset:26176\n"
    "buffer_load_dwordx4 v244, s[52:55], s29 offen offset:256 lds\n"
    "v_mfma_f32_16x16x32_bf16 v[48:51], a[36:39], v[216:219], v[48:51]\n"
    "ds_read_b64_tr_b16 a[62:63], v247 offset:27264\n"
    "v_sub_f32 v113, v113, v245\n"
    "v_mfma_f32_16x16x32_bf16 v[52:55], a[32:35], v[212:215], v[52:55]\n"
    "ds_read_b64_tr_b16 a[0:1], v247 offset:17504\n"
    "v_cvt_pk_bf16_f32 v230, v112, v113\n"
    "v_mfma_f32_16x16x32_bf16 v[52:55], a[36:39], v[220:223], v[52:55]\n"
    "s_waitcnt lgkmcnt(9)\n"
    "ds_read_b64_tr_b16 a[2:3], v247 offset:18592\n"
    "v_lshlrev_b32 v114, 16, v231\n"
    "v_mfma_f32_16x16x32_bf16 v[56:59], a[40:43], v[208:211], v[56:59]\n"
    "ds_read_b64_tr_b16 a[4:5], v247 offset:26208\n"
    "v_and_b32 v115, 0xffff0000, v231\n"
    "v_mfma_f32_16x16x32_bf16 v[56:59], a[44:47], v[216:219], v[56:59]\n"
    "ds_read_b64_tr_b16 a[6:7], v247 offset:27296\n"
    "v_sub_f32 v114, v114, v245\n"
    "v_mfma_f32_16x16x32_bf16 v[60:63], a[40:43], v[212:215], v[60:63]\n"
    "s_add_u32 m0, s87, 63328\n"
    "ds_read_b64_tr_b16 a[8:9], v247 offset:17536\n"
    "buffer_load_dwordx4 v244, s[52:55], s29 offen offset:384 lds\n"
    "v_mfma_f32_16x16x32_bf16 v[60:63], a[44:47], v[220:223], v[60:63]\n"
    "s_waitcnt lgkmcnt(9)\n"
    "ds_read_b64_tr_b16 a[10:11], v247 offset:18624\n"
    "v_sub_f32 v115, v115, v245\n"
    "v_mfma_f32_16x16x32_bf16 v[64:67], a[48:51], v[208:211], v[64:67]\n"
    "s_add_u32 s29, s29, 262144\n"
    "ds_read_b64_tr_b16 a[12:13], v247 offset:26240\n"
    "v_cvt_pk_bf16_f32 v231, v114, v115\n"
    "v_mfma_f32_16x16x32_bf16 v[64:67], a[52:55], v[216:219], v[64:67]\n"
    "ds_read_b64_tr_b16 a[14:15], v247 offset:27328\n"
    "v_lshlrev_b32 v112, 16, v236\n"
    "v_mfma_f32_16x16x32_bf16 v[68:71], a[48:51], v[212:215], v[68:71]\n"
    "ds_read_b64_tr_b16 a[16:17], v247 offset:17568\n"
    "v_and_b32 v113, 0xffff0000, v236\n"
    "v_mfma_f32_16x16x32_bf16 v[68:71], a[52:55], v[220:223], v[68:71]\n"
    "s_waitcnt lgkmcnt(9)\n"
    "ds_read_b64_tr_b16 a[18:19], v247 offset:18656\n"
    "v_sub_f32 v112, v112, v245\n"
    "v_mfma_f32_16x16x32_bf16 v[72:75], a[56:59], v[208:211], v[72:75]\n"
    "ds_read_b64_tr_b16 a[20:21], v247 offset:26272\n"
    "v_sub_f32 v113, v113, v245\n"
    "v_mfma_f32_16x16x32_bf16 v[72:75], a[60:63], v[216:219], v[72:75]\n"
    "ds_read_b64_tr_b16 a[22:23], v247 offset:27360\n"
    "v_cvt_pk_bf16_f32 v236, v112, v113\n"
    "v_mfma_f32_16x16x32_bf16 v[76:79], a[56:59], v[212:215], v[76:79]\n"
    "ds_read_b64_tr_b16 a[24:25], v247 offset:17600\n"
    "v_lshlrev_b32 v114, 16, v237\n"
    "v_mfma_f32_16x16x32_bf16 v[76:79], a[60:63], v[220:223], v[76:79]\n"
    "s_waitcnt lgkmcnt(9)\n"
    "ds_read_b64_tr_b16 a[26:27], v247 offset:18688\n"
    "v_and_b32 v115, 0xffff0000, v237\n"
    "v_mfma_f32_16x16x32_bf16 v[80:83], a[0:3], v[208:211], v[80:83]\n"
    "ds_read_b64_tr_b16 a[28:29], v247 offset:26304\n"
    "v_sub_f32 v114, v114, v245\n"
    "v_mfma_f32_16x16x32_bf16 v[80:83], a[4:7], v[216:219], v[80:83]\n"
    "ds_read_b64_tr_b16 a[30:31], v247 offset:27392\n"
    "v_sub_f32 v115, v115, v245\n"
    "v_mfma_f32_16x16x32_bf16 v[84:87], a[0:3], v[212:215], v[84:87]\n"
    "s_waitcnt vmcnt(8)\n"
    "v_cvt_pk_bf16_f32 v237, v114, v115\n"
    "v_lshlrev_b32 v112, 16, v238\n"
    "v_mfma_f32_16x16x32_bf16 v[84:87], a[4:7], v[220:223], v[84:87]\n"
    "s_waitcnt lgkmcnt(8)\n"
    "v_and_b32 v113, 0xffff0000, v238\n"
    "v_sub_f32 v112, v112, v245\n"
    "v_mfma_f32_16x16x32_bf16 v[88:91], a[8:11], v[208:211], v[88:91]\n"
    "s_barrier\n"
    "v_add_u32 v247, 0, v240\n"
    "ds_read_b64_tr_b16 a[32:33], v247 offset:0\n"
    "v_mfma_f32_16x16x32_bf16 v[88:91], a[12:15], v[216:219], v[88:91]\n"
    "ds_read_b64_tr_b16 a[34:35], v247 offset:1088\n"
    "ds_read_b64_tr_b16 a[36:37], v247 offset:8704\n"
    "v_mfma_f32_16x16x32_bf16 v[92:95], a[8:11], v[212:215], v[92:95]\n"
    "s_add_u32 m0, s87, 69632\n"
    "ds_read_b64_tr_b16 a[38:39], v247 offset:9792\n"
    "ds_read_b64_tr_b16 a[40:41], v247 offset:32\n"
    "v_mfma_f32_16x16x32_bf16 v[92:95], a[12:15], v[220:223], v[92:95]\n"
    "s_waitcnt lgkmcnt(9)\n"
    "buffer_load_dwordx4 v244, s[52:55], s29 offen offset:0 lds\n"
    "ds_read_b64_tr_b16 a[42:43], v247 offset:1120\n"
    "v_mfma_f32_16x16x32_bf16 v[96:99], a[16:19], v[208:211], v[96:99]\n"
    "ds_read_b64_tr_b16 a[44:45], v247 offset:8736\n"
    "v_sub_f32 v113, v113, v245\n"
    "v_mfma_f32_16x16x32_bf16 v[96:99], a[20:23], v[216:219], v[96:99]\n"
    "v_cvt_pk_bf16_f32 v238, v112, v113\n"
    "v_lshlrev_b32 v114, 16, v239\n"
    "v_mfma_f32_16x16x32_bf16 v[100:103], a[16:19], v[212:215], v[100:103]\n"
    "v_and_b32 v115, 0xffff0000, v239\n"
    "v_sub_f32 v114, v114, v245\n"
    "v_mfma_f32_16x16x32_bf16 v[100:103], a[20:23], v[220:223], v[100:103]\n"
    "s_waitcnt lgkmcnt(7)\n"
    "v_sub_f32 v115, v115, v245\n"
    "v_cvt_pk_bf16_f32 v239, v114, v115\n"
    "v_mfma_f32_16x16x32_bf16 v[104:107], a[24:27], v[208:211], v[104:107]\n"
    "s_add_u32 m0, s87, 73632\n"
    "ds_read_b64_tr_b16 a[46:47], v247 offset:9824\n"
    "ds_read_b64_tr_b16 a[48:49], v247 offset:64\n"
    "v_mfma_f32_16x16x32_bf16 v[104:107], a[28:31], v[216:219], v[104:107]\n"
    "ds_read_b64_tr_b16 a[50:51], v247 offset:1152\n"
    "buffer_load_dwordx4 v244, s[52:55], s29 offen offset:128 lds\n"
    "v_mfma_f32_16x16x32_bf16 v[108:111], a[24:27], v[212:215], v[108:111]\n"
    "ds_read_b64_tr_b16 a[52:53], v247 offset:8768\n"
    "ds_read_b64_tr_b16 a[54:55], v247 offset:9856\n"
    "v_mfma_f32_16x16x32_bf16 v[108:111], a[28:31], v[220:223], v[108:111]\n"
    "s_waitcnt lgkmcnt(8)\n"
    "ds_read_b64_tr_b16 a[56:57], v247 offset:96\n"
    "ds_read_b64_tr_b16 a[58:59], v247 offset:1184\n"
    "v_mfma_f32_16x16x32_bf16 v[0:3], a[32:35], v[224:227], v[0:3]\n"
    "ds_read_b64_tr_b16 a[60:61], v247 offset:8800\n"
    "v_mul_f32 v118, v118, v118\n"
    "v_mfma_f32_16x16x32_bf16 v[0:3], a[36:39], v[232:235], v[0:3]\n"
    "s_add_u32 m0, s87, 77632\n"
    "ds_read_b64_tr_b16 a[62:63], v247 offset:9888\n"
    "buffer_load_dwordx4 v244, s[52:55], s29 offen offset:256 lds\n"
    "v_mfma_f32_16x16x32_bf16 v[4:7], a[32:35], v[228:231], v[4:7]\n"
    "ds_read_b64_tr_b16 a[0:1], v247 offset:128\n"
    "v_mul_f32 v116, 0x3b124925, v116\n"
    "v_mfma_f32_16x16x32_bf16 v[4:7], a[36:39], v[236:239], v[4:7]\n"
    "s_waitcnt lgkmcnt(9)\n"
    "ds_read_b64_tr_b16 a[2:3], v247 offset:1216\n"
    "v_sub_f32 v116, v116, v118\n"
    "v_mfma_f32_16x16x32_bf16 v[8:11], a[40:43], v[224:227], v[8:11]\n"
    "ds_read_b64_tr_b16 a[4:5], v247 offset:8832\n"
    "v_max_f32 v116, 0, v116\n"
    "v_mfma_f32_16x16x32_bf16 v[8:11], a[44:47], v[232:235], v[8:11]\n"
    "ds_read_b64_tr_b16 a[6:7], v247 offset:9920\n"
    "v_cvt_pk_bf16_f32 v116, v116, v116\n"
    "v_mfma_f32_16x16x32_bf16 v[12:15], a[40:43], v[228:231], v[12:15]\n"
    "ds_read_b64_tr_b16 a[8:9], v247 offset:160\n"
    "v_and_b32 v116, 0xffff0000, v116\n"
    "v_mfma_f32_16x16x32_bf16 v[12:15], a[44:47], v[236:239], v[12:15]\n"
    "s_waitcnt lgkmcnt(9)\n"
    "ds_read_b64_tr_b16 a[10:11], v247 offset:1248\n"
    "v_add_f32 v116, 0x3727c5ac, v116\n"
    "v_mfma_f32_16x16x32_bf16 v[16:19], a[48:51], v[224:227], v[16:19]\n"
    "s_add_u32 m0, s87, 81632\n"
    "ds_read_b64_tr_b16 a[12:13], v247 offset:8864\n"
    "buffer_load_dwordx4 v244, s[52:55], s29 offen offset:384 lds\n"
    "v_mfma_f32_16x16x32_bf16 v[16:19], a[52:55], v[232:235], v[16:19]\n"
    "s_add_u32 s29, s29, 262144\n"
    "ds_read_b64_tr_b16 a[14:15], v247 offset:9952\n"
    "v_cvt_pk_bf16_f32 v116, v116, v116\n"
    "v_mfma_f32_16x16x32_bf16 v[20:23], a[48:51], v[228:231], v[20:23]\n"
    "ds_read_b64_tr_b16 a[16:17], v247 offset:192\n"
    "v_and_b32 v116, 0xffff0000, v116\n"
    "v_mfma_f32_16x16x32_bf16 v[20:23], a[52:55], v[236:239], v[20:23]\n"
    "s_waitcnt lgkmcnt(9)\n"
    "ds_read_b64_tr_b16 a[18:19], v247 offset:1280\n"
    "v_mul_f32 v119, v119, v119\n"
    "v_mfma_f32_16x16x32_bf16 v[24:27], a[56:59], v[224:227], v[24:27]\n"
    "ds_read_b64_tr_b16 a[20:21], v247 offset:8896\n"
    "v_mul_f32 v117, 0x3b124925, v117\n"
    "v_mfma_f32_16x16x32_bf16 v[24:27], a[60:63], v[232:235], v[24:27]\n"
    "s_add_u32 m0, s87, 86144\n"
    "ds_read_b64_tr_b16 a[22:23], v247 offset:9984\n"
    "buffer_load_dwordx4 v244, s[52:55], s29 offen offset:0 lds\n"
    "v_mfma_f32_16x16x32_bf16 v[28:31], a[56:59], v[228:231], v[28:31]\n"
    "ds_read_b64_tr_b16 a[24:25], v247 offset:17408\n"
    "v_sub_f32 v117, v117, v119\n"
    "v_mfma_f32_16x16x32_bf16 v[28:31], a[60:63], v[236:239], v[28:31]\n"
    "s_waitcnt lgkmcnt(9)\n"
    "ds_read_b64_tr_b16 a[26:27], v247 offset:18496\n"
    "v_max_f32 v117, 0, v117\n"
    "v_mfma_f32_16x16x32_bf16 v[32:35], a[0:3], v[224:227], v[32:35]\n"
    "ds_read_b64_tr_b16 a[28:29], v247 offset:26112\n"
    "v_cvt_pk_bf16_f32 v117, v117, v117\n"
    "v_mfma_f32_16x16x32_bf16 v[32:35], a[4:7], v[232:235], v[32:35]\n"
    "ds_read_b64_tr_b16 a[30:31], v247 offset:27200\n"
    "v_and_b32 v117, 0xffff0000, v117\n"
    "v_mfma_f32_16x16x32_bf16 v[36:39], a[0:3], v[228:231], v[36:39]\n"
    "s_add_u32 m0, s87, 90144\n"
    "ds_read_b64_tr_b16 a[32:33], v247 offset:17440\n"
    "buffer_load_dwordx4 v244, s[52:55], s29 offen offset:128 lds\n"
    "v_mfma_f32_16x16x32_bf16 v[36:39], a[4:7], v[236:239], v[36:39]\n"
    "s_waitcnt lgkmcnt(9)\n"
    "ds_read_b64_tr_b16 a[34:35], v247 offset:18528\n"
    "v_add_f32 v117, 0x3727c5ac, v117\n"
    "v_mfma_f32_16x16x32_bf16 v[40:43], a[8:11], v[224:227], v[40:43]\n"
    "ds_read_b64_tr_b16 a[36:37], v247 offset:26144\n"
    "v_cvt_pk_bf16_f32 v117, v117, v117\n"
    "v_mfma_f32_16x16x32_bf16 v[40:43], a[12:15], v[232:235], v[40:43]\n"
    "ds_read_b64_tr_b16 a[38:39], v247 offset:27232\n"
    "v_and_b32 v117, 0xffff0000, v117\n"
    "v_mfma_f32_16x16x32_bf16 v[44:47], a[8:11], v[228:231], v[44:47]\n"
    "v_sqrt_f32 v116, v116\n"
    "v_mfma_f32_16x16x32_bf16 v[44:47], a[12:15], v[236:239], v[44:47]\n"
    "s_waitcnt lgkmcnt(8)\n"
    "ds_read_b64_tr_b16 a[40:41], v247 offset:17472\n"
    "ds_read_b64_tr_b16 a[42:43], v247 offset:18560\n"
    "v_mfma_f32_16x16x32_bf16 v[48:51], a[16:19], v[224:227], v[48:51]\n"
    "s_add_u32 m0, s87, 94144\n"
    "ds_read_b64_tr_b16 a[44:45], v247 offset:26176\n"
    "buffer_load_dwordx4 v244, s[52:55], s29 offen offset:256 lds\n"
    "v_mfma_f32_16x16x32_bf16 v[48:51], a[20:23], v[232:235], v[48:51]\n"
    "ds_read_b64_tr_b16 a[46:47], v247 offset:27264\n"
    "v_cvt_pk_bf16_f32 v116, v116, v116\n"
    "v_mfma_f32_16x16x32_bf16 v[52:55], a[16:19], v[228:231], v[52:55]\n"
    "ds_read_b64_tr_b16 a[48:49], v247 offset:17504\n"
    "v_and_b32 v116, 0xffff0000, v116\n"
    "v_mfma_f32_16x16x32_bf16 v[52:55], a[20:23], v[236:239], v[52:55]\n"
    "s_waitcnt lgkmcnt(9)\n"
    "v_rcp_f32 v252, v116\n"
    "v_mfma_f32_16x16x32_bf16 v[56:59], a[24:27], v[224:227], v[56:59]\n"
    "ds_read_b64_tr_b16 a[50:51], v247 offset:18592\n"
    "ds_read_b64_tr_b16 a[52:53], v247 offset:26208\n"
    "v_mfma_f32_16x16x32_bf16 v[56:59], a[28:31], v[232:235], v[56:59]\n"
    "v_sqrt_f32 v117, v117\n"
    "v_mfma_f32_16x16x32_bf16 v[60:63], a[24:27], v[228:231], v[60:63]\n"
    "s_add_u32 m0, s87, 98144\n"
    "v_mul_f32 v248, 0x3db8aa3b, v252\n"
    "v_mul_f32 v250, 0xbfb8aa3b, v252\n"
    "v_mfma_f32_16x16x32_bf16 v[60:63], a[28:31], v[236:239], v[60:63]\n"
    "s_waitcnt lgkmcnt(7)\n"
    "v_cvt_pk_bf16_f32 v117, v117, v117\n"
    "v_and_b32 v117, 0xffff0000, v117\n"
    "v_mfma_f32_16x16x32_bf16 v[64:67], a[32:35], v[224:227], v[64:67]\n"
    "v_rcp_f32 v253, v117\n"
    "v_mfma_f32_16x16x32_bf16 v[64:67], a[36:39], v[232:235], v[64:67]\n"
    "v_mul_f32 v249, 0x3db8aa3b, v253\n"
    "v_mul_f32 v251, 0xbfb8aa3b, v253\n"
    "v_mfma_f32_16x16x32_bf16 v[68:71], a[32:35], v[228:231], v[68:71]\n"
    "ds_read_b64_tr_b16 a[54:55], v247 offset:27296\n"
    "ds_read_b64_tr_b16 a[56:57], v247 offset:17536\n"
    "v_mfma_f32_16x16x32_bf16 v[68:71], a[36:39], v[236:239], v[68:71]\n"
    "s_waitcnt lgkmcnt(5)\n"
    "buffer_load_dwordx4 v244, s[52:55], s29 offen offset:384 lds\n"
    "ds_read_b64_tr_b16 a[58:59], v247 offset:18624\n"
    "v_mfma_f32_16x16x32_bf16 v[72:75], a[40:43], v[224:227], v[72:75]\n"
    "s_add_u32 s29, s29, 262144\n"
    "ds_read_b64_tr_b16 a[60:61], v247 offset:26240\n"
    "ds_read_b64_tr_b16 a[62:63], v247 offset:27328\n"
    "v_mfma_f32_16x16x32_bf16 v[72:75], a[44:47], v[232:235], v[72:75]\n"
    "ds_read_b64_tr_b16 a[0:1], v247 offset:17568\n"
    "ds_read_b64_tr_b16 a[2:3], v247 offset:18656\n"
    "v_mfma_f32_16x16x32_bf16 v[76:79], a[40:43], v[228:231], v[76:79]\n"
    "ds_read_b64_tr_b16 a[4:5], v247 offset:26272\n"
    "ds_read_b64_tr_b16 a[6:7], v247 offset:27360\n"
    "v_mfma_f32_16x16x32_bf16 v[76:79], a[44:47], v[236:239], v[76:79]\n"
    "s_waitcnt lgkmcnt(8)\n"
    "ds_read_b64_tr_b16 a[8:9], v247 offset:17600\n"
    "ds_read_b64_tr_b16 a[10:11], v247 offset:18688\n"
    "v_mfma_f32_16x16x32_bf16 v[80:83], a[48:51], v[224:227], v[80:83]\n"
    "ds_read_b64_tr_b16 a[12:13], v247 offset:26304\n"
    "v_mul_f32 v0, v0, v250\n"
    "v_mfma_f32_16x16x32_bf16 v[80:83], a[52:55], v[232:235], v[80:83]\n"
    "ds_read_b64_tr_b16 a[14:15], v247 offset:27392\n"
    "v_mul_f32 v1, v1, v250\n"
    "v_mfma_f32_16x16x32_bf16 v[84:87], a[48:51], v[228:231], v[84:87]\n"
    "s_waitcnt vmcnt(8)\n"
    "v_exp_f32 v0, v0\n"
    "v_mfma_f32_16x16x32_bf16 v[84:87], a[52:55], v[236:239], v[84:87]\n"
    "s_waitcnt lgkmcnt(8)\n"
    "v_exp_f32 v1, v1\n"
    "v_mfma_f32_16x16x32_bf16 v[88:91], a[56:59], v[224:227], v[88:91]\n"
    "s_barrier\n"
    "v_add_u32 v247, 34816, v241\n"
    "ds_read_b64_tr_b16 a[16:17], v247 offset:0\n"
    "v_mfma_f32_16x16x32_bf16 v[88:91], a[60:63], v[232:235], v[88:91]\n"
    "ds_read_b64_tr_b16 a[18:19], v247 offset:256\n"
    "ds_read_b64_tr_b16 a[20:21], v247 offset:16512\n"
    "v_mfma_f32_16x16x32_bf16 v[92:95], a[56:59], v[228:231], v[92:95]\n"
    "s_add_u32 m0, s87, 104448\n"
    "ds_read_b64_tr_b16 a[22:23], v247 offset:16768\n"
    "ds_read_b64_tr_b16 a[24:25], v247 offset:8\n"
    "v_mfma_f32_16x16x32_bf16 v[92:95], a[60:63], v[236:239], v[92:95]\n"
    "s_waitcnt lgkmcnt(9)\n"
    "buffer_load_dwordx4 v244, s[52:55], s29 offen offset:0 lds\n"
    "ds_read_b64_tr_b16 a[26:27], v247 offset:264\n"
    "v_mfma_f32_16x16x32_bf16 v[96:99], a[0:3], v[224:227], v[96:99]\n"
    "ds_read_b64_tr_b16 a[28:29], v247 offset:16520\n"
    "v_add_f32 v0, 1.0, v0\n"
    "v_mfma_f32_16x16x32_bf16 v[96:99], a[4:7], v[232:235], v[96:99]\n"
    "ds_read_b64_tr_b16 a[30:31], v247 offset:16776\n"
    "v_add_f32 v1, 1.0, v1\n"
    "v_mfma_f32_16x16x32_bf16 v[100:103], a[0:3], v[228:231], v[100:103]\n"
    "ds_read_b64_tr_b16 a[32:33], v247 offset:512\n"
    "v_mul_f32 v2, v2, v250\n"
    "v_mfma_f32_16x16x32_bf16 v[100:103], a[4:7], v[236:239], v[100:103]\n"
    "s_waitcnt lgkmcnt(9)\n"
    "ds_read_b64_tr_b16 a[34:35], v247 offset:768\n"
    "v_mul_f32 v3, v3, v250\n"
    "v_mfma_f32_16x16x32_bf16 v[104:107], a[8:11], v[224:227], v[104:107]\n"
    "s_add_u32 m0, s87, 108448\n"
    "v_rcp_f32 v0, v0\n"
    "v_mfma_f32_16x16x32_bf16 v[104:107], a[12:15], v[232:235], v[104:107]\n"
    "ds_read_b64_tr_b16 a[36:37], v247 offset:17024\n"
    "buffer_load_dwordx4 v244, s[52:55], s29 offen offset:128 lds\n"
    "v_mfma_f32_16x16x32_bf16 v[108:111], a[8:11], v[228:231], v[108:111]\n"
    "ds_read_b64_tr_b16 a[38:39], v247 offset:17280\n"
    "ds_read_b64_tr_b16 a[40:41], v247 offset:520\n"
    "v_mfma_f32_16x16x32_bf16 v[108:111], a[12:15], v[236:239], v[108:111]\n"
    "s_waitcnt lgkmcnt(9)\n"
    "ds_read_b64_tr_b16 a[42:43], v247 offset:776\n"
    "v_mul_f32 v56, v56, v0\n"
    "v_mfma_f32_16x16x32_bf16 a[128:131], a[16:19], v[128:131], 0\n"
    "v_rcp_f32 v1, v1\n"
    "v_mfma_f32_16x16x32_bf16 a[128:131], a[20:23], v[136:139], a[128:131]\n"
    "ds_read_b64_tr_b16 a[44:45], v247 offset:17032\n"
    "ds_read_b64_tr_b16 a[46:47], v247 offset:17288\n"
    "v_mfma_f32_16x16x32_bf16 a[132:135], a[16:19], v[132:135], 0\n"
    "ds_read_b64_tr_b16 a[48:49], v247 offset:4128\n"
    "v_mul_f32 v57, v57, v1\n"
    "v_mfma_f32_16x16x32_bf16 a[132:135], a[20:23], v[140:143], a[132:135]\n"
    "s_waitcnt lgkmcnt(9)\n"
    "v_exp_f32 v2, v2\n"
    "v_mfma_f32_16x16x32_bf16 a[136:139], a[24:27], v[128:131], 0\n"
    "s_add_u32 m0, s87, 112448\n"
    "ds_read_b64_tr_b16 a[50:51], v247 offset:4384\n"
    "buffer_load_dwordx4 v244, s[52:55], s29 offen offset:256 lds\n"
    "v_mfma_f32_16x16x32_bf16 a[136:139], a[28:31], v[136:139], a[136:139]\n"
    "ds_read_b64_tr_b16 a[52:53], v247 offset:20640\n"
    "ds_read_b64_tr_b16 a[54:55], v247 offset:20896\n"
    "v_mfma_f32_16x16x32_bf16 a[140:143], a[24:27], v[132:135], 0\n"
    "ds_read_b64_tr_b16 a[56:57], v247 offset:4136\n"
    "v_add_f32 v2, 1.0, v2\n"
    "v_mfma_f32_16x16x32_bf16 a[140:143], a[28:31], v[140:143], a[140:143]\n"
    "s_waitcnt lgkmcnt(9)\n"
    "v_rcp_f32 v2, v2\n"
    "v_mfma_f32_16x16x32_bf16 a[144:147], a[32:35], v[128:131], 0\n"
    "ds_read_b64_tr_b16 a[58:59], v247 offset:4392\n"
    "ds_read_b64_tr_b16 a[60:61], v247 offset:20648\n"
    "v_mfma_f32_16x16x32_bf16 a[144:147], a[36:39], v[136:139], a[144:147]\n"
    "ds_read_b64_tr_b16 a[62:63], v247 offset:20904\n"
    "v_mul_f32 v58, v58, v2\n"
    "v_mfma_f32_16x16x32_bf16 a[148:151], a[32:35], v[132:135], 0\n"
    "s_add_u32 m0, s87, 116448\n"
    "ds_read_b64_tr_b16 a[0:1], v247 offset:4640\n"
    "buffer_load_dwordx4 v244, s[52:55], s29 offen offset:384 lds\n"
    "v_mfma_f32_16x16x32_bf16 a[148:151], a[36:39], v[140:143], a[148:151]\n"
    "s_waitcnt lgkmcnt(9)\n"
    "ds_read_b64_tr_b16 a[2:3], v247 offset:4896\n"
    "v_mul_f32 v4, v4, v251\n"
    "v_mfma_f32_16x16x32_bf16 a[152:155], a[40:43], v[128:131], 0\n"
    "s_add_u32 s29, s29, 262144\n"
    "ds_read_b64_tr_b16 a[4:5], v247 offset:21152\n"
    "v_mul_f32 v5, v5, v251\n"
    "v_mfma_f32_16x16x32_bf16 a[152:155], a[44:47], v[136:139], a[152:155]\n"
    "ds_read_b64_tr_b16 a[6:7], v247 offset:21408\n"
    "v_mul_f32 v6, v6, v251\n"
    "v_mfma_f32_16x16x32_bf16 a[156:159], a[40:43], v[132:135], 0\n"
    "ds_read_b64_tr_b16 a[8:9], v247 offset:4648\n"
    "v_mul_f32 v7, v7, v251\n"
    "v_mfma_f32_16x16x32_bf16 a[156:159], a[44:47], v[140:143], a[156:159]\n"
    "s_waitcnt lgkmcnt(9)\n"
    "v_exp_f32 v3, v3\n"
    "v_mfma_f32_16x16x32_bf16 a[160:163], a[48:51], v[128:131], 0\n"
    "ds_read_b64_tr_b16 a[10:11], v247 offset:4904\n"
    "ds_read_b64_tr_b16 a[12:13], v247 offset:21160\n"
    "v_mfma_f32_16x16x32_bf16 a[160:163], a[52:55], v[136:139], a[160:163]\n"
    "s_add_u32 m0, s87, 120960\n"
    "ds_read_b64_tr_b16 a[14:15], v247 offset:21416\n"
    "buffer_load_dwordx4 v244, s[52:55], s29 offen offset:0 lds\n"
    "v_mfma_f32_16x16x32_bf16 a[164:167], a[48:51], v[132:135], 0\n"
    "ds_read_b64_tr_b16 a[16:17], v247 offset:8256\n"
    "v_add_f32 v3, 1.0, v3\n"
    "v_mfma_f32_16x16x32_bf16 a[164:167], a[52:55], v[140:143], a[164:167]\n"
    "s_waitcnt lgkmcnt(9)\n"
    "v_rcp_f32 v3, v3\n"
    "v_mfma_f32_16x16x32_bf16 a[168:171], a[56:59], v[128:131], 0\n"
    "ds_read_b64_tr_b16 a[18:19], v247 offset:8512\n"
    "ds_read_b64_tr_b16 a[20:21], v247 offset:24768\n"
    "v_mfma_f32_16x16x32_bf16 a[168:171], a[60:63], v[136:139], a[168:171]\n"
    "ds_read_b64_tr_b16 a[22:23], v247 offset:25024\n"
    "v_mul_f32 v59, v59, v3\n"
    "v_mfma_f32_16x16x32_bf16 a[172:175], a[56:59], v[132:135], 0\n"
    "ds_read_b64_tr_b16 a[24:25], v247 offset:8264\n"
    "v_cvt_pk_bf16_f32 v0, v56, v57\n"
    "v_mfma_f32_16x16x32_bf16 a[172:175], a[60:63], v[140:143], a[172:175]\n"
    "s_waitcnt lgkmcnt(9)\n"
    "ds_read_b64_tr_b16 a[26:27], v247 offset:8520\n"
    "v_cvt_pk_bf16_f32 v1, v58, v59\n"
    "v_mfma_f32_16x16x32_bf16 a[176:179], a[0:3], v[128:131], 0\n"
    "s_add_u32 m0, s87, 124960\n"
    "v_exp_f32 v4, v4\n"
    "v_mfma_f32_16x16x32_bf16 a[176:179], a[4:7], v[136:139], a[176:179]\n"
    "buffer_load_dwordx4 v244, s[52:55], s29 offen offset:128 lds\n"
    "ds_read_b64_tr_b16 a[28:29], v247 offset:24776\n"
    "v_mfma_f32_16x16x32_bf16 a[180:183], a[0:3], v[132:135], 0\n"
    "ds_read_b64_tr_b16 a[30:31], v247 offset:25032\n"
    "ds_read_b64_tr_b16 a[32:33], v247 offset:8768\n"
    "v_mfma_f32_16x16x32_bf16 a[180:183], a[4:7], v[140:143], a[180:183]\n"
    "s_waitcnt lgkmcnt(9)\n"
    "ds_read_b64_tr_b16 a[34:35], v247 offset:9024\n"
    "v_add_f32 v4, 1.0, v4\n"
    "v_mfma_f32_16x16x32_bf16 a[184:187], a[8:11], v[128:131], 0\n"
    "v_rcp_f32 v4, v4\n"
    "v_mfma_f32_16x16x32_bf16 a[184:187], a[12:15], v[136:139], a[184:187]\n"
    "ds_read_b64_tr_b16 a[36:37], v247 offset:25280\n"
    "ds_read_b64_tr_b16 a[38:39], v247 offset:25536\n"
    "v_mfma_f32_16x16x32_bf16 a[188:191], a[8:11], v[132:135], 0\n"
    "s_add_u32 m0, s87, 128960\n"
    "ds_read_b64_tr_b16 a[40:41], v247 offset:8776\n"
    "buffer_load_dwordx4 v244, s[52:55], s29 offen offset:256 lds\n"
    "v_mfma_f32_16x16x32_bf16 a[188:191], a[12:15], v[140:143], a[188:191]\n"
    "s_waitcnt lgkmcnt(9)\n"
    "ds_read_b64_tr_b16 a[42:43], v247 offset:9032\n"
    "v_mul_f32 v60, v60, v4\n"
    "v_mfma_f32_16x16x32_bf16 a[192:195], a[16:19], v[128:131], 0\n"
    "v_exp_f32 v5, v5\n"
    "v_mfma_f32_16x16x32_bf16 a[192:195], a[20:23], v[136:139], a[192:195]\n"
    "ds_read_b64_tr_b16 a[44:45], v247 offset:25288\n"
    "ds_read_b64_tr_b16 a[46:47], v247 offset:25544\n"
    "v_mfma_f32_16x16x32_bf16 a[196:199], a[16:19], v[132:135], 0\n"
    "ds_read_b64_tr_b16 a[48:49], v247 offset:12384\n"
    "v_add_f32 v5, 1.0, v5\n"
    "v_mfma_f32_16x16x32_bf16 a[196:199], a[20:23], v[140:143], a[196:199]\n"
    "s_waitcnt lgkmcnt(9)\n"
    "v_rcp_f32 v5, v5\n"
    "v_mfma_f32_16x16x32_bf16 a[200:203], a[24:27], v[128:131], 0\n"
    "ds_read_b64_tr_b16 a[50:51], v247 offset:12640\n"
    "ds_read_b64_tr_b16 a[52:53], v247 offset:28896\n"
    "v_mfma_f32_16x16x32_bf16 a[200:203], a[28:31], v[136:139], a[200:203]\n"
    "s_add_u32 m0, s87, 132960\n"
    "ds_read_b64_tr_b16 a[54:55], v247 offset:29152\n"
    "buffer_load_dwordx4 v244, s[52:55], s29 offen offset:384 lds\n"
    "v_mfma_f32_16x16x32_bf16 a[204:207], a[24:27], v[132:135], 0\n"
    "s_add_u32 s29, s29, 262144\n"
    "ds_read_b64_tr_b16 a[56:57], v247 offset:12392\n"
    "v_mul_f32 v61, v61, v5\n"
    "v_mfma_f32_16x16x32_bf16 a[204:207], a[28:31], v[140:143], a[204:207]\n"
    "s_waitcnt lgkmcnt(9)\n"
    "v_exp_f32 v6, v6\n"
    "v_mfma_f32_16x16x32_bf16 a[208:211], a[32:35], v[128:131], 0\n"
    "ds_read_b64_tr_b16 a[58:59], v247 offset:12648\n"
    "ds_read_b64_tr_b16 a[60:61], v247 offset:28904\n"
    "v_mfma_f32_16x16x32_bf16 a[208:211], a[36:39], v[136:139], a[208:211]\n"
    "ds_read_b64_tr_b16 a[62:63], v247 offset:29160\n"
    "v_add_f32 v6, 1.0, v6\n"
    "v_mfma_f32_16x16x32_bf16 a[212:215], a[32:35], v[132:135], 0\n"
    "ds_read_b64_tr_b16 a[0:1], v247 offset:12896\n"
    "v_mul_f32 v8, v8, v250\n"
    "v_mfma_f32_16x16x32_bf16 a[212:215], a[36:39], v[140:143], a[212:215]\n"
    "s_waitcnt lgkmcnt(9)\n"
    "ds_read_b64_tr_b16 a[2:3], v247 offset:13152\n"
    "v_mul_f32 v9, v9, v250\n"
    "v_mfma_f32_16x16x32_bf16 a[216:219], a[40:43], v[128:131], 0\n"
    "ds_read_b64_tr_b16 a[4:5], v247 offset:29408\n"
    "v_mul_f32 v10, v10, v250\n"
    "v_mfma_f32_16x16x32_bf16 a[216:219], a[44:47], v[136:139], a[216:219]\n"
    "ds_read_b64_tr_b16 a[6:7], v247 offset:29664\n"
    "v_mul_f32 v11, v11, v250\n"
    "v_mfma_f32_16x16x32_bf16 a[220:223], a[40:43], v[132:135], 0\n"
    "v_rcp_f32 v6, v6\n"
    "v_mfma_f32_16x16x32_bf16 a[220:223], a[44:47], v[140:143], a[220:223]\n"
    "s_waitcnt lgkmcnt(8)\n"
    "ds_read_b64_tr_b16 a[8:9], v247 offset:12904\n"
    "ds_read_b64_tr_b16 a[10:11], v247 offset:13160\n"
    "v_mfma_f32_16x16x32_bf16 a[224:227], a[48:51], v[128:131], 0\n"
    "ds_read_b64_tr_b16 a[12:13], v247 offset:29416\n"
    "v_mul_f32 v62, v62, v6\n"
    "v_mfma_f32_16x16x32_bf16 a[224:227], a[52:55], v[136:139], a[224:227]\n"
    "v_exp_f32 v7, v7\n"
    "v_mfma_f32_16x16x32_bf16 a[228:231], a[48:51], v[132:135], 0\n"
    "ds_read_b64_tr_b16 a[14:15], v247 offset:29672\n"
    "v_add_f32 v7, 1.0, v7\n"
    "v_mfma_f32_16x16x32_bf16 a[228:231], a[52:55], v[140:143], a[228:231]\n"
    "s_waitcnt lgkmcnt(8)\n"
    "v_rcp_f32 v7, v7\n"
    "v_mfma_f32_16x16x32_bf16 a[232:235], a[56:59], v[128:131], 0\n"
    "s_waitcnt vmcnt(8)\n"
    "v_exp_f32 v8, v8\n"
    "v_mfma_f32_16x16x32_bf16 a[232:235], a[60:63], v[136:139], a[232:235]\n"
    "s_barrier\n"
    "v_add_u32 v247, 69632, v241\n"
    "ds_read_b64_tr_b16 a[16:17], v247 offset:0\n"
    "v_mfma_f32_16x16x32_bf16 a[236:239], a[56:59], v[132:135], 0\n"
    "s_add_u32 m0, s87, 0\n"
    "ds_read_b64_tr_b16 a[18:19], v247 offset:256\n"
    "ds_read_b64_tr_b16 a[20:21], v247 offset:16512\n"
    "v_mfma_f32_16x16x32_bf16 a[236:239], a[60:63], v[140:143], a[236:239]\n"
    "s_waitcnt lgkmcnt(7)\n"
    "ds_read_b64_tr_b16 a[22:23], v247 offset:16768\n"
    "ds_read_b64_tr_b16 a[24:25], v247 offset:8\n"
    "v_mfma_f32_16x16x32_bf16 a[240:243], a[0:3], v[128:131], 0\n"
    "buffer_load_dwordx4 v244, s[52:55], s29 offen offset:0 lds\n"
    "ds_read_b64_tr_b16 a[26:27], v247 offset:264\n"
    "v_mfma_f32_16x16x32_bf16 a[240:243], a[4:7], v[136:139], a[240:243]\n"
    "ds_read_b64_tr_b16 a[28:29], v247 offset:16520\n"
    "ds_read_b64_tr_b16 a[30:31], v247 offset:16776\n"
    "v_mfma_f32_16x16x32_bf16 a[244:247], a[0:3], v[132:135], 0\n"
    "ds_read_b64_tr_b16 a[32:33], v247 offset:512\n"
    "v_mul_f32 v63, v63, v7\n"
    "v_mfma_f32_16x16x32_bf16 a[244:247], a[4:7], v[140:143], a[244:247]\n"
    "s_waitcnt lgkmcnt(9)\n"
    "ds_read_b64_tr_b16 a[34:35], v247 offset:768\n"
    "v_cvt_pk_bf16_f32 v4, v60, v61\n"
    "v_mfma_f32_16x16x32_bf16 a[248:251], a[8:11], v[128:131], 0\n"
    "s_add_u32 m0, s87, 4000\n"
    "ds_read_b64_tr_b16 a[36:37], v247 offset:17024\n"
    "buffer_load_dwordx4 v244, s[52:55], s29 offen offset:128 lds\n"
    "v_mfma_f32_16x16x32_bf16 a[248:251], a[12:15], v[136:139], a[248:251]\n"
    "ds_read_b64_tr_b16 a[38:39], v247 offset:17280\n"
    "v_cvt_pk_bf16_f32 v5, v62, v63\n"
    "v_mfma_f32_16x16x32_bf16 a[252:255], a[8:11], v[132:135], 0\n"
    "ds_read_b64_tr_b16 a[40:41], v247 offset:520\n"
    "v_add_f32 v8, 1.0, v8\n"
    "v_mfma_f32_16x16x32_bf16 a[252:255], a[12:15], v[140:143], a[252:255]\n"
    "s_waitcnt lgkmcnt(9)\n"
    "v_rcp_f32 v8, v8\n"
    "v_mfma_f32_16x16x32_bf16 a[128:131], a[16:19], v[144:147], a[128:131]\n"
    "ds_read_b64_tr_b16 a[42:43], v247 offset:776\n"
    "ds_read_b64_tr_b16 a[44:45], v247 offset:17032\n"
    "v_mfma_f32_16x16x32_bf16 a[128:131], a[20:23], v[152:155], a[128:131]\n"
    "ds_read_b64_tr_b16 a[46:47], v247 offset:17288\n"
    "v_mul_f32 v64, v64, v8\n"
    "v_mfma_f32_16x16x32_bf16 a[132:135], a[16:19], v[148:151], a[132:135]\n"
    "v_exp_f32 v9, v9\n"
    "v_mfma_f32_16x16x32_bf16 a[132:135], a[20:23], v[156:159], a[132:135]\n"
    "s_waitcnt lgkmcnt(8)\n"
    "ds_read_b64_tr_b16 a[48:49], v247 offset:4128\n"
    "ds_read_b64_tr_b16 a[50:51], v247 offset:4384\n"
    "v_mfma_f32_16x16x32_bf16 a[136:139], a[24:27], v[144:147], a[136:139]\n"
    "s_add_u32 m0, s87, 8000\n"
    "ds_read_b64_tr_b16 a[52:53], v247 offset:20640\n"
    "buffer_load_dwordx4 v244, s[52:55], s29 offen offset:256 lds\n"
    "v_mfma_f32_16x16x32_bf16 a[136:139], a[28:31], v[152:155], a[136:139]\n"
    "ds_read_b64_tr_b16 a[54:55], v247 offset:20896\n"
    "v_add_f32 v9, 1.0, v9\n"
    "v_mfma_f32_16x16x32_bf16 a[140:143], a[24:27], v[148:151], a[140:143]\n"
    "ds_read_b64_tr_b16 a[56:57], v247 offset:4136\n"
    "v_mul_f32 v12, v12, v251\n"
    "v_mfma_f32_16x16x32_bf16 a[140:143], a[28:31], v[156:159], a[140:143]\n"
    "s_waitcnt lgkmcnt(9)\n"
    "ds_read_b64_tr_b16 a[58:59], v247 offset:4392\n"
    "v_mul_f32 v13, v13, v251\n"
    "v_mfma_f32_16x16x32_bf16 a[144:147], a[32:35], v[144:147], a[144:147]\n"
    "ds_read_b64_tr_b16 a[60:61], v247 offset:20648\n"
    "v_mul_f32 v14, v14, v251\n"
    "v_mfma_f32_16x16x32_bf16 a[144:147], a[36:39], v[152:155], a[144:147]\n"
    "ds_read_b64_tr_b16 a[62:63], v247 offset:20904\n"
    "v_mul_f32 v15, v15, v251\n"
    "v_mfma_f32_16x16x32_bf16 a[148:151], a[32:35], v[148:151], a[148:151]\n"
    "s_add_u32 m0, s87, 12000\n"
    "v_rcp_f32 v9, v9\n"
    "v_mfma_f32_16x16x32_bf16 a[148:151], a[36:39], v[156:159], a[148:151]\n"
    "s_waitcnt lgkmcnt(8)\n"
    "ds_read_b64_tr_b16 a[0:1], v247 offset:4640\n"
    "buffer_load_dwordx4 v244, s[52:55], s29 offen offset:384 lds\n"
    "v_mfma_f32_16x16x32_bf16 a[152:155], a[40:43], v[144:147], a[152:155]\n"
    "s_add_u32 s29, s29, 262144\n"
    "ds_read_b64_tr_b16 a[2:3], v247 offset:4896\n"
    "ds_read_b64_tr_b16 a[4:5], v247 offset:21152\n"
    "v_mfma_f32_16x16x32_bf16 a[152:155], a[44:47], v[152:155], a[152:155]\n"
    "ds_read_b64_tr_b16 a[6:7], v247 offset:21408\n"
    "v_mul_f32 v65, v65, v9\n"
    "v_mfma_f32_16x16x32_bf16 a[156:159], a[40:43], v[148:151], a[156:159]\n"
    "v_exp_f32 v10, v10\n"
    "v_mfma_f32_16x16x32_bf16 a[156:159], a[44:47], v[156:159], a[156:159]\n"
    "s_waitcnt lgkmcnt(8)\n"
    "ds_read_b64_tr_b16 a[8:9], v247 offset:4648\n"
    "ds_read_b64_tr_b16 a[10:11], v247 offset:4904\n"
    "v_mfma_f32_16x16x32_bf16 a[160:163], a[48:51], v[144:147], a[160:163]\n"
    "ds_read_b64_tr_b16 a[12:13], v247 offset:21160\n"
    "v_add_f32 v10, 1.0, v10\n"
    "v_mfma_f32_16x16x32_bf16 a[160:163], a[52:55], v[152:155], a[160:163]\n"
    "s_add_u32 m0, s87, 16512\n"
    "v_rcp_f32 v10, v10\n"
    "v_mfma_f32_16x16x32_bf16 a[164:167], a[48:51], v[148:151], a[164:167]\n"
    "ds_read_b64_tr_b16 a[14:15], v247 offset:21416\n"
    "buffer_load_dwordx4 v244, s[52:55], s29 offen offset:0 lds\n"
    "v_mfma_f32_16x16x32_bf16 a[164:167], a[52:55], v[156:159], a[164:167]\n"
    "s_waitcnt lgkmcnt(8)\n"
    "ds_read_b64_tr_b16 a[16:17], v247 offset:8256\n"
    "ds_read_b64_tr_b16 a[18:19], v247 offset:8512\n"
    "v_mfma_f32_16x16x32_bf16 a[168:171], a[56:59], v[144:147], a[168:171]\n"
    "ds_read_b64_tr_b16 a[20:21], v247 offset:24768\n"
    "v_mul_f32 v66, v66, v10\n"
    "v_mfma_f32_16x16x32_bf16 a[168:171], a[60:63], v[152:155], a[168:171]\n"
    "v_exp_f32 v11, v11\n"
    "v_mfma_f32_16x16x32_bf16 a[172:175], a[56:59], v[148:151], a[172:175]\n"
    "ds_read_b64_tr_b16 a[22:23], v247 offset:25024\n"
    "ds_read_b64_tr_b16 a[24:25], v247 offset:8264\n"
    "v_mfma_f32_16x16x32_bf16 a[172:175], a[60:63], v[156:159], a[172:175]\n"
    "s_waitcnt lgkmcnt(9)\n"
    "ds_read_b64_tr_b16 a[26:27], v247 offset:8520\n"
    "v_add_f32 v11, 1.0, v11\n"
    "v_mfma_f32_16x16x32_bf16 a[176:179], a[0:3], v[144:147], a[176:179]\n"
    "s_add_u32 m0, s87, 20512\n"
    "v_rcp_f32 v11, v11\n"
    "v_mfma_f32_16x16x32_bf16 a[176:179], a[4:7], v[152:155], a[176:179]\n"
    "buffer_load_dwordx4 v244, s[52:55], s29 offen offset:128 lds\n"
    "ds_read_b64_tr_b16 a[28:29], v247 offset:24776\n"
    "v_mfma_f32_16x16x32_bf16 a[180:183], a[0:3], v[148:151], a[180:183]\n"
    "ds_read_b64_tr_b16 a[30:31], v247 offset:25032\n"
    "ds_read_b64_tr_b16 a[32:33], v247 offset:8768\n"
    "v_mfma_f32_16x16x32_bf16 a[180:183], a[4:7], v[156:159], a[180:183]\n"
    "s_waitcnt lgkmcnt(9)\n"
    "ds_read_b64_tr_b16 a[34:35], v247 offset:9024\n"
    "v_mul_f32 v67, v67, v11\n"
    "v_mfma_f32_16x16x32_bf16 a[184:187], a[8:11], v[144:147], a[184:187]\n"
    "ds_read_b64_tr_b16 a[36:37], v247 offset:25280\n"
    "v_cvt_pk_bf16_f32 v2, v64, v65\n"
    "v_mfma_f32_16x16x32_bf16 a[184:187], a[12:15], v[152:155], a[184:187]\n"
    "ds_read_b64_tr_b16 a[38:39], v247 offset:25536\n"
    "v_cvt_pk_bf16_f32 v3, v66, v67\n"
    "v_mfma_f32_16x16x32_bf16 a[188:191], a[8:11], v[148:151], a[188:191]\n"
    "s_add_u32 m0, s87, 24512\n"
    "ds_read_b64_tr_b16 a[40:41], v247 offset:8776\n"
    "buffer_load_dwordx4 v244, s[52:55], s29 offen offset:256 lds\n"
    "v_mfma_f32_16x16x32_bf16 a[188:191], a[12:15], v[156:159], a[188:191]\n"
    "s_waitcnt lgkmcnt(9)\n"
    "ds_read_b64_tr_b16 a[42:43], v247 offset:9032\n"
    "buffer_store_dwordx4 v[0:3], v246, s[60:63], s92 offen offset:0 sc1\n"
    "v_mfma_f32_16x16x32_bf16 a[192:195], a[16:19], v[144:147], a[192:195]\n"
    "v_exp_f32 v12, v12\n"
    "v_mfma_f32_16x16x32_bf16 a[192:195], a[20:23], v[152:155], a[192:195]\n"
    "ds_read_b64_tr_b16 a[44:45], v247 offset:25288\n"
    "ds_read_b64_tr_b16 a[46:47], v247 offset:25544\n"
    "v_mfma_f32_16x16x32_bf16 a[196:199], a[16:19], v[148:151], a[196:199]\n"
    "ds_read_b64_tr_b16 a[48:49], v247 offset:12384\n"
    "v_add_f32 v12, 1.0, v12\n"
    "v_mfma_f32_16x16x32_bf16 a[196:199], a[20:23], v[156:159], a[196:199]\n"
    "s_waitcnt lgkmcnt(9)\n"
    "v_rcp_f32 v12, v12\n"
    "v_mfma_f32_16x16x32_bf16 a[200:203], a[24:27], v[144:147], a[200:203]\n"
    "ds_read_b64_tr_b16 a[50:51], v247 offset:12640\n"
    "ds_read_b64_tr_b16 a[52:53], v247 offset:28896\n"
    "v_mfma_f32_16x16x32_bf16 a[200:203], a[28:31], v[152:155], a[200:203]\n"
    "s_add_u32 m0, s87, 28512\n"
    "ds_read_b64_tr_b16 a[54:55], v247 offset:29152\n"
    "buffer_load_dwordx4 v244, s[52:55], s29 offen offset:384 lds\n"
    "v_mfma_f32_16x16x32_bf16 a[204:207], a[24:27], v[148:151], a[204:207]\n"
    "s_add_u32 s29, s29, 262144\n"
    "ds_read_b64_tr_b16 a[56:57], v247 offset:12392\n"
    "v_mul_f32 v68, v68, v12\n"
    "v_mfma_f32_16x16x32_bf16 a[204:207], a[28:31], v[156:159], a[204:207]\n"
    "s_waitcnt lgkmcnt(9)\n"
    "ds_read_b64_tr_b16 a[58:59], v247 offset:12648\n"
    "v_mul_f32 v16, v16, v250\n"
    "v_mfma_f32_16x16x32_bf16 a[208:211], a[32:35], v[144:147], a[208:211]\n"
    "ds_read_b64_tr_b16 a[60:61], v247 offset:28904\n"
    "v_mul_f32 v17, v17, v250\n"
    "v_mfma_f32_16x16x32_bf16 a[208:211], a[36:39], v[152:155], a[208:211]\n"
    "ds_read_b64_tr_b16 a[62:63], v247 offset:29160\n"
    "v_mul_f32 v18, v18, v250\n"
    "v_mfma_f32_16x16x32_bf16 a[212:215], a[32:35], v[148:151], a[212:215]\n"
    "ds_read_b64_tr_b16 a[0:1], v247 offset:12896\n"
    "v_mul_f32 v19, v19, v250\n"
    "v_mfma_f32_16x16x32_bf16 a[212:215], a[36:39], v[156:159], a[212:215]\n"
    "s_waitcnt lgkmcnt(9)\n"
    "v_exp_f32 v13, v13\n"
    "v_mfma_f32_16x16x32_bf16 a[216:219], a[40:43], v[144:147], a[216:219]\n"
    "ds_read_b64_tr_b16 a[2:3], v247 offset:13152\n"
    "ds_read_b64_tr_b16 a[4:5], v247 offset:29408\n"
    "v_mfma_f32_16x16x32_bf16 a[216:219], a[44:47], v[152:155], a[216:219]\n"
    "ds_read_b64_tr_b16 a[6:7], v247 offset:29664\n"
    "v_add_f32 v13, 1.0, v13\n"
    "v_mfma_f32_16x16x32_bf16 a[220:223], a[40:43], v[148:151], a[220:223]\n"
    "v_rcp_f32 v13, v13\n"
    "v_mfma_f32_16x16x32_bf16 a[220:223], a[44:47], v[156:159], a[220:223]\n"
    "s_waitcnt lgkmcnt(8)\n"
    "ds_read_b64_tr_b16 a[8:9], v247 offset:12904\n"
    "ds_read_b64_tr_b16 a[10:11], v247 offset:13160\n"
    "v_mfma_f32_16x16x32_bf16 a[224:227], a[48:51], v[144:147], a[224:227]\n"
    "ds_read_b64_tr_b16 a[12:13], v247 offset:29416\n"
    "v_mul_f32 v69, v69, v13\n"
    "v_mfma_f32_16x16x32_bf16 a[224:227], a[52:55], v[152:155], a[224:227]\n"
    "v_exp_f32 v14, v14\n"
    "v_mfma_f32_16x16x32_bf16 a[228:231], a[48:51], v[148:151], a[228:231]\n"
    "ds_read_b64_tr_b16 a[14:15], v247 offset:29672\n"
    "v_add_f32 v14, 1.0, v14\n"
    "v_mfma_f32_16x16x32_bf16 a[228:231], a[52:55], v[156:159], a[228:231]\n"
    "s_waitcnt lgkmcnt(8)\n"
    "v_rcp_f32 v14, v14\n"
    "v_mfma_f32_16x16x32_bf16 a[232:235], a[56:59], v[144:147], a[232:235]\n"
    "s_waitcnt vmcnt(9)\n"
    "v_exp_f32 v15, v15\n"
    "v_mfma_f32_16x16x32_bf16 a[232:235], a[60:63], v[152:155], a[232:235]\n"
    "s_barrier\n"
    "v_add_u32 v247, 104448, v241\n"
    "ds_read_b64_tr_b16 a[16:17], v247 offset:0\n"
    "v_mfma_f32_16x16x32_bf16 a[236:239], a[56:59], v[148:151], a[236:239]\n"
    "s_add_u32 m0, s87, 34816\n"
    "ds_read_b64_tr_b16 a[18:19], v247 offset:256\n"
    "ds_read_b64_tr_b16 a[20:21], v247 offset:16512\n"
    "v_mfma_f32_16x16x32_bf16 a[236:239], a[60:63], v[156:159], a[236:239]\n"
    "s_waitcnt lgkmcnt(7)\n"
    "ds_read_b64_tr_b16 a[22:23], v247 offset:16768\n"
    "ds_read_b64_tr_b16 a[24:25], v247 offset:8\n"
    "v_mfma_f32_16x16x32_bf16 a[240:243], a[0:3], v[144:147], a[240:243]\n"
    "buffer_load_dwordx4 v244, s[52:55], s29 offen offset:0 lds\n"
    "ds_read_b64_tr_b16 a[26:27], v247 offset:264\n"
    "v_mfma_f32_16x16x32_bf16 a[240:243], a[4:7], v[152:155], a[240:243]\n"
    "ds_read_b64_tr_b16 a[28:29], v247 offset:16520\n"
    "ds_read_b64_tr_b16 a[30:31], v247 offset:16776\n"
    "v_mfma_f32_16x16x32_bf16 a[244:247], a[0:3], v[148:151], a[244:247]\n"
    "ds_read_b64_tr_b16 a[32:33], v247 offset:512\n"
    "v_mul_f32 v70, v70, v14\n"
    "v_mfma_f32_16x16x32_bf16 a[244:247], a[4:7], v[156:159], a[244:247]\n"
    "s_waitcnt lgkmcnt(9)\n"
    "ds_read_b64_tr_b16 a[34:35], v247 offset:768\n"
    "v_add_f32 v15, 1.0, v15\n"
    "v_mfma_f32_16x16x32_bf16 a[248:251], a[8:11], v[144:147], a[248:251]\n"
    "s_add_u32 m0, s87, 38816\n"
    "v_rcp_f32 v15, v15\n"
    "v_mfma_f32_16x16x32_bf16 a[248:251], a[12:15], v[152:155], a[248:251]\n"
    "ds_read_b64_tr_b16 a[36:37], v247 offset:17024\n"
    "buffer_load_dwordx4 v244, s[52:55], s29 offen offset:128 lds\n"
    "v_mfma_f32_16x16x32_bf16 a[252:255], a[8:11], v[148:151], a[252:255]\n"
    "ds_read_b64_tr_b16 a[38:39], v247 offset:17280\n"
    "ds_read_b64_tr_b16 a[40:41], v247 offset:520\n"
    "v_mfma_f32_16x16x32_bf16 a[252:255], a[12:15], v[156:159], a[252:255]\n"
    "s_waitcnt lgkmcnt(9)\n"
    "ds_read_b64_tr_b16 a[42:43], v247 offset:776\n"
    "v_mul_f32 v71, v71, v15\n"
    "v_mfma_f32_16x16x32_bf16 a[128:131], a[16:19], v[160:163], a[128:131]\n"
    "s_waitcnt vmcnt(59)\n"
    "ds_read_b64_tr_b16 a[44:45], v247 offset:17032\n"
    "v_readfirstlane_b32 s99, v124\n"
    "v_mfma_f32_16x16x32_bf16 a[128:131], a[20:23], v[168:171], a[128:131]\n"
    "ds_read_b64_tr_b16 a[46:47], v247 offset:17288\n"
    "v_cvt_pk_bf16_f32 v6, v68, v69\n"
    "v_mfma_f32_16x16x32_bf16 a[132:135], a[16:19], v[164:167], a[132:135]\n"
    "ds_read_b64_tr_b16 a[48:49], v247 offset:4128\n"
    "v_readfirstlane_b32 s35, v127\n"
    "v_mfma_f32_16x16x32_bf16 a[132:135], a[20:23], v[172:175], a[132:135]\n"
    "s_waitcnt lgkmcnt(9)\n"
    "ds_read_b64_tr_b16 a[50:51], v247 offset:4384\n"
    "v_cvt_pk_bf16_f32 v7, v70, v71\n"
    "v_mfma_f32_16x16x32_bf16 a[136:139], a[24:27], v[160:163], a[136:139]\n"
    "s_add_u32 m0, s87, 42816\n"
    "ds_read_b64_tr_b16 a[52:53], v247 offset:20640\n"
    "buffer_load_dwordx4 v244, s[52:55], s29 offen offset:256 lds\n"
    "v_mfma_f32_16x16x32_bf16 a[136:139], a[28:31], v[168:171], a[136:139]\n"
    "s_mul_i32 s33, s35, 0x9e3779b1\n"
    "ds_read_b64_tr_b16 a[54:55], v247 offset:20896\n"
    "buffer_store_dwordx4 v[4:7], v246, s[60:63], s92 offen offset:1024 sc1\n"
    "v_mfma_f32_16x16x32_bf16 a[140:143], a[24:27], v[164:167], a[140:143]\n"
    "s_add_u32 s33, s33, 0x7f4a7c15\n"
    "ds_read_b64_tr_b16 a[56:57], v247 offset:4136\n"
    "v_mul_f32 v20, v20, v251\n"
    "v_mfma_f32_16x16x32_bf16 a[140:143], a[28:31], v[172:175], a[140:143]\n"
    "s_waitcnt lgkmcnt(9)\n"
    "ds_read_b64_tr_b16 a[58:59], v247 offset:4392\n"
    "v_mul_f32 v21, v21, v251\n"
    "v_mfma_f32_16x16x32_bf16 a[144:147], a[32:35], v[160:163], a[144:147]\n"
    "s_xor_b32 s34, s35, 0x5bd1e995\n"
    "ds_read_b64_tr_b16 a[60:61], v247 offset:20648\n"
    "v_mul_f32 v22, v22, v251\n"
    "v_mfma_f32_16x16x32_bf16 a[144:147], a[36:39], v[168:171], a[144:147]\n"
    "s_add_u32 s35, s35, 1\n"
    "ds_read_b64_tr_b16 a[62:63], v247 offset:20904\n"
    "v_mul_f32 v23, v23, v251\n"
    "v_mfma_f32_16x16x32_bf16 a[148:151], a[32:35], v[164:167], a[148:151]\n"
    "s_add_u32 m0, s87, 46816\n"
    "v_exp_f32 v16, v16\n"
    "v_mfma_f32_16x16x32_bf16 a[148:151], a[36:39], v[172:175], a[148:151]\n"
    "s_waitcnt lgkmcnt(8)\n"
    "ds_read_b64_tr_b16 a[0:1], v247 offset:4640\n"
    "buffer_load_dwordx4 v244, s[52:55], s29 offen offset:384 lds\n"
    "v_mfma_f32_16x16x32_bf16 a[152:155], a[40:43], v[160:163], a[152:155]\n"
    "s_add_u32 s29, s29, 262144\n"
    "ds_read_b64_tr_b16 a[2:3], v247 offset:4896\n"
    "ds_read_b64_tr_b16 a[4:5], v247 offset:21152\n"
    "v_mfma_f32_16x16x32_bf16 a[152:155], a[44:47], v[168:171], a[152:155]\n"
    "ds_read_b64_tr_b16 a[6:7], v247 offset:21408\n"
    "v_add_f32 v16, 1.0, v16\n"
    "v_mfma_f32_16x16x32_bf16 a[156:159], a[40:43], v[164:167], a[156:159]\n"
    "v_rcp_f32 v16, v16\n"
    "v_mfma_f32_16x16x32_bf16 a[156:159], a[44:47], v[172:175], a[156:159]\n"
    "s_waitcnt lgkmcnt(8)\n"
    "ds_read_b64_tr_b16 a[8:9], v247 offset:4648\n"
    "ds_read_b64_tr_b16 a[10:11], v247 offset:4904\n"
    "v_mfma_f32_16x16x32_bf16 a[160:163], a[48:51], v[160:163], a[160:163]\n"
    "ds_read_b64_tr_b16 a[12:13], v247 offset:21160\n"
    "v_mul_f32 v72, v72, v16\n"
    "v_mfma_f32_16x16x32_bf16 a[160:163], a[52:55], v[168:171], a[160:163]\n"
    "s_add_u32 m0, s87, 51328\n"
    "v_exp_f32 v17, v17\n"
    "v_mfma_f32_16x16x32_bf16 a[164:167], a[48:51], v[164:167], a[164:167]\n"
    "ds_read_b64_tr_b16 a[14:15], v247 offset:21416\n"
    "buffer_load_dwordx4 v244, s[52:55], s29 offen offset:0 lds\n"
    "v_mfma_f32_16x16x32_bf16 a[164:167], a[52:55], v[172:175], a[164:167]\n"
    "s_waitcnt lgkmcnt(8)\n"
    "ds_read_b64_tr_b16 a[16:17], v247 offset:8256\n"
    "ds_read_b64_tr_b16 a[18:19], v247 offset:8512\n"
    "v_mfma_f32_16x16x32_bf16 a[168:171], a[56:59], v[160:163], a[168:171]\n"
    "ds_read_b64_tr_b16 a[20:21], v247 offset:24768\n"
    "v_add_f32 v17, 1.0, v17\n"
    "v_mfma_f32_16x16x32_bf16 a[168:171], a[60:63], v[168:171], a[168:171]\n"
    "v_rcp_f32 v17, v17\n"
    "v_mfma_f32_16x16x32_bf16 a[172:175], a[56:59], v[164:167], a[172:175]\n"
    "ds_read_b64_tr_b16 a[22:23], v247 offset:25024\n"
    "ds_read_b64_tr_b16 a[24:25], v247 offset:8264\n"
    "v_mfma_f32_16x16x32_bf16 a[172:175], a[60:63], v[172:175], a[172:175]\n"
    "s_waitcnt lgkmcnt(9)\n"
    "ds_read_b64_tr_b16 a[26:27], v247 offset:8520\n"
    "v_mul_f32 v73, v73, v17\n"
    "v_mfma_f32_16x16x32_bf16 a[176:179], a[0:3], v[160:163], a[176:179]\n"
    "s_add_u32 m0, s87, 55328\n"
    "v_exp_f32 v18, v18\n"
    "v_mfma_f32_16x16x32_bf16 a[176:179], a[4:7], v[168:171], a[176:179]\n"
    "buffer_load_dwordx4 v244, s[52:55], s29 offen offset:128 lds\n"
    "ds_read_b64_tr_b16 a[28:29], v247 offset:24776\n"
    "v_mfma_f32_16x16x32_bf16 a[180:183], a[0:3], v[164:167], a[180:183]\n"
    "ds_read_b64_tr_b16 a[30:31], v247 offset:25032\n"
    "ds_read_b64_tr_b16 a[32:33], v247 offset:8768\n"
    "v_mfma_f32_16x16x32_bf16 a[180:183], a[4:7], v[172:175], a[180:183]\n"
    "s_waitcnt lgkmcnt(9)\n"
    "ds_read_b64_tr_b16 a[34:35], v247 offset:9024\n"
    "v_add_f32 v18, 1.0, v18\n"
    "v_mfma_f32_16x16x32_bf16 a[184:187], a[8:11], v[160:163], a[184:187]\n"
    "v_rcp_f32 v18, v18\n"
    "v_mfma_f32_16x16x32_bf16 a[184:187], a[12:15], v[168:171], a[184:187]\n"
    "ds_read_b64_tr_b16 a[36:37], v247 offset:25280\n"
    "ds_read_b64_tr_b16 a[38:39], v247 offset:25536\n"
    "v_mfma_f32_16x16x32_bf16 a[188:191], a[8:11], v[164:167], a[188:191]\n"
    "s_add_u32 m0, s87, 59328\n"
    "ds_read_b64_tr_b16 a[40:41], v247 offset:8776\n"
    "buffer_load_dwordx4 v244, s[52:55], s29 offen offset:256 lds\n"
    "v_mfma_f32_16x16x32_bf16 a[188:191], a[12:15], v[172:175], a[188:191]\n"
    "s_waitcnt lgkmcnt(9)\n"
    "ds_read_b64_tr_b16 a[42:43], v247 offset:9032\n"
    "v_mul_f32 v74, v74, v18\n"
    "v_mfma_f32_16x16x32_bf16 a[192:195], a[16:19], v[160:163], a[192:195]\n"
    "v_exp_f32 v19, v19\n"
    "v_mfma_f32_16x16x32_bf16 a[192:195], a[20:23], v[168:171], a[192:195]\n"
    "ds_read_b64_tr_b16 a[44:45], v247 offset:25288\n"
    "ds_read_b64_tr_b16 a[46:47], v247 offset:25544\n"
    "v_mfma_f32_16x16x32_bf16 a[196:199], a[16:19], v[164:167], a[196:199]\n"
    "ds_read_b64_tr_b16 a[48:49], v247 offset:12384\n"
    "v_add_f32 v19, 1.0, v19\n"
    "v_mfma_f32_16x16x32_bf16 a[196:199], a[20:23], v[172:175], a[196:199]\n"
    "s_waitcnt lgkmcnt(9)\n"
    "ds_read_b64_tr_b16 a[50:51], v247 offset:12640\n"
    "v_mul_f32 v24, v24, v250\n"
    "v_mfma_f32_16x16x32_bf16 a[200:203], a[24:27], v[160:163], a[200:203]\n"
    "ds_read_b64_tr_b16 a[52:53], v247 offset:28896\n"
    "v_mul_f32 v25, v25, v250\n"
    "v_mfma_f32_16x16x32_bf16 a[200:203], a[28:31], v[168:171], a[200:203]\n"
    "s_add_u32 m0, s87, 63328\n"
    "ds_read_b64_tr_b16 a[54:55], v247 offset:29152\n"
    "buffer_load_dwordx4 v244, s[52:55], s29 offen offset:384 lds\n"
    "v_mfma_f32_16x16x32_bf16 a[204:207], a[24:27], v[164:167], a[204:207]\n"
    "s_add_u32 s29, s29, 262144\n"
    "ds_read_b64_tr_b16 a[56:57], v247 offset:12392\n"
    "v_mul_f32 v26, v26, v250\n"
    "v_mfma_f32_16x16x32_bf16 a[204:207], a[28:31], v[172:175], a[204:207]\n"
    "s_waitcnt lgkmcnt(9)\n"
    "ds_read_b64_tr_b16 a[58:59], v247 offset:12648\n"
    "v_mul_f32 v27, v27, v250\n"
    "v_mfma_f32_16x16x32_bf16 a[208:211], a[32:35], v[160:163], a[208:211]\n"
    "v_rcp_f32 v19, v19\n"
    "v_mfma_f32_16x16x32_bf16 a[208:211], a[36:39], v[168:171], a[208:211]\n"
    "ds_read_b64_tr_b16 a[60:61], v247 offset:28904\n"
    "ds_read_b64_tr_b16 a[62:63], v247 offset:29160\n"
    "v_mfma_f32_16x16x32_bf16 a[212:215], a[32:35], v[164:167], a[212:215]\n"
    "ds_read_b64_tr_b16 a[0:1], v247 offset:12896\n"
    "v_mul_f32 v75, v75, v19\n"
    "v_mfma_f32_16x16x32_bf16 a[212:215], a[36:39], v[172:175], a[212:215]\n"
    "s_waitcnt lgkmcnt(9)\n"
    "ds_read_b64_tr_b16 a[2:3], v247 offset:13152\n"
    "v_cvt_pk_bf16_f32 v16, v72, v73\n"
    "v_mfma_f32_16x16x32_bf16 a[216:219], a[40:43], v[160:163], a[216:219]\n"
    "ds_read_b64_tr_b16 a[4:5], v247 offset:29408\n"
    "v_cvt_pk_bf16_f32 v17, v74, v75\n"
    "v_mfma_f32_16x16x32_bf16 a[216:219], a[44:47], v[168:171], a[216:219]\n"
    "v_exp_f32 v20, v20\n"
    "v_mfma_f32_16x16x32_bf16 a[220:223], a[40:43], v[164:167], a[220:223]\n"
    "ds_read_b64_tr_b16 a[6:7], v247 offset:29664\n"
    "ds_read_b64_tr_b16 a[8:9], v247 offset:12904\n"
    "v_mfma_f32_16x16x32_bf16 a[220:223], a[44:47], v[172:175], a[220:223]\n"
    "s_waitcnt lgkmcnt(9)\n"
    "ds_read_b64_tr_b16 a[10:11], v247 offset:13160\n"
    "v_add_f32 v20, 1.0, v20\n"
    "v_mfma_f32_16x16x32_bf16 a[224:227], a[48:51], v[160:163], a[224:227]\n"
    "v_rcp_f32 v20, v20\n"
    "v_mfma_f32_16x16x32_bf16 a[224:227], a[52:55], v[168:171], a[224:227]\n"
    "ds_read_b64_tr_b16 a[12:13], v247 offset:29416\n"
    "ds_read_b64_tr_b16 a[14:15], v247 offset:29672\n"
    "v_mfma_f32_16x16x32_bf16 a[228:231], a[48:51], v[164:167], a[228:231]\n"
    "s_waitcnt vmcnt(9)\n"
    "v_exp_f32 v21, v21\n"
    "v_mfma_f32_16x16x32_bf16 a[228:231], a[52:55], v[172:175], a[228:231]\n"
    "s_waitcnt lgkmcnt(8)\n"
    "v_exp_f32 v22, v22\n"
    "v_mfma_f32_16x16x32_bf16 a[232:235], a[56:59], v[160:163], a[232:235]\n"
    "s_barrier\n"
    "v_add_u32 v247, 0, v241\n"
    "ds_read_b64_tr_b16 a[16:17], v247 offset:0\n"
    "v_mfma_f32_16x16x32_bf16 a[232:235], a[60:63], v[168:171], a[232:235]\n"
    "ds_read_b64_tr_b16 a[18:19], v247 offset:256\n"
    "ds_read_b64_tr_b16 a[20:21], v247 offset:16512\n"
    "v_mfma_f32_16x16x32_bf16 a[236:239], a[56:59], v[164:167], a[236:239]\n"
    "s_add_u32 m0, s87, 69632\n"
    "ds_read_b64_tr_b16 a[22:23], v247 offset:16768\n"
    "ds_read_b64_tr_b16 a[24:25], v247 offset:8\n"
    "v_mfma_f32_16x16x32_bf16 a[236:239], a[60:63], v[172:175], a[236:239]\n"
    "s_waitcnt lgkmcnt(9)\n"
    "buffer_load_dwordx4 v244, s[52:55], s29 offen offset:0 lds\n"
    "ds_read_b64_tr_b16 a[26:27], v247 offset:264\n"
    "v_mfma_f32_16x16x32_bf16 a[240:243], a[0:3], v[160:163], a[240:243]\n"
    "ds_read_b64_tr_b16 a[28:29], v247 offset:16520\n"
    "v_mul_f32 v76, v76, v20\n"
    "v_mfma_f32_16x16x32_bf16 a[240:243], a[4:7], v[168:171], a[240:243]\n"
    "ds_read_b64_tr_b16 a[30:31], v247 offset:16776\n"
    "v_add_f32 v21, 1.0, v21\n"
    "v_mfma_f32_16x16x32_bf16 a[244:247], a[0:3], v[164:167], a[244:247]\n"
    "ds_read_b64_tr_b16 a[32:33], v247 offset:512\n"
    "v_add_f32 v22, 1.0, v22\n"
    "v_mfma_f32_16x16x32_bf16 a[244:247], a[4:7], v[172:175], a[244:247]\n"
    "s_waitcnt lgkmcnt(9)\n"
    "v_rcp_f32 v21, v21\n"
    "v_mfma_f32_16x16x32_bf16 a[248:251], a[8:11], v[160:163], a[248:251]\n"
    "s_add_u32 m0, s87, 73632\n"
    "ds_read_b64_tr_b16 a[34:35], v247 offset:768\n"
    "ds_read_b64_tr_b16 a[36:37], v247 offset:17024\n"
    "v_mfma_f32_16x16x32_bf16 a[248:251], a[12:15], v[168:171], a[248:251]\n"
    "buffer_load_dwordx4 v244, s[52:55], s29 offen offset:128 lds\n"
    "ds_read_b64_tr_b16 a[38:39], v247 offset:17280\n"
    "v_mfma_f32_16x16x32_bf16 a[252:255], a[8:11], v[164:167], a[252:255]\n"
    "ds_read_b64_tr_b16 a[40:41], v247 offset:520\n"
    "v_mul_f32 v77, v77, v21\n"
    "v_mfma_f32_16x16x32_bf16 a[252:255], a[12:15], v[172:175], a[252:255]\n"
    "s_waitcnt lgkmcnt(9)\n"
    "ds_read_b64_tr_b16 a[42:43], v247 offset:776\n"
    "v_mul_f32 v28, v28, v251\n"
    "v_mfma_f32_16x16x32_bf16 a[128:131], a[16:19], v[176:179], a[128:131]\n"
    "ds_read_b64_tr_b16 a[44:45], v247 offset:17032\n"
    "v_mul_f32 v29, v29, v251\n"
    "v_mfma_f32_16x16x32_bf16 a[128:131], a[20:23], v[184:187], a[128:131]\n"
    "ds_read_b64_tr_b16 a[46:47], v247 offset:17288\n"
    "v_mul_f32 v30, v30, v251\n"
    "v_mfma_f32_16x16x32_bf16 a[132:135], a[16:19], v[180:183], a[132:135]\n"
    "ds_read_b64_tr_b16 a[48:49], v247 offset:4128\n"
    "v_mul_f32 v31, v31, v251\n"
    "v_mfma_f32_16x16x32_bf16 a[132:135], a[20:23], v[188:191], a[132:135]\n"
    "s_waitcnt lgkmcnt(9)\n"
    "v_rcp_f32 v22, v22\n"
    "v_mfma_f32_16x16x32_bf16 a[136:139], a[24:27], v[176:179], a[136:139]\n"
    "s_add_u32 m0, s87, 77632\n"
    "ds_read_b64_tr_b16 a[50:51], v247 offset:4384\n"
    "buffer_load_dwordx4 v244, s[52:55], s29 offen offset:256 lds\n"
    "v_mfma_f32_16x16x32_bf16 a[136:139], a[28:31], v[184:187], a[136:139]\n"
    "ds_read_b64_tr_b16 a[52:53], v247 offset:20640\n"
    "ds_read_b64_tr_b16 a[54:55], v247 offset:20896\n"
    "v_mfma_f32_16x16x32_bf16 a[140:143], a[24:27], v[180:183], a[140:143]\n"
    "ds_read_b64_tr_b16 a[56:57], v247 offset:4136\n"
    "v_mul_f32 v78, v78, v22\n"
    "v_mfma_f32_16x16x32_bf16 a[140:143], a[28:31], v[188:191], a[140:143]\n"
    "s_waitcnt lgkmcnt(9)\n"
    "v_exp_f32 v23, v23\n"
    "v_mfma_f32_16x16x32_bf16 a[144:147], a[32:35], v[176:179], a[144:147]\n"
    "ds_read_b64_tr_b16 a[58:59], v247 offset:4392\n"
    "ds_read_b64_tr_b16 a[60:61], v247 offset:20648\n"
    "v_mfma_f32_16x16x32_bf16 a[144:147], a[36:39], v[184:187], a[144:147]\n"
    "ds_read_b64_tr_b16 a[62:63], v247 offset:20904\n"
    "v_add_f32 v23, 1.0, v23\n"
    "v_mfma_f32_16x16x32_bf16 a[148:151], a[32:35], v[180:183], a[148:151]\n"
    "s_add_u32 m0, s87, 81632\n"
    "v_rcp_f32 v23, v23\n"
    "v_mfma_f32_16x16x32_bf16 a[148:151], a[36:39], v[188:191], a[148:151]\n"
    "s_waitcnt lgkmcnt(8)\n"
    "ds_read_b64_tr_b16 a[0:1], v247 offset:4640\n"
    "buffer_load_dwordx4 v244, s[52:55], s29 offen offset:384 lds\n"
    "v_mfma_f32_16x16x32_bf16 a[152:155], a[40:43], v[176:179], a[152:155]\n"
    "s_add_u32 s29, s29, 262144\n"
    "ds_read_b64_tr_b16 a[2:3], v247 offset:4896\n"
    "ds_read_b64_tr_b16 a[4:5], v247 offset:21152\n"
    "v_mfma_f32_16x16x32_bf16 a[152:155], a[44:47], v[184:187], a[152:155]\n"
    "ds_read_b64_tr_b16 a[6:7], v247 offset:21408\n"
    "v_mul_f32 v79, v79, v23\n"
    "v_mfma_f32_16x16x32_bf16 a[156:159], a[40:43], v[180:183], a[156:159]\n"
    "ds_read_b64_tr_b16 a[8:9], v247 offset:4648\n"
    "v_cvt_pk_bf16_f32 v20, v76, v77\n"
    "v_mfma_f32_16x16x32_bf16 a[156:159], a[44:47], v[188:191], a[156:159]\n"
    "s_waitcnt lgkmcnt(9)\n"
    "ds_read_b64_tr_b16 a[10:11], v247 offset:4904\n"
    "v_cvt_pk_bf16_f32 v21, v78, v79\n"
    "v_mfma_f32_16x16x32_bf16 a[160:163], a[48:51], v[176:179], a[160:163]\n"
    "v_exp_f32 v24, v24\n"
    "v_mfma_f32_16x16x32_bf16 a[160:163], a[52:55], v[184:187], a[160:163]\n"
    "s_add_u32 m0, s87, 86144\n"
    "ds_read_b64_tr_b16 a[12:13], v247 offset:21160\n"
    "ds_read_b64_tr_b16 a[14:15], v247 offset:21416\n"
    "v_mfma_f32_16x16x32_bf16 a[164:167], a[48:51], v[180:183], a[164:167]\n"
    "buffer_load_dwordx4 v244, s[52:55], s29 offen offset:0 lds\n"
    "ds_read_b64_tr_b16 a[16:17], v247 offset:8256\n"
    "v_mfma_f32_16x16x32_bf16 a[164:167], a[52:55], v[188:191], a[164:167]\n"
    "s_waitcnt lgkmcnt(9)\n"
    "ds_read_b64_tr_b16 a[18:19], v247 offset:8512\n"
    "v_add_f32 v24, 1.0, v24\n"
    "v_mfma_f32_16x16x32_bf16 a[168:171], a[56:59], v[176:179], a[168:171]\n"
    "v_rcp_f32 v24, v24\n"
    "v_mfma_f32_16x16x32_bf16 a[168:171], a[60:63], v[184:187], a[168:171]\n"
    "ds_read_b64_tr_b16 a[20:21], v247 offset:24768\n"
    "ds_read_b64_tr_b16 a[22:23], v247 offset:25024\n"
    "v_mfma_f32_16x16x32_bf16 a[172:175], a[56:59], v[180:183], a[172:175]\n"
    "ds_read_b64_tr_b16 a[24:25], v247 offset:8264\n"
    "v_mul_f32 v80, v80, v24\n"
    "v_mfma_f32_16x16x32_bf16 a[172:175], a[60:63], v[188:191], a[172:175]\n"
    "s_waitcnt lgkmcnt(9)\n"
    "v_exp_f32 v25, v25\n"
    "v_mfma_f32_16x16x32_bf16 a[176:179], a[0:3], v[176:179], a[176:179]\n"
    "s_add_u32 m0, s87, 90144\n"
    "ds_read_b64_tr_b16 a[26:27], v247 offset:8520\n"
    "buffer_load_dwordx4 v244, s[52:55], s29 offen offset:128 lds\n"
    "v_mfma_f32_16x16x32_bf16 a[176:179], a[4:7], v[184:187], a[176:179]\n"
    "ds_read_b64_tr_b16 a[28:29], v247 offset:24776\n"
    "ds_read_b64_tr_b16 a[30:31], v247 offset:25032\n"
    "v_mfma_f32_16x16x32_bf16 a[180:183], a[0:3], v[180:183], a[180:183]\n"
    "ds_read_b64_tr_b16 a[32:33], v247 offset:8768\n"
    "v_add_f32 v25, 1.0, v25\n"
    "v_mfma_f32_16x16x32_bf16 a[180:183], a[4:7], v[188:191], a[180:183]\n"
    "s_waitcnt lgkmcnt(9)\n"
    "v_rcp_f32 v25, v25\n"
    "v_mfma_f32_16x16x32_bf16 a[184:187], a[8:11], v[176:179], a[184:187]\n"
    "ds_read_b64_tr_b16 a[34:35], v247 offset:9024\n"
    "ds_read_b64_tr_b16 a[36:37], v247 offset:25280\n"
    "v_mfma_f32_16x16x32_bf16 a[184:187], a[12:15], v[184:187], a[184:187]\n"
    "ds_read_b64_tr_b16 a[38:39], v247 offset:25536\n"
    "v_mul_f32 v81, v81, v25\n"
    "v_mfma_f32_16x16x32_bf16 a[188:191], a[8:11], v[180:183], a[188:191]\n"
    "s_add_u32 m0, s87, 94144\n"
    "ds_read_b64_tr_b16 a[40:41], v247 offset:8776\n"
    "buffer_load_dwordx4 v244, s[52:55], s29 offen offset:256 lds\n"
    "v_mfma_f32_16x16x32_bf16 a[188:191], a[12:15], v[188:191], a[188:191]\n"
    "s_waitcnt lgkmcnt(9)\n"
    "ds_read_b64_tr_b16 a[42:43], v247 offset:9032\n"
    "v_mul_f32 v32, v32, v250\n"
    "v_mfma_f32_16x16x32_bf16 a[192:195], a[16:19], v[176:179], a[192:195]\n"
    "ds_read_b64_tr_b16 a[44:45], v247 offset:25288\n"
    "v_mul_f32 v33, v33, v250\n"
    "v_mfma_f32_16x16x32_bf16 a[192:195], a[20:23], v[184:187], a[192:195]\n"
    "ds_read_b64_tr_b16 a[46:47], v247 offset:25544\n"
    "v_mul_f32 v34, v34, v250\n"
    "v_mfma_f32_16x16x32_bf16 a[196:199], a[16:19], v[180:183], a[196:199]\n"
    "ds_read_b64_tr_b16 a[48:49], v247 offset:12384\n"
    "v_mul_f32 v35, v35, v250\n"
    "v_mfma_f32_16x16x32_bf16 a[196:199], a[20:23], v[188:191], a[196:199]\n"
    "s_waitcnt lgkmcnt(9)\n"
    "v_exp_f32 v26, v26\n"
    "v_mfma_f32_16x16x32_bf16 a[200:203], a[24:27], v[176:179], a[200:203]\n"
    "ds_read_b64_tr_b16 a[50:51], v247 offset:12640\n"
    "ds_read_b64_tr_b16 a[52:53], v247 offset:28896\n"
    "v_mfma_f32_16x16x32_bf16 a[200:203], a[28:31], v[184:187], a[200:203]\n"
    "s_add_u32 m0, s87, 98144\n"
    "ds_read_b64_tr_b16 a[54:55], v247 offset:29152\n"
    "buffer_load_dwordx4 v244, s[52:55], s29 offen offset:384 lds\n"
    "v_mfma_f32_16x16x32_bf16 a[204:207], a[24:27], v[180:183], a[204:207]\n"
    "s_add_u32 s29, s29, 262144\n"
    "ds_read_b64_tr_b16 a[56:57], v247 offset:12392\n"
    "v_add_f32 v26, 1.0, v26\n"
    "v_mfma_f32_16x16x32_bf16 a[204:207], a[28:31], v[188:191], a[204:207]\n"
    "s_waitcnt lgkmcnt(9)\n"
    "v_rcp_f32 v26, v26\n"
    "v_mfma_f32_16x16x32_bf16 a[208:211], a[32:35], v[176:179], a[208:211]\n"
    "ds_read_b64_tr_b16 a[58:59], v247 offset:12648\n"
    "ds_read_b64_tr_b16 a[60:61], v247 offset:28904\n"
    "v_mfma_f32_16x16x32_bf16 a[208:211], a[36:39], v[184:187], a[208:211]\n"
    "ds_read_b64_tr_b16 a[62:63], v247 offset:29160\n"
    "v_mul_f32 v82, v82, v26\n"
    "v_mfma_f32_16x16x32_bf16 a[212:215], a[32:35], v[180:183], a[212:215]\n"
    "v_exp_f32 v27, v27\n"
    "v_mfma_f32_16x16x32_bf16 a[212:215], a[36:39], v[188:191], a[212:215]\n"
    "s_waitcnt lgkmcnt(8)\n"
    "ds_read_b64_tr_b16 a[0:1], v247 offset:12896\n"
    "ds_read_b64_tr_b16 a[2:3], v247 offset:13152\n"
    "v_mfma_f32_16x16x32_bf16 a[216:219], a[40:43], v[176:179], a[216:219]\n"
    "ds_read_b64_tr_b16 a[4:5], v247 offset:29408\n"
    "v_add_f32 v27, 1.0, v27\n"
    "v_mfma_f32_16x16x32_bf16 a[216:219], a[44:47], v[184:187], a[216:219]\n"
    "v_rcp_f32 v27, v27\n"
    "v_mfma_f32_16x16x32_bf16 a[220:223], a[40:43], v[180:183], a[220:223]\n"
    "ds_read_b64_tr_b16 a[6:7], v247 offset:29664\n"
    "ds_read_b64_tr_b16 a[8:9], v247 offset:12904\n"
    "v_mfma_f32_16x16x32_bf16 a[220:223], a[44:47], v[188:191], a[220:223]\n"
    "s_waitcnt lgkmcnt(9)\n"
    "ds_read_b64_tr_b16 a[10:11], v247 offset:13160\n"
    "v_mul_f32 v83, v83, v27\n"
    "v_mfma_f32_16x16x32_bf16 a[224:227], a[48:51], v[176:179], a[224:227]\n"
    "ds_read_b64_tr_b16 a[12:13], v247 offset:29416\n"
    "v_cvt_pk_bf16_f32 v18, v80, v81\n"
    "v_mfma_f32_16x16x32_bf16 a[224:227], a[52:55], v[184:187], a[224:227]\n"
    "ds_read_b64_tr_b16 a[14:15], v247 offset:29672\n"
    "v_cvt_pk_bf16_f32 v19, v82, v83\n"
    "v_mfma_f32_16x16x32_bf16 a[228:231], a[48:51], v[180:183], a[228:231]\n"
    "s_waitcnt vmcnt(8)\n"
    "v_exp_f32 v28, v28\n"
    "v_mfma_f32_16x16x32_bf16 a[228:231], a[52:55], v[188:191], a[228:231]\n"
    "s_waitcnt lgkmcnt(8)\n"
    "v_exp_f32 v29, v29\n"
    "v_mfma_f32_16x16x32_bf16 a[232:235], a[56:59], v[176:179], a[232:235]\n"
    "s_barrier\n"
    "v_add_u32 v247, 34816, v241\n"
    "ds_read_b64_tr_b16 a[16:17], v247 offset:0\n"
    "v_mfma_f32_16x16x32_bf16 a[232:235], a[60:63], v[184:187], a[232:235]\n"
    "ds_read_b64_tr_b16 a[18:19], v247 offset:256\n"
    "ds_read_b64_tr_b16 a[20:21], v247 offset:16512\n"
    "v_mfma_f32_16x16x32_bf16 a[236:239], a[56:59], v[180:183], a[236:239]\n"
    "s_add_u32 m0, s87, 104448\n"
    "ds_read_b64_tr_b16 a[22:23], v247 offset:16768\n"
    "ds_read_b64_tr_b16 a[24:25], v247 offset:8\n"
    "v_mfma_f32_16x16x32_bf16 a[236:239], a[60:63], v[188:191], a[236:239]\n"
    "s_waitcnt lgkmcnt(9)\n"
    "buffer_load_dwordx4 v244, s[52:55], s29 offen offset:0 lds\n"
    "ds_read_b64_tr_b16 a[26:27], v247 offset:264\n"
    "v_mfma_f32_16x16x32_bf16 a[240:243], a[0:3], v[176:179], a[240:243]\n"
    "ds_read_b64_tr_b16 a[28:29], v247 offset:16520\n"
    "buffer_store_dwordx4 v[16:19], v246, s[60:63], s92 offen offset:2048 sc1\n"
    "v_mfma_f32_16x16x32_bf16 a[240:243], a[4:7], v[184:187], a[240:243]\n"
    "ds_read_b64_tr_b16 a[30:31], v247 offset:16776\n"
    "v_add_f32 v28, 1.0, v28\n"
    "v_mfma_f32_16x16x32_bf16 a[244:247], a[0:3], v[180:183], a[244:247]\n"
    "ds_read_b64_tr_b16 a[32:33], v247 offset:512\n"
    "v_add_f32 v29, 1.0, v29\n"
    "v_mfma_f32_16x16x32_bf16 a[244:247], a[4:7], v[188:191], a[244:247]\n"
    "s_waitcnt lgkmcnt(9)\n"
    "ds_read_b64_tr_b16 a[34:35], v247 offset:768\n"
    "v_mul_f32 v36, v36, v251\n"
    "v_mfma_f32_16x16x32_bf16 a[248:251], a[8:11], v[176:179], a[248:251]\n"
    "s_add_u32 m0, s87, 108448\n"
    "ds_read_b64_tr_b16 a[36:37], v247 offset:17024\n"
    "buffer_load_dwordx4 v244, s[52:55], s29 offen offset:128 lds\n"
    "v_mfma_f32_16x16x32_bf16 a[248:251], a[12:15], v[184:187], a[248:251]\n"
    "ds_read_b64_tr_b16 a[38:39], v247 offset:17280\n"
    "v_mul_f32 v37, v37, v251\n"
    "v_mfma_f32_16x16x32_bf16 a[252:255], a[8:11], v[180:183], a[252:255]\n"
    "ds_read_b64_tr_b16 a[40:41], v247 offset:520\n"
    "v_mul_f32 v38, v38, v251\n"
    "v_mfma_f32_16x16x32_bf16 a[252:255], a[12:15], v[188:191], a[252:255]\n"
    "s_waitcnt lgkmcnt(9)\n"
    "ds_read_b64_tr_b16 a[42:43], v247 offset:776\n"
    "v_mul_f32 v39, v39, v251\n"
    "v_mfma_f32_16x16x32_bf16 a[128:131], a[16:19], v[192:195], a[128:131]\n"
    "v_rcp_f32 v28, v28\n"
    "v_mfma_f32_16x16x32_bf16 a[128:131], a[20:23], v[200:203], a[128:131]\n"
    "ds_read_b64_tr_b16 a[44:45], v247 offset:17032\n"
    "ds_read_b64_tr_b16 a[46:47], v247 offset:17288\n"
    "v_mfma_f32_16x16x32_bf16 a[132:135], a[16:19], v[196:199], a[132:135]\n"
    "ds_read_b64_tr_b16 a[48:49], v247 offset:4128\n"
    "v_mul_f32 v84, v84, v28\n"
    "v_mfma_f32_16x16x32_bf16 a[132:135], a[20:23], v[204:207], a[132:135]\n"
    "s_waitcnt lgkmcnt(9)\n"
    "v_rcp_f32 v29, v29\n"
    "v_mfma_f32_16x16x32_bf16 a[136:139], a[24:27], v[192:195], a[136:139]\n"
    "s_add_u32 m0, s87, 112448\n"
    "ds_read_b64_tr_b16 a[50:51], v247 offset:4384\n"
    "buffer_load_dwordx4 v244, s[52:55], s29 offen offset:256 lds\n"
    "v_mfma_f32_16x16x32_bf16 a[136:139], a[28:31], v[200:203], a[136:139]\n"
    "ds_read_b64_tr_b16 a[52:53], v247 offset:20640\n"
    "ds_read_b64_tr_b16 a[54:55], v247 offset:20896\n"
    "v_mfma_f32_16x16x32_bf16 a[140:143], a[24:27], v[196:199], a[140:143]\n"
    "ds_read_b64_tr_b16 a[56:57], v247 offset:4136\n"
    "v_mul_f32 v85, v85, v29\n"
    "v_mfma_f32_16x16x32_bf16 a[140:143], a[28:31], v[204:207], a[140:143]\n"
    "s_waitcnt lgkmcnt(9)\n"
    "v_exp_f32 v30, v30\n"
    "v_mfma_f32_16x16x32_bf16 a[144:147], a[32:35], v[192:195], a[144:147]\n"
    "ds_read_b64_tr_b16 a[58:59], v247 offset:4392\n"
    "ds_read_b64_tr_b16 a[60:61], v247 offset:20648\n"
    "v_mfma_f32_16x16x32_bf16 a[144:147], a[36:39], v[200:203], a[144:147]\n"
    "ds_read_b64_tr_b16 a[62:63], v247 offset:20904\n"
    "v_add_f32 v30, 1.0, v30\n"
    "v_mfma_f32_16x16x32_bf16 a[148:151], a[32:35], v[196:199], a[148:151]\n"
    "s_add_u32 m0, s87, 116448\n"
    "v_rcp_f32 v30, v30\n"
    "v_mfma_f32_16x16x32_bf16 a[148:151], a[36:39], v[204:207], a[148:151]\n"
    "s_waitcnt lgkmcnt(8)\n"
    "ds_read_b64_tr_b16 a[0:1], v247 offset:4640\n"
    "buffer_load_dwordx4 v244, s[52:55], s29 offen offset:384 lds\n"
    "v_mfma_f32_16x16x32_bf16 a[152:155], a[40:43], v[192:195], a[152:155]\n"
    "s_add_u32 s29, s29, 262144\n"
    "ds_read_b64_tr_b16 a[2:3], v247 offset:4896\n"
    "ds_read_b64_tr_b16 a[4:5], v247 offset:21152\n"
    "v_mfma_f32_16x16x32_bf16 a[152:155], a[44:47], v[200:203], a[152:155]\n"
    "ds_read_b64_tr_b16 a[6:7], v247 offset:21408\n"
    "v_mul_f32 v86, v86, v30\n"
    "v_mfma_f32_16x16x32_bf16 a[156:159], a[40:43], v[196:199], a[156:159]\n"
    "v_exp_f32 v31, v31\n"
    "v_mfma_f32_16x16x32_bf16 a[156:159], a[44:47], v[204:207], a[156:159]\n"
    "s_waitcnt lgkmcnt(8)\n"
    "ds_read_b64_tr_b16 a[8:9], v247 offset:4648\n"
    "ds_read_b64_tr_b16 a[10:11], v247 offset:4904\n"
    "v_mfma_f32_16x16x32_bf16 a[160:163], a[48:51], v[192:195], a[160:163]\n"
    "ds_read_b64_tr_b16 a[12:13], v247 offset:21160\n"
    "v_add_f32 v31, 1.0, v31\n"
    "v_mfma_f32_16x16x32_bf16 a[160:163], a[52:55], v[200:203], a[160:163]\n"
    "s_add_u32 m0, s87, 120960\n"
    "v_rcp_f32 v31, v31\n"
    "v_mfma_f32_16x16x32_bf16 a[164:167], a[48:51], v[196:199], a[164:167]\n"
    "ds_read_b64_tr_b16 a[14:15], v247 offset:21416\n"
    "buffer_load_dwordx4 v244, s[52:55], s29 offen offset:0 lds\n"
    "v_mfma_f32_16x16x32_bf16 a[164:167], a[52:55], v[204:207], a[164:167]\n"
    "s_waitcnt lgkmcnt(8)\n"
    "ds_read_b64_tr_b16 a[16:17], v247 offset:8256\n"
    "ds_read_b64_tr_b16 a[18:19], v247 offset:8512\n"
    "v_mfma_f32_16x16x32_bf16 a[168:171], a[56:59], v[192:195], a[168:171]\n"
    "ds_read_b64_tr_b16 a[20:21], v247 offset:24768\n"
    "v_mul_f32 v87, v87, v31\n"
    "v_mfma_f32_16x16x32_bf16 a[168:171], a[60:63], v[200:203], a[168:171]\n"
    "ds_read_b64_tr_b16 a[22:23], v247 offset:25024\n"
    "v_cvt_pk_bf16_f32 v22, v84, v85\n"
    "v_mfma_f32_16x16x32_bf16 a[172:175], a[56:59], v[196:199], a[172:175]\n"
    "ds_read_b64_tr_b16 a[24:25], v247 offset:8264\n"
    "v_cvt_pk_bf16_f32 v23, v86, v87\n"
    "v_mfma_f32_16x16x32_bf16 a[172:175], a[60:63], v[204:207], a[172:175]\n"
    "s_waitcnt lgkmcnt(9)\n"
    "ds_read_b64_tr_b16 a[26:27], v247 offset:8520\n"
    "buffer_store_dwordx4 v[20:23], v246, s[60:63], s92 offen offset:3072 sc1\n"
    "v_mfma_f32_16x16x32_bf16 a[176:179], a[0:3], v[192:195], a[176:179]\n"
    "s_add_u32 m0, s87, 124960\n"
    "v_exp_f32 v32, v32\n"
    "v_mfma_f32_16x16x32_bf16 a[176:179], a[4:7], v[200:203], a[176:179]\n"
    "buffer_load_dwordx4 v244, s[52:55], s29 offen offset:128 lds\n"
    "ds_read_b64_tr_b16 a[28:29], v247 offset:24776\n"
    "v_mfma_f32_16x16x32_bf16 a[180:183], a[0:3], v[196:199], a[180:183]\n"
    "ds_read_b64_tr_b16 a[30:31], v247 offset:25032\n"
    "ds_read_b64_tr_b16 a[32:33], v247 offset:8768\n"
    "v_mfma_f32_16x16x32_bf16 a[180:183], a[4:7], v[204:207], a[180:183]\n"
    "s_waitcnt lgkmcnt(9)\n"
    "ds_read_b64_tr_b16 a[34:35], v247 offset:9024\n"
    "v_add_f32 v32, 1.0, v32\n"
    "v_mfma_f32_16x16x32_bf16 a[184:187], a[8:11], v[192:195], a[184:187]\n"
    "ds_read_b64_tr_b16 a[36:37], v247 offset:25280\n"
    "v_mul_f32 v40, v40, v250\n"
    "v_mfma_f32_16x16x32_bf16 a[184:187], a[12:15], v[200:203], a[184:187]\n"
    "ds_read_b64_tr_b16 a[38:39], v247 offset:25536\n"
    "v_mul_f32 v41, v41, v250\n"
    "v_mfma_f32_16x16x32_bf16 a[188:191], a[8:11], v[196:199], a[188:191]\n"
    "s_add_u32 m0, s87, 128960\n"
    "ds_read_b64_tr_b16 a[40:41], v247 offset:8776\n"
    "buffer_load_dwordx4 v244, s[52:55], s29 offen offset:256 lds\n"
    "v_mfma_f32_16x16x32_bf16 a[188:191], a[12:15], v[204:207], a[188:191]\n"
    "s_waitcnt lgkmcnt(9)\n"
    "ds_read_b64_tr_b16 a[42:43], v247 offset:9032\n"
    "v_mul_f32 v42, v42, v250\n"
    "v_mfma_f32_16x16x32_bf16 a[192:195], a[16:19], v[192:195], a[192:195]\n"
    "ds_read_b64_tr_b16 a[44:45], v247 offset:25288\n"
    "v_mul_f32 v43, v43, v250\n"
    "v_mfma_f32_16x16x32_bf16 a[192:195], a[20:23], v[200:203], a[192:195]\n"
    "v_rcp_f32 v32, v32\n"
    "v_mfma_f32_16x16x32_bf16 a[196:199], a[16:19], v[196:199], a[196:199]\n"
    "ds_read_b64_tr_b16 a[46:47], v247 offset:25544\n"
    "ds_read_b64_tr_b16 a[48:49], v247 offset:12384\n"
    "v_mfma_f32_16x16x32_bf16 a[196:199], a[20:23], v[204:207], a[196:199]\n"
    "s_waitcnt lgkmcnt(9)\n"
    "ds_read_b64_tr_b16 a[50:51], v247 offset:12640\n"
    "v_mul_f32 v88, v88, v32\n"
    "v_mfma_f32_16x16x32_bf16 a[200:203], a[24:27], v[192:195], a[200:203]\n"
    "v_exp_f32 v33, v33\n"
    "v_mfma_f32_16x16x32_bf16 a[200:203], a[28:31], v[200:203], a[200:203]\n"
    "s_add_u32 m0, s87, 132960\n"
    "ds_read_b64_tr_b16 a[52:53], v247 offset:28896\n"
    "ds_read_b64_tr_b16 a[54:55], v247 offset:29152\n"
    "v_mfma_f32_16x16x32_bf16 a[204:207], a[24:27], v[196:199], a[204:207]\n"
    "buffer_load_dwordx4 v244, s[52:55], s29 offen offset:384 lds\n"
    "ds_read_b64_tr_b16 a[56:57], v247 offset:12392\n"
    "v_mfma_f32_16x16x32_bf16 a[204:207], a[28:31], v[204:207], a[204:207]\n"
    "s_waitcnt lgkmcnt(9)\n"
    "ds_read_b64_tr_b16 a[58:59], v247 offset:12648\n"
    "v_add_f32 v33, 1.0, v33\n"
    "v_mfma_f32_16x16x32_bf16 a[208:211], a[32:35], v[192:195], a[208:211]\n"
    "s_add_u32 s29, s29, 262144\n"
    "v_rcp_f32 v33, v33\n"
    "v_mfma_f32_16x16x32_bf16 a[208:211], a[36:39], v[200:203], a[208:211]\n"
    "ds_read_b64_tr_b16 a[60:61], v247 offset:28904\n"
    "ds_read_b64_tr_b16 a[62:63], v247 offset:29160\n"
    "v_mfma_f32_16x16x32_bf16 a[212:215], a[32:35], v[196:199], a[212:215]\n"
    "ds_read_b64_tr_b16 a[0:1], v247 offset:12896\n"
    "v_mul_f32 v89, v89, v33\n"
    "v_mfma_f32_16x16x32_bf16 a[212:215], a[36:39], v[204:207], a[212:215]\n"
    "s_waitcnt lgkmcnt(9)\n"
    "v_exp_f32 v34, v34\n"
    "v_mfma_f32_16x16x32_bf16 a[216:219], a[40:43], v[192:195], a[216:219]\n"
    "ds_read_b64_tr_b16 a[2:3], v247 offset:13152\n"
    "ds_read_b64_tr_b16 a[4:5], v247 offset:29408\n"
    "v_mfma_f32_16x16x32_bf16 a[216:219], a[44:47], v[200:203], a[216:219]\n"
    "ds_read_b64_tr_b16 a[6:7], v247 offset:29664\n"
    "v_add_f32 v34, 1.0, v34\n"
    "v_mfma_f32_16x16x32_bf16 a[220:223], a[40:43], v[196:199], a[220:223]\n"
    "v_rcp_f32 v34, v34\n"
    "v_mfma_f32_16x16x32_bf16 a[220:223], a[44:47], v[204:207], a[220:223]\n"
    "s_waitcnt lgkmcnt(8)\n"
    "ds_read_b64_tr_b16 a[8:9], v247 offset:12904\n"
    "ds_read_b64_tr_b16 a[10:11], v247 offset:13160\n"
    "v_mfma_f32_16x16x32_bf16 a[224:227], a[48:51], v[192:195], a[224:227]\n"
    "ds_read_b64_tr_b16 a[12:13], v247 offset:29416\n"
    "v_mul_f32 v90, v90, v34\n"
    "v_mfma_f32_16x16x32_bf16 a[224:227], a[52:55], v[200:203], a[224:227]\n"
    "v_exp_f32 v35, v35\n"
    "v_mfma_f32_16x16x32_bf16 a[228:231], a[48:51], v[196:199], a[228:231]\n"
    "ds_read_b64_tr_b16 a[14:15], v247 offset:29672\n"
    "v_add_f32 v35, 1.0, v35\n"
    "v_mfma_f32_16x16x32_bf16 a[228:231], a[52:55], v[204:207], a[228:231]\n"
    "s_waitcnt lgkmcnt(8)\n"
    "v_rcp_f32 v35, v35\n"
    "v_mfma_f32_16x16x32_bf16 a[232:235], a[56:59], v[192:195], a[232:235]\n"
    "s_waitcnt vmcnt(10)\n"
    "v_exp_f32 v36, v36\n"
    "v_mfma_f32_16x16x32_bf16 a[232:235], a[60:63], v[200:203], a[232:235]\n"
    "s_barrier\n"
    "v_add_u32 v247, 69632, v241\n"
    "ds_read_b64_tr_b16 a[16:17], v247 offset:0\n"
    "v_mfma_f32_16x16x32_bf16 a[236:239], a[56:59], v[196:199], a[236:239]\n"
    "s_add_u32 m0, s77, 0\n"
    "ds_read_b64_tr_b16 a[18:19], v247 offset:256\n"
    "ds_read_b64_tr_b16 a[20:21], v247 offset:16512\n"
    "v_mfma_f32_16x16x32_bf16 a[236:239], a[60:63], v[204:207], a[236:239]\n"
    "s_waitcnt lgkmcnt(7)\n"
    "ds_read_b64_tr_b16 a[22:23], v247 offset:16768\n"
    "ds_read_b64_tr_b16 a[24:25], v247 offset:8\n"
    "v_mfma_f32_16x16x32_bf16 a[240:243], a[0:3], v[192:195], a[240:243]\n"
    "buffer_load_dwordx4 v123, s[24:27], s72 offen offset:0 nt lds\n"
    "ds_read_b64_tr_b16 a[26:27], v247 offset:264\n"
    "v_mfma_f32_16x16x32_bf16 a[240:243], a[4:7], v[200:203], a[240:243]\n"
    "ds_read_b64_tr_b16 a[28:29], v247 offset:16520\n"
    "ds_read_b64_tr_b16 a[30:31], v247 offset:16776\n"
    "v_mfma_f32_16x16x32_bf16 a[244:247], a[0:3], v[196:199], a[244:247]\n"
    "ds_read_b64_tr_b16 a[32:33], v247 offset:512\n"
    "v_mul_f32 v91, v91, v35\n"
    "v_mfma_f32_16x16x32_bf16 a[244:247], a[4:7], v[204:207], a[244:247]\n"
    "s_waitcnt lgkmcnt(9)\n"
    "ds_read_b64_tr_b16 a[34:35], v247 offset:768\n"
    "v_cvt_pk_bf16_f32 v32, v88, v89\n"
    "v_mfma_f32_16x16x32_bf16 a[248:251], a[8:11], v[192:195], a[248:251]\n"
    "s_add_u32 m0, s77, 1984\n"
    "ds_read_b64_tr_b16 a[36:37], v247 offset:17024\n"
    "buffer_load_dwordx4 v123, s[24:27], s72 offen offset:128 nt lds\n"
    "v_mfma_f32_16x16x32_bf16 a[248:251], a[12:15], v[200:203], a[248:251]\n"
    "ds_read_b64_tr_b16 a[38:39], v247 offset:17280\n"
    "v_cvt_pk_bf16_f32 v33, v90, v91\n"
    "v_mfma_f32_16x16x32_bf16 a[252:255], a[8:11], v[196:199], a[252:255]\n"
    "ds_read_b64_tr_b16 a[40:41], v247 offset:520\n"
    "v_add_f32 v36, 1.0, v36\n"
    "v_mfma_f32_16x16x32_bf16 a[252:255], a[12:15], v[204:207], a[252:255]\n"
    "s_waitcnt lgkmcnt(9)\n"
    "ds_read_b64_tr_b16 a[42:43], v247 offset:776\n"
    "v_mul_f32 v44, v44, v251\n"
    "v_mfma_f32_16x16x32_bf16 a[128:131], a[16:19], v[208:211], a[128:131]\n"
    "ds_read_b64_tr_b16 a[44:45], v247 offset:17032\n"
    "v_mul_f32 v45, v45, v251\n"
    "v_mfma_f32_16x16x32_bf16 a[128:131], a[20:23], v[216:219], a[128:131]\n"
    "ds_read_b64_tr_b16 a[46:47], v247 offset:17288\n"
    "v_mul_f32 v46, v46, v251\n"
    "v_mfma_f32_16x16x32_bf16 a[132:135], a[16:19], v[212:215], a[132:135]\n"
    "ds_read_b64_tr_b16 a[48:49], v247 offset:4128\n"
    "v_mul_f32 v47, v47, v251\n"
    "v_mfma_f32_16x16x32_bf16 a[132:135], a[20:23], v[220:223], a[132:135]\n"
    "s_waitcnt lgkmcnt(9)\n"
    "v_rcp_f32 v36, v36\n"
    "v_mfma_f32_16x16x32_bf16 a[136:139], a[24:27], v[208:211], a[136:139]\n"
    "s_add_u32 m0, s77, 3968\n"
    "ds_read_b64_tr_b16 a[50:51], v247 offset:4384\n"
    "buffer_load_dwordx4 v123, s[24:27], s72 offen offset:256 nt lds\n"
    "v_mfma_f32_16x16x32_bf16 a[136:139], a[28:31], v[216:219], a[136:139]\n"
    "ds_read_b64_tr_b16 a[52:53], v247 offset:20640\n"
    "ds_read_b64_tr_b16 a[54:55], v247 offset:20896\n"
    "v_mfma_f32_16x16x32_bf16 a[140:143], a[24:27], v[212:215], a[140:143]\n"
    "ds_read_b64_tr_b16 a[56:57], v247 offset:4136\n"
    "v_mul_f32 v92, v92, v36\n"
    "v_mfma_f32_16x16x32_bf16 a[140:143], a[28:31], v[220:223], a[140:143]\n"
    "s_waitcnt lgkmcnt(9)\n"
    "v_exp_f32 v37, v37\n"
    "v_mfma_f32_16x16x32_bf16 a[144:147], a[32:35], v[208:211], a[144:147]\n"
    "ds_read_b64_tr_b16 a[58:59], v247 offset:4392\n"
    "ds_read_b64_tr_b16 a[60:61], v247 offset:20648\n"
    "v_mfma_f32_16x16x32_bf16 a[144:147], a[36:39], v[216:219], a[144:147]\n"
    "ds_read_b64_tr_b16 a[62:63], v247 offset:20904\n"
    "v_add_f32 v37, 1.0, v37\n"
    "v_mfma_f32_16x16x32_bf16 a[148:151], a[32:35], v[212:215], a[148:151]\n"
    "s_add_u32 m0, s77, 5952\n"
    "v_rcp_f32 v37, v37\n"
    "v_mfma_f32_16x16x32_bf16 a[148:151], a[36:39], v[220:223], a[148:151]\n"
    "s_waitcnt lgkmcnt(8)\n"
    "ds_read_b64_tr_b16 a[0:1], v247 offset:4640\n"
    "buffer_load_dwordx4 v123, s[24:27], s72 offen offset:384 nt lds\n"
    "v_mfma_f32_16x16x32_bf16 a[152:155], a[40:43], v[208:211], a[152:155]\n"
    "s_add_u32 s72, s72, 2048\n"
    "ds_read_b64_tr_b16 a[2:3], v247 offset:4896\n"
    "ds_read_b64_tr_b16 a[4:5], v247 offset:21152\n"
    "v_mfma_f32_16x16x32_bf16 a[152:155], a[44:47], v[216:219], a[152:155]\n"
    "ds_read_b64_tr_b16 a[6:7], v247 offset:21408\n"
    "v_mul_f32 v93, v93, v37\n"
    "v_mfma_f32_16x16x32_bf16 a[156:159], a[40:43], v[212:215], a[156:159]\n"
    "v_exp_f32 v38, v38\n"
    "v_mfma_f32_16x16x32_bf16 a[156:159], a[44:47], v[220:223], a[156:159]\n"
    "s_waitcnt lgkmcnt(8)\n"
    "ds_read_b64_tr_b16 a[8:9], v247 offset:4648\n"
    "ds_read_b64_tr_b16 a[10:11], v247 offset:4904\n"
    "v_mfma_f32_16x16x32_bf16 a[160:163], a[48:51], v[208:211], a[160:163]\n"
    "ds_read_b64_tr_b16 a[12:13], v247 offset:21160\n"
    "v_add_f32 v38, 1.0, v38\n"
    "v_mfma_f32_16x16x32_bf16 a[160:163], a[52:55], v[216:219], a[160:163]\n"
    "s_add_u32 m0, s77, 16896\n"
    "v_rcp_f32 v38, v38\n"
    "v_mfma_f32_16x16x32_bf16 a[164:167], a[48:51], v[212:215], a[164:167]\n"
    "ds_read_b64_tr_b16 a[14:15], v247 offset:21416\n"
    "buffer_load_dwordx4 v123, s[24:27], s72 offen offset:0 nt lds\n"
    "v_mfma_f32_16x16x32_bf16 a[164:167], a[52:55], v[220:223], a[164:167]\n"
    "s_waitcnt lgkmcnt(8)\n"
    "ds_read_b64_tr_b16 a[16:17], v247 offset:8256\n"
    "ds_read_b64_tr_b16 a[18:19], v247 offset:8512\n"
    "v_mfma_f32_16x16x32_bf16 a[168:171], a[56:59], v[208:211], a[168:171]\n"
    "ds_read_b64_tr_b16 a[20:21], v247 offset:24768\n"
    "v_mul_f32 v94, v94, v38\n"
    "v_mfma_f32_16x16x32_bf16 a[168:171], a[60:63], v[216:219], a[168:171]\n"
    "v_exp_f32 v39, v39\n"
    "v_mfma_f32_16x16x32_bf16 a[172:175], a[56:59], v[212:215], a[172:175]\n"
    "ds_read_b64_tr_b16 a[22:23], v247 offset:25024\n"
    "ds_read_b64_tr_b16 a[24:25], v247 offset:8264\n"
    "v_mfma_f32_16x16x32_bf16 a[172:175], a[60:63], v[220:223], a[172:175]\n"
    "s_waitcnt lgkmcnt(9)\n"
    "ds_read_b64_tr_b16 a[26:27], v247 offset:8520\n"
    "v_add_f32 v39, 1.0, v39\n"
    "v_mfma_f32_16x16x32_bf16 a[176:179], a[0:3], v[208:211], a[176:179]\n"
    "s_add_u32 m0, s77, 18880\n"
    "ds_read_b64_tr_b16 a[28:29], v247 offset:24776\n"
    "buffer_load_dwordx4 v123, s[24:27], s72 offen offset:128 nt lds\n"
    "v_mfma_f32_16x16x32_bf16 a[176:179], a[4:7], v[216:219], a[176:179]\n"
    "ds_read_b64_tr_b16 a[30:31], v247 offset:25032\n"
    "v_mul_f32 v48, v48, v250\n"
    "v_mfma_f32_16x16x32_bf16 a[180:183], a[0:3], v[212:215], a[180:183]\n"
    "ds_read_b64_tr_b16 a[32:33], v247 offset:8768\n"
    "v_mul_f32 v49, v49, v250\n"
    "v_mfma_f32_16x16x32_bf16 a[180:183], a[4:7], v[220:223], a[180:183]\n"
    "s_waitcnt lgkmcnt(9)\n"
    "ds_read_b64_tr_b16 a[34:35], v247 offset:9024\n"
    "v_mul_f32 v50, v50, v250\n"
    "v_mfma_f32_16x16x32_bf16 a[184:187], a[8:11], v[208:211], a[184:187]\n"
    "ds_read_b64_tr_b16 a[36:37], v247 offset:25280\n"
    "v_mul_f32 v51, v51, v250\n"
    "v_mfma_f32_16x16x32_bf16 a[184:187], a[12:15], v[216:219], a[184:187]\n"
    "v_rcp_f32 v39, v39\n"
    "v_mfma_f32_16x16x32_bf16 a[188:191], a[8:11], v[212:215], a[188:191]\n"
    "s_add_u32 m0, s77, 20864\n"
    "ds_read_b64_tr_b16 a[38:39], v247 offset:25536\n"
    "ds_read_b64_tr_b16 a[40:41], v247 offset:8776\n"
    "v_mfma_f32_16x16x32_bf16 a[188:191], a[12:15], v[220:223], a[188:191]\n"
    "s_waitcnt lgkmcnt(9)\n"
    "buffer_load_dwordx4 v123, s[24:27], s72 offen offset:256 nt lds\n"
    "ds_read_b64_tr_b16 a[42:43], v247 offset:9032\n"
    "v_mfma_f32_16x16x32_bf16 a[192:195], a[16:19], v[208:211], a[192:195]\n"
    "ds_read_b64_tr_b16 a[44:45], v247 offset:25288\n"
    "v_mul_f32 v95, v95, v39\n"
    "v_mfma_f32_16x16x32_bf16 a[192:195], a[20:23], v[216:219], a[192:195]\n"
    "ds_read_b64_tr_b16 a[46:47], v247 offset:25544\n"
    "v_cvt_pk_bf16_f32 v36, v92, v93\n"
    "v_mfma_f32_16x16x32_bf16 a[196:199], a[16:19], v[212:215], a[196:199]\n"
    "ds_read_b64_tr_b16 a[48:49], v247 offset:12384\n"
    "v_cvt_pk_bf16_f32 v37, v94, v95\n"
    "v_mfma_f32_16x16x32_bf16 a[196:199], a[20:23], v[220:223], a[196:199]\n"
    "s_waitcnt lgkmcnt(9)\n"
    "v_exp_f32 v40, v40\n"
    "v_mfma_f32_16x16x32_bf16 a[200:203], a[24:27], v[208:211], a[200:203]\n"
    "ds_read_b64_tr_b16 a[50:51], v247 offset:12640\n"
    "ds_read_b64_tr_b16 a[52:53], v247 offset:28896\n"
    "v_mfma_f32_16x16x32_bf16 a[200:203], a[28:31], v[216:219], a[200:203]\n"
    "s_add_u32 m0, s77, 22848\n"
    "ds_read_b64_tr_b16 a[54:55], v247 offset:29152\n"
    "buffer_load_dwordx4 v123, s[24:27], s72 offen offset:384 nt lds\n"
    "v_mfma_f32_16x16x32_bf16 a[204:207], a[24:27], v[212:215], a[204:207]\n"
    "s_add_u32 s72, s72, 14336\n"
    "ds_read_b64_tr_b16 a[56:57], v247 offset:12392\n"
    "v_add_f32 v40, 1.0, v40\n"
    "v_mfma_f32_16x16x32_bf16 a[204:207], a[28:31], v[220:223], a[204:207]\n"
    "s_waitcnt lgkmcnt(9)\n"
    "v_rcp_f32 v40, v40\n"
    "v_mfma_f32_16x16x32_bf16 a[208:211], a[32:35], v[208:211], a[208:211]\n"
    "ds_read_b64_tr_b16 a[58:59], v247 offset:12648\n"
    "ds_read_b64_tr_b16 a[60:61], v247 offset:28904\n"
    "v_mfma_f32_16x16x32_bf16 a[208:211], a[36:39], v[216:219], a[208:211]\n"
    "ds_read_b64_tr_b16 a[62:63], v247 offset:29160\n"
    "v_mul_f32 v96, v96, v40\n"
    "v_mfma_f32_16x16x32_bf16 a[212:215], a[32:35], v[212:215], a[212:215]\n"
    "v_exp_f32 v41, v41\n"
    "v_mfma_f32_16x16x32_bf16 a[212:215], a[36:39], v[220:223], a[212:215]\n"
    "s_waitcnt lgkmcnt(8)\n"
    "ds_read_b64_tr_b16 a[0:1], v247 offset:12896\n"
    "ds_read_b64_tr_b16 a[2:3], v247 offset:13152\n"
    "v_mfma_f32_16x16x32_bf16 a[216:219], a[40:43], v[208:211], a[216:219]\n"
    "ds_read_b64_tr_b16 a[4:5], v247 offset:29408\n"
    "v_add_f32 v41, 1.0, v41\n"
    "v_mfma_f32_16x16x32_bf16 a[216:219], a[44:47], v[216:219], a[216:219]\n"
    "v_rcp_f32 v41, v41\n"
    "v_mfma_f32_16x16x32_bf16 a[220:223], a[40:43], v[212:215], a[220:223]\n"
    "ds_read_b64_tr_b16 a[6:7], v247 offset:29664\n"
    "ds_read_b64_tr_b16 a[8:9], v247 offset:12904\n"
    "v_mfma_f32_16x16x32_bf16 a[220:223], a[44:47], v[220:223], a[220:223]\n"
    "s_waitcnt lgkmcnt(9)\n"
    "ds_read_b64_tr_b16 a[10:11], v247 offset:13160\n"
    "v_mul_f32 v97, v97, v41\n"
    "v_mfma_f32_16x16x32_bf16 a[224:227], a[48:51], v[208:211], a[224:227]\n"
    "v_exp_f32 v42, v42\n"
    "v_mfma_f32_16x16x32_bf16 a[224:227], a[52:55], v[216:219], a[224:227]\n"
    "ds_read_b64_tr_b16 a[12:13], v247 offset:29416\n"
    "ds_read_b64_tr_b16 a[14:15], v247 offset:29672\n"
    "v_mfma_f32_16x16x32_bf16 a[228:231], a[48:51], v[212:215], a[228:231]\n"
    "s_waitcnt vmcnt(8)\n"
    "v_exp_f32 v43, v43\n"
    "v_mfma_f32_16x16x32_bf16 a[228:231], a[52:55], v[220:223], a[228:231]\n"
    "s_waitcnt lgkmcnt(8)\n"
    "v_exp_f32 v44, v44\n"
    "v_mfma_f32_16x16x32_bf16 a[232:235], a[56:59], v[208:211], a[232:235]\n"
    "s_barrier\n"
    "v_add_u32 v247, 104448, v241\n"
    "ds_read_b64_tr_b16 a[16:17], v247 offset:0\n"
    "v_mfma_f32_16x16x32_bf16 a[232:235], a[60:63], v[216:219], a[232:235]\n"
    "ds_read_b64_tr_b16 a[18:19], v247 offset:256\n"
    "ds_read_b64_tr_b16 a[20:21], v247 offset:16512\n"
    "v_mfma_f32_16x16x32_bf16 a[236:239], a[56:59], v[212:215], a[236:239]\n"
    "s_add_u32 m0, s77, 33792\n"
    "ds_read_b64_tr_b16 a[22:23], v247 offset:16768\n"
    "ds_read_b64_tr_b16 a[24:25], v247 offset:8\n"
    "v_mfma_f32_16x16x32_bf16 a[236:239], a[60:63], v[220:223], a[236:239]\n"
    "s_waitcnt lgkmcnt(9)\n"
    "buffer_load_dwordx4 v123, s[24:27], s72 offen offset:0 nt lds\n"
    "ds_read_b64_tr_b16 a[26:27], v247 offset:264\n"
    "v_mfma_f32_16x16x32_bf16 a[240:243], a[0:3], v[208:211], a[240:243]\n"
    "ds_read_b64_tr_b16 a[28:29], v247 offset:16520\n"
    "v_add_f32 v42, 1.0, v42\n"
    "v_mfma_f32_16x16x32_bf16 a[240:243], a[4:7], v[216:219], a[240:243]\n"
    "ds_read_b64_tr_b16 a[30:31], v247 offset:16776\n"
    "v_add_f32 v43, 1.0, v43\n"
    "v_mfma_f32_16x16x32_bf16 a[244:247], a[0:3], v[212:215], a[244:247]\n"
    "ds_read_b64_tr_b16 a[32:33], v247 offset:512\n"
    "v_add_f32 v44, 1.0, v44\n"
    "v_mfma_f32_16x16x32_bf16 a[244:247], a[4:7], v[220:223], a[244:247]\n"
    "s_waitcnt lgkmcnt(9)\n"
    "ds_read_b64_tr_b16 a[34:35], v247 offset:768\n"
    "v_mul_f32 v52, v52, v251\n"
    "v_mfma_f32_16x16x32_bf16 a[248:251], a[8:11], v[208:211], a[248:251]\n"
    "s_add_u32 m0, s77, 35776\n"
    "ds_read_b64_tr_b16 a[36:37], v247 offset:17024\n"
    "buffer_load_dwordx4 v123, s[24:27], s72 offen offset:128 nt lds\n"
    "v_mfma_f32_16x16x32_bf16 a[248:251], a[12:15], v[216:219], a[248:251]\n"
    "ds_read_b64_tr_b16 a[38:39], v247 offset:17280\n"
    "v_mul_f32 v53, v53, v251\n"
    "v_mfma_f32_16x16x32_bf16 a[252:255], a[8:11], v[212:215], a[252:255]\n"
    "ds_read_b64_tr_b16 a[40:41], v247 offset:520\n"
    "v_mul_f32 v54, v54, v251\n"
    "v_mfma_f32_16x16x32_bf16 a[252:255], a[12:15], v[220:223], a[252:255]\n"
    "s_waitcnt lgkmcnt(9)\n"
    "ds_read_b64_tr_b16 a[42:43], v247 offset:776\n"
    "v_mul_f32 v55, v55, v251\n"
    "v_mfma_f32_16x16x32_bf16 a[128:131], a[16:19], v[224:227], a[128:131]\n"
    "v_rcp_f32 v42, v42\n"
    "v_mfma_f32_16x16x32_bf16 a[128:131], a[20:23], v[232:235], a[128:131]\n"
    "ds_read_b64_tr_b16 a[44:45], v247 offset:17032\n"
    "ds_read_b64_tr_b16 a[46:47], v247 offset:17288\n"
    "v_mfma_f32_16x16x32_bf16 a[132:135], a[16:19], v[228:231], a[132:135]\n"
    "ds_read_b64_tr_b16 a[48:49], v247 offset:4128\n"
    "v_mul_f32 v98, v98, v42\n"
    "v_mfma_f32_16x16x32_bf16 a[132:135], a[20:23], v[236:239], a[132:135]\n"
    "s_waitcnt lgkmcnt(9)\n"
    "v_rcp_f32 v43, v43\n"
    "v_mfma_f32_16x16x32_bf16 a[136:139], a[24:27], v[224:227], a[136:139]\n"
    "s_add_u32 m0, s77, 37760\n"
    "ds_read_b64_tr_b16 a[50:51], v247 offset:4384\n"
    "buffer_load_dwordx4 v123, s[24:27], s72 offen offset:256 nt lds\n"
    "v_mfma_f32_16x16x32_bf16 a[136:139], a[28:31], v[232:235], a[136:139]\n"
    "ds_read_b64_tr_b16 a[52:53], v247 offset:20640\n"
    "ds_read_b64_tr_b16 a[54:55], v247 offset:20896\n"
    "v_mfma_f32_16x16x32_bf16 a[140:143], a[24:27], v[228:231], a[140:143]\n"
    "ds_read_b64_tr_b16 a[56:57], v247 offset:4136\n"
    "v_mul_f32 v99, v99, v43\n"
    "v_mfma_f32_16x16x32_bf16 a[140:143], a[28:31], v[236:239], a[140:143]\n"
    "s_waitcnt lgkmcnt(9)\n"
    "ds_read_b64_tr_b16 a[58:59], v247 offset:4392\n"
    "v_cvt_pk_bf16_f32 v34, v96, v97\n"
    "v_mfma_f32_16x16x32_bf16 a[144:147], a[32:35], v[224:227], a[144:147]\n"
    "ds_read_b64_tr_b16 a[60:61], v247 offset:20648\n"
    "v_cvt_pk_bf16_f32 v35, v98, v99\n"
    "v_mfma_f32_16x16x32_bf16 a[144:147], a[36:39], v[232:235], a[144:147]\n"
    "ds_read_b64_tr_b16 a[62:63], v247 offset:20904\n"
    "buffer_store_dwordx4 v[32:35], v246, s[60:63], s93 offen offset:0 sc1\n"
    "v_mfma_f32_16x16x32_bf16 a[148:151], a[32:35], v[228:231], a[148:151]\n"
    "s_add_u32 m0, s77, 39744\n"
    "v_rcp_f32 v44, v44\n"
    "v_mfma_f32_16x16x32_bf16 a[148:151], a[36:39], v[236:239], a[148:151]\n"
    "s_waitcnt lgkmcnt(8)\n"
    "ds_read_b64_tr_b16 a[0:1], v247 offset:4640\n"
    "buffer_load_dwordx4 v123, s[24:27], s72 offen offset:384 nt lds\n"
    "v_mfma_f32_16x16x32_bf16 a[152:155], a[40:43], v[224:227], a[152:155]\n"
    "s_add_u32 s72, s72, 2048\n"
    "ds_read_b64_tr_b16 a[2:3], v247 offset:4896\n"
    "ds_read_b64_tr_b16 a[4:5], v247 offset:21152\n"
    "v_mfma_f32_16x16x32_bf16 a[152:155], a[44:47], v[232:235], a[152:155]\n"
    "ds_read_b64_tr_b16 a[6:7], v247 offset:21408\n"
    "v_mul_f32 v100, v100, v44\n"
    "v_mfma_f32_16x16x32_bf16 a[156:159], a[40:43], v[228:231], a[156:159]\n"
    "v_exp_f32 v45, v45\n"
    "v_mfma_f32_16x16x32_bf16 a[156:159], a[44:47], v[236:239], a[156:159]\n"
    "s_waitcnt lgkmcnt(8)\n"
    "ds_read_b64_tr_b16 a[8:9], v247 offset:4648\n"
    "ds_read_b64_tr_b16 a[10:11], v247 offset:4904\n"
    "v_mfma_f32_16x16x32_bf16 a[160:163], a[48:51], v[224:227], a[160:163]\n"
    "ds_read_b64_tr_b16 a[12:13], v247 offset:21160\n"
    "v_add_f32 v45, 1.0, v45\n"
    "v_mfma_f32_16x16x32_bf16 a[160:163], a[52:55], v[232:235], a[160:163]\n"
    "s_add_u32 m0, s77, 50688\n"
    "v_rcp_f32 v45, v45\n"
    "v_mfma_f32_16x16x32_bf16 a[164:167], a[48:51], v[228:231], a[164:167]\n"
    "ds_read_b64_tr_b16 a[14:15], v247 offset:21416\n"
    "buffer_load_dwordx4 v123, s[24:27], s72 offen offset:0 nt lds\n"
    "v_mfma_f32_16x16x32_bf16 a[164:167], a[52:55], v[236:239], a[164:167]\n"
    "s_waitcnt lgkmcnt(8)\n"
    "ds_read_b64_tr_b16 a[16:17], v247 offset:8256\n"
    "ds_read_b64_tr_b16 a[18:19], v247 offset:8512\n"
    "v_mfma_f32_16x16x32_bf16 a[168:171], a[56:59], v[224:227], a[168:171]\n"
    "ds_read_b64_tr_b16 a[20:21], v247 offset:24768\n"
    "v_mul_f32 v101, v101, v45\n"
    "v_mfma_f32_16x16x32_bf16 a[168:171], a[60:63], v[232:235], a[168:171]\n"
    "v_exp_f32 v46, v46\n"
    "v_mfma_f32_16x16x32_bf16 a[172:175], a[56:59], v[228:231], a[172:175]\n"
    "ds_read_b64_tr_b16 a[22:23], v247 offset:25024\n"
    "ds_read_b64_tr_b16 a[24:25], v247 offset:8264\n"
    "v_mfma_f32_16x16x32_bf16 a[172:175], a[60:63], v[236:239], a[172:175]\n"
    "s_waitcnt lgkmcnt(9)\n"
    "ds_read_b64_tr_b16 a[26:27], v247 offset:8520\n"
    "v_add_f32 v46, 1.0, v46\n"
    "v_mfma_f32_16x16x32_bf16 a[176:179], a[0:3], v[224:227], a[176:179]\n"
    "s_add_u32 m0, s77, 52672\n"
    "v_rcp_f32 v46, v46\n"
    "v_mfma_f32_16x16x32_bf16 a[176:179], a[4:7], v[232:235], a[176:179]\n"
    "buffer_load_dwordx4 v123, s[24:27], s72 offen offset:128 nt lds\n"
    "ds_read_b64_tr_b16 a[28:29], v247 offset:24776\n"
    "v_mfma_f32_16x16x32_bf16 a[180:183], a[0:3], v[228:231], a[180:183]\n"
    "ds_read_b64_tr_b16 a[30:31], v247 offset:25032\n"
    "ds_read_b64_tr_b16 a[32:33], v247 offset:8768\n"
    "v_mfma_f32_16x16x32_bf16 a[180:183], a[4:7], v[236:239], a[180:183]\n"
    "s_waitcnt lgkmcnt(9)\n"
    "ds_read_b64_tr_b16 a[34:35], v247 offset:9024\n"
    "v_mul_f32 v102, v102, v46\n"
    "v_mfma_f32_16x16x32_bf16 a[184:187], a[8:11], v[224:227], a[184:187]\n"
    "v_exp_f32 v47, v47\n"
    "v_mfma_f32_16x16x32_bf16 a[184:187], a[12:15], v[232:235], a[184:187]\n"
    "ds_read_b64_tr_b16 a[36:37], v247 offset:25280\n"
    "ds_read_b64_tr_b16 a[38:39], v247 offset:25536\n"
    "v_mfma_f32_16x16x32_bf16 a[188:191], a[8:11], v[228:231], a[188:191]\n"
    "s_add_u32 m0, s77, 54656\n"
    "ds_read_b64_tr_b16 a[40:41], v247 offset:8776\n"
    "buffer_load_dwordx4 v123, s[24:27], s72 offen offset:256 nt lds\n"
    "v_mfma_f32_16x16x32_bf16 a[188:191], a[12:15], v[236:239], a[188:191]\n"
    "s_waitcnt lgkmcnt(9)\n"
    "ds_read_b64_tr_b16 a[42:43], v247 offset:9032\n"
    "v_add_f32 v47, 1.0, v47\n"
    "v_mfma_f32_16x16x32_bf16 a[192:195], a[16:19], v[224:227], a[192:195]\n"
    "v_rcp_f32 v47, v47\n"
    "v_mfma_f32_16x16x32_bf16 a[192:195], a[20:23], v[232:235], a[192:195]\n"
    "ds_read_b64_tr_b16 a[44:45], v247 offset:25288\n"
    "ds_read_b64_tr_b16 a[46:47], v247 offset:25544\n"
    "v_mfma_f32_16x16x32_bf16 a[196:199], a[16:19], v[228:231], a[196:199]\n"
    "ds_read_b64_tr_b16 a[48:49], v247 offset:12384\n"
    "v_mul_f32 v103, v103, v47\n"
    "v_mfma_f32_16x16x32_bf16 a[196:199], a[20:23], v[236:239], a[196:199]\n"
    "s_waitcnt lgkmcnt(9)\n"
    "ds_read_b64_tr_b16 a[50:51], v247 offset:12640\n"
    "v_cvt_pk_bf16_f32 v38, v100, v101\n"
    "v_mfma_f32_16x16x32_bf16 a[200:203], a[24:27], v[224:227], a[200:203]\n"
    "ds_read_b64_tr_b16 a[52:53], v247 offset:28896\n"
    "v_cvt_pk_bf16_f32 v39, v102, v103\n"
    "v_mfma_f32_16x16x32_bf16 a[200:203], a[28:31], v[232:235], a[200:203]\n"
    "s_add_u32 m0, s77, 56640\n"
    "ds_read_b64_tr_b16 a[54:55], v247 offset:29152\n"
    "buffer_load_dwordx4 v123, s[24:27], s72 offen offset:384 nt lds\n"
    "v_mfma_f32_16x16x32_bf16 a[204:207], a[24:27], v[228:231], a[204:207]\n"
    "s_add_u32 s72, s72, 14336\n"
    "ds_read_b64_tr_b16 a[56:57], v247 offset:12392\n"
    "buffer_store_dwordx4 v[36:39], v246, s[60:63], s93 offen offset:1024 sc1\n"
    "v_mfma_f32_16x16x32_bf16 a[204:207], a[28:31], v[236:239], a[204:207]\n"
    "s_waitcnt lgkmcnt(9)\n"
    "v_exp_f32 v48, v48\n"
    "v_mfma_f32_16x16x32_bf16 a[208:211], a[32:35], v[224:227], a[208:211]\n"
    "ds_read_b64_tr_b16 a[58:59], v247 offset:12648\n"
    "ds_read_b64_tr_b16 a[60:61], v247 offset:28904\n"
    "v_mfma_f32_16x16x32_bf16 a[208:211], a[36:39], v[232:235], a[208:211]\n"
    "ds_read_b64_tr_b16 a[62:63], v247 offset:29160\n"
    "v_add_f32 v48, 1.0, v48\n"
    "v_mfma_f32_16x16x32_bf16 a[212:215], a[32:35], v[228:231], a[212:215]\n"
    "v_rcp_f32 v48, v48\n"
    "v_mfma_f32_16x16x32_bf16 a[212:215], a[36:39], v[236:239], a[212:215]\n"
    "s_waitcnt lgkmcnt(8)\n"
    "ds_read_b64_tr_b16 a[0:1], v247 offset:12896\n"
    "ds_read_b64_tr_b16 a[2:3], v247 offset:13152\n"
    "v_mfma_f32_16x16x32_bf16 a[216:219], a[40:43], v[224:227], a[216:219]\n"
    "ds_read_b64_tr_b16 a[4:5], v247 offset:29408\n"
    "v_mul_f32 v104, v104, v48\n"
    "v_mfma_f32_16x16x32_bf16 a[216:219], a[44:47], v[232:235], a[216:219]\n"
    "v_exp_f32 v49, v49\n"
    "v_mfma_f32_16x16x32_bf16 a[220:223], a[40:43], v[228:231], a[220:223]\n"
    "ds_read_b64_tr_b16 a[6:7], v247 offset:29664\n"
    "ds_read_b64_tr_b16 a[8:9], v247 offset:12904\n"
    "v_mfma_f32_16x16x32_bf16 a[220:223], a[44:47], v[236:239], a[220:223]\n"
    "s_waitcnt lgkmcnt(9)\n"
    "ds_read_b64_tr_b16 a[10:11], v247 offset:13160\n"
    "v_add_f32 v49, 1.0, v49\n"
    "v_mfma_f32_16x16x32_bf16 a[224:227], a[48:51], v[224:227], a[224:227]\n"
    "v_rcp_f32 v49, v49\n"
    "v_mfma_f32_16x16x32_bf16 a[224:227], a[52:55], v[232:235], a[224:227]\n"
    "ds_read_b64_tr_b16 a[12:13], v247 offset:29416\n"
    "ds_read_b64_tr_b16 a[14:15], v247 offset:29672\n"
    "v_mfma_f32_16x16x32_bf16 a[228:231], a[48:51], v[228:231], a[228:231]\n"
    "v_exp_f32 v50, v50\n"
    "v_mfma_f32_16x16x32_bf16 a[228:231], a[52:55], v[236:239], a[228:231]\n"
    "s_waitcnt lgkmcnt(8)\n"
    "v_exp_f32 v51, v51\n"
    "v_mfma_f32_16x16x32_bf16 a[232:235], a[56:59], v[224:227], a[232:235]\n"
    "v_exp_f32 v52, v52\n"
    "v_mfma_f32_16x16x32_bf16 a[232:235], a[60:63], v[232:235], a[232:235]\n"
    "v_exp_f32 v53, v53\n"
    "v_mfma_f32_16x16x32_bf16 a[236:239], a[56:59], v[228:231], a[236:239]\n"
    "v_exp_f32 v54, v54\n"
    "v_mfma_f32_16x16x32_bf16 a[236:239], a[60:63], v[236:239], a[236:239]\n"
    "s_waitcnt lgkmcnt(4)\n"
    "v_exp_f32 v55, v55\n"
    "v_mfma_f32_16x16x32_bf16 a[240:243], a[0:3], v[224:227], a[240:243]\n"
    "v_mul_f32 v105, v105, v49\n"
    "v_add_f32 v50, 1.0, v50\n"
    "v_mfma_f32_16x16x32_bf16 a[240:243], a[4:7], v[232:235], a[240:243]\n"
    "v_rcp_f32 v50, v50\n"
    "v_mfma_f32_16x16x32_bf16 a[244:247], a[0:3], v[228:231], a[244:247]\n"
    "v_mul_f32 v106, v106, v50\n"
    "v_add_f32 v51, 1.0, v51\n"
    "v_mfma_f32_16x16x32_bf16 a[244:247], a[4:7], v[236:239], a[244:247]\n"
    "s_waitcnt lgkmcnt(0)\n"
    "v_rcp_f32 v51, v51\n"
    "v_mfma_f32_16x16x32_bf16 a[248:251], a[8:11], v[224:227], a[248:251]\n"
    "v_mul_f32 v107, v107, v51\n"
    "v_cvt_pk_bf16_f32 v48, v104, v105\n"
    "v_mfma_f32_16x16x32_bf16 a[248:251], a[12:15], v[232:235], a[248:251]\n"
    "v_cvt_pk_bf16_f32 v49, v106, v107\n"
    "v_add_f32 v52, 1.0, v52\n"
    "v_mfma_f32_16x16x32_bf16 a[252:255], a[8:11], v[228:231], a[252:255]\n"
    "v_rcp_f32 v52, v52\n"
    "v_mfma_f32_16x16x32_bf16 a[252:255], a[12:15], v[236:239], a[252:255]\n"
    "v_mul_f32 v108, v108, v52\n"
    "v_add_f32 v53, 1.0, v53\n"
    "v_rcp_f32 v53, v53\n"
    "v_add_f32 v54, 1.0, v54\n"
    "v_mul_f32 v109, v109, v53\n"
    "v_rcp_f32 v54, v54\n"
    "v_add_f32 v55, 1.0, v55\n"
    "v_mul_f32 v110, v110, v54\n"
    "v_rcp_f32 v55, v55\n"
    "s_nop 0\n"
    "v_mul_f32 v111, v111, v55\n"
    "v_cvt_pk_bf16_f32 v50, v108, v109\n"
    "v_cvt_pk_bf16_f32 v51, v110, v111\n"
    "buffer_store_dwordx4 v[48:51], v246, s[60:63], s93 offen offset:2048 sc1\n"
    "buffer_load_dword v125, v125, s[56:59], 0 offen\n"
    "buffer_load_dword v126, v126, s[48:51], 0 offen\n"
    "s_barrier\n"
    "s_add_u32 m0, s78, 0\n"
    "s_nop 0\n"
    "buffer_load_dwordx4 v122, s[36:39], s73 offen offset:0 nt lds\n"
    "s_add_u32 m0, s78, 1056\n"
    "s_nop 0\n"
    "buffer_load_dwordx4 v122, s[36:39], s74 offen offset:0 nt lds\n"
    "s_add_u32 m0, s78, 2112\n"
    "s_nop 0\n"
    "buffer_load_dwordx4 v122, s[36:39], s75 offen offset:0 nt lds\n"
    "s_add_u32 m0, s78, 3168\n"
    "s_nop 0\n"
    "buffer_load_dwordx4 v122, s[36:39], s76 offen offset:0 nt lds\n"
    "v_accvgpr_read_b32 v80, a128\n"
    "v_accvgpr_read_b32 v81, a129\n"
    "v_accvgpr_read_b32 v82, a130\n"
    "v_accvgpr_read_b32 v83, a131\n"
    "v_pk_mul_f32 v[80:81], v[80:81], v[248:249] op_sel_hi:[1,0]\n"
    "v_pk_mul_f32 v[82:83], v[82:83], v[248:249] op_sel_hi:[1,0]\n"
    "v_cvt_pk_bf16_f32 v64, v80, v81\n"
    "v_cvt_pk_bf16_f32 v65, v82, v83\n"
    "v_accvgpr_read_b32 v84, a136\n"
    "v_accvgpr_read_b32 v85, a137\n"
    "v_accvgpr_read_b32 v86, a138\n"
    "v_accvgpr_read_b32 v87, a139\n"
    "v_pk_mul_f32 v[84:85], v[84:85], v[248:249] op_sel_hi:[1,0]\n"
    "v_pk_mul_f32 v[86:87], v[86:87], v[248:249] op_sel_hi:[1,0]\n"
    "v_cvt_pk_bf16_f32 v66, v84, v85\n"
    "v_cvt_pk_bf16_f32 v67, v86, v87\n"
    "ds_write_b128 v254, v[64:67] offset:0\n"
    "v_accvgpr_read_b32 v88, a132\n"
    "v_accvgpr_read_b32 v89, a133\n"
    "v_accvgpr_read_b32 v90, a134\n"
    "v_accvgpr_read_b32 v91, a135\n"
    "v_pk_mul_f32 v[88:89], v[88:89], v[248:249] op_sel:[0,1]\n"
    "v_pk_mul_f32 v[90:91], v[90:91], v[248:249] op_sel:[0,1]\n"
    "v_cvt_pk_bf16_f32 v68, v88, v89\n"
    "v_cvt_pk_bf16_f32 v69, v90, v91\n"
    "v_accvgpr_read_b32 v92, a140\n"
    "v_accvgpr_read_b32 v93, a141\n"
    "v_accvgpr_read_b32 v94, a142\n"
    "v_accvgpr_read_b32 v95, a143\n"
    "v_pk_mul_f32 v[92:93], v[92:93], v[248:249] op_sel:[0,1]\n"
    "v_pk_mul_f32 v[94:95], v[94:95], v[248:249] op_sel:[0,1]\n"
    "v_cvt_pk_bf16_f32 v70, v92, v93\n"
    "v_cvt_pk_bf16_f32 v71, v94, v95\n"
    "ds_write_b128 v254, v[68:71] offset:1024\n"
    "v_accvgpr_read_b32 v96, a144\n"
    "v_accvgpr_read_b32 v97, a145\n"
    "v_accvgpr_read_b32 v98, a146\n"
    "v_accvgpr_read_b32 v99, a147\n"
    "v_pk_mul_f32 v[96:97], v[96:97], v[248:249] op_sel_hi:[1,0]\n"
    "v_pk_mul_f32 v[98:99], v[98:99], v[248:249] op_sel_hi:[1,0]\n"
    "v_cvt_pk_bf16_f32 v72, v96, v97\n"
    "v_cvt_pk_bf16_f32 v73, v98, v99\n"
    "v_accvgpr_read_b32 v100, a152\n"
    "v_accvgpr_read_b32 v101, a153\n"
    "v_accvgpr_read_b32 v102, a154\n"
    "v_accvgpr_read_b32 v103, a155\n"
    "v_pk_mul_f32 v[100:101], v[100:101], v[248:249] op_sel_hi:[1,0]\n"
    "v_pk_mul_f32 v[102:103], v[102:103], v[248:249] op_sel_hi:[1,0]\n"
    "v_cvt_pk_bf16_f32 v74, v100, v101\n"
    "v_cvt_pk_bf16_f32 v75, v102, v103\n"
    "ds_write_b128 v254, v[72:75] offset:8192\n"
    "v_accvgpr_read_b32 v104, a148\n"
    "v_accvgpr_read_b32 v105, a149\n"
    "v_accvgpr_read_b32 v106, a150\n"
    "v_accvgpr_read_b32 v107, a151\n"
    "v_pk_mul_f32 v[104:105], v[104:105], v[248:249] op_sel:[0,1]\n"
    "v_pk_mul_f32 v[106:107], v[106:107], v[248:249] op_sel:[0,1]\n"
    "v_cvt_pk_bf16_f32 v76, v104, v105\n"
    "v_cvt_pk_bf16_f32 v77, v106, v107\n"
    "v_accvgpr_read_b32 v108, a156\n"
    "v_accvgpr_read_b32 v109, a157\n"
    "v_accvgpr_read_b32 v110, a158\n"
    "v_accvgpr_read_b32 v111, a159\n"
    "v_pk_mul_f32 v[108:109], v[108:109], v[248:249] op_sel:[0,1]\n"
    "v_pk_mul_f32 v[110:111], v[110:111], v[248:249] op_sel:[0,1]\n"
    "v_cvt_pk_bf16_f32 v78, v108, v109\n"
    "v_cvt_pk_bf16_f32 v79, v110, v111\n"
    "ds_write_b128 v254, v[76:79] offset:9216\n"
    "v_accvgpr_read_b32 v80, a160\n"
    "v_accvgpr_read_b32 v81, a161\n"
    "v_accvgpr_read_b32 v82, a162\n"
    "v_accvgpr_read_b32 v83, a163\n"
    "v_pk_mul_f32 v[80:81], v[80:81], v[248:249] op_sel_hi:[1,0]\n"
    "v_pk_mul_f32 v[82:83], v[82:83], v[248:249] op_sel_hi:[1,0]\n"
    "v_cvt_pk_bf16_f32 v64, v80, v81\n"
    "v_cvt_pk_bf16_f32 v65, v82, v83\n"
    "v_accvgpr_read_b32 v84, a168\n"
    "v_accvgpr_read_b32 v85, a169\n"
    "v_accvgpr_read_b32 v86, a170\n"
    "v_accvgpr_read_b32 v87, a171\n"
    "v_pk_mul_f32 v[84:85], v[84:85], v[248:249] op_sel_hi:[1,0]\n"
    "v_pk_mul_f32 v[86:87], v[86:87], v[248:249] op_sel_hi:[1,0]\n"
    "v_cvt_pk_bf16_f32 v66, v84, v85\n"
    "v_cvt_pk_bf16_f32 v67, v86, v87\n"
    "ds_write_b128 v254, v[64:67] offset:16384\n"
    "v_accvgpr_read_b32 v88, a164\n"
    "v_accvgpr_read_b32 v89, a165\n"
    "v_accvgpr_read_b32 v90, a166\n"
    "v_accvgpr_read_b32 v91, a167\n"
    "v_pk_mul_f32 v[88:89], v[88:89], v[248:249] op_sel:[0,1]\n"
    "v_pk_mul_f32 v[90:91], v[90:91], v[248:249] op_sel:[0,1]\n"
    "v_cvt_pk_bf16_f32 v68, v88, v89\n"
    "v_cvt_pk_bf16_f32 v69, v90, v91\n"
    "v_accvgpr_read_b32 v92, a172\n"
    "v_accvgpr_read_b32 v93, a173\n"
    "v_accvgpr_read_b32 v94, a174\n"
    "v_accvgpr_read_b32 v95, a175\n"
    "v_pk_mul_f32 v[92:93], v[92:93], v[248:249] op_sel:[0,1]\n"
    "v_pk_mul_f32 v[94:95], v[94:95], v[248:249] op_sel:[0,1]\n"
    "v_cvt_pk_bf16_f32 v70, v92, v93\n"
    "v_cvt_pk_bf16_f32 v71, v94, v95\n"
    "ds_write_b128 v254, v[68:71] offset:17408\n"
    "v_accvgpr_read_b32 v96, a176\n"
    "v_accvgpr_read_b32 v97, a177\n"
    "v_accvgpr_read_b32 v98, a178\n"
    "v_accvgpr_read_b32 v99, a179\n"
    "v_pk_mul_f32 v[96:97], v[96:97], v[248:249] op_sel_hi:[1,0]\n"
    "v_pk_mul_f32 v[98:99], v[98:99], v[248:249] op_sel_hi:[1,0]\n"
    "v_cvt_pk_bf16_f32 v72, v96, v97\n"
    "v_cvt_pk_bf16_f32 v73, v98, v99\n"
    "v_accvgpr_read_b32 v100, a184\n"
    "v_accvgpr_read_b32 v101, a185\n"
    "v_accvgpr_read_b32 v102, a186\n"
    "v_accvgpr_read_b32 v103, a187\n"
    "v_pk_mul_f32 v[100:101], v[100:101], v[248:249] op_sel_hi:[1,0]\n"
    "v_pk_mul_f32 v[102:103], v[102:103], v[248:249] op_sel_hi:[1,0]\n"
    "v_cvt_pk_bf16_f32 v74, v100, v101\n"
    "v_cvt_pk_bf16_f32 v75, v102, v103\n"
    "ds_write_b128 v254, v[72:75] offset:24576\n"
    "v_accvgpr_read_b32 v104, a180\n"
    "v_accvgpr_read_b32 v105, a181\n"
    "v_accvgpr_read_b32 v106, a182\n"
    "v_accvgpr_read_b32 v107, a183\n"
    "v_pk_mul_f32 v[104:105], v[104:105], v[248:249] op_sel:[0,1]\n"
    "v_pk_mul_f32 v[106:107], v[106:107], v[248:249] op_sel:[0,1]\n"
    "v_cvt_pk_bf16_f32 v76, v104, v105\n"
    "v_cvt_pk_bf16_f32 v77, v106, v107\n"
    "v_accvgpr_read_b32 v108, a188\n"
    "v_accvgpr_read_b32 v109, a189\n"
    "v_accvgpr_read_b32 v110, a190\n"
    "v_accvgpr_read_b32 v111, a191\n"
    "v_pk_mul_f32 v[108:109], v[108:109], v[248:249] op_sel:[0,1]\n"
    "v_pk_mul_f32 v[110:111], v[110:111], v[248:249] op_sel:[0,1]\n"
    "v_cvt_pk_bf16_f32 v78, v108, v109\n"
    "v_cvt_pk_bf16_f32 v79, v110, v111\n"
    "ds_write_b128 v254, v[76:79] offset:25600\n"
    "v_accvgpr_read_b32 v80, a192\n"
    "v_accvgpr_read_b32 v81, a193\n"
    "v_accvgpr_read_b32 v82, a194\n"
    "v_accvgpr_read_b32 v83, a195\n"
    "v_pk_mul_f32 v[80:81], v[80:81], v[248:249] op_sel_hi:[1,0]\n"
    "v_pk_mul_f32 v[82:83], v[82:83], v[248:249] op_sel_hi:[1,0]\n"
    "v_cvt_pk_bf16_f32 v64, v80, v81\n"
    "v_cvt_pk_bf16_f32 v65, v82, v83\n"
    "v_accvgpr_read_b32 v84, a200\n"
    "v_accvgpr_read_b32 v85, a201\n"
    "v_accvgpr_read_b32 v86, a202\n"
    "v_accvgpr_read_b32 v87, a203\n"
    "v_pk_mul_f32 v[84:85], v[84:85], v[248:249] op_sel_hi:[1,0]\n"
    "v_pk_mul_f32 v[86:87], v[86:87], v[248:249] op_sel_hi:[1,0]\n"
    "v_cvt_pk_bf16_f32 v66, v84, v85\n"
    "v_cvt_pk_bf16_f32 v67, v86, v87\n"
    "ds_write_b128 v254, v[64:67] offset:32768\n"
    "v_accvgpr_read_b32 v88, a196\n"
    "v_accvgpr_read_b32 v89, a197\n"
    "v_accvgpr_read_b32 v90, a198\n"
    "v_accvgpr_read_b32 v91, a199\n"
    "v_pk_mul_f32 v[88:89], v[88:89], v[248:249] op_sel:[0,1]\n"
    "v_pk_mul_f32 v[90:91], v[90:91], v[248:249] op_sel:[0,1]\n"
    "v_cvt_pk_bf16_f32 v68, v88, v89\n"
    "v_cvt_pk_bf16_f32 v69, v90, v91\n"
    "v_accvgpr_read_b32 v92, a204\n"
    "v_accvgpr_read_b32 v93, a205\n"
    "v_accvgpr_read_b32 v94, a206\n"
    "v_accvgpr_read_b32 v95, a207\n"
    "v_pk_mul_f32 v[92:93], v[92:93], v[248:249] op_sel:[0,1]\n"
    "v_pk_mul_f32 v[94:95], v[94:95], v[248:249] op_sel:[0,1]\n"
    "v_cvt_pk_bf16_f32 v70, v92, v93\n"
    "v_cvt_pk_bf16_f32 v71, v94, v95\n"
    "ds_write_b128 v254, v[68:71] offset:33792\n"
    "v_accvgpr_read_b32 v96, a208\n"
    "v_accvgpr_read_b32 v97, a209\n"
    "v_accvgpr_read_b32 v98, a210\n"
    "v_accvgpr_read_b32 v99, a211\n"
    "v_pk_mul_f32 v[96:97], v[96:97], v[248:249] op_sel_hi:[1,0]\n"
    "v_pk_mul_f32 v[98:99], v[98:99], v[248:249] op_sel_hi:[1,0]\n"
    "v_cvt_pk_bf16_f32 v72, v96, v97\n"
    "v_cvt_pk_bf16_f32 v73, v98, v99\n"
    "v_accvgpr_read_b32 v100, a216\n"
    "v_accvgpr_read_b32 v101, a217\n"
    "v_accvgpr_read_b32 v102, a218\n"
    "v_accvgpr_read_b32 v103, a219\n"
    "v_pk_mul_f32 v[100:101], v[100:101], v[248:249] op_sel_hi:[1,0]\n"
    "v_pk_mul_f32 v[102:103], v[102:103], v[248:249] op_sel_hi:[1,0]\n"
    "v_cvt_pk_bf16_f32 v74, v100, v101\n"
    "v_cvt_pk_bf16_f32 v75, v102, v103\n"
    "ds_write_b128 v254, v[72:75] offset:40960\n"
    "v_accvgpr_read_b32 v104, a212\n"
    "v_accvgpr_read_b32 v105, a213\n"
    "v_accvgpr_read_b32 v106, a214\n"
    "v_accvgpr_read_b32 v107, a215\n"
    "v_pk_mul_f32 v[104:105], v[104:105], v[248:249] op_sel:[0,1]\n"
    "v_pk_mul_f32 v[106:107], v[106:107], v[248:249] op_sel:[0,1]\n"
    "v_cvt_pk_bf16_f32 v76, v104, v105\n"
    "v_cvt_pk_bf16_f32 v77, v106, v107\n"
    "v_accvgpr_read_b32 v108, a220\n"
    "v_accvgpr_read_b32 v109, a221\n"
    "v_accvgpr_read_b32 v110, a222\n"
    "v_accvgpr_read_b32 v111, a223\n"
    "v_pk_mul_f32 v[108:109], v[108:109], v[248:249] op_sel:[0,1]\n"
    "v_pk_mul_f32 v[110:111], v[110:111], v[248:249] op_sel:[0,1]\n"
    "v_cvt_pk_bf16_f32 v78, v108, v109\n"
    "v_cvt_pk_bf16_f32 v79, v110, v111\n"
    "ds_write_b128 v254, v[76:79] offset:41984\n"
    "v_accvgpr_read_b32 v80, a224\n"
    "v_accvgpr_read_b32 v81, a225\n"
    "v_accvgpr_read_b32 v82, a226\n"
    "v_accvgpr_read_b32 v83, a227\n"
    "v_pk_mul_f32 v[80:81], v[80:81], v[248:249] op_sel_hi:[1,0]\n"
    "v_pk_mul_f32 v[82:83], v[82:83], v[248:249] op_sel_hi:[1,0]\n"
    "v_cvt_pk_bf16_f32 v64, v80, v81\n"
    "v_cvt_pk_bf16_f32 v65, v82, v83\n"
    "v_accvgpr_read_b32 v84, a232\n"
    "v_accvgpr_read_b32 v85, a233\n"
    "v_accvgpr_read_b32 v86, a234\n"
    "v_accvgpr_read_b32 v87, a235\n"
    "v_pk_mul_f32 v[84:85], v[84:85], v[248:249] op_sel_hi:[1,0]\n"
    "v_pk_mul_f32 v[86:87], v[86:87], v[248:249] op_sel_hi:[1,0]\n"
    "v_cvt_pk_bf16_f32 v66, v84, v85\n"
    "v_cvt_pk_bf16_f32 v67, v86, v87\n"
    "ds_write_b128 v254, v[64:67] offset:49152\n"
    "v_accvgpr_read_b32 v88, a228\n"
    "v_accvgpr_read_b32 v89, a229\n"
    "v_accvgpr_read_b32 v90, a230\n"
    "v_accvgpr_read_b32 v91, a231\n"
    "v_pk_mul_f32 v[88:89], v[88:89], v[248:249] op_sel:[0,1]\n"
    "v_pk_mul_f32 v[90:91], v[90:91], v[248:249] op_sel:[0,1]\n"
    "v_cvt_pk_bf16_f32 v68, v88, v89\n"
    "v_cvt_pk_bf16_f32 v69, v90, v91\n"
    "v_accvgpr_read_b32 v92, a236\n"
    "v_accvgpr_read_b32 v93, a237\n"
    "v_accvgpr_read_b32 v94, a238\n"
    "v_accvgpr_read_b32 v95, a239\n"
    "v_pk_mul_f32 v[92:93], v[92:93], v[248:249] op_sel:[0,1]\n"
    "v_pk_mul_f32 v[94:95], v[94:95], v[248:249] op_sel:[0,1]\n"
    "v_cvt_pk_bf16_f32 v70, v92, v93\n"
    "v_cvt_pk_bf16_f32 v71, v94, v95\n"
    "ds_write_b128 v254, v[68:71] offset:50176\n"
    "v_accvgpr_read_b32 v96, a240\n"
    "v_accvgpr_read_b32 v97, a241\n"
    "v_accvgpr_read_b32 v98, a242\n"
    "v_accvgpr_read_b32 v99, a243\n"
    "v_pk_mul_f32 v[96:97], v[96:97], v[248:249] op_sel_hi:[1,0]\n"
    "v_pk_mul_f32 v[98:99], v[98:99], v[248:249] op_sel_hi:[1,0]\n"
    "v_cvt_pk_bf16_f32 v72, v96, v97\n"
    "v_cvt_pk_bf16_f32 v73, v98, v99\n"
    "v_accvgpr_read_b32 v100, a248\n"
    "v_accvgpr_read_b32 v101, a249\n"
    "v_accvgpr_read_b32 v102, a250\n"
    "v_accvgpr_read_b32 v103, a251\n"
    "v_pk_mul_f32 v[100:101], v[100:101], v[248:249] op_sel_hi:[1,0]\n"
    "v_pk_mul_f32 v[102:103], v[102:103], v[248:249] op_sel_hi:[1,0]\n"
    "v_cvt_pk_bf16_f32 v74, v100, v101\n"
    "v_cvt_pk_bf16_f32 v75, v102, v103\n"
    "ds_write_b128 v254, v[72:75] offset:57344\n"
    "v_accvgpr_read_b32 v104, a244\n"
    "v_accvgpr_read_b32 v105, a245\n"
    "v_accvgpr_read_b32 v106, a246\n"
    "v_accvgpr_read_b32 v107, a247\n"
    "v_pk_mul_f32 v[104:105], v[104:105], v[248:249] op_sel:[0,1]\n"
    "v_pk_mul_f32 v[106:107], v[106:107], v[248:249] op_sel:[0,1]\n"
    "v_cvt_pk_bf16_f32 v76, v104, v105\n"
    "v_cvt_pk_bf16_f32 v77, v106, v107\n"
    "v_accvgpr_read_b32 v108, a252\n"
    "v_accvgpr_read_b32 v109, a253\n"
    "v_accvgpr_read_b32 v110, a254\n"
    "v_accvgpr_read_b32 v111, a255\n"
    "v_pk_mul_f32 v[108:109], v[108:109], v[248:249] op_sel:[0,1]\n"
    "v_pk_mul_f32 v[110:111], v[110:111], v[248:249] op_sel:[0,1]\n"
    "v_cvt_pk_bf16_f32 v78, v108, v109\n"
    "v_cvt_pk_bf16_f32 v79, v110, v111\n"
    "ds_write_b128 v254, v[76:79] offset:58368\n"
    "s_waitcnt vmcnt(17) lgkmcnt(0)\n"
    "s_barrier\n"
    "ds_read_b128 v[80:83], v120 offset:0\n"
    "ds_read_b128 v[84:87], v120 offset:512\n"
    "ds_read_b128 a[0:3], v254 offset:0\n"
    "ds_read_b128 a[4:7], v254 offset:1024\n"
    "ds_read_b128 a[8:11], v255 offset:0\n"
    "ds_read_b128 a[12:15], v255 offset:1024\n"
    "ds_read_b128 a[16:19], v254 offset:8192\n"
    "ds_read_b128 a[20:23], v254 offset:9216\n"
    "ds_read_b128 a[24:27], v255 offset:8192\n"
    "ds_read_b128 a[28:31], v255 offset:9216\n"
    "ds_read_b128 a[32:35], v254 offset:16384\n"
    "ds_read_b128 a[36:39], v254 offset:17408\n"
    "ds_read_b128 a[40:43], v255 offset:16384\n"
    "ds_read_b128 a[44:47], v255 offset:17408\n"
    "ds_read_b128 a[48:51], v254 offset:24576\n"
    "ds_read_b128 a[52:55], v254 offset:25600\n"
    "ds_read_b128 a[56:59], v255 offset:24576\n"
    "ds_read_b128 a[60:63], v255 offset:25600\n"
    "ds_read_b128 v[88:91], v120 offset:2112\n"
    "ds_read_b128 v[92:95], v120 offset:2624\n"
    "ds_read_b128 v[96:99], v120 offset:4224\n"
    "ds_read_b128 v[100:103], v120 offset:4736\n"
    "s_waitcnt lgkmcnt(12)\n"
    "v_mfma_f32_16x16x32_bf16 v[128:131], v[80:83], a[0:3], 0\n"
    "ds_read_b128 a[64:67], v254 offset:32768\n"
    "ds_read_b128 a[68:71], v254 offset:33792\n"
    "v_mfma_f32_16x16x32_bf16 v[128:131], v[84:87], a[16:19], v[128:131]\n"
    "ds_read_b128 a[72:75], v255 offset:32768\n"
    "ds_read_b128 a[76:79], v255 offset:33792\n"
    "v_mfma_f32_16x16x32_bf16 v[132:135], v[80:83], a[4:7], 0\n"
    "ds_read_b128 a[80:83], v254 offset:40960\n"
    "v_mfma_f32_16x16x32_bf16 v[132:135], v[84:87], a[20:23], v[132:135]\n"
    "ds_read_b128 a[84:87], v254 offset:41984\n"
    "ds_read_b128 a[88:91], v255 offset:40960\n"
    "v_mfma_f32_16x16x32_bf16 v[136:139], v[80:83], a[8:11], 0\n"
    "ds_read_b128 a[92:95], v255 offset:41984\n"
    "ds_read_b128 v[104:107], v120 offset:6336\n"
    "v_mfma_f32_16x16x32_bf16 v[136:139], v[84:87], a[24:27], v[136:139]\n"
    "ds_read_b128 v[108:111], v120 offset:6848\n"
    "v_mfma_f32_16x16x32_bf16 v[140:143], v[80:83], a[12:15], 0\n"
    "v_mfma_f32_16x16x32_bf16 v[140:143], v[84:87], a[28:31], v[140:143]\n"
    "s_waitcnt lgkmcnt(12)\n"
    "v_mfma_f32_16x16x32_bf16 v[128:131], v[88:91], a[32:35], v[128:131]\n"
    "ds_read_b128 a[96:99], v254 offset:49152\n"
    "ds_read_b128 a[100:103], v254 offset:50176\n"
    "v_mfma_f32_16x16x32_bf16 v[128:131], v[92:95], a[48:51], v[128:131]\n"
    "ds_read_b128 a[104:107], v255 offset:49152\n"
    "ds_read_b128 a[108:111], v255 offset:50176\n"
    "v_mfma_f32_16x16x32_bf16 v[132:135], v[88:91], a[36:39], v[132:135]\n"
    "ds_read_b128 a[112:115], v254 offset:57344\n"
    "v_mfma_f32_16x16x32_bf16 v[132:135], v[92:95], a[52:55], v[132:135]\n"
    "ds_read_b128 a[116:119], v254 offset:58368\n"
    "ds_read_b128 a[120:123], v255 offset:57344\n"
    "v_mfma_f32_16x16x32_bf16 v[136:139], v[88:91], a[40:43], v[136:139]\n"
    "ds_read_b128 a[124:127], v255 offset:58368\n"
    "ds_read_b128 v[80:83], v120 offset:16896\n"
    "v_mfma_f32_16x16x32_bf16 v[136:139], v[92:95], a[56:59], v[136:139]\n"
    "ds_read_b128 v[84:87], v120 offset:17408\n"
    "v_mfma_f32_16x16x32_bf16 v[140:143], v[88:91], a[44:47], v[140:143]\n"
    "v_mfma_f32_16x16x32_bf16 v[140:143], v[92:95], a[60:63], v[140:143]\n"
    "s_waitcnt lgkmcnt(12)\n"
    "v_mfma_f32_16x16x32_bf16 v[128:131], v[96:99], a[64:67], v[128:131]\n"
    "ds_read_b128 v[88:91], v120 offset:19008\n"
    "v_mfma_f32_16x16x32_bf16 v[128:131], v[100:103], a[80:83], v[128:131]\n"
    "v_mfma_f32_16x16x32_bf16 v[132:135], v[96:99], a[68:71], v[132:135]\n"
    "v_mfma_f32_16x16x32_bf16 v[132:135], v[100:103], a[84:87], v[132:135]\n"
    "ds_read_b128 v[92:95], v120 offset:19520\n"
    "v_mfma_f32_16x16x32_bf16 v[136:139], v[96:99], a[72:75], v[136:139]\n"
    "v_mfma_f32_16x16x32_bf16 v[136:139], v[100:103], a[88:91], v[136:139]\n"
    "v_mfma_f32_16x16x32_bf16 v[140:143], v[96:99], a[76:79], v[140:143]\n"
    "v_mfma_f32_16x16x32_bf16 v[140:143], v[100:103], a[92:95], v[140:143]\n"
    "s_waitcnt lgkmcnt(4)\n"
    "v_mfma_f32_16x16x32_bf16 v[128:131], v[104:107], a[96:99], v[128:131]\n"
    "ds_read_b128 v[96:99], v120 offset:21120\n"
    "v_mfma_f32_16x16x32_bf16 v[128:131], v[108:111], a[112:115], v[128:131]\n"
    "v_mfma_f32_16x16x32_bf16 v[132:135], v[104:107], a[100:103], v[132:135]\n"
    "v_mfma_f32_16x16x32_bf16 v[132:135], v[108:111], a[116:119], v[132:135]\n"
    "ds_read_b128 v[100:103], v120 offset:21632\n"
    "v_mfma_f32_16x16x32_bf16 v[136:139], v[104:107], a[104:107], v[136:139]\n"
    "v_mfma_f32_16x16x32_bf16 v[136:139], v[108:111], a[120:123], v[136:139]\n"
    "v_mfma_f32_16x16x32_bf16 v[140:143], v[104:107], a[108:111], v[140:143]\n"
    "v_mfma_f32_16x16x32_bf16 v[140:143], v[108:111], a[124:127], v[140:143]\n"
    "s_waitcnt lgkmcnt(4)\n"
    "v_max3_f32 v112, v128, v129, v130\n"
    "v_max_f32 v112, v112, v131\n"
    "v_mfma_f32_16x16x32_bf16 v[144:147], v[80:83], a[0:3], 0\n"
    "ds_read_b128 v[104:107], v120 offset:23232\n"
    "v_max3_f32 v113, v132, v133, v134\n"
    "v_mfma_f32_16x16x32_bf16 v[144:147], v[84:87], a[16:19], v[144:147]\n"
    "v_max3_f32 v114, v136, v137, v138\n"
    "v_max3_f32 v115, v140, v141, v142\n"
    "v_mfma_f32_16x16x32_bf16 v[148:151], v[80:83], a[4:7], 0\n"
    "v_max_f32 v113, v113, v135\n"
    "v_max_f32 v114, v114, v139\n"
    "v_mfma_f32_16x16x32_bf16 v[148:151], v[84:87], a[20:23], v[148:151]\n"
    "ds_read_b128 v[108:111], v120 offset:23744\n"
    "v_max_f32 v115, v115, v143\n"
    "v_mfma_f32_16x16x32_bf16 v[152:155], v[80:83], a[8:11], 0\n"
    "v_mov_b32 v160, v112\n"
    "v_mov_b32 v161, v113\n"
    "v_mfma_f32_16x16x32_bf16 v[152:155], v[84:87], a[24:27], v[152:155]\n"
    "v_mov_b32 v162, v114\n"
    "v_mov_b32 v163, v115\n"
    "v_mfma_f32_16x16x32_bf16 v[156:159], v[80:83], a[12:15], 0\n"
    "v_mfma_f32_16x16x32_bf16 v[156:159], v[84:87], a[28:31], v[156:159]\n"
    "s_waitcnt lgkmcnt(4)\n"
    "v_permlane16_swap_b32 v112, v160\n"
    "v_mfma_f32_16x16x32_bf16 v[144:147], v[88:91], a[32:35], v[144:147]\n"
    "s_waitcnt vmcnt(12)\n"
    "v_permlane16_swap_b32 v113, v161\n"
    "v_mfma_f32_16x16x32_bf16 v[144:147], v[92:95], a[48:51], v[144:147]\n"
    "s_barrier\n"
    "v_permlane16_swap_b32 v114, v162\n"
    "v_mfma_f32_16x16x32_bf16 v[148:151], v[88:91], a[36:39], v[148:151]\n"
    "s_add_u32 m0, s77, 0\n"
    "ds_read_b128 v[80:83], v120 offset:33792\n"
    "buffer_load_dwordx4 v123, s[24:27], s72 offen offset:0 nt lds\n"
    "v_mfma_f32_16x16x32_bf16 v[148:151], v[92:95], a[52:55], v[148:151]\n"
    "v_permlane16_swap_b32 v115, v163\n"
    "v_mfma_f32_16x16x32_bf16 v[152:155], v[88:91], a[40:43], v[152:155]\n"
    "ds_read_b128 v[84:87], v120 offset:34304\n"
    "v_max_f32 v112, v112, v160\n"
    "v_mfma_f32_16x16x32_bf16 v[152:155], v[92:95], a[56:59], v[152:155]\n"
    "s_add_u32 m0, s77, 1984\n"
    "v_max_f32 v113, v113, v161\n"
    "buffer_load_dwordx4 v123, s[24:27], s72 offen offset:128 nt lds\n"
    "v_mfma_f32_16x16x32_bf16 v[156:159], v[88:91], a[44:47], v[156:159]\n"
    "v_max_f32 v114, v114, v162\n"
    "v_max_f32 v115, v115, v163\n"
    "v_mfma_f32_16x16x32_bf16 v[156:159], v[92:95], a[60:63], v[156:159]\n"
    "s_waitcnt lgkmcnt(4)\n"
    "v_mov_b32 v160, v112\n"
    "v_mov_b32 v161, v113\n"
    "v_mfma_f32_16x16x32_bf16 v[144:147], v[96:99], a[64:67], v[144:147]\n"
    "ds_read_b128 v[88:91], v120 offset:35904\n"
    "v_mov_b32 v162, v114\n"
    "v_mfma_f32_16x16x32_bf16 v[144:147], v[100:103], a[80:83], v[144:147]\n"
    "s_add_u32 m0, s77, 3968\n"
    "v_mov_b32 v163, v115\n"
    "buffer_load_dwordx4 v123, s[24:27], s72 offen offset:256 nt lds\n"
    "v_mfma_f32_16x16x32_bf16 v[148:151], v[96:99], a[68:71], v[148:151]\n"
    "v_permlane32_swap_b32 v112, v160\n"
    "v_mfma_f32_16x16x32_bf16 v[148:151], v[100:103], a[84:87], v[148:151]\n"
    "ds_read_b128 v[92:95], v120 offset:36416\n"
    "v_mbcnt_lo_u32_b32 v164, -1, 0\n"
    "v_mfma_f32_16x16x32_bf16 v[152:155], v[96:99], a[72:75], v[152:155]\n"
    "s_add_u32 m0, s77, 5952\n"
    "v_permlane32_swap_b32 v113, v161\n"
    "v_mfma_f32_16x16x32_bf16 v[152:155], v[100:103], a[88:91], v[152:155]\n"
    "buffer_load_dwordx4 v123, s[24:27], s72 offen offset:384 nt lds\n"
    "v_mbcnt_hi_u32_b32 v164, -1, v164\n"
    "v_mfma_f32_16x16x32_bf16 v[156:159], v[96:99], a[76:79], v[156:159]\n"
    "s_add_u32 s72, s72, 2048\n"
    "v_permlane32_swap_b32 v114, v162\n"
    "v_mfma_f32_16x16x32_bf16 v[156:159], v[100:103], a[92:95], v[156:159]\n"
    "s_waitcnt lgkmcnt(4)\n"
    "v_permlane32_swap_b32 v115, v163\n"
    "v_mfma_f32_16x16x32_bf16 v[144:147], v[104:107], a[96:99], v[144:147]\n"
    "s_add_u32 m0, s78, 16768\n"
    "v_max_f32 v112, v112, v160\n"
    "v_max_f32 v113, v113, v161\n"
    "v_max_f32 v114, v114, v162\n"
    "v_max_f32 v115, v115, v163\n"
    "v_mfma_f32_16x16x32_bf16 v[144:147], v[108:111], a[112:115], v[144:147]\n"
    "buffer_load_dwordx4 v122, s[36:39], s73 offen offset:128 nt lds\n"
    "v_sub_f32 v64, 0, v112\n"
    "v_mfma_f32_16x16x32_bf16 v[148:151], v[104:107], a[100:103], v[148:151]\n"
    "v_sub_f32 v65, 0, v112\n"
    "v_sub_f32 v66, 0, v112\n"
    "v_mfma_f32_16x16x32_bf16 v[148:151], v[108:111], a[116:119], v[148:151]\n"
    "s_add_u32 m0, s78, 17824\n"
    "v_sub_f32 v67, 0, v112\n"
    "ds_read_b128 v[96:99], v120 offset:38016\n"
    "v_mfma_f32_16x16x32_bf16 v[152:155], v[104:107], a[104:107], v[152:155]\n"
    "ds_read_b128 v[100:103], v120 offset:38528\n"
    "buffer_load_dwordx4 v122, s[36:39], s74 offen offset:128 nt lds\n"
    "v_mfma_f32_16x16x32_bf16 v[152:155], v[108:111], a[120:123], v[152:155]\n"
    "v_sub_f32 v68, 0, v113\n"
    "v_sub_f32 v69, 0, v113\n"
    "v_mfma_f32_16x16x32_bf16 v[156:159], v[104:107], a[108:111], v[156:159]\n"
    "v_sub_f32 v70, 0, v113\n"
    "v_sub_f32 v71, 0, v113\n"
    "v_mfma_f32_16x16x32_bf16 v[156:159], v[108:111], a[124:127], v[156:159]\n"
    "s_waitcnt lgkmcnt(4)\n"
    "v_sub_f32 v72, 0, v114\n"
    "v_sub_f32 v73, 0, v114\n"
    "v_mfma_f32_16x16x32_bf16 v[0:3], v[80:83], a[0:3], v[64:67]\n"
    "s_add_u32 m0, s78, 18880\n"
    "v_sub_f32 v74, 0, v114\n"
    "v_sub_f32 v75, 0, v114\n"
    "v_mfma_f32_16x16x32_bf16 v[0:3], v[84:87], a[16:19], v[0:3]\n"
    "buffer_load_dwordx4 v122, s[36:39], s75 offen offset:128 nt lds\n"
    "v_sub_f32 v76, 0, v115\n"
    "v_mfma_f32_16x16x32_bf16 v[4:7], v[80:83], a[4:7], v[68:71]\n"
    "s_add_u32 m0, s78, 19936\n"
    "v_sub_f32 v77, 0, v115\n"
    "v_sub_f32 v78, 0, v115\n"
    "v_mfma_f32_16x16x32_bf16 v[4:7], v[84:87], a[20:23], v[4:7]\n"
    "v_sub_f32 v79, 0, v115\n"
    "ds_read_b128 v[104:107], v120 offset:40128\n"
    "v_mfma_f32_16x16x32_bf16 v[8:11], v[80:83], a[8:11], v[72:75]\n"
    "ds_read_b128 v[108:111], v120 offset:40640\n"
    "buffer_load_dwordx4 v122, s[36:39], s76 offen offset:128 nt lds\n"
    "v_mfma_f32_16x16x32_bf16 v[8:11], v[84:87], a[24:27], v[8:11]\n"
    "v_and_b32 v164, 15, v164\n"
    "v_lshlrev_b32 v164, 2, v164\n"
    "v_mfma_f32_16x16x32_bf16 v[12:15], v[80:83], a[12:15], v[76:79]\n"
    "s_lshl_b32 s84, s81, 7\n"
    "v_add_u32 v164, s84, v164\n"
    "v_add_u32 v164, 0x27800, v164\n"
    "v_mfma_f32_16x16x32_bf16 v[12:15], v[84:87], a[28:31], v[12:15]\n"
    "s_waitcnt lgkmcnt(4)\n"
    "ds_write_b32 v164, v252\n"
    "ds_write_b32 v164, v253 offset:64\n"
    "v_mfma_f32_16x16x32_bf16 v[0:3], v[88:91], a[32:35], v[0:3]\n"
    "s_waitcnt vmcnt(4)\n"
    "v_mov_b32 v112, 0\n"
    "v_mov_b32 v113, 0\n"
    "v_mfma_f32_16x16x32_bf16 v[0:3], v[92:95], a[48:51], v[0:3]\n"
    "s_barrier\n"
    "ds_read_b128 v[80:83], v120 offset:50688\n"
    "v_mov_b32 v114, 0\n"
    "v_mfma_f32_16x16x32_bf16 v[4:7], v[88:91], a[36:39], v[4:7]\n"
    "s_add_u32 m0, s77, 16896\n"
    "v_mov_b32 v115, 0\n"
    "buffer_load_dwordx4 v123, s[24:27], s72 offen offset:0 nt lds\n"
    "v_mfma_f32_16x16x32_bf16 v[4:7], v[92:95], a[52:55], v[4:7]\n"
    "s_add_u32 m0, s77, 18880\n"
    "v_add_f32 v128, v128, v64\n"
    "buffer_load_dwordx4 v123, s[24:27], s72 offen offset:128 nt lds\n"
    "v_mfma_f32_16x16x32_bf16 v[8:11], v[88:91], a[40:43], v[8:11]\n"
    "ds_read_b128 v[84:87], v120 offset:51200\n"
    "v_add_f32 v129, v129, v64\n"
    "v_mfma_f32_16x16x32_bf16 v[8:11], v[92:95], a[56:59], v[8:11]\n"
    "s_add_u32 m0, s77, 20864\n"
    "v_add_f32 v130, v130, v64\n"
    "buffer_load_dwordx4 v123, s[24:27], s72 offen offset:256 nt lds\n"
    "v_mfma_f32_16x16x32_bf16 v[12:15], v[88:91], a[44:47], v[12:15]\n"
    "v_add_f32 v131, v131, v64\n"
    "v_add_f32 v132, v132, v68\n"
    "v_mfma_f32_16x16x32_bf16 v[12:15], v[92:95], a[60:63], v[12:15]\n"
    "s_waitcnt lgkmcnt(6)\n"
    "v_add_f32 v133, v133, v68\n"
    "v_add_f32 v134, v134, v68\n"
    "v_mfma_f32_16x16x32_bf16 v[0:3], v[96:99], a[64:67], v[0:3]\n"
    "s_add_u32 m0, s77, 22848\n"
    "ds_read_b128 v[88:91], v120 offset:52800\n"
    "buffer_load_dwordx4 v123, s[24:27], s72 offen offset:384 nt lds\n"
    "v_mfma_f32_16x16x32_bf16 v[0:3], v[100:103], a[80:83], v[0:3]\n"
    "s_add_u32 s72, s72, 14336\n"
    "v_add_f32 v135, v135, v68\n"
    "v_add_f32 v136, v136, v72\n"
    "v_mfma_f32_16x16x32_bf16 v[4:7], v[96:99], a[68:71], v[4:7]\n"
    "s_add_u32 m0, s78, 33536\n"
    "v_add_f32 v137, v137, v72\n"
    "buffer_load_dwordx4 v122, s[36:39], s73 offen offset:256 nt lds\n"
    "v_mfma_f32_16x16x32_bf16 v[4:7], v[100:103], a[84:87], v[4:7]\n"
    "s_add_u32 m0, s78, 34592\n"
    "ds_read_b128 v[92:95], v120 offset:53312\n"
    "buffer_load_dwordx4 v122, s[36:39], s74 offen offset:256 nt lds\n"
    "v_mfma_f32_16x16x32_bf16 v[8:11], v[96:99], a[72:75], v[8:11]\n"
    "v_add_f32 v138, v138, v72\n"
    "v_add_f32 v139, v139, v72\n"
    "v_mfma_f32_16x16x32_bf16 v[8:11], v[100:103], a[88:91], v[8:11]\n"
    "s_add_u32 m0, s78, 35648\n"
    "v_add_f32 v140, v140, v76\n"
    "buffer_load_dwordx4 v122, s[36:39], s75 offen offset:256 nt lds\n"
    "v_mfma_f32_16x16x32_bf16 v[12:15], v[96:99], a[76:79], v[12:15]\n"
    "s_add_u32 m0, s78, 36704\n"
    "v_add_f32 v141, v141, v76\n"
    "buffer_load_dwordx4 v122, s[36:39], s76 offen offset:256 nt lds\n"
    "v_mfma_f32_16x16x32_bf16 v[12:15], v[100:103], a[92:95], v[12:15]\n"
    "s_waitcnt lgkmcnt(6)\n"
    "v_add_f32 v142, v142, v76\n"
    "v_add_f32 v143, v143, v76\n"
    "v_mfma_f32_16x16x32_bf16 v[0:3], v[104:107], a[96:99], v[0:3]\n"
    "s_add_u32 m0, s78, 50304\n"
    "ds_read_b128 v[96:99], v120 offset:54912\n"
    "buffer_load_dwordx4 v122, s[36:39], s73 offen offset:384 nt lds\n"
    "v_mfma_f32_16x16x32_bf16 v[0:3], v[108:111], a[112:115], v[0:3]\n"
    "s_add_u32 s73, s73, 32768\n"
    "v_add_f32 v144, v144, v64\n"
    "v_add_f32 v145, v145, v64\n"
    "v_mfma_f32_16x16x32_bf16 v[4:7], v[104:107], a[100:103], v[4:7]\n"
    "s_add_u32 m0, s78, 51360\n"
    "v_add_f32 v146, v146, v64\n"
    "buffer_load_dwordx4 v122, s[36:39], s74 offen offset:384 nt lds\n"
    "v_mfma_f32_16x16x32_bf16 v[4:7], v[108:111], a[116:119], v[4:7]\n"
    "s_add_u32 s74, s74, 32768\n"
    "ds_read_b128 v[100:103], v120 offset:55424\n"
    "v_add_f32 v147, v147, v64\n"
    "v_mfma_f32_16x16x32_bf16 v[8:11], v[104:107], a[104:107], v[8:11]\n"
    "s_add_u32 m0, s78, 52416\n"
    "v_exp_f32 v0, v0\n"
    "v_mfma_f32_16x16x32_bf16 v[8:11], v[108:111], a[120:123], v[8:11]\n"
    "buffer_load_dwordx4 v122, s[36:39], s75 offen offset:384 nt lds\n"
    "v_add_f32 v148, v148, v68\n"
    "v_mfma_f32_16x16x32_bf16 v[12:15], v[104:107], a[108:111], v[12:15]\n"
    "s_add_u32 s75, s75, 32768\n"
    "v_exp_f32 v1, v1\n"
    "v_mfma_f32_16x16x32_bf16 v[12:15], v[108:111], a[124:127], v[12:15]\n"
    "s_waitcnt lgkmcnt(4)\n"
    "v_exp_f32 v2, v2\n"
    "v_mfma_f32_16x16x32_bf16 v[16:19], v[80:83], a[0:3], v[64:67]\n"
    "s_add_u32 m0, s78, 53472\n"
    "ds_read_b128 v[104:107], v120 offset:57024\n"
    "buffer_load_dwordx4 v122, s[36:39], s76 offen offset:384 nt lds\n"
    "v_mfma_f32_16x16x32_bf16 v[16:19], v[84:87], a[16:19], v[16:19]\n"
    "s_add_u32 s76, s76, 32768\n"
    "v_exp_f32 v3, v3\n"
    "v_mfma_f32_16x16x32_bf16 v[20:23], v[80:83], a[4:7], v[68:71]\n"
    "v_exp_f32 v4, v4\n"
    "v_mfma_f32_16x16x32_bf16 v[20:23], v[84:87], a[20:23], v[20:23]\n"
    "ds_read_b128 v[108:111], v120 offset:57536\n"
    "v_add_f32 v116, v0, v1\n"
    "v_mfma_f32_16x16x32_bf16 v[24:27], v[80:83], a[8:11], v[72:75]\n"
    "v_exp_f32 v5, v5\n"
    "v_mfma_f32_16x16x32_bf16 v[24:27], v[84:87], a[24:27], v[24:27]\n"
    "v_exp_f32 v6, v6\n"
    "v_mfma_f32_16x16x32_bf16 v[28:31], v[80:83], a[12:15], v[76:79]\n"
    "v_exp_f32 v7, v7\n"
    "v_mfma_f32_16x16x32_bf16 v[28:31], v[84:87], a[28:31], v[28:31]\n"
    "s_waitcnt lgkmcnt(4)\n"
    "v_add_f32 v116, v116, v2\n"
    "v_add_f32 v116, v116, v3\n"
    "v_mfma_f32_16x16x32_bf16 v[16:19], v[88:91], a[32:35], v[16:19]\n"
    "ds_read_b128 v[80:83], v120 offset:0\n"
    "v_cvt_pk_bf16_f32 v48, v0, v1\n"
    "v_mfma_f32_16x16x32_bf16 v[16:19], v[92:95], a[48:51], v[16:19]\n"
    "v_cvt_pk_bf16_f32 v49, v2, v3\n"
    "v_add_f32 v117, v4, v5\n"
    "v_mfma_f32_16x16x32_bf16 v[20:23], v[88:91], a[36:39], v[20:23]\n"
    "v_exp_f32 v8, v8\n"
    "v_mfma_f32_16x16x32_bf16 v[20:23], v[92:95], a[52:55], v[20:23]\n"
    "ds_read_b128 v[84:87], v120 offset:512\n"
    "v_add_f32 v117, v117, v6\n"
    "v_mfma_f32_16x16x32_bf16 v[24:27], v[88:91], a[40:43], v[24:27]\n"
    "v_exp_f32 v9, v9\n"
    "v_mfma_f32_16x16x32_bf16 v[24:27], v[92:95], a[56:59], v[24:27]\n"
    "v_exp_f32 v10, v10\n"
    "v_mfma_f32_16x16x32_bf16 v[28:31], v[88:91], a[44:47], v[28:31]\n"
    "v_exp_f32 v11, v11\n"
    "v_mfma_f32_16x16x32_bf16 v[28:31], v[92:95], a[60:63], v[28:31]\n"
    "s_waitcnt lgkmcnt(4)\n"
    "v_add_f32 v117, v117, v7\n"
    "v_cvt_pk_bf16_f32 v52, v4, v5\n"
    "v_mfma_f32_16x16x32_bf16 v[16:19], v[96:99], a[64:67], v[16:19]\n"
    "ds_read_b128 v[88:91], v120 offset:2112\n"
    "v_cvt_pk_bf16_f32 v53, v6, v7\n"
    "v_mfma_f32_16x16x32_bf16 v[16:19], v[100:103], a[80:83], v[16:19]\n"
    "v_exp_f32 v12, v12\n"
    "v_mfma_f32_16x16x32_bf16 v[20:23], v[96:99], a[68:71], v[20:23]\n"
    "v_exp_f32 v13, v13\n"
    "v_mfma_f32_16x16x32_bf16 v[20:23], v[100:103], a[84:87], v[20:23]\n"
    "ds_read_b128 v[92:95], v120 offset:2624\n"
    "v_add_f32 v118, v8, v9\n"
    "v_mfma_f32_16x16x32_bf16 v[24:27], v[96:99], a[72:75], v[24:27]\n"
    "v_exp_f32 v14, v14\n"
    "v_mfma_f32_16x16x32_bf16 v[24:27], v[100:103], a[88:91], v[24:27]\n"
    "v_exp_f32 v15, v15\n"
    "v_mfma_f32_16x16x32_bf16 v[28:31], v[96:99], a[76:79], v[28:31]\n"
    "v_add_f32 v118, v118, v10\n"
    "v_add_f32 v118, v118, v11\n"
    "v_mfma_f32_16x16x32_bf16 v[28:31], v[100:103], a[92:95], v[28:31]\n"
    "s_waitcnt lgkmcnt(4)\n"
    "v_cvt_pk_bf16_f32 v56, v8, v9\n"
    "v_cvt_pk_bf16_f32 v57, v10, v11\n"
    "v_mfma_f32_16x16x32_bf16 v[16:19], v[104:107], a[96:99], v[16:19]\n"
    "ds_read_b128 v[96:99], v120 offset:4224\n"
    "v_add_f32 v119, v12, v13\n"
    "v_mfma_f32_16x16x32_bf16 v[16:19], v[108:111], a[112:115], v[16:19]\n"
    "v_add_f32 v119, v119, v14\n"
    "v_add_f32 v119, v119, v15\n"
    "v_mfma_f32_16x16x32_bf16 v[20:23], v[104:107], a[100:103], v[20:23]\n"
    "v_cvt_pk_bf16_f32 v60, v12, v13\n"
    "v_cvt_pk_bf16_f32 v61, v14, v15\n"
    "v_mfma_f32_16x16x32_bf16 v[20:23], v[108:111], a[116:119], v[20:23]\n"
    "ds_read_b128 v[100:103], v120 offset:4736\n"
    "v_add_f32 v149, v149, v68\n"
    "v_mfma_f32_16x16x32_bf16 v[24:27], v[104:107], a[104:107], v[24:27]\n"
    "v_exp_f32 v16, v16\n"
    "v_mfma_f32_16x16x32_bf16 v[24:27], v[108:111], a[120:123], v[24:27]\n"
    "v_exp_f32 v17, v17\n"
    "v_mfma_f32_16x16x32_bf16 v[28:31], v[104:107], a[108:111], v[28:31]\n"
    "v_exp_f32 v18, v18\n"
    "v_mfma_f32_16x16x32_bf16 v[28:31], v[108:111], a[124:127], v[28:31]\n"
    "s_waitcnt lgkmcnt(4)\n"
    "v_exp_f32 v19, v19\n"
    "v_mfma_f32_16x16x32_bf16 v[0:3], v[80:83], a[0:3], v[64:67]\n"
    "ds_read_b128 v[104:107], v120 offset:6336\n"
    "v_add_f32 v116, v116, v16\n"
    "v_mfma_f32_16x16x32_bf16 v[0:3], v[84:87], a[16:19], v[0:3]\n"
    "v_exp_f32 v20, v20\n"
    "v_mfma_f32_16x16x32_bf16 v[4:7], v[80:83], a[4:7], v[68:71]\n"
    "v_exp_f32 v21, v21\n"
    "v_mfma_f32_16x16x32_bf16 v[4:7], v[84:87], a[20:23], v[4:7]\n"
    "ds_read_b128 v[108:111], v120 offset:6848\n"
    "v_add_f32 v116, v116, v17\n"
    "v_mfma_f32_16x16x32_bf16 v[8:11], v[80:83], a[8:11], v[72:75]\n"
    "v_exp_f32 v22, v22\n"
    "v_mfma_f32_16x16x32_bf16 v[8:11], v[84:87], a[24:27], v[8:11]\n"
    "v_exp_f32 v23, v23\n"
    "v_mfma_f32_16x16x32_bf16 v[12:15], v[80:83], a[12:15], v[76:79]\n"
    "v_add_f32 v116, v116, v18\n"
    "v_add_f32 v116, v116, v19\n"
    "v_mfma_f32_16x16x32_bf16 v[12:15], v[84:87], a[28:31], v[12:15]\n"
    "s_waitcnt lgkmcnt(4)\n"
    "v_cvt_pk_bf16_f32 v50, v16, v17\n"
    "v_cvt_pk_bf16_f32 v51, v18, v19\n"
    "v_mfma_f32_16x16x32_bf16 v[0:3], v[88:91], a[32:35], v[0:3]\n"
    "s_waitcnt vmcnt(8)\n"
    "v_exp_f32 v24, v24\n"
    "v_mfma_f32_16x16x32_bf16 v[0:3], v[92:95], a[48:51], v[0:3]\n"
    "s_barrier\n"
    "ds_read_b128 v[80:83], v120 offset:16896\n"
    "v_add_f32 v117, v117, v20\n"
    "v_mfma_f32_16x16x32_bf16 v[4:7], v[88:91], a[36:39], v[4:7]\n"
    "v_exp_f32 v25, v25\n"
    "v_mfma_f32_16x16x32_bf16 v[4:7], v[92:95], a[52:55], v[4:7]\n"
    "v_exp_f32 v26, v26\n"
    "v_mfma_f32_16x16x32_bf16 v[8:11], v[88:91], a[40:43], v[8:11]\n"
    "ds_read_b128 v[84:87], v120 offset:17408\n"
    "v_add_f32 v117, v117, v21\n"
    "v_mfma_f32_16x16x32_bf16 v[8:11], v[92:95], a[56:59], v[8:11]\n"
    "v_exp_f32 v27, v27\n"
    "v_mfma_f32_16x16x32_bf16 v[12:15], v[88:91], a[44:47], v[12:15]\n"
    "v_add_f32 v117, v117, v22\n"
    "v_add_f32 v117, v117, v23\n"
    "v_mfma_f32_16x16x32_bf16 v[12:15], v[92:95], a[60:63], v[12:15]\n"
    "s_waitcnt lgkmcnt(4)\n"
    "v_cvt_pk_bf16_f32 v54, v20, v21\n"
    "v_cvt_pk_bf16_f32 v55, v22, v23\n"
    "v_mfma_f32_16x16x32_bf16 v[0:3], v[96:99], a[64:67], v[0:3]\n"
    "ds_read_b128 v[88:91], v120 offset:19008\n"
    "v_add_f32 v118, v118, v24\n"
    "v_mfma_f32_16x16x32_bf16 v[0:3], v[100:103], a[80:83], v[0:3]\n"
    "v_exp_f32 v28, v28\n"
    "v_mfma_f32_16x16x32_bf16 v[4:7], v[96:99], a[68:71], v[4:7]\n"
    "v_exp_f32 v29, v29\n"
    "v_mfma_f32_16x16x32_bf16 v[4:7], v[100:103], a[84:87], v[4:7]\n"
    "ds_read_b128 v[92:95], v120 offset:19520\n"
    "v_add_f32 v118, v118, v25\n"
    "v_mfma_f32_16x16x32_bf16 v[8:11], v[96:99], a[72:75], v[8:11]\n"
    "v_exp_f32 v30, v30\n"
    "v_mfma_f32_16x16x32_bf16 v[8:11], v[100:103], a[88:91], v[8:11]\n"
    "v_exp_f32 v31, v31\n"
    "v_mfma_f32_16x16x32_bf16 v[12:15], v[96:99], a[76:79], v[12:15]\n"
    "v_add_f32 v118, v118, v26\n"
    "v_add_f32 v118, v118, v27\n"
    "v_mfma_f32_16x16x32_bf16 v[12:15], v[100:103], a[92:95], v[12:15]\n"
    "s_waitcnt lgkmcnt(4)\n"
    "v_cvt_pk_bf16_f32 v58, v24, v25\n"
    "v_cvt_pk_bf16_f32 v59, v26, v27\n"
    "v_mfma_f32_16x16x32_bf16 v[0:3], v[104:107], a[96:99], v[0:3]\n"
    "ds_read_b128 v[96:99], v120 offset:21120\n"
    "v_add_f32 v119, v119, v28\n"
    "v_mfma_f32_16x16x32_bf16 v[0:3], v[108:111], a[112:115], v[0:3]\n"
    "v_add_f32 v119, v119, v29\n"
    "v_add_f32 v119, v119, v30\n"
    "v_mfma_f32_16x16x32_bf16 v[4:7], v[104:107], a[100:103], v[4:7]\n"
    "v_add_f32 v119, v119, v31\n"
    "v_cvt_pk_bf16_f32 v62, v28, v29\n"
    "v_mfma_f32_16x16x32_bf16 v[4:7], v[108:111], a[116:119], v[4:7]\n"
    "ds_read_b128 v[100:103], v120 offset:21632\n"
    "v_cvt_pk_bf16_f32 v63, v30, v31\n"
    "v_mfma_f32_16x16x32_bf16 v[8:11], v[104:107], a[104:107], v[8:11]\n"
    "v_add_f32 v150, v150, v68\n"
    "v_add_f32 v151, v151, v68\n"
    "v_mfma_f32_16x16x32_bf16 v[8:11], v[108:111], a[120:123], v[8:11]\n"
    "v_add_f32 v152, v152, v72\n"
    "v_add_f32 v153, v153, v72\n"
    "v_mfma_f32_16x16x32_bf16 v[12:15], v[104:107], a[108:111], v[12:15]\n"
    "v_add_f32 v154, v154, v72\n"
    "v_add_f32 v155, v155, v72\n"
    "v_mfma_f32_16x16x32_bf16 v[12:15], v[108:111], a[124:127], v[12:15]\n"
    "s_waitcnt lgkmcnt(4)\n"
    "v_add_f32 v156, v156, v76\n"
    "v_add_f32 v157, v157, v76\n"
    "v_mfma_f32_16x16x32_bf16 v[16:19], v[80:83], a[0:3], v[64:67]\n"
    "ds_read_b128 v[104:107], v120 offset:23232\n"
    "v_add_f32 v158, v158, v76\n"
    "v_mfma_f32_16x16x32_bf16 v[16:19], v[84:87], a[16:19], v[16:19]\n"
    "s_waitcnt vmcnt(0)\n"
    "v_exp_f32 v128, v128\n"
    "v_mfma_f32_16x16x32_bf16 v[20:23], v[80:83], a[4:7], v[68:71]\n"
    "s_add_u32 m0, s77, 33792\n"
    "v_exp_f32 v129, v129\n"
    "v_mfma_f32_16x16x32_bf16 v[20:23], v[84:87], a[20:23], v[20:23]\n"
    "ds_read_b128 v[108:111], v120 offset:23744\n"
    "buffer_load_dwordx4 v123, s[24:27], s72 offen offset:0 nt lds\n"
    "v_mfma_f32_16x16x32_bf16 v[24:27], v[80:83], a[8:11], v[72:75]\n"
    "v_add_f32 v159, v159, v76\n"
    "v_add_f32 v168, v128, v129\n"
    "v_mfma_f32_16x16x32_bf16 v[24:27], v[84:87], a[24:27], v[24:27]\n"
    "v_exp_f32 v130, v130\n"
    "v_mfma_f32_16x16x32_bf16 v[28:31], v[80:83], a[12:15], v[76:79]\n"
    "s_add_u32 m0, s77, 35776\n"
    "v_exp_f32 v131, v131\n"
    "v_mfma_f32_16x16x32_bf16 v[28:31], v[84:87], a[28:31], v[28:31]\n"
    "s_waitcnt lgkmcnt(4)\n"
    "buffer_load_dwordx4 v123, s[24:27], s72 offen offset:128 nt lds\n"
    "v_add_f32 v168, v168, v130\n"
    "v_mfma_f32_16x16x32_bf16 v[16:19], v[88:91], a[32:35], v[16:19]\n"
    "ds_read_b64_tr_b16 v[80:81], v121 offset:0\n"
    "v_add_f32 v168, v168, v131\n"
    "v_mfma_f32_16x16x32_bf16 v[16:19], v[92:95], a[48:51], v[16:19]\n"
    "ds_read_b64_tr_b16 v[82:83], v121 offset:256\n"
    "v_cvt_pk_bf16_f32 v32, v128, v129\n"
    "v_mfma_f32_16x16x32_bf16 v[20:23], v[88:91], a[36:39], v[20:23]\n"
    "s_add_u32 m0, s77, 37760\n"
    "v_exp_f32 v132, v132\n"
    "v_mfma_f32_16x16x32_bf16 v[20:23], v[92:95], a[52:55], v[20:23]\n"
    "ds_read_b64_tr_b16 v[84:85], v121 offset:4224\n"
    "buffer_load_dwordx4 v123, s[24:27], s72 offen offset:256 nt lds\n"
    "v_mfma_f32_16x16x32_bf16 v[24:27], v[88:91], a[40:43], v[24:27]\n"
    "ds_read_b64_tr_b16 v[86:87], v121 offset:4480\n"
    "v_cvt_pk_bf16_f32 v33, v130, v131\n"
    "v_mfma_f32_16x16x32_bf16 v[24:27], v[92:95], a[56:59], v[24:27]\n"
    "v_exp_f32 v133, v133\n"
    "v_mfma_f32_16x16x32_bf16 v[28:31], v[88:91], a[44:47], v[28:31]\n"
    "s_add_u32 m0, s77, 39744\n"
    "v_exp_f32 v134, v134\n"
    "v_mfma_f32_16x16x32_bf16 v[28:31], v[92:95], a[60:63], v[28:31]\n"
    "s_waitcnt lgkmcnt(6)\n"
    "v_exp_f32 v135, v135\n"
    "v_mfma_f32_16x16x32_bf16 v[16:19], v[96:99], a[64:67], v[16:19]\n"
    "v_exp_f32 v136, v136\n"
    "v_mfma_f32_16x16x32_bf16 v[16:19], v[100:103], a[80:83], v[16:19]\n"
    "v_exp_f32 v137, v137\n"
    "v_exp_f32 v138, v138\n"
    "v_exp_f32 v139, v139\n"
    "v_exp_f32 v140, v140\n"
    "v_exp_f32 v141, v141\n"
    "v_exp_f32 v142, v142\n"
    "v_exp_f32 v143, v143\n"
    "v_exp_f32 v144, v144\n"
    "v_exp_f32 v145, v145\n"
    "v_exp_f32 v146, v146\n"
    "v_exp_f32 v147, v147\n"
    "v_exp_f32 v148, v148\n"
    "v_exp_f32 v149, v149\n"
    "v_exp_f32 v150, v150\n"
    "v_exp_f32 v151, v151\n"
    "v_exp_f32 v152, v152\n"
    "v_exp_f32 v153, v153\n"
    "v_exp_f32 v154, v154\n"
    "v_exp_f32 v155, v155\n"
    "v_exp_f32 v156, v156\n"
    "v_exp_f32 v157, v157\n"
    "v_exp_f32 v158, v158\n"
    "v_exp_f32 v159, v159\n"
    "v_mfma_f32_16x16x32_bf16 v[20:23], v[96:99], a[68:71], v[20:23]\n"
    "buffer_load_dwordx4 v123, s[24:27], s72 offen offset:384 nt lds\n"
    "v_add_f32 v169, v132, v133\n"
    "v_mfma_f32_16x16x32_bf16 v[20:23], v[100:103], a[84:87], v[20:23]\n"
    "s_add_u32 s72, s72, 2048\n"
    "v_add_f32 v169, v169, v134\n"
    "v_add_f32 v169, v169, v135\n"
    "v_mfma_f32_16x16x32_bf16 v[24:27], v[96:99], a[72:75], v[24:27]\n"
    "s_add_u32 m0, s77, 50688\n"
    "v_cvt_pk_bf16_f32 v36, v132, v133\n"
    "v_cvt_pk_bf16_f32 v37, v134, v135\n"
    "v_add_f32 v170, v136, v137\n"
    "v_add_f32 v170, v170, v138\n"
    "v_add_f32 v170, v170, v139\n"
    "v_cvt_pk_bf16_f32 v40, v136, v137\n"
    "v_cvt_pk_bf16_f32 v41, v138, v139\n"
    "v_add_f32 v171, v140, v141\n"
    "v_add_f32 v171, v171, v142\n"
    "v_add_f32 v171, v171, v143\n"
    "v_cvt_pk_bf16_f32 v44, v140, v141\n"
    "v_cvt_pk_bf16_f32 v45, v142, v143\n"
    "v_add_f32 v168, v168, v144\n"
    "v_add_f32 v168, v168, v145\n"
    "v_add_f32 v168, v168, v146\n"
    "v_add_f32 v168, v168, v147\n"
    "v_cvt_pk_bf16_f32 v34, v144, v145\n"
    "v_cvt_pk_bf16_f32 v35, v146, v147\n"
    "v_add_f32 v169, v169, v148\n"
    "v_add_f32 v169, v169, v149\n"
    "v_add_f32 v169, v169, v150\n"
    "v_add_f32 v169, v169, v151\n"
    "v_cvt_pk_bf16_f32 v38, v148, v149\n"
    "v_cvt_pk_bf16_f32 v39, v150, v151\n"
    "v_add_f32 v170, v170, v152\n"
    "v_add_f32 v170, v170, v153\n"
    "v_add_f32 v170, v170, v154\n"
    "v_add_f32 v170, v170, v155\n"
    "v_cvt_pk_bf16_f32 v42, v152, v153\n"
    "v_cvt_pk_bf16_f32 v43, v154, v155\n"
    "v_add_f32 v171, v171, v156\n"
    "v_add_f32 v171, v171, v157\n"
    "v_add_f32 v171, v171, v158\n"
    "v_add_f32 v171, v171, v159\n"
    "v_cvt_pk_bf16_f32 v46, v156, v157\n"
    "v_cvt_pk_bf16_f32 v47, v158, v159\n"
    "v_mfma_f32_16x16x32_bf16 v[24:27], v[100:103], a[88:91], v[24:27]\n"
    "ds_read_b64_tr_b16 v[88:89], v121 offset:32\n"
    "ds_read_b64_tr_b16 v[90:91], v121 offset:288\n"
    "v_mfma_f32_16x16x32_bf16 v[28:31], v[96:99], a[76:79], v[28:31]\n"
    "ds_read_b64_tr_b16 v[92:93], v121 offset:4256\n"
    "ds_read_b64_tr_b16 v[94:95], v121 offset:4512\n"
    "v_mfma_f32_16x16x32_bf16 v[28:31], v[100:103], a[92:95], v[28:31]\n"
    "s_waitcnt lgkmcnt(8)\n"
    "buffer_load_dwordx4 v123, s[24:27], s72 offen offset:0 nt lds\n"
    "v_add_f32 v116, v116, v168\n"
    "v_add_f32 v117, v117, v169\n"
    "v_add_f32 v118, v118, v170\n"
    "v_add_f32 v119, v119, v171\n"
    "v_mfma_f32_16x16x32_bf16 v[16:19], v[104:107], a[96:99], v[16:19]\n"
    "s_add_u32 m0, s77, 52672\n"
    "v_max3_f32 v124, v116, v117, v118\n"
    "v_max_f32 v124, v124, v119\n"
    "v_mfma_f32_16x16x32_bf16 v[16:19], v[108:111], a[112:115], v[16:19]\n"
    "v_cmp_nge_f32 vcc, s80, v124\n"
    "buffer_load_dwordx4 v123, s[24:27], s72 offen offset:128 nt lds\n"
    "v_mfma_f32_16x16x32_bf16 v[20:23], v[104:107], a[100:103], v[20:23]\n"
    "s_cbranch_vccnz Lslow%=\n"
    "LbackF%=:\n"
    "ds_read_b64_tr_b16 v[96:97], v121 offset:512\n"
    "ds_read_b64_tr_b16 v[98:99], v121 offset:768\n"
    "v_mfma_f32_16x16x32_bf16 v[20:23], v[108:111], a[116:119], v[20:23]\n"
    "s_add_u32 s28, s28, 32768\n"
    "ds_read_b64_tr_b16 v[100:101], v121 offset:4736\n"
    "v_add_f32 v112, v112, v116\n"
    "v_mfma_f32_16x16x32_bf16 v[24:27], v[104:107], a[104:107], v[24:27]\n"
    "ds_read_b64_tr_b16 v[102:103], v121 offset:4992\n"
    "v_add_f32 v113, v113, v117\n"
    "v_mfma_f32_16x16x32_bf16 v[24:27], v[108:111], a[120:123], v[24:27]\n"
    "s_add_u32 m0, s77, 54656\n"
    "v_add_f32 v114, v114, v118\n"
    "buffer_load_dwordx4 v123, s[24:27], s72 offen offset:256 nt lds\n"
    "v_mfma_f32_16x16x32_bf16 v[28:31], v[104:107], a[108:111], v[28:31]\n"
    "v_exp_f32 v0, v0\n"
    "v_mfma_f32_16x16x32_bf16 v[28:31], v[108:111], a[124:127], v[28:31]\n"
    "s_waitcnt lgkmcnt(8)\n"
    "v_exp_f32 v1, v1\n"
    "v_mfma_f32_16x16x32_bf16 v[128:131], v[80:83], v[32:35], 0\n"
    "v_add_f32 v115, v115, v119\n"
    "ds_read_b64_tr_b16 v[104:105], v121 offset:544\n"
    "v_mfma_f32_16x16x32_bf16 v[128:131], v[84:87], v[48:51], v[128:131]\n"
    "ds_read_b64_tr_b16 v[106:107], v121 offset:800\n"
    "v_mbcnt_lo_u32_b32 v126, -1, 0\n"
    "v_mfma_f32_16x16x32_bf16 v[132:135], v[80:83], v[36:39], 0\n"
    "s_add_u32 m0, s77, 56640\n"
    "v_mbcnt_hi_u32_b32 v126, -1, v126\n"
    "buffer_load_dwordx4 v123, s[24:27], s72 offen offset:384 nt lds\n"
    "v_mfma_f32_16x16x32_bf16 v[132:135], v[84:87], v[52:55], v[132:135]\n"
    "s_add_u32 s72, s72, 14336\n"
    "ds_read_b64_tr_b16 v[108:109], v121 offset:4768\n"
    "v_add_f32 v116, v0, v1\n"
    "v_mfma_f32_16x16x32_bf16 a[128:131], v[80:83], v[40:43], 0\n"
    "s_and_b32 s84, %1, 7\n"
    "v_exp_f32 v2, v2\n"
    "v_mfma_f32_16x16x32_bf16 a[128:131], v[84:87], v[56:59], a[128:131]\n"
    "s_lshl_b32 s84, s84, 1\n"
    "ds_read_b64_tr_b16 v[110:111], v121 offset:5024\n"
    "v_add_f32 v116, v116, v2\n"
    "v_mfma_f32_16x16x32_bf16 a[132:135], v[80:83], v[44:47], 0\n"
    "s_bfe_u32 s85, %1, 0x10003\n"
    "v_exp_f32 v3, v3\n"
    "v_mfma_f32_16x16x32_bf16 a[132:135], v[84:87], v[60:63], a[132:135]\n"
    "s_waitcnt lgkmcnt(8)\n"
    "v_add_f32 v116, v116, v3\n"
    "v_mfma_f32_16x16x32_bf16 v[136:139], v[88:91], v[32:35], 0\n"
    "s_waitcnt vmcnt(12)\n"
    "v_exp_f32 v4, v4\n"
    "v_mfma_f32_16x16x32_bf16 v[136:139], v[92:95], v[48:51], v[136:139]\n"
    "s_barrier\n"
    "v_exp_f32 v5, v5\n"
    "v_mfma_f32_16x16x32_bf16 v[140:143], v[88:91], v[36:39], 0\n"
    "s_add_u32 s84, s84, s85\n"
    "ds_read_b64_tr_b16 v[80:81], v121 offset:16896\n"
    "ds_read_b64_tr_b16 v[82:83], v121 offset:17152\n"
    "v_mfma_f32_16x16x32_bf16 v[140:143], v[92:95], v[52:55], v[140:143]\n"
    "s_lshl_b32 s84, s84, 3\n"
    "v_exp_f32 v6, v6\n"
    "v_mfma_f32_16x16x32_bf16 a[136:139], v[88:91], v[40:43], 0\n"
    "s_add_u32 s84, s84, 128\n"
    "v_lshlrev_b32 v125, 12, v126\n"
    "v_add_u32 v125, s84, v125\n"
    "v_mov_b32 v126, s33\n"
    "v_mov_b32 v127, s34\n"
    "v_mfma_f32_16x16x32_bf16 a[136:139], v[92:95], v[56:59], a[136:139]\n"
    "s_add_u32 m0, s77, 0\n"
    "ds_read_b64_tr_b16 v[84:85], v121 offset:21120\n"
    "ds_read_b64_tr_b16 v[86:87], v121 offset:21376\n"
    "v_mfma_f32_16x16x32_bf16 a[140:143], v[88:91], v[44:47], 0\n"
    "buffer_load_dwordx4 v123, s[24:27], s72 offen offset:0 nt lds\n"
    "buffer_store_dwordx2 v[126:127], v125, s[64:67], 0 offen sc1\n"
    "v_mfma_f32_16x16x32_bf16 a[140:143], v[92:95], v[60:63], a[140:143]\n"
    "s_waitcnt lgkmcnt(8)\n"
    "v_exp_f32 v7, v7\n"
    "v_mfma_f32_16x16x32_bf16 v[144:147], v[96:99], v[32:35], 0\n"
    "ds_read_b64_tr_b16 v[88:89], v121 offset:16928\n"
    "v_add_f32 v117, v4, v5\n"
    "v_mfma_f32_16x16x32_bf16 v[144:147], v[100:103], v[48:51], v[144:147]\n"
    "ds_read_b64_tr_b16 v[90:91], v121 offset:17184\n"
    "v_add_f32 v117, v117, v6\n"
    "v_mfma_f32_16x16x32_bf16 v[148:151], v[96:99], v[36:39], 0\n"
    "s_add_u32 m0, s77, 1984\n"
    "v_add_f32 v117, v117, v7\n"
    "buffer_load_dwordx4 v123, s[24:27], s72 offen offset:128 nt lds\n"
    "v_mfma_f32_16x16x32_bf16 v[148:151], v[100:103], v[52:55], v[148:151]\n"
    "ds_read_b64_tr_b16 v[92:93], v121 offset:21152\n"
    "v_mfma_f32_16x16x32_bf16 a[144:147], v[96:99], v[40:43], 0\n"
    "ds_read_b64_tr_b16 v[94:95], v121 offset:21408\n"
    "v_mfma_f32_16x16x32_bf16 a[144:147], v[100:103], v[56:59], a[144:147]\n"
    "v_mfma_f32_16x16x32_bf16 a[148:151], v[96:99], v[44:47], 0\n"
    "s_add_u32 s90, s90, LbackM%=-LbackF%=\n"
    "s_addc_u32 s91, s91, 0\n"
    "v_exp_f32 v8, v8\n"
    "v_mfma_f32_16x16x32_bf16 a[148:151], v[100:103], v[60:63], a[148:151]\n"
    "s_waitcnt lgkmcnt(8)\n"
    "v_exp_f32 v9, v9\n"
    "v_mfma_f32_16x16x32_bf16 v[152:155], v[104:107], v[32:35], 0\n"
    "ds_read_b64_tr_b16 v[96:97], v121 offset:17408\n"
    "v_add_f32 v118, v8, v9\n"
    "v_mfma_f32_16x16x32_bf16 v[152:155], v[108:111], v[48:51], v[152:155]\n"
    "s_add_u32 m0, s77, 3968\n"
    "v_exp_f32 v10, v10\n"
    "v_mfma_f32_16x16x32_bf16 v[156:159], v[104:107], v[36:39], 0\n"
    "buffer_load_dwordx4 v123, s[24:27], s72 offen offset:256 nt lds\n"
    "ds_read_b64_tr_b16 v[98:99], v121 offset:17664\n"
    "v_mfma_f32_16x16x32_bf16 v[156:159], v[108:111], v[52:55], v[156:159]\n"
    "ds_read_b64_tr_b16 v[100:101], v121 offset:21632\n"
    "v_add_f32 v118, v118, v10\n"
    "v_mfma_f32_16x16x32_bf16 a[152:155], v[104:107], v[40:43], 0\n"
    "v_exp_f32 v11, v11\n"
    "v_mfma_f32_16x16x32_bf16 a[152:155], v[108:111], v[56:59], a[152:155]\n"
    "ds_read_b64_tr_b16 v[102:103], v121 offset:21888\n"
    "v_add_f32 v118, v118, v11\n"
    "v_mfma_f32_16x16x32_bf16 a[156:159], v[104:107], v[44:47], 0\n"
    "v_mfma_f32_16x16x32_bf16 a[156:159], v[108:111], v[60:63], a[156:159]\n"
    "s_waitcnt lgkmcnt(8)\n"
    "v_mfma_f32_16x16x32_bf16 v[160:163], v[80:83], v[32:35], 0\n"
    "s_add_u32 m0, s77, 5952\n"
    "ds_read_b64_tr_b16 v[104:105], v121 offset:17440\n"
    "buffer_load_dwordx4 v123, s[24:27], s72 offen offset:384 nt lds\n"
    "v_mfma_f32_16x16x32_bf16 v[160:163], v[84:87], v[48:51], v[160:163]\n"
    "s_add_u32 s72, s72, 2048\n"
    "ds_read_b64_tr_b16 v[106:107], v121 offset:17696\n"
    "v_mfma_f32_16x16x32_bf16 v[164:167], v[80:83], v[36:39], 0\n"
    "v_mfma_f32_16x16x32_bf16 v[164:167], v[84:87], v[52:55], v[164:167]\n"
    "ds_read_b64_tr_b16 v[108:109], v121 offset:21664\n"
    "v_mfma_f32_16x16x32_bf16 a[160:163], v[80:83], v[40:43], 0\n"
    "v_exp_f32 v12, v12\n"
    "v_mfma_f32_16x16x32_bf16 a[160:163], v[84:87], v[56:59], a[160:163]\n"
    "v_exp_f32 v13, v13\n"
    "v_mfma_f32_16x16x32_bf16 a[164:167], v[80:83], v[44:47], 0\n"
    "ds_read_b64_tr_b16 v[110:111], v121 offset:21920\n"
    "v_add_f32 v119, v12, v13\n"
    "v_mfma_f32_16x16x32_bf16 a[164:167], v[84:87], v[60:63], a[164:167]\n"
    "s_waitcnt lgkmcnt(8)\n"
    "v_exp_f32 v14, v14\n"
    "v_mfma_f32_16x16x32_bf16 v[168:171], v[88:91], v[32:35], 0\n"
    "s_add_u32 m0, s77, 16896\n"
    "ds_read_b64_tr_b16 v[80:81], v121 offset:33792\n"
    "buffer_load_dwordx4 v123, s[24:27], s72 offen offset:0 nt lds\n"
    "v_mfma_f32_16x16x32_bf16 v[168:171], v[92:95], v[48:51], v[168:171]\n"
    "ds_read_b64_tr_b16 v[82:83], v121 offset:34048\n"
    "v_add_f32 v119, v119, v14\n"
    "v_mfma_f32_16x16x32_bf16 v[172:175], v[88:91], v[36:39], 0\n"
    "v_exp_f32 v15, v15\n"
    "v_mfma_f32_16x16x32_bf16 v[172:175], v[92:95], v[52:55], v[172:175]\n"
    "ds_read_b64_tr_b16 v[84:85], v121 offset:38016\n"
    "v_add_f32 v119, v119, v15\n"
    "v_mfma_f32_16x16x32_bf16 a[168:171], v[88:91], v[40:43], 0\n"
    "ds_read_b64_tr_b16 v[86:87], v121 offset:38272\n"
    "v_mfma_f32_16x16x32_bf16 a[168:171], v[92:95], v[56:59], a[168:171]\n"
    "v_mfma_f32_16x16x32_bf16 a[172:175], v[88:91], v[44:47], 0\n"
    "s_add_u32 m0, s77, 18880\n"
    "v_mfma_f32_16x16x32_bf16 a[172:175], v[92:95], v[60:63], a[172:175]\n"
    "s_waitcnt lgkmcnt(8)\n"
    "buffer_load_dwordx4 v123, s[24:27], s72 offen offset:128 nt lds\n"
    "v_mfma_f32_16x16x32_bf16 v[176:179], v[96:99], v[32:35], 0\n"
    "ds_read_b64_tr_b16 v[88:89], v121 offset:33824\n"
    "v_mfma_f32_16x16x32_bf16 v[176:179], v[100:103], v[48:51], v[176:179]\n"
    "ds_read_b64_tr_b16 v[90:91], v121 offset:34080\n"
    "v_mfma_f32_16x16x32_bf16 v[180:183], v[96:99], v[36:39], 0\n"
    "v_exp_f32 v16, v16\n"
    "v_mfma_f32_16x16x32_bf16 v[180:183], v[100:103], v[52:55], v[180:183]\n"
    "ds_read_b64_tr_b16 v[92:93], v121 offset:38048\n"
    "v_add_f32 v116, v116, v16\n"
    "v_mfma_f32_16x16x32_bf16 a[176:179], v[96:99], v[40:43], 0\n"
    "v_exp_f32 v17, v17\n"
    "v_mfma_f32_16x16x32_bf16 a[176:179], v[100:103], v[56:59], a[176:179]\n"
    "s_add_u32 m0, s77, 20864\n"
    "ds_read_b64_tr_b16 v[94:95], v121 offset:38304\n"
    "buffer_load_dwordx4 v123, s[24:27], s72 offen offset:256 nt lds\n"
    "v_mfma_f32_16x16x32_bf16 a[180:183], v[96:99], v[44:47], 0\n"
    "v_exp_f32 v18, v18\n"
    "v_mfma_f32_16x16x32_bf16 a[180:183], v[100:103], v[60:63], a[180:183]\n"
    "s_waitcnt lgkmcnt(8)\n"
    "v_exp_f32 v19, v19\n"
    "v_mfma_f32_16x16x32_bf16 v[184:187], v[104:107], v[32:35], 0\n"
    "ds_read_b64_tr_b16 v[96:97], v121 offset:34304\n"
    "v_add_f32 v116, v116, v17\n"
    "v_mfma_f32_16x16x32_bf16 v[184:187], v[108:111], v[48:51], v[184:187]\n"
    "ds_read_b64_tr_b16 v[98:99], v121 offset:34560\n"
    "v_add_f32 v116, v116, v18\n"
    "v_mfma_f32_16x16x32_bf16 v[188:191], v[104:107], v[36:39], 0\n"
    "v_add_f32 v116, v116, v19\n"
    "v_mfma_f32_16x16x32_bf16 v[188:191], v[108:111], v[52:55], v[188:191]\n"
    "ds_read_b64_tr_b16 v[100:101], v121 offset:38528\n"
    "v_mfma_f32_16x16x32_bf16 a[184:187], v[104:107], v[40:43], 0\n"
    "s_add_u32 m0, s77, 22848\n"
    "ds_read_b64_tr_b16 v[102:103], v121 offset:38784\n"
    "buffer_load_dwordx4 v123, s[24:27], s72 offen offset:384 nt lds\n"
    "v_mfma_f32_16x16x32_bf16 a[184:187], v[108:111], v[56:59], a[184:187]\n"
    "s_add_u32 s72, s72, 14336\n"
    "v_mfma_f32_16x16x32_bf16 a[188:191], v[104:107], v[44:47], 0\n"
    "v_mfma_f32_16x16x32_bf16 a[188:191], v[108:111], v[60:63], a[188:191]\n"
    "s_waitcnt lgkmcnt(8)\n"
    "v_mfma_f32_16x16x32_bf16 v[192:195], v[80:83], v[32:35], 0\n"
    "ds_read_b64_tr_b16 v[104:105], v121 offset:34336\n"
    "v_mfma_f32_16x16x32_bf16 v[192:195], v[84:87], v[48:51], v[192:195]\n"
    "v_exp_f32 v20, v20\n"
    "v_mfma_f32_16x16x32_bf16 v[196:199], v[80:83], v[36:39], 0\n"
    "ds_read_b64_tr_b16 v[106:107], v121 offset:34592\n"
    "v_add_f32 v117, v117, v20\n"
    "v_mfma_f32_16x16x32_bf16 v[196:199], v[84:87], v[52:55], v[196:199]\n"
    "v_exp_f32 v21, v21\n"
    "v_mfma_f32_16x16x32_bf16 a[192:195], v[80:83], v[40:43], 0\n"
    "ds_read_b64_tr_b16 v[108:109], v121 offset:38560\n"
    "ds_read_b64_tr_b16 v[110:111], v121 offset:38816\n"
    "v_mfma_f32_16x16x32_bf16 a[192:195], v[84:87], v[56:59], a[192:195]\n"
    "v_exp_f32 v22, v22\n"
    "v_mfma_f32_16x16x32_bf16 a[196:199], v[80:83], v[44:47], 0\n"
    "v_exp_f32 v23, v23\n"
    "v_mfma_f32_16x16x32_bf16 a[196:199], v[84:87], v[60:63], a[196:199]\n"
    "s_waitcnt lgkmcnt(8)\n"
    "v_add_f32 v117, v117, v21\n"
    "v_add_f32 v117, v117, v22\n"
    "v_mfma_f32_16x16x32_bf16 v[200:203], v[88:91], v[32:35], 0\n"
    "s_waitcnt vmcnt(13)\n"
    "v_add_f32 v117, v117, v23\n"
    "v_mfma_f32_16x16x32_bf16 v[200:203], v[92:95], v[48:51], v[200:203]\n"
    "s_barrier\n"
    "ds_read_b64_tr_b16 v[80:81], v121 offset:50688\n"
    "v_mfma_f32_16x16x32_bf16 v[204:207], v[88:91], v[36:39], 0\n"
    "ds_read_b64_tr_b16 v[82:83], v121 offset:50944\n"
    "v_mfma_f32_16x16x32_bf16 v[204:207], v[92:95], v[52:55], v[204:207]\n"
    "s_add_u32 m0, s78, 0\n"
    "v_mfma_f32_16x16x32_bf16 a[200:203], v[88:91], v[40:43], 0\n"
    "ds_read_b64_tr_b16 v[84:85], v121 offset:54912\n"
    "buffer_load_dwordx4 v122, s[36:39], s73 offen offset:0 nt lds\n"
    "v_mfma_f32_16x16x32_bf16 a[200:203], v[92:95], v[56:59], a[200:203]\n"
    "ds_read_b64_tr_b16 v[86:87], v121 offset:55168\n"
    "v_mfma_f32_16x16x32_bf16 a[204:207], v[88:91], v[44:47], 0\n"
    "v_mfma_f32_16x16x32_bf16 a[204:207], v[92:95], v[60:63], a[204:207]\n"
    "s_waitcnt lgkmcnt(8)\n"
    "v_mfma_f32_16x16x32_bf16 v[208:211], v[96:99], v[32:35], 0\n"
    "ds_read_b64_tr_b16 v[88:89], v121 offset:50720\n"
    "v_mfma_f32_16x16x32_bf16 v[208:211], v[100:103], v[48:51], v[208:211]\n"
    "s_add_u32 m0, s78, 1056\n"
    "v_exp_f32 v24, v24\n"
    "v_mfma_f32_16x16x32_bf16 v[212:215], v[96:99], v[36:39], 0\n"
    "buffer_load_dwordx4 v122, s[36:39], s74 offen offset:0 nt lds\n"
    "ds_read_b64_tr_b16 v[90:91], v121 offset:50976\n"
    "v_mfma_f32_16x16x32_bf16 v[212:215], v[100:103], v[52:55], v[212:215]\n"
    "ds_read_b64_tr_b16 v[92:93], v121 offset:54944\n"
    "v_add_f32 v118, v118, v24\n"
    "v_mfma_f32_16x16x32_bf16 a[208:211], v[96:99], v[40:43], 0\n"
    "v_exp_f32 v25, v25\n"
    "v_mfma_f32_16x16x32_bf16 a[208:211], v[100:103], v[56:59], a[208:211]\n"
    "ds_read_b64_tr_b16 v[94:95], v121 offset:55200\n"
    "v_add_f32 v118, v118, v25\n"
    "v_mfma_f32_16x16x32_bf16 a[212:215], v[96:99], v[44:47], 0\n"
    "v_exp_f32 v26, v26\n"
    "v_mfma_f32_16x16x32_bf16 a[212:215], v[100:103], v[60:63], a[212:215]\n"
    "s_waitcnt lgkmcnt(8)\n"
    "v_exp_f32 v27, v27\n"
    "v_mfma_f32_16x16x32_bf16 v[216:219], v[104:107], v[32:35], 0\n"
    "s_add_u32 m0, s78, 2112\n"
    "ds_read_b64_tr_b16 v[96:97], v121 offset:51200\n"
    "buffer_load_dwordx4 v122, s[36:39], s75 offen offset:0 nt lds\n"
    "v_mfma_f32_16x16x32_bf16 v[216:219], v[108:111], v[48:51], v[216:219]\n"
    "ds_read_b64_tr_b16 v[98:99], v121 offset:51456\n"
    "v_add_f32 v118, v118, v26\n"
    "v_mfma_f32_16x16x32_bf16 v[220:223], v[104:107], v[36:39], 0\n"
    "v_add_f32 v118, v118, v27\n"
    "v_mfma_f32_16x16x32_bf16 v[220:223], v[108:111], v[52:55], v[220:223]\n"
    "ds_read_b64_tr_b16 v[100:101], v121 offset:55424\n"
    "v_mfma_f32_16x16x32_bf16 a[216:219], v[104:107], v[40:43], 0\n"
    "ds_read_b64_tr_b16 v[102:103], v121 offset:55680\n"
    "v_mfma_f32_16x16x32_bf16 a[216:219], v[108:111], v[56:59], a[216:219]\n"
    "s_add_u32 m0, s78, 3168\n"
    "v_mfma_f32_16x16x32_bf16 a[220:223], v[104:107], v[44:47], 0\n"
    "buffer_load_dwordx4 v122, s[36:39], s76 offen offset:0 nt lds\n"
    "v_mfma_f32_16x16x32_bf16 a[220:223], v[108:111], v[60:63], a[220:223]\n"
    "s_waitcnt lgkmcnt(8)\n"
    "v_mfma_f32_16x16x32_bf16 v[224:227], v[80:83], v[32:35], 0\n"
    "v_exp_f32 v28, v28\n"
    "v_mfma_f32_16x16x32_bf16 v[224:227], v[84:87], v[48:51], v[224:227]\n"
    "ds_read_b64_tr_b16 v[104:105], v121 offset:51232\n"
    "ds_read_b64_tr_b16 v[106:107], v121 offset:51488\n"
    "v_mfma_f32_16x16x32_bf16 v[228:231], v[80:83], v[36:39], 0\n"
    "v_exp_f32 v29, v29\n"
    "v_mfma_f32_16x16x32_bf16 v[228:231], v[84:87], v[52:55], v[228:231]\n"
    "s_add_u32 m0, s78, 16768\n"
    "ds_read_b64_tr_b16 v[108:109], v121 offset:55456\n"
    "buffer_load_dwordx4 v122, s[36:39], s73 offen offset:128 nt lds\n"
    "v_mfma_f32_16x16x32_bf16 a[224:227], v[80:83], v[40:43], 0\n"
    "ds_read_b64_tr_b16 v[110:111], v121 offset:55712\n"
    "v_add_f32 v119, v119, v28\n"
    "v_mfma_f32_16x16x32_bf16 a[224:227], v[84:87], v[56:59], a[224:227]\n"
    "v_exp_f32 v30, v30\n"
    "v_mfma_f32_16x16x32_bf16 a[228:231], v[80:83], v[44:47], 0\n"
    "v_exp_f32 v31, v31\n"
    "v_mfma_f32_16x16x32_bf16 a[228:231], v[84:87], v[60:63], a[228:231]\n"
    "s_waitcnt lgkmcnt(8)\n"
    "v_add_f32 v119, v119, v29\n"
    "v_add_f32 v119, v119, v30\n"
    "v_mfma_f32_16x16x32_bf16 v[232:235], v[88:91], v[32:35], 0\n"
    "ds_read_b128 v[80:83], v120 offset:33792\n"
    "v_add_f32 v119, v119, v31\n"
    "v_mfma_f32_16x16x32_bf16 v[232:235], v[92:95], v[48:51], v[232:235]\n"
    "s_add_u32 m0, s78, 17824\n"
    "v_mfma_f32_16x16x32_bf16 v[236:239], v[88:91], v[36:39], 0\n"
    "buffer_load_dwordx4 v122, s[36:39], s74 offen offset:128 nt lds\n"
    "v_mfma_f32_16x16x32_bf16 v[236:239], v[92:95], v[52:55], v[236:239]\n"
    "ds_read_b128 v[84:87], v120 offset:34304\n"
    "v_mfma_f32_16x16x32_bf16 a[232:235], v[88:91], v[40:43], 0\n"
    "v_mfma_f32_16x16x32_bf16 a[232:235], v[92:95], v[56:59], a[232:235]\n"
    "v_mfma_f32_16x16x32_bf16 a[236:239], v[88:91], v[44:47], 0\n"
    "v_mfma_f32_16x16x32_bf16 a[236:239], v[92:95], v[60:63], a[236:239]\n"
    "s_waitcnt lgkmcnt(6)\n"
    "v_mfma_f32_16x16x32_bf16 v[240:243], v[96:99], v[32:35], 0\n"
    "s_add_u32 m0, s78, 18880\n"
    "ds_read_b128 v[88:91], v120 offset:35904\n"
    "buffer_load_dwordx4 v122, s[36:39], s75 offen offset:128 nt lds\n"
    "v_mfma_f32_16x16x32_bf16 v[240:243], v[100:103], v[48:51], v[240:243]\n"
    "v_mfma_f32_16x16x32_bf16 v[244:247], v[96:99], v[36:39], 0\n"
    "v_mfma_f32_16x16x32_bf16 v[244:247], v[100:103], v[52:55], v[244:247]\n"
    "ds_read_b128 v[92:95], v120 offset:36416\n"
    "v_mfma_f32_16x16x32_bf16 a[240:243], v[96:99], v[40:43], 0\n"
    "s_add_u32 m0, s78, 19936\n"
    "v_mfma_f32_16x16x32_bf16 a[240:243], v[100:103], v[56:59], a[240:243]\n"
    "buffer_load_dwordx4 v122, s[36:39], s76 offen offset:128 nt lds\n"
    "v_mfma_f32_16x16x32_bf16 a[244:247], v[96:99], v[44:47], 0\n"
    "v_mfma_f32_16x16x32_bf16 a[244:247], v[100:103], v[60:63], a[244:247]\n"
    "s_waitcnt lgkmcnt(4)\n"
    "v_mfma_f32_16x16x32_bf16 v[248:251], v[104:107], v[32:35], 0\n"
    "v_cvt_pk_bf16_f32 v32, v0, v1\n"
    "v_cvt_pk_bf16_f32 v33, v2, v3\n"
    "v_mfma_f32_16x16x32_bf16 v[248:251], v[108:111], v[48:51], v[248:251]\n"
    "ds_read_b128 v[96:99], v120 offset:38016\n"
    "v_mfma_f32_16x16x32_bf16 v[252:255], v[104:107], v[36:39], 0\n"
    "v_cvt_pk_bf16_f32 v36, v4, v5\n"
    "v_cvt_pk_bf16_f32 v37, v6, v7\n"
    "v_mfma_f32_16x16x32_bf16 v[252:255], v[108:111], v[52:55], v[252:255]\n"
    "ds_read_b128 v[100:103], v120 offset:38528\n"
    "v_mfma_f32_16x16x32_bf16 a[248:251], v[104:107], v[40:43], 0\n"
    "v_cvt_pk_bf16_f32 v40, v8, v9\n"
    "v_cvt_pk_bf16_f32 v41, v10, v11\n"
    "v_mfma_f32_16x16x32_bf16 a[248:251], v[108:111], v[56:59], a[248:251]\n"
    "v_mfma_f32_16x16x32_bf16 a[252:255], v[104:107], v[44:47], 0\n"
    "v_cvt_pk_bf16_f32 v44, v12, v13\n"
    "v_cvt_pk_bf16_f32 v45, v14, v15\n"
    "v_mfma_f32_16x16x32_bf16 a[252:255], v[108:111], v[60:63], a[252:255]\n"
    "s_waitcnt lgkmcnt(4)\n"
    ".p2align 6\n"
    "Lloop%=:\n"
    "v_mfma_f32_16x16x32_bf16 v[0:3], v[80:83], a[0:3], v[64:67]\n"
    "ds_read_b128 v[104:107], v120 offset:40128\n"
    "v_cvt_pk_bf16_f32 v34, v16, v17\n"
    "v_mfma_f32_16x16x32_bf16 v[0:3], v[84:87], a[16:19], v[0:3]\n"
    "v_cvt_pk_bf16_f32 v35, v18, v19\n"
    "v_cvt_pk_bf16_f32 v38, v20, v21\n"
    "v_mfma_f32_16x16x32_bf16 v[4:7], v[80:83], a[4:7], v[68:71]\n"
    "v_cvt_pk_bf16_f32 v39, v22, v23\n"
    "v_cvt_pk_bf16_f32 v42, v24, v25\n"
    "v_mfma_f32_16x16x32_bf16 v[4:7], v[84:87], a[20:23], v[4:7]\n"
    "ds_read_b128 v[108:111], v120 offset:40640\n"
    "v_cvt_pk_bf16_f32 v43, v26, v27\n"
    "v_mfma_f32_16x16x32_bf16 v[8:11], v[80:83], a[8:11], v[72:75]\n"
    "v_cvt_pk_bf16_f32 v46, v28, v29\n"
    "v_cvt_pk_bf16_f32 v47, v30, v31\n"
    "v_mfma_f32_16x16x32_bf16 v[8:11], v[84:87], a[24:27], v[8:11]\n"
    "v_mfma_f32_16x16x32_bf16 v[12:15], v[80:83], a[12:15], v[76:79]\n"
    "v_mfma_f32_16x16x32_bf16 v[12:15], v[84:87], a[28:31], v[12:15]\n"
    "s_waitcnt lgkmcnt(4)\n"
    "v_mfma_f32_16x16x32_bf16 v[0:3], v[88:91], a[32:35], v[0:3]\n"
    "s_waitcnt vmcnt(12)\n"
    "v_mfma_f32_16x16x32_bf16 v[0:3], v[92:95], a[48:51], v[0:3]\n"
    "s_barrier\n"
    "ds_read_b128 v[80:83], v120 offset:50688\n"
    "v_mfma_f32_16x16x32_bf16 v[4:7], v[88:91], a[36:39], v[4:7]\n"
    "s_add_u32 m0, s78, 33536\n"
    "v_mfma_f32_16x16x32_bf16 v[4:7], v[92:95], a[52:55], v[4:7]\n"
    "buffer_load_dwordx4 v122, s[36:39], s73 offen offset:256 nt lds\n"
    "v_mfma_f32_16x16x32_bf16 v[8:11], v[88:91], a[40:43], v[8:11]\n"
    "s_add_u32 m0, s78, 34592\n"
    "ds_read_b128 v[84:87], v120 offset:51200\n"
    "buffer_load_dwordx4 v122, s[36:39], s74 offen offset:256 nt lds\n"
    "v_mfma_f32_16x16x32_bf16 v[8:11], v[92:95], a[56:59], v[8:11]\n"
    "v_mfma_f32_16x16x32_bf16 v[12:15], v[88:91], a[44:47], v[12:15]\n"
    "v_mfma_f32_16x16x32_bf16 v[12:15], v[92:95], a[60:63], v[12:15]\n"
    "s_waitcnt lgkmcnt(4)\n"
    "v_mfma_f32_16x16x32_bf16 v[0:3], v[96:99], a[64:67], v[0:3]\n"
    "s_add_u32 m0, s78, 35648\n"
    "ds_read_b128 v[88:91], v120 offset:52800\n"
    "buffer_load_dwordx4 v122, s[36:39], s75 offen offset:256 nt lds\n"
    "v_mfma_f32_16x16x32_bf16 v[0:3], v[100:103], a[80:83], v[0:3]\n"
    "s_add_u32 m0, s78, 36704\n"
    "v_mfma_f32_16x16x32_bf16 v[4:7], v[96:99], a[68:71], v[4:7]\n"
    "buffer_load_dwordx4 v122, s[36:39], s76 offen offset:256 nt lds\n"
    "v_mfma_f32_16x16x32_bf16 v[4:7], v[100:103], a[84:87], v[4:7]\n"
    "ds_read_b128 v[92:95], v120 offset:53312\n"
    "v_mfma_f32_16x16x32_bf16 v[8:11], v[96:99], a[72:75], v[8:11]\n"
    "s_add_u32 m0, s78, 50304\n"
    "v_mfma_f32_16x16x32_bf16 v[8:11], v[100:103], a[88:91], v[8:11]\n"
    "buffer_load_dwordx4 v122, s[36:39], s73 offen offset:384 nt lds\n"
    "v_mfma_f32_16x16x32_bf16 v[12:15], v[96:99], a[76:79], v[12:15]\n"
    "s_add_u32 s73, s73, 32768\n"
    "v_mfma_f32_16x16x32_bf16 v[12:15], v[100:103], a[92:95], v[12:15]\n"
    "s_waitcnt lgkmcnt(4)\n"
    "v_mfma_f32_16x16x32_bf16 v[0:3], v[104:107], a[96:99], v[0:3]\n"
    "s_add_u32 m0, s78, 51360\n"
    "ds_read_b128 v[96:99], v120 offset:54912\n"
    "buffer_load_dwordx4 v122, s[36:39], s74 offen offset:384 nt lds\n"
    "v_mfma_f32_16x16x32_bf16 v[0:3], v[108:111], a[112:115], v[0:3]\n"
    "s_add_u32 s74, s74, 32768\n"
    "v_mfma_f32_16x16x32_bf16 v[4:7], v[104:107], a[100:103], v[4:7]\n"
    "s_add_u32 m0, s78, 52416\n"
    "v_mfma_f32_16x16x32_bf16 v[4:7], v[108:111], a[116:119], v[4:7]\n"
    "buffer_load_dwordx4 v122, s[36:39], s75 offen offset:384 nt lds\n"
    "ds_read_b128 v[100:103], v120 offset:55424\n"
    "v_mfma_f32_16x16x32_bf16 v[8:11], v[104:107], a[104:107], v[8:11]\n"
    "s_add_u32 s75, s75, 32768\n"
    "v_exp_f32 v0, v0\n"
    "v_mfma_f32_16x16x32_bf16 v[8:11], v[108:111], a[120:123], v[8:11]\n"
    "s_add_u32 m0, s78, 53472\n"
    "v_exp_f32 v1, v1\n"
    "v_mfma_f32_16x16x32_bf16 v[12:15], v[104:107], a[108:111], v[12:15]\n"
    "s_cmp_lt_u32 s79, 4\n"
    "s_cbranch_scc1 Lside%=\n"
    "Lsret%=:\n"
    "buffer_load_dwordx4 v122, s[36:39], s76 offen offset:384 nt lds\n"
    "v_add_f32 v116, v116, v0\n"
    "v_mfma_f32_16x16x32_bf16 v[12:15], v[108:111], a[124:127], v[12:15]\n"
    "s_waitcnt lgkmcnt(4)\n"
    "v_exp_f32 v2, v2\n"
    "v_mfma_f32_16x16x32_bf16 v[16:19], v[80:83], a[0:3], v[64:67]\n"
    "s_add_u32 s76, s76, 32768\n"
    "ds_read_b128 v[104:107], v120 offset:57024\n"
    "v_add_f32 v116, v116, v1\n"
    "v_mfma_f32_16x16x32_bf16 v[16:19], v[84:87], a[16:19], v[16:19]\n"
    "v_exp_f32 v3, v3\n"
    "v_mfma_f32_16x16x32_bf16 v[20:23], v[80:83], a[4:7], v[68:71]\n"
    "v_exp_f32 v4, v4\n"
    "v_mfma_f32_16x16x32_bf16 v[20:23], v[84:87], a[20:23], v[20:23]\n"
    "ds_read_b128 v[108:111], v120 offset:57536\n"
    "v_add_f32 v116, v116, v2\n"
    "v_mfma_f32_16x16x32_bf16 v[24:27], v[80:83], a[8:11], v[72:75]\n"
    "v_exp_f32 v5, v5\n"
    "v_mfma_f32_16x16x32_bf16 v[24:27], v[84:87], a[24:27], v[24:27]\n"
    "v_exp_f32 v6, v6\n"
    "v_mfma_f32_16x16x32_bf16 v[28:31], v[80:83], a[12:15], v[76:79]\n"
    "v_exp_f32 v7, v7\n"
    "v_mfma_f32_16x16x32_bf16 v[28:31], v[84:87], a[28:31], v[28:31]\n"
    "s_waitcnt lgkmcnt(4)\n"
    "v_add_f32 v116, v116, v3\n"
    "v_cvt_pk_bf16_f32 v48, v0, v1\n"
    "v_mfma_f32_16x16x32_bf16 v[16:19], v[88:91], a[32:35], v[16:19]\n"
    "ds_read_b128 v[80:83], v120 offset:0\n"
    "v_cvt_pk_bf16_f32 v49, v2, v3\n"
    "v_mfma_f32_16x16x32_bf16 v[16:19], v[92:95], a[48:51], v[16:19]\n"
    "v_exp_f32 v8, v8\n"
    "v_mfma_f32_16x16x32_bf16 v[20:23], v[88:91], a[36:39], v[20:23]\n"
    "v_exp_f32 v9, v9\n"
    "v_mfma_f32_16x16x32_bf16 v[20:23], v[92:95], a[52:55], v[20:23]\n"
    "ds_read_b128 v[84:87], v120 offset:512\n"
    "v_add_f32 v117, v117, v4\n"
    "v_mfma_f32_16x16x32_bf16 v[24:27], v[88:91], a[40:43], v[24:27]\n"
    "v_exp_f32 v10, v10\n"
    "v_mfma_f32_16x16x32_bf16 v[24:27], v[92:95], a[56:59], v[24:27]\n"
    "v_exp_f32 v11, v11\n"
    "v_mfma_f32_16x16x32_bf16 v[28:31], v[88:91], a[44:47], v[28:31]\n"
    "v_add_f32 v117, v117, v5\n"
    "v_add_f32 v117, v117, v6\n"
    "v_mfma_f32_16x16x32_bf16 v[28:31], v[92:95], a[60:63], v[28:31]\n"
    "s_waitcnt lgkmcnt(4)\n"
    "v_add_f32 v117, v117, v7\n"
    "v_cvt_pk_bf16_f32 v52, v4, v5\n"
    "v_mfma_f32_16x16x32_bf16 v[16:19], v[96:99], a[64:67], v[16:19]\n"
    "ds_read_b128 v[88:91], v120 offset:2112\n"
    "v_cvt_pk_bf16_f32 v53, v6, v7\n"
    "v_mfma_f32_16x16x32_bf16 v[16:19], v[100:103], a[80:83], v[16:19]\n"
    "v_exp_f32 v12, v12\n"
    "v_mfma_f32_16x16x32_bf16 v[20:23], v[96:99], a[68:71], v[20:23]\n"
    "v_exp_f32 v13, v13\n"
    "v_mfma_f32_16x16x32_bf16 v[20:23], v[100:103], a[84:87], v[20:23]\n"
    "ds_read_b128 v[92:95], v120 offset:2624\n"
    "v_add_f32 v118, v118, v8\n"
    "v_mfma_f32_16x16x32_bf16 v[24:27], v[96:99], a[72:75], v[24:27]\n"
    "v_exp_f32 v14, v14\n"
    "v_mfma_f32_16x16x32_bf16 v[24:27], v[100:103], a[88:91], v[24:27]\n"
    "v_exp_f32 v15, v15\n"
    "v_mfma_f32_16x16x32_bf16 v[28:31], v[96:99], a[76:79], v[28:31]\n"
    "v_add_f32 v118, v118, v9\n"
    "v_add_f32 v118, v118, v10\n"
    "v_mfma_f32_16x16x32_bf16 v[28:31], v[100:103], a[92:95], v[28:31]\n"
    "s_waitcnt lgkmcnt(4)\n"
    "v_add_f32 v118, v118, v11\n"
    "v_cvt_pk_bf16_f32 v56, v8, v9\n"
    "v_mfma_f32_16x16x32_bf16 v[16:19], v[104:107], a[96:99], v[16:19]\n"
    "ds_read_b128 v[96:99], v120 offset:4224\n"
    "v_cvt_pk_bf16_f32 v57, v10, v11\n"
    "v_mfma_f32_16x16x32_bf16 v[16:19], v[108:111], a[112:115], v[16:19]\n"
    "v_add_f32 v119, v119, v12\n"
    "v_add_f32 v119, v119, v13\n"
    "v_mfma_f32_16x16x32_bf16 v[20:23], v[104:107], a[100:103], v[20:23]\n"
    "v_add_f32 v119, v119, v14\n"
    "v_add_f32 v119, v119, v15\n"
    "v_mfma_f32_16x16x32_bf16 v[20:23], v[108:111], a[116:119], v[20:23]\n"
    "ds_read_b128 v[100:103], v120 offset:4736\n"
    "v_cvt_pk_bf16_f32 v60, v12, v13\n"
    "v_mfma_f32_16x16x32_bf16 v[24:27], v[104:107], a[104:107], v[24:27]\n"
    "v_exp_f32 v16, v16\n"
    "v_mfma_f32_16x16x32_bf16 v[24:27], v[108:111], a[120:123], v[24:27]\n"
    "v_cvt_pk_bf16_f32 v61, v14, v15\n"
    "v_add_f32 v116, v116, v16\n"
    "v_mfma_f32_16x16x32_bf16 v[28:31], v[104:107], a[108:111], v[28:31]\n"
    "v_exp_f32 v17, v17\n"
    "v_mfma_f32_16x16x32_bf16 v[28:31], v[108:111], a[124:127], v[28:31]\n"
    "s_waitcnt lgkmcnt(4)\n"
    "v_exp_f32 v18, v18\n"
    "v_mfma_f32_16x16x32_bf16 v[0:3], v[80:83], a[0:3], v[64:67]\n"
    "ds_read_b128 v[104:107], v120 offset:6336\n"
    "v_add_f32 v116, v116, v17\n"
    "v_mfma_f32_16x16x32_bf16 v[0:3], v[84:87], a[16:19], v[0:3]\n"
    "v_exp_f32 v19, v19\n"
    "v_mfma_f32_16x16x32_bf16 v[4:7], v[80:83], a[4:7], v[68:71]\n"
    "v_exp_f32 v20, v20\n"
    "v_mfma_f32_16x16x32_bf16 v[4:7], v[84:87], a[20:23], v[4:7]\n"
    "ds_read_b128 v[108:111], v120 offset:6848\n"
    "v_add_f32 v116, v116, v18\n"
    "v_mfma_f32_16x16x32_bf16 v[8:11], v[80:83], a[8:11], v[72:75]\n"
    "v_exp_f32 v21, v21\n"
    "v_mfma_f32_16x16x32_bf16 v[8:11], v[84:87], a[24:27], v[8:11]\n"
    "v_exp_f32 v22, v22\n"
    "v_mfma_f32_16x16x32_bf16 v[12:15], v[80:83], a[12:15], v[76:79]\n"
    "v_exp_f32 v23, v23\n"
    "v_mfma_f32_16x16x32_bf16 v[12:15], v[84:87], a[28:31], v[12:15]\n"
    "s_waitcnt lgkmcnt(4)\n"
    "v_add_f32 v116, v116, v19\n"
    "v_cvt_pk_bf16_f32 v50, v16, v17\n"
    "v_mfma_f32_16x16x32_bf16 v[0:3], v[88:91], a[32:35], v[0:3]\n"
    "s_waitcnt vmcnt(12)\n"
    "v_cvt_pk_bf16_f32 v51, v18, v19\n"
    "v_add_f32 v117, v117, v20\n"
    "v_mfma_f32_16x16x32_bf16 v[0:3], v[92:95], a[48:51], v[0:3]\n"
    "s_barrier\n"
    "ds_read_b128 v[80:83], v120 offset:16896\n"
    "v_add_f32 v117, v117, v21\n"
    "v_mfma_f32_16x16x32_bf16 v[4:7], v[88:91], a[36:39], v[4:7]\n"
    "v_exp_f32 v24, v24\n"
    "v_mfma_f32_16x16x32_bf16 v[4:7], v[92:95], a[52:55], v[4:7]\n"
    "v_exp_f32 v25, v25\n"
    "v_mfma_f32_16x16x32_bf16 v[8:11], v[88:91], a[40:43], v[8:11]\n"
    "ds_read_b128 v[84:87], v120 offset:17408\n"
    "v_add_f32 v117, v117, v22\n"
    "v_mfma_f32_16x16x32_bf16 v[8:11], v[92:95], a[56:59], v[8:11]\n"
    "v_exp_f32 v26, v26\n"
    "v_mfma_f32_16x16x32_bf16 v[12:15], v[88:91], a[44:47], v[12:15]\n"
    "v_exp_f32 v27, v27\n"
    "v_mfma_f32_16x16x32_bf16 v[12:15], v[92:95], a[60:63], v[12:15]\n"
    "s_waitcnt lgkmcnt(4)\n"
    "v_add_f32 v117, v117, v23\n"
    "v_cvt_pk_bf16_f32 v54, v20, v21\n"
    "v_mfma_f32_16x16x32_bf16 v[0:3], v[96:99], a[64:67], v[0:3]\n"
    "ds_read_b128 v[88:91], v120 offset:19008\n"
    "v_cvt_pk_bf16_f32 v55, v22, v23\n"
    "v_mfma_f32_16x16x32_bf16 v[0:3], v[100:103], a[80:83], v[0:3]\n"
    "v_exp_f32 v28, v28\n"
    "v_mfma_f32_16x16x32_bf16 v[4:7], v[96:99], a[68:71], v[4:7]\n"
    "v_exp_f32 v29, v29\n"
    "v_mfma_f32_16x16x32_bf16 v[4:7], v[100:103], a[84:87], v[4:7]\n"
    "ds_read_b128 v[92:95], v120 offset:19520\n"
    "v_add_f32 v118, v118, v24\n"
    "v_mfma_f32_16x16x32_bf16 v[8:11], v[96:99], a[72:75], v[8:11]\n"
    "v_exp_f32 v30, v30\n"
    "v_mfma_f32_16x16x32_bf16 v[8:11], v[100:103], a[88:91], v[8:11]\n"
    "v_exp_f32 v31, v31\n"
    "v_mfma_f32_16x16x32_bf16 v[12:15], v[96:99], a[76:79], v[12:15]\n"
    "v_add_f32 v118, v118, v25\n"
    "v_add_f32 v118, v118, v26\n"
    "v_mfma_f32_16x16x32_bf16 v[12:15], v[100:103], a[92:95], v[12:15]\n"
    "s_waitcnt lgkmcnt(4)\n"
    "v_add_f32 v118, v118, v27\n"
    "v_cvt_pk_bf16_f32 v58, v24, v25\n"
    "v_mfma_f32_16x16x32_bf16 v[0:3], v[104:107], a[96:99], v[0:3]\n"
    "ds_read_b128 v[96:99], v120 offset:21120\n"
    "v_cvt_pk_bf16_f32 v59, v26, v27\n"
    "v_mfma_f32_16x16x32_bf16 v[0:3], v[108:111], a[112:115], v[0:3]\n"
    "v_add_f32 v119, v119, v28\n"
    "v_add_f32 v119, v119, v29\n"
    "v_mfma_f32_16x16x32_bf16 v[4:7], v[104:107], a[100:103], v[4:7]\n"
    "v_add_f32 v119, v119, v30\n"
    "v_add_f32 v119, v119, v31\n"
    "v_mfma_f32_16x16x32_bf16 v[4:7], v[108:111], a[116:119], v[4:7]\n"
    "ds_read_b128 v[100:103], v120 offset:21632\n"
    "v_cvt_pk_bf16_f32 v62, v28, v29\n"
    "v_mfma_f32_16x16x32_bf16 v[8:11], v[104:107], a[104:107], v[8:11]\n"
    "v_cvt_pk_bf16_f32 v63, v30, v31\n"
    "v_mfma_f32_16x16x32_bf16 v[8:11], v[108:111], a[120:123], v[8:11]\n"
    "v_mfma_f32_16x16x32_bf16 v[12:15], v[104:107], a[108:111], v[12:15]\n"
    "v_mfma_f32_16x16x32_bf16 v[12:15], v[108:111], a[124:127], v[12:15]\n"
    "s_waitcnt lgkmcnt(4)\n"
    "v_mfma_f32_16x16x32_bf16 v[16:19], v[80:83], a[0:3], v[64:67]\n"
    "ds_read_b128 v[104:107], v120 offset:23232\n"
    "v_max3_f32 v124, v116, v117, v118\n"
    "v_mfma_f32_16x16x32_bf16 v[16:19], v[84:87], a[16:19], v[16:19]\n"
    "v_max_f32 v124, v124, v119\n"
    "v_cmp_nge_f32 vcc, s80, v124\n"
    "v_mfma_f32_16x16x32_bf16 v[20:23], v[80:83], a[4:7], v[68:71]\n"
    "s_add_u32 m0, s77, 33792\n"
    "v_mfma_f32_16x16x32_bf16 v[20:23], v[84:87], a[20:23], v[20:23]\n"
    "ds_read_b128 v[108:111], v120 offset:23744\n"
    "buffer_load_dwordx4 v123, s[24:27], s72 offen offset:0 nt lds\n"
    "v_mfma_f32_16x16x32_bf16 v[24:27], v[80:83], a[8:11], v[72:75]\n"
    "v_mfma_f32_16x16x32_bf16 v[24:27], v[84:87], a[24:27], v[24:27]\n"
    "v_mfma_f32_16x16x32_bf16 v[28:31], v[80:83], a[12:15], v[76:79]\n"
    "s_add_u32 m0, s77, 35776\n"
    "v_mfma_f32_16x16x32_bf16 v[28:31], v[84:87], a[28:31], v[28:31]\n"
    "s_waitcnt lgkmcnt(4)\n"
    "buffer_load_dwordx4 v123, s[24:27], s72 offen offset:128 nt lds\n"
    "v_mfma_f32_16x16x32_bf16 v[16:19], v[88:91], a[32:35], v[16:19]\n"
    "s_cbranch_vccnz Lslow%=\n"
    "LbackM%=:\n"
    "ds_read_b64_tr_b16 v[80:81], v121 offset:0\n"
    "v_add_f32 v112, v112, v116\n"
    "v_mfma_f32_16x16x32_bf16 v[16:19], v[92:95], a[48:51], v[16:19]\n"
    "ds_read_b64_tr_b16 v[82:83], v121 offset:256\n"
    "v_add_f32 v113, v113, v117\n"
    "v_mfma_f32_16x16x32_bf16 v[20:23], v[88:91], a[36:39], v[20:23]\n"
    "s_add_u32 m0, s77, 37760\n"
    "v_add_f32 v114, v114, v118\n"
    "buffer_load_dwordx4 v123, s[24:27], s72 offen offset:256 nt lds\n"
    "v_mfma_f32_16x16x32_bf16 v[20:23], v[92:95], a[52:55], v[20:23]\n"
    "s_add_u32 s28, s28, 32768\n"
    "ds_read_b64_tr_b16 v[84:85], v121 offset:4224\n"
    "v_add_f32 v115, v115, v119\n"
    "v_mfma_f32_16x16x32_bf16 v[24:27], v[88:91], a[40:43], v[24:27]\n"
    "v_exp_f32 v0, v0\n"
    "v_mfma_f32_16x16x32_bf16 v[24:27], v[92:95], a[56:59], v[24:27]\n"
    "v_exp_f32 v1, v1\n"
    "v_mfma_f32_16x16x32_bf16 v[28:31], v[88:91], a[44:47], v[28:31]\n"
    "s_add_u32 m0, s77, 39744\n"
    "ds_read_b64_tr_b16 v[86:87], v121 offset:4480\n"
    "buffer_load_dwordx4 v123, s[24:27], s72 offen offset:384 nt lds\n"
    "v_mfma_f32_16x16x32_bf16 v[28:31], v[92:95], a[60:63], v[28:31]\n"
    "s_waitcnt lgkmcnt(6)\n"
    "v_exp_f32 v2, v2\n"
    "v_mfma_f32_16x16x32_bf16 v[16:19], v[96:99], a[64:67], v[16:19]\n"
    "s_add_u32 s72, s72, 2048\n"
    "ds_read_b64_tr_b16 v[88:89], v121 offset:32\n"
    "v_add_f32 v116, v0, v1\n"
    "v_mfma_f32_16x16x32_bf16 v[16:19], v[100:103], a[80:83], v[16:19]\n"
    "ds_read_b64_tr_b16 v[90:91], v121 offset:288\n"
    "v_add_f32 v116, v116, v2\n"
    "v_mfma_f32_16x16x32_bf16 v[20:23], v[96:99], a[68:71], v[20:23]\n"
    "v_exp_f32 v3, v3\n"
    "v_mfma_f32_16x16x32_bf16 v[20:23], v[100:103], a[84:87], v[20:23]\n"
    "s_add_u32 m0, s77, 50688\n"
    "ds_read_b64_tr_b16 v[92:93], v121 offset:4256\n"
    "buffer_load_dwordx4 v123, s[24:27], s72 offen offset:0 nt lds\n"
    "v_mfma_f32_16x16x32_bf16 v[24:27], v[96:99], a[72:75], v[24:27]\n"
    "ds_read_b64_tr_b16 v[94:95], v121 offset:4512\n"
    "v_add_f32 v116, v116, v3\n"
    "v_mfma_f32_16x16x32_bf16 v[24:27], v[100:103], a[88:91], v[24:27]\n"
    "v_mfma_f32_16x16x32_bf16 v[28:31], v[96:99], a[76:79], v[28:31]\n"
    "v_mfma_f32_16x16x32_bf16 v[28:31], v[100:103], a[92:95], v[28:31]\n"
    "s_waitcnt lgkmcnt(8)\n"
    "v_mfma_f32_16x16x32_bf16 v[16:19], v[104:107], a[96:99], v[16:19]\n"
    "s_add_u32 m0, s77, 52672\n"
    "ds_read_b64_tr_b16 v[96:97], v121 offset:512\n"
    "buffer_load_dwordx4 v123, s[24:27], s72 offen offset:128 nt lds\n"
    "v_mfma_f32_16x16x32_bf16 v[16:19], v[108:111], a[112:115], v[16:19]\n"
    "ds_read_b64_tr_b16 v[98:99], v121 offset:768\n"
    "v_mfma_f32_16x16x32_bf16 v[20:23], v[104:107], a[100:103], v[20:23]\n"
    "v_exp_f32 v4, v4\n"
    "v_mfma_f32_16x16x32_bf16 v[20:23], v[108:111], a[116:119], v[20:23]\n"
    "v_exp_f32 v5, v5\n"
    "v_mfma_f32_16x16x32_bf16 v[24:27], v[104:107], a[104:107], v[24:27]\n"
    "ds_read_b64_tr_b16 v[100:101], v121 offset:4736\n"
    "ds_read_b64_tr_b16 v[102:103], v121 offset:4992\n"
    "v_mfma_f32_16x16x32_bf16 v[24:27], v[108:111], a[120:123], v[24:27]\n"
    "s_add_u32 m0, s77, 54656\n"
    "v_exp_f32 v6, v6\n"
    "v_mfma_f32_16x16x32_bf16 v[28:31], v[104:107], a[108:111], v[28:31]\n"
    "buffer_load_dwordx4 v123, s[24:27], s72 offen offset:256 nt lds\n"
    "v_add_f32 v117, v4, v5\n"
    "v_mfma_f32_16x16x32_bf16 v[28:31], v[108:111], a[124:127], v[28:31]\n"
    "s_waitcnt lgkmcnt(8)\n"
    "v_exp_f32 v7, v7\n"
    "v_mfma_f32_16x16x32_bf16 v[128:131], v[80:83], v[32:35], v[128:131]\n"
    "ds_read_b64_tr_b16 v[104:105], v121 offset:544\n"
    "v_add_f32 v117, v117, v6\n"
    "v_mfma_f32_16x16x32_bf16 v[128:131], v[84:87], v[48:51], v[128:131]\n"
    "ds_read_b64_tr_b16 v[106:107], v121 offset:800\n"
    "v_add_f32 v117, v117, v7\n"
    "v_mfma_f32_16x16x32_bf16 v[132:135], v[80:83], v[36:39], v[132:135]\n"
    "s_add_u32 m0, s77, 56640\n"
    "v_mfma_f32_16x16x32_bf16 v[132:135], v[84:87], v[52:55], v[132:135]\n"
    "ds_read_b64_tr_b16 v[108:109], v121 offset:4768\n"
    "buffer_load_dwordx4 v123, s[24:27], s72 offen offset:384 nt lds\n"
    "v_mfma_f32_16x16x32_bf16 a[128:131], v[80:83], v[40:43], a[128:131]\n"
    "s_add_u32 s72, s72, 14336\n"
    "ds_read_b64_tr_b16 v[110:111], v121 offset:5024\n"
    "v_mfma_f32_16x16x32_bf16 a[128:131], v[84:87], v[56:59], a[128:131]\n"
    "v_mfma_f32_16x16x32_bf16 a[132:135], v[80:83], v[44:47], a[132:135]\n"
    "v_mfma_f32_16x16x32_bf16 a[132:135], v[84:87], v[60:63], a[132:135]\n"
    "s_waitcnt lgkmcnt(8)\n"
    "v_mfma_f32_16x16x32_bf16 v[136:139], v[88:91], v[32:35], v[136:139]\n"
    "s_waitcnt vmcnt(12)\n"
    "v_exp_f32 v8, v8\n"
    "v_mfma_f32_16x16x32_bf16 v[136:139], v[92:95], v[48:51], v[136:139]\n"
    "s_barrier\n"
    "v_exp_f32 v9, v9\n"
    "v_mfma_f32_16x16x32_bf16 v[140:143], v[88:91], v[36:39], v[140:143]\n"
    "ds_read_b64_tr_b16 v[80:81], v121 offset:16896\n"
    "ds_read_b64_tr_b16 v[82:83], v121 offset:17152\n"
    "v_mfma_f32_16x16x32_bf16 v[140:143], v[92:95], v[52:55], v[140:143]\n"
    "s_add_u32 m0, s77, 0\n"
    "v_exp_f32 v10, v10\n"
    "v_mfma_f32_16x16x32_bf16 a[136:139], v[88:91], v[40:43], a[136:139]\n"
    "ds_read_b64_tr_b16 v[84:85], v121 offset:21120\n"
    "buffer_load_dwordx4 v123, s[24:27], s72 offen offset:0 nt lds\n"
    "v_mfma_f32_16x16x32_bf16 a[136:139], v[92:95], v[56:59], a[136:139]\n"
    "ds_read_b64_tr_b16 v[86:87], v121 offset:21376\n"
    "v_add_f32 v118, v8, v9\n"
    "v_mfma_f32_16x16x32_bf16 a[140:143], v[88:91], v[44:47], a[140:143]\n"
    "v_exp_f32 v11, v11\n"
    "v_mfma_f32_16x16x32_bf16 a[140:143], v[92:95], v[60:63], a[140:143]\n"
    "s_waitcnt lgkmcnt(8)\n"
    "v_add_f32 v118, v118, v10\n"
    "v_add_f32 v118, v118, v11\n"
    "v_mfma_f32_16x16x32_bf16 v[144:147], v[96:99], v[32:35], v[144:147]\n"
    "ds_read_b64_tr_b16 v[88:89], v121 offset:16928\n"
    "v_mfma_f32_16x16x32_bf16 v[144:147], v[100:103], v[48:51], v[144:147]\n"
    "ds_read_b64_tr_b16 v[90:91], v121 offset:17184\n"
    "v_mfma_f32_16x16x32_bf16 v[148:151], v[96:99], v[36:39], v[148:151]\n"
    "s_add_u32 m0, s77, 1984\n"
    "v_mfma_f32_16x16x32_bf16 v[148:151], v[100:103], v[52:55], v[148:151]\n"
    "ds_read_b64_tr_b16 v[92:93], v121 offset:21152\n"
    "buffer_load_dwordx4 v123, s[24:27], s72 offen offset:128 nt lds\n"
    "v_mfma_f32_16x16x32_bf16 a[144:147], v[96:99], v[40:43], a[144:147]\n"
    "ds_read_b64_tr_b16 v[94:95], v121 offset:21408\n"
    "v_mfma_f32_16x16x32_bf16 a[144:147], v[100:103], v[56:59], a[144:147]\n"
    "v_mfma_f32_16x16x32_bf16 a[148:151], v[96:99], v[44:47], a[148:151]\n"
    "v_exp_f32 v12, v12\n"
    "v_mfma_f32_16x16x32_bf16 a[148:151], v[100:103], v[60:63], a[148:151]\n"
    "s_waitcnt lgkmcnt(8)\n"
    "v_exp_f32 v13, v13\n"
    "v_mfma_f32_16x16x32_bf16 v[152:155], v[104:107], v[32:35], v[152:155]\n"
    "ds_read_b64_tr_b16 v[96:97], v121 offset:17408\n"
    "v_add_f32 v119, v12, v13\n"
    "v_mfma_f32_16x16x32_bf16 v[152:155], v[108:111], v[48:51], v[152:155]\n"
    "s_add_u32 m0, s77, 3968\n"
    "v_exp_f32 v14, v14\n"
    "v_mfma_f32_16x16x32_bf16 v[156:159], v[104:107], v[36:39], v[156:159]\n"
    "buffer_load_dwordx4 v123, s[24:27], s72 offen offset:256 nt lds\n"
    "ds_read_b64_tr_b16 v[98:99], v121 offset:17664\n"
    "v_mfma_f32_16x16x32_bf16 v[156:159], v[108:111], v[52:55], v[156:159]\n"
    "ds_read_b64_tr_b16 v[100:101], v121 offset:21632\n"
    "v_add_f32 v119, v119, v14\n"
    "v_mfma_f32_16x16x32_bf16 a[152:155], v[104:107], v[40:43], a[152:155]\n"
    "v_exp_f32 v15, v15\n"
    "v_mfma_f32_16x16x32_bf16 a[152:155], v[108:111], v[56:59], a[152:155]\n"
    "ds_read_b64_tr_b16 v[102:103], v121 offset:21888\n"
    "v_add_f32 v119, v119, v15\n"
    "v_mfma_f32_16x16x32_bf16 a[156:159], v[104:107], v[44:47], a[156:159]\n"
    "v_mfma_f32_16x16x32_bf16 a[156:159], v[108:111], v[60:63], a[156:159]\n"
    "s_waitcnt lgkmcnt(8)\n"
    "v_mfma_f32_16x16x32_bf16 v[160:163], v[80:83], v[32:35], v[160:163]\n"
    "s_add_u32 m0, s77, 5952\n"
    "ds_read_b64_tr_b16 v[104:105], v121 offset:17440\n"
    "buffer_load_dwordx4 v123, s[24:27], s72 offen offset:384 nt lds\n"
    "v_mfma_f32_16x16x32_bf16 v[160:163], v[84:87], v[48:51], v[160:163]\n"
    "s_add_u32 s72, s72, 2048\n"
    "ds_read_b64_tr_b16 v[106:107], v121 offset:17696\n"
    "v_mfma_f32_16x16x32_bf16 v[164:167], v[80:83], v[36:39], v[164:167]\n"
    "v_mfma_f32_16x16x32_bf16 v[164:167], v[84:87], v[52:55], v[164:167]\n"
    "ds_read_b64_tr_b16 v[108:109], v121 offset:21664\n"
    "v_mfma_f32_16x16x32_bf16 a[160:163], v[80:83], v[40:43], a[160:163]\n"
    "ds_read_b64_tr_b16 v[110:111], v121 offset:21920\n"
    "v_mfma_f32_16x16x32_bf16 a[160:163], v[84:87], v[56:59], a[160:163]\n"
    "v_mfma_f32_16x16x32_bf16 a[164:167], v[80:83], v[44:47], a[164:167]\n"
    "v_mfma_f32_16x16x32_bf16 a[164:167], v[84:87], v[60:63], a[164:167]\n"
    "s_waitcnt lgkmcnt(8)\n"
    "v_mfma_f32_16x16x32_bf16 v[168:171], v[88:91], v[32:35], v[168:171]\n"
    "s_add_u32 m0, s77, 16896\n"
    "ds_read_b64_tr_b16 v[80:81], v121 offset:33792\n"
    "buffer_load_dwordx4 v123, s[24:27], s72 offen offset:0 nt lds\n"
    "v_mfma_f32_16x16x32_bf16 v[168:171], v[92:95], v[48:51], v[168:171]\n"
    "ds_read_b64_tr_b16 v[82:83], v121 offset:34048\n"
    "v_mfma_f32_16x16x32_bf16 v[172:175], v[88:91], v[36:39], v[172:175]\n"
    "v_mfma_f32_16x16x32_bf16 v[172:175], v[92:95], v[52:55], v[172:175]\n"
    "ds_read_b64_tr_b16 v[84:85], v121 offset:38016\n"
    "v_mfma_f32_16x16x32_bf16 a[168:171], v[88:91], v[40:43], a[168:171]\n"
    "ds_read_b64_tr_b16 v[86:87], v121 offset:38272\n"
    "v_mfma_f32_16x16x32_bf16 a[168:171], v[92:95], v[56:59], a[168:171]\n"
    "v_mfma_f32_16x16x32_bf16 a[172:175], v[88:91], v[44:47], a[172:175]\n"
    "s_add_u32 m0, s77, 18880\n"
    "v_mfma_f32_16x16x32_bf16 a[172:175], v[92:95], v[60:63], a[172:175]\n"
    "s_waitcnt lgkmcnt(8)\n"
    "buffer_load_dwordx4 v123, s[24:27], s72 offen offset:128 nt lds\n"
    "v_mfma_f32_16x16x32_bf16 v[176:179], v[96:99], v[32:35], v[176:179]\n"
    "ds_read_b64_tr_b16 v[88:89], v121 offset:33824\n"
    "v_mfma_f32_16x16x32_bf16 v[176:179], v[100:103], v[48:51], v[176:179]\n"
    "ds_read_b64_tr_b16 v[90:91], v121 offset:34080\n"
    "v_mfma_f32_16x16x32_bf16 v[180:183], v[96:99], v[36:39], v[180:183]\n"
    "v_exp_f32 v16, v16\n"
    "v_mfma_f32_16x16x32_bf16 v[180:183], v[100:103], v[52:55], v[180:183]\n"
    "ds_read_b64_tr_b16 v[92:93], v121 offset:38048\n"
    "v_add_f32 v116, v116, v16\n"
    "v_mfma_f32_16x16x32_bf16 a[176:179], v[96:99], v[40:43], a[176:179]\n"
    "v_exp_f32 v17, v17\n"
    "v_mfma_f32_16x16x32_bf16 a[176:179], v[100:103], v[56:59], a[176:179]\n"
    "s_add_u32 m0, s77, 20864\n"
    "ds_read_b64_tr_b16 v[94:95], v121 offset:38304\n"
    "buffer_load_dwordx4 v123, s[24:27], s72 offen offset:256 nt lds\n"
    "v_mfma_f32_16x16x32_bf16 a[180:183], v[96:99], v[44:47], a[180:183]\n"
    "v_exp_f32 v18, v18\n"
    "v_mfma_f32_16x16x32_bf16 a[180:183], v[100:103], v[60:63], a[180:183]\n"
    "s_waitcnt lgkmcnt(8)\n"
    "v_exp_f32 v19, v19\n"
    "v_mfma_f32_16x16x32_bf16 v[184:187], v[104:107], v[32:35], v[184:187]\n"
    "ds_read_b64_tr_b16 v[96:97], v121 offset:34304\n"
    "v_add_f32 v116, v116, v17\n"
    "v_mfma_f32_16x16x32_bf16 v[184:187], v[108:111], v[48:51], v[184:187]\n"
    "ds_read_b64_tr_b16 v[98:99], v121 offset:34560\n"
    "v_add_f32 v116, v116, v18\n"
    "v_mfma_f32_16x16x32_bf16 v[188:191], v[104:107], v[36:39], v[188:191]\n"
    "v_add_f32 v116, v116, v19\n"
    "v_mfma_f32_16x16x32_bf16 v[188:191], v[108:111], v[52:55], v[188:191]\n"
    "ds_read_b64_tr_b16 v[100:101], v121 offset:38528\n"
    "v_mfma_f32_16x16x32_bf16 a[184:187], v[104:107], v[40:43], a[184:187]\n"
    "s_add_u32 m0, s77, 22848\n"
    "ds_read_b64_tr_b16 v[102:103], v121 offset:38784\n"
    "buffer_load_dwordx4 v123, s[24:27], s72 offen offset:384 nt lds\n"
    "v_mfma_f32_16x16x32_bf16 a[184:187], v[108:111], v[56:59], a[184:187]\n"
    "s_add_u32 s72, s72, 14336\n"
    "v_mfma_f32_16x16x32_bf16 a[188:191], v[104:107], v[44:47], a[188:191]\n"
    "v_mfma_f32_16x16x32_bf16 a[188:191], v[108:111], v[60:63], a[188:191]\n"
    "s_waitcnt lgkmcnt(8)\n"
    "v_mfma_f32_16x16x32_bf16 v[192:195], v[80:83], v[32:35], v[192:195]\n"
    "ds_read_b64_tr_b16 v[104:105], v121 offset:34336\n"
    "v_mfma_f32_16x16x32_bf16 v[192:195], v[84:87], v[48:51], v[192:195]\n"
    "v_exp_f32 v20, v20\n"
    "v_mfma_f32_16x16x32_bf16 v[196:199], v[80:83], v[36:39], v[196:199]\n"
    "ds_read_b64_tr_b16 v[106:107], v121 offset:34592\n"
    "v_add_f32 v117, v117, v20\n"
    "v_mfma_f32_16x16x32_bf16 v[196:199], v[84:87], v[52:55], v[196:199]\n"
    "v_exp_f32 v21, v21\n"
    "v_mfma_f32_16x16x32_bf16 a[192:195], v[80:83], v[40:43], a[192:195]\n"
    "ds_read_b64_tr_b16 v[108:109], v121 offset:38560\n"
    "ds_read_b64_tr_b16 v[110:111], v121 offset:38816\n"
    "v_mfma_f32_16x16x32_bf16 a[192:195], v[84:87], v[56:59], a[192:195]\n"
    "v_exp_f32 v22, v22\n"
    "v_mfma_f32_16x16x32_bf16 a[196:199], v[80:83], v[44:47], a[196:199]\n"
    "v_exp_f32 v23, v23\n"
    "v_mfma_f32_16x16x32_bf16 a[196:199], v[84:87], v[60:63], a[196:199]\n"
    "s_waitcnt lgkmcnt(8)\n"
    "v_add_f32 v117, v117, v21\n"
    "v_add_f32 v117, v117, v22\n"
    "v_mfma_f32_16x16x32_bf16 v[200:203], v[88:91], v[32:35], v[200:203]\n"
    "s_waitcnt vmcnt(12)\n"
    "v_add_f32 v117, v117, v23\n"
    "v_mfma_f32_16x16x32_bf16 v[200:203], v[92:95], v[48:51], v[200:203]\n"
    "s_barrier\n"
    "ds_read_b64_tr_b16 v[80:81], v121 offset:50688\n"
    "v_mfma_f32_16x16x32_bf16 v[204:207], v[88:91], v[36:39], v[204:207]\n"
    "ds_read_b64_tr_b16 v[82:83], v121 offset:50944\n"
    "v_mfma_f32_16x16x32_bf16 v[204:207], v[92:95], v[52:55], v[204:207]\n"
    "s_add_u32 m0, s78, 0\n"
    "v_mfma_f32_16x16x32_bf16 a[200:203], v[88:91], v[40:43], a[200:203]\n"
    "ds_read_b64_tr_b16 v[84:85], v121 offset:54912\n"
    "buffer_load_dwordx4 v122, s[36:39], s73 offen offset:0 nt lds\n"
    "v_mfma_f32_16x16x32_bf16 a[200:203], v[92:95], v[56:59], a[200:203]\n"
    "ds_read_b64_tr_b16 v[86:87], v121 offset:55168\n"
    "v_mfma_f32_16x16x32_bf16 a[204:207], v[88:91], v[44:47], a[204:207]\n"
    "v_mfma_f32_16x16x32_bf16 a[204:207], v[92:95], v[60:63], a[204:207]\n"
    "s_waitcnt lgkmcnt(8)\n"
    "v_mfma_f32_16x16x32_bf16 v[208:211], v[96:99], v[32:35], v[208:211]\n"
    "ds_read_b64_tr_b16 v[88:89], v121 offset:50720\n"
    "v_mfma_f32_16x16x32_bf16 v[208:211], v[100:103], v[48:51], v[208:211]\n"
    "s_add_u32 m0, s78, 1056\n"
    "v_exp_f32 v24, v24\n"
    "v_mfma_f32_16x16x32_bf16 v[212:215], v[96:99], v[36:39], v[212:215]\n"
    "buffer_load_dwordx4 v122, s[36:39], s74 offen offset:0 nt lds\n"
    "ds_read_b64_tr_b16 v[90:91], v121 offset:50976\n"
    "v_mfma_f32_16x16x32_bf16 v[212:215], v[100:103], v[52:55], v[212:215]\n"
    "ds_read_b64_tr_b16 v[92:93], v121 offset:54944\n"
    "v_add_f32 v118, v118, v24\n"
    "v_mfma_f32_16x16x32_bf16 a[208:211], v[96:99], v[40:43], a[208:211]\n"
    "v_exp_f32 v25, v25\n"
    "v_mfma_f32_16x16x32_bf16 a[208:211], v[100:103], v[56:59], a[208:211]\n"
    "ds_read_b64_tr_b16 v[94:95], v121 offset:55200\n"
    "v_add_f32 v118, v118, v25\n"
    "v_mfma_f32_16x16x32_bf16 a[212:215], v[96:99], v[44:47], a[212:215]\n"
    "v_exp_f32 v26, v26\n"
    "v_mfma_f32_16x16x32_bf16 a[212:215], v[100:103], v[60:63], a[212:215]\n"
    "s_waitcnt lgkmcnt(8)\n"
    "v_exp_f32 v27, v27\n"
    "v_mfma_f32_16x16x32_bf16 v[216:219], v[104:107], v[32:35], v[216:219]\n"
    "s_add_u32 m0, s78, 2112\n"
    "ds_read_b64_tr_b16 v[96:97], v121 offset:51200\n"
    "buffer_load_dwordx4 v122, s[36:39], s75 offen offset:0 nt lds\n"
    "v_mfma_f32_16x16x32_bf16 v[216:219], v[108:111], v[48:51], v[216:219]\n"
    "ds_read_b64_tr_b16 v[98:99], v121 offset:51456\n"
    "v_add_f32 v118, v118, v26\n"
    "v_mfma_f32_16x16x32_bf16 v[220:223], v[104:107], v[36:39], v[220:223]\n"
    "v_add_f32 v118, v118, v27\n"
    "v_mfma_f32_16x16x32_bf16 v[220:223], v[108:111], v[52:55], v[220:223]\n"
    "ds_read_b64_tr_b16 v[100:101], v121 offset:55424\n"
    "v_mfma_f32_16x16x32_bf16 a[216:219], v[104:107], v[40:43], a[216:219]\n"
    "ds_read_b64_tr_b16 v[102:103], v121 offset:55680\n"
    "v_mfma_f32_16x16x32_bf16 a[216:219], v[108:111], v[56:59], a[216:219]\n"
    "s_add_u32 m0, s78, 3168\n"
    "v_mfma_f32_16x16x32_bf16 a[220:223], v[104:107], v[44:47], a[220:223]\n"
    "buffer_load_dwordx4 v122, s[36:39], s76 offen offset:0 nt lds\n"
    "v_mfma_f32_16x16x32_bf16 a[220:223], v[108:111], v[60:63], a[220:223]\n"
    "s_waitcnt lgkmcnt(8)\n"
    "v_mfma_f32_16x16x32_bf16 v[224:227], v[80:83], v[32:35], v[224:227]\n"
    "v_exp_f32 v28, v28\n"
    "v_mfma_f32_16x16x32_bf16 v[224:227], v[84:87], v[48:51], v[224:227]\n"
    "ds_read_b64_tr_b16 v[104:105], v121 offset:51232\n"
    "ds_read_b64_tr_b16 v[106:107], v121 offset:51488\n"
    "v_mfma_f32_16x16x32_bf16 v[228:231], v[80:83], v[36:39], v[228:231]\n"
    "v_exp_f32 v29, v29\n"
    "v_mfma_f32_16x16x32_bf16 v[228:231], v[84:87], v[52:55], v[228:231]\n"
    "s_add_u32 m0, s78, 16768\n"
    "ds_read_b64_tr_b16 v[108:109], v121 offset:55456\n"
    "buffer_load_dwordx4 v122, s[36:39], s73 offen offset:128 nt lds\n"
    "v_mfma_f32_16x16x32_bf16 a[224:227], v[80:83], v[40:43], a[224:227]\n"
    "ds_read_b64_tr_b16 v[110:111], v121 offset:55712\n"
    "v_add_f32 v119, v119, v28\n"
    "v_mfma_f32_16x16x32_bf16 a[224:227], v[84:87], v[56:59], a[224:227]\n"
    "v_exp_f32 v30, v30\n"
    "v_mfma_f32_16x16x32_bf16 a[228:231], v[80:83], v[44:47], a[228:231]\n"
    "v_exp_f32 v31, v31\n"
    "v_mfma_f32_16x16x32_bf16 a[228:231], v[84:87], v[60:63], a[228:231]\n"
    "s_waitcnt lgkmcnt(8)\n"
    "v_add_f32 v119, v119, v29\n"
    "v_add_f32 v119, v119, v30\n"
    "v_mfma_f32_16x16x32_bf16 v[232:235], v[88:91], v[32:35], v[232:235]\n"
    "ds_read_b128 v[80:83], v120 offset:33792\n"
    "v_add_f32 v119, v119, v31\n"
    "v_mfma_f32_16x16x32_bf16 v[232:235], v[92:95], v[48:51], v[232:235]\n"
    "s_add_u32 m0, s78, 17824\n"
    "v_mfma_f32_16x16x32_bf16 v[236:239], v[88:91], v[36:39], v[236:239]\n"
    "buffer_load_dwordx4 v122, s[36:39], s74 offen offset:128 nt lds\n"
    "v_mfma_f32_16x16x32_bf16 v[236:239], v[92:95], v[52:55], v[236:239]\n"
    "ds_read_b128 v[84:87], v120 offset:34304\n"
    "v_mfma_f32_16x16x32_bf16 a[232:235], v[88:91], v[40:43], a[232:235]\n"
    "v_mfma_f32_16x16x32_bf16 a[232:235], v[92:95], v[56:59], a[232:235]\n"
    "v_mfma_f32_16x16x32_bf16 a[236:239], v[88:91], v[44:47], a[236:239]\n"
    "v_mfma_f32_16x16x32_bf16 a[236:239], v[92:95], v[60:63], a[236:239]\n"
    "s_waitcnt lgkmcnt(6)\n"
    "v_mfma_f32_16x16x32_bf16 v[240:243], v[96:99], v[32:35], v[240:243]\n"
    "s_add_u32 m0, s78, 18880\n"
    "ds_read_b128 v[88:91], v120 offset:35904\n"
    "buffer_load_dwordx4 v122, s[36:39], s75 offen offset:128 nt lds\n"
    "v_mfma_f32_16x16x32_bf16 v[240:243], v[100:103], v[48:51], v[240:243]\n"
    "v_mfma_f32_16x16x32_bf16 v[244:247], v[96:99], v[36:39], v[244:247]\n"
    "v_mfma_f32_16x16x32_bf16 v[244:247], v[100:103], v[52:55], v[244:247]\n"
    "ds_read_b128 v[92:95], v120 offset:36416\n"
    "v_mfma_f32_16x16x32_bf16 a[240:243], v[96:99], v[40:43], a[240:243]\n"
    "s_add_u32 m0, s78, 19936\n"
    "v_mfma_f32_16x16x32_bf16 a[240:243], v[100:103], v[56:59], a[240:243]\n"
    "buffer_load_dwordx4 v122, s[36:39], s76 offen offset:128 nt lds\n"
    "v_mfma_f32_16x16x32_bf16 a[244:247], v[96:99], v[44:47], a[244:247]\n"
    "v_mfma_f32_16x16x32_bf16 a[244:247], v[100:103], v[60:63], a[244:247]\n"
    "s_waitcnt lgkmcnt(4)\n"
    "v_mfma_f32_16x16x32_bf16 v[248:251], v[104:107], v[32:35], v[248:251]\n"
    "v_cvt_pk_bf16_f32 v32, v0, v1\n"
    "v_cvt_pk_bf16_f32 v33, v2, v3\n"
    "v_mfma_f32_16x16x32_bf16 v[248:251], v[108:111], v[48:51], v[248:251]\n"
    "ds_read_b128 v[96:99], v120 offset:38016\n"
    "v_mfma_f32_16x16x32_bf16 v[252:255], v[104:107], v[36:39], v[252:255]\n"
    "s_sub_u32 s79, s79, 1\n"
    "v_cvt_pk_bf16_f32 v36, v4, v5\n"
    "v_cvt_pk_bf16_f32 v37, v6, v7\n"
    "v_mfma_f32_16x16x32_bf16 v[252:255], v[108:111], v[52:55], v[252:255]\n"
    "ds_read_b128 v[100:103], v120 offset:38528\n"
    "v_mfma_f32_16x16x32_bf16 a[248:251], v[104:107], v[40:43], a[248:251]\n"
    "v_cvt_pk_bf16_f32 v40, v8, v9\n"
    "v_cvt_pk_bf16_f32 v41, v10, v11\n"
    "v_mfma_f32_16x16x32_bf16 a[248:251], v[108:111], v[56:59], a[248:251]\n"
    "v_mfma_f32_16x16x32_bf16 a[252:255], v[104:107], v[44:47], a[252:255]\n"
    "v_cvt_pk_bf16_f32 v44, v12, v13\n"
    "v_cvt_pk_bf16_f32 v45, v14, v15\n"
    "v_mfma_f32_16x16x32_bf16 a[252:255], v[108:111], v[60:63], a[252:255]\n"
    "s_waitcnt lgkmcnt(4)\n"
    "s_cmp_lg_u32 s79, 0\n"
    "s_cbranch_scc1 Lloop%=\n"
    "s_add_u32 s90, s90, LbackL%=-LbackM%=\n"
    "s_addc_u32 s91, s91, 0\n"
    "v_mfma_f32_16x16x32_bf16 v[0:3], v[80:83], a[0:3], v[64:67]\n"
    "s_lshl_b32 s86, s81, 2\n"
    "ds_read_b128 v[104:107], v120 offset:40128\n"
    "v_cvt_pk_bf16_f32 v34, v16, v17\n"
    "v_mfma_f32_16x16x32_bf16 v[0:3], v[84:87], a[16:19], v[0:3]\n"
    "v_cvt_pk_bf16_f32 v35, v18, v19\n"
    "v_cvt_pk_bf16_f32 v38, v20, v21\n"
    "v_mfma_f32_16x16x32_bf16 v[4:7], v[80:83], a[4:7], v[68:71]\n"
    "v_cvt_pk_bf16_f32 v39, v22, v23\n"
    "v_cvt_pk_bf16_f32 v42, v24, v25\n"
    "v_mfma_f32_16x16x32_bf16 v[4:7], v[84:87], a[20:23], v[4:7]\n"
    "ds_read_b128 v[108:111], v120 offset:40640\n"
    "v_cvt_pk_bf16_f32 v43, v26, v27\n"
    "v_mfma_f32_16x16x32_bf16 v[8:11], v[80:83], a[8:11], v[72:75]\n"
    "v_cvt_pk_bf16_f32 v46, v28, v29\n"
    "v_cvt_pk_bf16_f32 v47, v30, v31\n"
    "v_mfma_f32_16x16x32_bf16 v[8:11], v[84:87], a[24:27], v[8:11]\n"
    "v_mbcnt_lo_u32_b32 v126, -1, 0\n"
    "v_mbcnt_hi_u32_b32 v126, -1, v126\n"
    "v_mfma_f32_16x16x32_bf16 v[12:15], v[80:83], a[12:15], v[76:79]\n"
    "v_bfe_u32 v125, v126, 4, 1\n"
    "v_lshlrev_b32 v125, 4, v125\n"
    "v_mfma_f32_16x16x32_bf16 v[12:15], v[84:87], a[28:31], v[12:15]\n"
    "s_waitcnt lgkmcnt(4)\n"
    "v_bfe_u32 v127, v126, 2, 2\n"
    "v_add_u32 v125, v125, v127\n"
    "v_mfma_f32_16x16x32_bf16 v[0:3], v[88:91], a[32:35], v[0:3]\n"
    "s_waitcnt vmcnt(16)\n"
    "v_add_u32 v125, s86, v125\n"
    "v_mul_u32_u24 v125, 896, v125\n"
    "v_mfma_f32_16x16x32_bf16 v[0:3], v[92:95], a[48:51], v[0:3]\n"
    "s_barrier\n"
    "ds_read_b128 v[80:83], v120 offset:50688\n"
    "v_lshrrev_b32 v127, 5, v126\n"
    "v_mfma_f32_16x16x32_bf16 v[4:7], v[88:91], a[36:39], v[4:7]\n"
    "s_add_u32 m0, s78, 33536\n"
    "v_lshl_add_u32 v125, v127, 6, v125\n"
    "buffer_load_dwordx4 v122, s[36:39], s73 offen offset:256 nt lds\n"
    "v_mfma_f32_16x16x32_bf16 v[4:7], v[92:95], a[52:55], v[4:7]\n"
    "v_and_b32 v127, 3, v126\n"
    "v_lshl_add_u32 v125, v127, 4, v125\n"
    "v_mfma_f32_16x16x32_bf16 v[8:11], v[88:91], a[40:43], v[8:11]\n"
    "s_add_u32 m0, s78, 34592\n"
    "ds_read_b128 v[84:87], v120 offset:51200\n"
    "buffer_load_dwordx4 v122, s[36:39], s74 offen offset:256 nt lds\n"
    "v_mfma_f32_16x16x32_bf16 v[8:11], v[92:95], a[56:59], v[8:11]\n"
    "v_mfma_f32_16x16x32_bf16 v[12:15], v[88:91], a[44:47], v[12:15]\n"
    "v_mfma_f32_16x16x32_bf16 v[12:15], v[92:95], a[60:63], v[12:15]\n"
    "s_waitcnt lgkmcnt(4)\n"
    "v_mfma_f32_16x16x32_bf16 v[0:3], v[96:99], a[64:67], v[0:3]\n"
    "s_add_u32 m0, s78, 35648\n"
    "ds_read_b128 v[88:91], v120 offset:52800\n"
    "buffer_load_dwordx4 v122, s[36:39], s75 offen offset:256 nt lds\n"
    "v_mfma_f32_16x16x32_bf16 v[0:3], v[100:103], a[80:83], v[0:3]\n"
    "s_add_u32 m0, s78, 36704\n"
    "v_mfma_f32_16x16x32_bf16 v[4:7], v[96:99], a[68:71], v[4:7]\n"
    "buffer_load_dwordx4 v122, s[36:39], s76 offen offset:256 nt lds\n"
    "v_mfma_f32_16x16x32_bf16 v[4:7], v[100:103], a[84:87], v[4:7]\n"
    "ds_read_b128 v[92:95], v120 offset:53312\n"
    "v_mfma_f32_16x16x32_bf16 v[8:11], v[96:99], a[72:75], v[8:11]\n"
    "s_add_u32 m0, s78, 50304\n"
    "v_mfma_f32_16x16x32_bf16 v[8:11], v[100:103], a[88:91], v[8:11]\n"
    "buffer_load_dwordx4 v122, s[36:39], s73 offen offset:384 nt lds\n"
    "v_mfma_f32_16x16x32_bf16 v[12:15], v[96:99], a[76:79], v[12:15]\n"
    "s_add_u32 s73, s73, 32768\n"
    "v_mfma_f32_16x16x32_bf16 v[12:15], v[100:103], a[92:95], v[12:15]\n"
    "s_waitcnt lgkmcnt(4)\n"
    "v_mfma_f32_16x16x32_bf16 v[0:3], v[104:107], a[96:99], v[0:3]\n"
    "s_add_u32 m0, s78, 51360\n"
    "ds_read_b128 v[96:99], v120 offset:54912\n"
    "buffer_load_dwordx4 v122, s[36:39], s74 offen offset:384 nt lds\n"
    "v_mfma_f32_16x16x32_bf16 v[0:3], v[108:111], a[112:115], v[0:3]\n"
    "s_add_u32 s74, s74, 32768\n"
    "v_mfma_f32_16x16x32_bf16 v[4:7], v[104:107], a[100:103], v[4:7]\n"
    "s_add_u32 m0, s78, 52416\n"
    "v_mfma_f32_16x16x32_bf16 v[4:7], v[108:111], a[116:119], v[4:7]\n"
    "buffer_load_dwordx4 v122, s[36:39], s75 offen offset:384 nt lds\n"
    "ds_read_b128 v[100:103], v120 offset:55424\n"
    "v_mfma_f32_16x16x32_bf16 v[8:11], v[104:107], a[104:107], v[8:11]\n"
    "s_add_u32 s75, s75, 32768\n"
    "v_exp_f32 v0, v0\n"
    "v_mfma_f32_16x16x32_bf16 v[8:11], v[108:111], a[120:123], v[8:11]\n"
    "s_add_u32 m0, s78, 53472\n"
    "v_exp_f32 v1, v1\n"
    "v_mfma_f32_16x16x32_bf16 v[12:15], v[104:107], a[108:111], v[12:15]\n"
    "buffer_load_dwordx4 v122, s[36:39], s76 offen offset:384 nt lds\n"
    "v_add_f32 v116, v116, v0\n"
    "v_mfma_f32_16x16x32_bf16 v[12:15], v[108:111], a[124:127], v[12:15]\n"
    "s_waitcnt lgkmcnt(4)\n"
    "v_exp_f32 v2, v2\n"
    "v_mfma_f32_16x16x32_bf16 v[16:19], v[80:83], a[0:3], v[64:67]\n"
    "s_add_u32 s76, s76, 32768\n"
    "ds_read_b128 v[104:107], v120 offset:57024\n"
    "v_add_f32 v116, v116, v1\n"
    "v_mfma_f32_16x16x32_bf16 v[16:19], v[84:87], a[16:19], v[16:19]\n"
    "v_exp_f32 v3, v3\n"
    "v_mfma_f32_16x16x32_bf16 v[20:23], v[80:83], a[4:7], v[68:71]\n"
    "v_exp_f32 v4, v4\n"
    "v_mfma_f32_16x16x32_bf16 v[20:23], v[84:87], a[20:23], v[20:23]\n"
    "ds_read_b128 v[108:111], v120 offset:57536\n"
    "v_add_f32 v116, v116, v2\n"
    "v_mfma_f32_16x16x32_bf16 v[24:27], v[80:83], a[8:11], v[72:75]\n"
    "v_exp_f32 v5, v5\n"
    "v_mfma_f32_16x16x32_bf16 v[24:27], v[84:87], a[24:27], v[24:27]\n"
    "v_exp_f32 v6, v6\n"
    "v_mfma_f32_16x16x32_bf16 v[28:31], v[80:83], a[12:15], v[76:79]\n"
    "v_exp_f32 v7, v7\n"
    "v_mfma_f32_16x16x32_bf16 v[28:31], v[84:87], a[28:31], v[28:31]\n"
    "s_waitcnt lgkmcnt(4)\n"
    "v_exp_f32 v8, v8\n"
    "v_mfma_f32_16x16x32_bf16 v[16:19], v[88:91], a[32:35], v[16:19]\n"
    "s_waitcnt vmcnt(12)\n"
    "v_exp_f32 v9, v9\n"
    "v_mfma_f32_16x16x32_bf16 v[16:19], v[92:95], a[48:51], v[16:19]\n"
    "s_barrier\n"
    "ds_read_b64_tr_b16 v[80:81], v121 offset:0\n"
    "v_add_f32 v116, v116, v3\n"
    "v_mfma_f32_16x16x32_bf16 v[20:23], v[88:91], a[36:39], v[20:23]\n"
    "ds_read_b64_tr_b16 v[82:83], v121 offset:256\n"
    "v_cvt_pk_bf16_f32 v48, v0, v1\n"
    "v_mfma_f32_16x16x32_bf16 v[20:23], v[92:95], a[52:55], v[20:23]\n"
    "v_exp_f32 v10, v10\n"
    "v_mfma_f32_16x16x32_bf16 v[24:27], v[88:91], a[40:43], v[24:27]\n"
    "ds_read_b64_tr_b16 v[84:85], v121 offset:4224\n"
    "v_cvt_pk_bf16_f32 v49, v2, v3\n"
    "v_mfma_f32_16x16x32_bf16 v[24:27], v[92:95], a[56:59], v[24:27]\n"
    "ds_read_b64_tr_b16 v[86:87], v121 offset:4480\n"
    "v_add_f32 v117, v117, v4\n"
    "v_mfma_f32_16x16x32_bf16 v[28:31], v[88:91], a[44:47], v[28:31]\n"
    "v_exp_f32 v11, v11\n"
    "v_mfma_f32_16x16x32_bf16 v[28:31], v[92:95], a[60:63], v[28:31]\n"
    "s_waitcnt lgkmcnt(6)\n"
    "v_exp_f32 v12, v12\n"
    "v_mfma_f32_16x16x32_bf16 v[16:19], v[96:99], a[64:67], v[16:19]\n"
    "ds_read_b64_tr_b16 v[88:89], v121 offset:32\n"
    "v_add_f32 v117, v117, v5\n"
    "v_mfma_f32_16x16x32_bf16 v[16:19], v[100:103], a[80:83], v[16:19]\n"
    "ds_read_b64_tr_b16 v[90:91], v121 offset:288\n"
    "v_add_f32 v117, v117, v6\n"
    "v_mfma_f32_16x16x32_bf16 v[20:23], v[96:99], a[68:71], v[20:23]\n"
    "v_exp_f32 v13, v13\n"
    "v_mfma_f32_16x16x32_bf16 v[20:23], v[100:103], a[84:87], v[20:23]\n"
    "ds_read_b64_tr_b16 v[92:93], v121 offset:4256\n"
    "v_add_f32 v117, v117, v7\n"
    "v_mfma_f32_16x16x32_bf16 v[24:27], v[96:99], a[72:75], v[24:27]\n"
    "ds_read_b64_tr_b16 v[94:95], v121 offset:4512\n"
    "v_cvt_pk_bf16_f32 v52, v4, v5\n"
    "v_mfma_f32_16x16x32_bf16 v[24:27], v[100:103], a[88:91], v[24:27]\n"
    "v_exp_f32 v14, v14\n"
    "v_mfma_f32_16x16x32_bf16 v[28:31], v[96:99], a[76:79], v[28:31]\n"
    "v_exp_f32 v15, v15\n"
    "v_mfma_f32_16x16x32_bf16 v[28:31], v[100:103], a[92:95], v[28:31]\n"
    "s_waitcnt lgkmcnt(8)\n"
    "v_cvt_pk_bf16_f32 v53, v6, v7\n"
    "v_add_f32 v118, v118, v8\n"
    "v_mfma_f32_16x16x32_bf16 v[16:19], v[104:107], a[96:99], v[16:19]\n"
    "v_add_f32 v118, v118, v9\n"
    "v_add_f32 v118, v118, v10\n"
    "v_mfma_f32_16x16x32_bf16 v[16:19], v[108:111], a[112:115], v[16:19]\n"
    "v_add_f32 v118, v118, v11\n"
    "v_cvt_pk_bf16_f32 v56, v8, v9\n"
    "v_mfma_f32_16x16x32_bf16 v[20:23], v[104:107], a[100:103], v[20:23]\n"
    "v_cvt_pk_bf16_f32 v57, v10, v11\n"
    "v_add_f32 v119, v119, v12\n"
    "v_add_f32 v119, v119, v13\n"
    "v_add_f32 v119, v119, v14\n"
    "v_add_f32 v119, v119, v15\n"
    "v_cvt_pk_bf16_f32 v60, v12, v13\n"
    "v_cvt_pk_bf16_f32 v61, v14, v15\n"
    "v_mfma_f32_16x16x32_bf16 v[20:23], v[108:111], a[116:119], v[20:23]\n"
    "v_exp_f32 v16, v16\n"
    "v_mfma_f32_16x16x32_bf16 v[24:27], v[104:107], a[104:107], v[24:27]\n"
    "ds_read_b64_tr_b16 v[96:97], v121 offset:512\n"
    "ds_read_b64_tr_b16 v[98:99], v121 offset:768\n"
    "v_mfma_f32_16x16x32_bf16 v[24:27], v[108:111], a[120:123], v[24:27]\n"
    "ds_read_b64_tr_b16 v[100:101], v121 offset:4736\n"
    "ds_read_b64_tr_b16 v[102:103], v121 offset:4992\n"
    "v_mfma_f32_16x16x32_bf16 v[28:31], v[104:107], a[108:111], v[28:31]\n"
    "v_exp_f32 v17, v17\n"
    "v_mfma_f32_16x16x32_bf16 v[28:31], v[108:111], a[124:127], v[28:31]\n"
    "s_waitcnt lgkmcnt(8)\n"
    "v_exp_f32 v18, v18\n"
    "v_exp_f32 v19, v19\n"
    "v_add_f32 v116, v116, v16\n"
    "v_add_f32 v116, v116, v17\n"
    "v_add_f32 v116, v116, v18\n"
    "v_add_f32 v116, v116, v19\n"
    "v_cvt_pk_bf16_f32 v50, v16, v17\n"
    "v_cvt_pk_bf16_f32 v51, v18, v19\n"
    "v_exp_f32 v20, v20\n"
    "v_exp_f32 v21, v21\n"
    "v_exp_f32 v22, v22\n"
    "v_exp_f32 v23, v23\n"
    "v_add_f32 v117, v117, v20\n"
    "v_add_f32 v117, v117, v21\n"
    "v_add_f32 v117, v117, v22\n"
    "v_add_f32 v117, v117, v23\n"
    "v_cvt_pk_bf16_f32 v54, v20, v21\n"
    "v_cvt_pk_bf16_f32 v55, v22, v23\n"
    "v_exp_f32 v24, v24\n"
    "v_exp_f32 v25, v25\n"
    "v_exp_f32 v26, v26\n"
    "v_exp_f32 v27, v27\n"
    "v_add_f32 v118, v118, v24\n"
    "v_add_f32 v118, v118, v25\n"
    "v_add_f32 v118, v118, v26\n"
    "v_add_f32 v118, v118, v27\n"
    "v_cvt_pk_bf16_f32 v58, v24, v25\n"
    "v_cvt_pk_bf16_f32 v59, v26, v27\n"
    "v_exp_f32 v28, v28\n"
    "v_exp_f32 v29, v29\n"
    "v_exp_f32 v30, v30\n"
    "v_exp_f32 v31, v31\n"
    "v_add_f32 v119, v119, v28\n"
    "v_add_f32 v119, v119, v29\n"
    "v_add_f32 v119, v119, v30\n"
    "v_add_f32 v119, v119, v31\n"
    "v_cvt_pk_bf16_f32 v62, v28, v29\n"
    "v_cvt_pk_bf16_f32 v63, v30, v31\n"
    "v_max3_f32 v124, v116, v117, v118\n"
    "v_max_f32 v124, v124, v119\n"
    "v_cmp_nge_f32 vcc, s80, v124\n"
    "s_nop 0\n"
    "s_cbranch_vccnz Lslow%=\n"
    "LbackL%=:\n"
    "v_mfma_f32_16x16x32_bf16 v[128:131], v[80:83], v[32:35], v[128:131]\n"
    "s_nop 0\n"
    "ds_read_b64_tr_b16 v[104:105], v121 offset:544\n"
    "v_add_f32 v112, v112, v116\n"
    "v_mfma_f32_16x16x32_bf16 v[128:131], v[84:87], v[48:51], v[128:131]\n"
    "ds_read_b64_tr_b16 v[106:107], v121 offset:800\n"
    "v_add_f32 v113, v113, v117\n"
    "v_mfma_f32_16x16x32_bf16 v[132:135], v[80:83], v[36:39], v[132:135]\n"
    "s_mov_b32 s88, 0\n"
    "s_add_u32 m0, s89, 0\n"
    "v_add_f32 v114, v114, v118\n"
    "buffer_load_dwordx4 v125, s[56:59], s88 offen offset:0 lds\n"
    "v_mfma_f32_16x16x32_bf16 v[132:135], v[84:87], v[52:55], v[132:135]\n"
    "s_add_u32 s88, s88, 28672\n"
    "ds_read_b64_tr_b16 v[108:109], v121 offset:4768\n"
    "v_add_f32 v115, v115, v119\n"
    "v_mfma_f32_16x16x32_bf16 a[128:131], v[80:83], v[40:43], a[128:131]\n"
    "s_nop 0\n"
    "ds_read_b64_tr_b16 v[110:111], v121 offset:5024\n"
    "v_mbcnt_lo_u32_b32 v126, -1, 0\n"
    "v_mfma_f32_16x16x32_bf16 a[128:131], v[84:87], v[56:59], a[128:131]\n"
    "v_mbcnt_hi_u32_b32 v126, -1, v126\n"
    "v_mov_b32 v116, v112\n"
    "v_mfma_f32_16x16x32_bf16 a[132:135], v[80:83], v[44:47], a[132:135]\n"
    "s_add_u32 m0, s89, 4224\n"
    "v_mov_b32 v117, v113\n"
    "buffer_load_dwordx4 v125, s[56:59], s88 offen offset:0 lds\n"
    "v_mfma_f32_16x16x32_bf16 a[132:135], v[84:87], v[60:63], a[132:135]\n"
    "s_waitcnt lgkmcnt(8)\n"
    "v_mov_b32 v118, v114\n"
    "v_mov_b32 v119, v115\n"
    "v_mfma_f32_16x16x32_bf16 v[136:139], v[88:91], v[32:35], v[136:139]\n"
    "s_waitcnt vmcnt(10)\n"
    "v_permlane16_swap_b32 v112, v116\n"
    "v_mfma_f32_16x16x32_bf16 v[136:139], v[92:95], v[48:51], v[136:139]\n"
    "s_barrier\n"
    "v_permlane16_swap_b32 v113, v117\n"
    "v_mfma_f32_16x16x32_bf16 v[140:143], v[88:91], v[36:39], v[140:143]\n"
    "s_add_u32 s88, s88, 28672\n"
    "v_permlane16_swap_b32 v114, v118\n"
    "v_mfma_f32_16x16x32_bf16 v[140:143], v[92:95], v[52:55], v[140:143]\n"
    "s_add_u32 m0, s89, 8448\n"
    "v_permlane16_swap_b32 v115, v119\n"
    "v_mfma_f32_16x16x32_bf16 a[136:139], v[88:91], v[40:43], a[136:139]\n"
    "s_lshl_b32 s84, s81, 11\n"
    "ds_read_b64_tr_b16 v[80:81], v121 offset:16896\n"
    "ds_read_b64_tr_b16 v[82:83], v121 offset:17152\n"
    "v_mfma_f32_16x16x32_bf16 a[136:139], v[92:95], v[56:59], a[136:139]\n"
    "s_add_u32 s84, s84, 67584\n"
    "ds_read_b64_tr_b16 v[84:85], v121 offset:21120\n"
    "ds_read_b64_tr_b16 v[86:87], v121 offset:21376\n"
    "v_mfma_f32_16x16x32_bf16 a[140:143], v[88:91], v[44:47], a[140:143]\n"
    "buffer_load_dwordx4 v125, s[56:59], s88 offen offset:0 lds\n"
    "v_lshlrev_b32 v127, 4, v126\n"
    "v_mfma_f32_16x16x32_bf16 a[140:143], v[92:95], v[60:63], a[140:143]\n"
    "s_waitcnt lgkmcnt(8)\n"
    "v_add_u32 v124, s84, v127\n"
    "v_add_u32 v16, 49152, v124\n"
    "v_mfma_f32_16x16x32_bf16 v[144:147], v[96:99], v[32:35], v[144:147]\n"
    "s_add_u32 s88, s88, 28672\n"
    "s_add_u32 m0, s89, 12672\n"
    "ds_read_b64_tr_b16 v[88:89], v121 offset:16928\n"
    "v_mfma_f32_16x16x32_bf16 v[144:147], v[100:103], v[48:51], v[144:147]\n"
    "s_lshl_b32 s84, s81, 10\n"
    "buffer_load_dwordx4 v125, s[56:59], s88 offen offset:0 lds\n"
    "ds_read_b64_tr_b16 v[90:91], v121 offset:17184\n"
    "v_mfma_f32_16x16x32_bf16 v[148:151], v[96:99], v[36:39], v[148:151]\n"
    "s_add_u32 s84, s84, 157696\n"
    "v_add_u32 v17, s84, v127\n"
    "v_add_f32 v112, v112, v116\n"
    "v_mfma_f32_16x16x32_bf16 v[148:151], v[100:103], v[52:55], v[148:151]\n"
    "s_mov_b32 s88, 114688\n"
    "s_add_u32 m0, s89, 16896\n"
    "ds_read_b64_tr_b16 v[92:93], v121 offset:21152\n"
    "buffer_load_dwordx4 v125, s[56:59], s88 offen offset:0 lds\n"
    "v_mfma_f32_16x16x32_bf16 a[144:147], v[96:99], v[40:43], a[144:147]\n"
    "s_add_u32 s88, s88, 28672\n"
    "ds_read_b64_tr_b16 v[94:95], v121 offset:21408\n"
    "v_add_f32 v113, v113, v117\n"
    "v_mfma_f32_16x16x32_bf16 a[144:147], v[100:103], v[56:59], a[144:147]\n"
    "v_add_f32 v114, v114, v118\n"
    "v_add_f32 v115, v115, v119\n"
    "v_mfma_f32_16x16x32_bf16 a[148:151], v[96:99], v[44:47], a[148:151]\n"
    "v_mov_b32 v116, v112\n"
    "v_mov_b32 v117, v113\n"
    "v_mfma_f32_16x16x32_bf16 a[148:151], v[100:103], v[60:63], a[148:151]\n"
    "s_waitcnt lgkmcnt(8)\n"
    "v_mov_b32 v118, v114\n"
    "v_mov_b32 v119, v115\n"
    "v_mfma_f32_16x16x32_bf16 v[152:155], v[104:107], v[32:35], v[152:155]\n"
    "s_add_u32 m0, s89, 21120\n"
    "ds_read_b64_tr_b16 v[96:97], v121 offset:17408\n"
    "buffer_load_dwordx4 v125, s[56:59], s88 offen offset:0 lds\n"
    "v_mfma_f32_16x16x32_bf16 v[152:155], v[108:111], v[48:51], v[152:155]\n"
    "s_add_u32 s88, s88, 28672\n"
    "ds_read_b64_tr_b16 v[98:99], v121 offset:17664\n"
    "v_mov_b32 v18, 0x27a00\n"
    "v_mfma_f32_16x16x32_bf16 v[156:159], v[104:107], v[36:39], v[156:159]\n"
    "v_permlane32_swap_b32 v112, v116\n"
    "v_mfma_f32_16x16x32_bf16 v[156:159], v[108:111], v[52:55], v[156:159]\n"
    "s_add_u32 m0, s89, 25344\n"
    "ds_read_b64_tr_b16 v[100:101], v121 offset:21632\n"
    "buffer_load_dwordx4 v125, s[56:59], s88 offen offset:0 lds\n"
    "v_mfma_f32_16x16x32_bf16 a[152:155], v[104:107], v[40:43], a[152:155]\n"
    "s_add_u32 s88, s88, 28672\n"
    "ds_read_b64_tr_b16 v[102:103], v121 offset:21888\n"
    "ds_read_b32 v18, v18\n"
    "v_mfma_f32_16x16x32_bf16 a[152:155], v[108:111], v[56:59], a[152:155]\n"
    "s_waitcnt lgkmcnt(0)\n"
    "v_permlane32_swap_b32 v113, v117\n"
    "v_mfma_f32_16x16x32_bf16 a[156:159], v[104:107], v[44:47], a[156:159]\n"
    "v_readfirstlane_b32 s79, v18\n"
    "ds_write_b128 v16, a[128:131] offset:24576\n"
    "v_mfma_f32_16x16x32_bf16 a[156:159], v[108:111], v[60:63], a[156:159]\n"
    "s_waitcnt lgkmcnt(10)\n"
    "v_permlane32_swap_b32 v114, v118\n"
    "v_mfma_f32_16x16x32_bf16 v[160:163], v[80:83], v[32:35], v[160:163]\n"
    "s_add_u32 m0, s89, 29568\n"
    "v_permlane32_swap_b32 v115, v119\n"
    "v_add_f32 v112, v112, v116\n"
    "v_add_f32 v113, v113, v117\n"
    "v_add_f32 v114, v114, v118\n"
    "v_add_f32 v115, v115, v119\n"
    "v_mov_b32 v0, v72\n"
    "v_mov_b32 v1, v114\n"
    "v_mov_b32 v2, v76\n"
    "v_mov_b32 v3, v115\n"
    "v_mfma_f32_16x16x32_bf16 v[160:163], v[84:87], v[48:51], v[160:163]\n"
    "s_sub_u32 s79, s79, s99\n"
    "ds_write_b128 v17, v[0:3]\n"
    "buffer_load_dwordx4 v125, s[56:59], s88 offen offset:0 lds\n"
    "v_mfma_f32_16x16x32_bf16 v[164:167], v[80:83], v[36:39], v[164:167]\n"
    "s_and_b32 s79, s79, 15\n"
    "ds_read_b64_tr_b16 v[104:105], v121 offset:17440\n"
    "ds_read_b64_tr_b16 v[106:107], v121 offset:17696\n"
    "v_mfma_f32_16x16x32_bf16 v[164:167], v[84:87], v[52:55], v[164:167]\n"
    "s_mul_i32 s77, s79, 43\n"
    "ds_read_b64_tr_b16 v[108:109], v121 offset:21664\n"
    "v_xor_b32 v17, 1024, v17\n"
    "v_mfma_f32_16x16x32_bf16 a[160:163], v[80:83], v[40:43], a[160:163]\n"
    "s_lshr_b32 s77, s77, 7\n"
    "ds_read_b64_tr_b16 v[110:111], v121 offset:21920\n"
    "ds_write_b128 v16, a[132:135] offset:25600\n"
    "v_mfma_f32_16x16x32_bf16 a[160:163], v[84:87], v[56:59], a[160:163]\n"
    "s_mul_i32 s84, s77, 3\n"
    "ds_write_b128 v16, a[136:139] offset:32768\n"
    "ds_write_b128 v16, a[140:143] offset:33792\n"
    "v_mfma_f32_16x16x32_bf16 a[164:167], v[80:83], v[44:47], a[164:167]\n"
    "s_sub_u32 s84, s79, s84\n"
    "v_mfma_f32_16x16x32_bf16 a[164:167], v[84:87], v[60:63], a[164:167]\n"
    "s_waitcnt lgkmcnt(14)\n"
    "s_waitcnt lgkmcnt(7)\n"
    "v_mfma_f32_16x16x32_bf16 v[168:171], v[88:91], v[32:35], v[168:171]\n"
    "s_waitcnt vmcnt(12)\n"
    "v_mfma_f32_16x16x32_bf16 v[168:171], v[92:95], v[48:51], v[168:171]\n"
    "s_barrier\n"
    "ds_read_b64_tr_b16 v[80:81], v121 offset:33792\n"
    "ds_write_b128 v124, a[144:147] offset:0\n"
    "v_mfma_f32_16x16x32_bf16 v[172:175], v[88:91], v[36:39], v[172:175]\n"
    "s_sub_u32 s85, s79, 12\n"
    "ds_read_b64_tr_b16 v[82:83], v121 offset:34048\n"
    "ds_read_b128 v[4:7], v17\n"
    "v_mfma_f32_16x16x32_bf16 v[172:175], v[92:95], v[52:55], v[172:175]\n"
    "s_cmp_ge_u32 s79, 12\n"
    "s_cselect_b32 s77, s85, s77\n"
    "s_cselect_b32 s79, 3, s84\n"
    "ds_write_b128 v124, a[148:151] offset:1024\n"
    "ds_write_b128 v124, a[152:155] offset:8192\n"
    "v_mfma_f32_16x16x32_bf16 a[168:171], v[88:91], v[40:43], a[168:171]\n"
    "s_lshl_b32 s84, s79, 8\n"
    "s_add_u32 s48, s48, s84\n"
    "s_addc_u32 s49, s49, 0\n"
    "s_sub_u32 s50, s50, s84\n"
    "s_lshl_b32 s84, s79, 3\n"
    "s_lshl_b32 s78, 0xff, s84\n"
    "s_cmp_eq_u32 s81, s77\n"
    "s_cselect_b32 s78, s78, 0\n"
    "v_mfma_f32_16x16x32_bf16 a[168:171], v[92:95], v[56:59], a[168:171]\n"
    "s_mov_b32 s88, 0\n"
    "s_add_u32 m0, s89, 33664\n"
    "ds_read_b64_tr_b16 v[84:85], v121 offset:38016\n"
    "ds_read_b64_tr_b16 v[86:87], v121 offset:38272\n"
    "v_mfma_f32_16x16x32_bf16 a[172:175], v[88:91], v[44:47], a[172:175]\n"
    "s_mul_i32 s84, s77, 7168\n"
    "buffer_load_dwordx4 v125, s[56:59], s88 offen offset:128 lds\n"
    "ds_write_b128 v124, a[156:159] offset:9216\n"
    "v_mfma_f32_16x16x32_bf16 a[172:175], v[92:95], v[60:63], a[172:175]\n"
    "s_waitcnt lgkmcnt(15)\n"
    "v_mfma_f32_16x16x32_bf16 v[176:179], v[96:99], v[32:35], v[176:179]\n"
    "s_add_u32 s88, s88, 28672\n"
    "ds_read_b64_tr_b16 v[88:89], v121 offset:33824\n"
    "v_mfma_f32_16x16x32_bf16 v[176:179], v[100:103], v[48:51], v[176:179]\n"
    "s_add_u32 m0, s89, 37888\n"
    "ds_read_b64_tr_b16 v[90:91], v121 offset:34080\n"
    "buffer_load_dwordx4 v125, s[56:59], s88 offen offset:128 lds\n"
    "v_mfma_f32_16x16x32_bf16 v[180:183], v[96:99], v[36:39], v[180:183]\n"
    "s_add_u32 s84, s84, 114688\n"
    "v_add_u32 v120, s84, v127\n"
    "v_add_u32 v123, 0x25200, v127\n"
    "v_mfma_f32_16x16x32_bf16 v[180:183], v[100:103], v[52:55], v[180:183]\n"
    "s_add_u32 s88, s88, 28672\n"
    "ds_read_b64_tr_b16 v[92:93], v121 offset:38048\n"
    "v_mfma_f32_16x16x32_bf16 a[176:179], v[96:99], v[40:43], a[176:179]\n"
    "ds_read_b64_tr_b16 v[94:95], v121 offset:38304\n"
    "v_mfma_f32_16x16x32_bf16 a[176:179], v[100:103], v[56:59], a[176:179]\n"
    "v_mfma_f32_16x16x32_bf16 a[180:183], v[96:99], v[44:47], a[180:183]\n"
    "s_waitcnt lgkmcnt(9)\n"
    "v_min_f32 v8, v64, v4\n"
    "v_sub_f32 v9, v8, v64\n"
    "v_mfma_f32_16x16x32_bf16 a[180:183], v[100:103], v[60:63], a[180:183]\n"
    "s_waitcnt lgkmcnt(15)\n"
    "v_sub_f32 v10, v8, v4\n"
    "v_min_f32 v12, v68, v6\n"
    "v_mfma_f32_16x16x32_bf16 v[184:187], v[104:107], v[32:35], v[184:187]\n"
    "s_add_u32 m0, s89, 42112\n"
    "ds_read_b64_tr_b16 v[96:97], v121 offset:34304\n"
    "buffer_load_dwordx4 v125, s[56:59], s88 offen offset:128 lds\n"
    "v_mfma_f32_16x16x32_bf16 v[184:187], v[108:111], v[48:51], v[184:187]\n"
    "s_add_u32 s88, s88, 28672\n"
    "ds_read_b64_tr_b16 v[98:99], v121 offset:34560\n"
    "v_sub_f32 v13, v12, v68\n"
    "v_mfma_f32_16x16x32_bf16 v[188:191], v[104:107], v[36:39], v[188:191]\n"
    "s_mul_i32 s28, s81, 573440\n"
    "v_exp_f32 v9, v9\n"
    "v_mfma_f32_16x16x32_bf16 v[188:191], v[108:111], v[52:55], v[188:191]\n"
    "s_add_u32 m0, s89, 46336\n"
    "ds_read_b64_tr_b16 v[100:101], v121 offset:38528\n"
    "buffer_load_dwordx4 v125, s[56:59], s88 offen offset:128 lds\n"
    "v_mfma_f32_16x16x32_bf16 a[184:187], v[104:107], v[40:43], a[184:187]\n"
    "ds_read_b64_tr_b16 v[102:103], v121 offset:38784\n"
    "v_sub_f32 v14, v12, v6\n"
    "v_mfma_f32_16x16x32_bf16 a[184:187], v[108:111], v[56:59], a[184:187]\n"
    "v_exp_f32 v10, v10\n"
    "v_mfma_f32_16x16x32_bf16 a[188:191], v[104:107], v[44:47], a[188:191]\n"
    "buffer_load_dwordx4 a[0:3], v120, s[44:47], s28 offen offset:0 sc1\n"
    "v_mul_f32 v8, v112, v9\n"
    "v_mfma_f32_16x16x32_bf16 a[188:191], v[108:111], v[60:63], a[188:191]\n"
    "s_waitcnt lgkmcnt(9)\n"
    "v_exp_f32 v13, v13\n"
    "v_mfma_f32_16x16x32_bf16 v[192:195], v[80:83], v[32:35], v[192:195]\n"
    "ds_read_b64_tr_b16 v[104:105], v121 offset:34336\n"
    "v_fmac_f32 v8, v5, v10\n"
    "v_mfma_f32_16x16x32_bf16 v[192:195], v[84:87], v[48:51], v[192:195]\n"
    "s_mov_b32 s88, 114688\n"
    "s_add_u32 m0, s89, 50560\n"
    "v_rcp_f32 v8, v8\n"
    "v_mfma_f32_16x16x32_bf16 v[196:199], v[80:83], v[36:39], v[196:199]\n"
    "buffer_load_dwordx4 v125, s[56:59], s88 offen offset:128 lds\n"
    "ds_read_b64_tr_b16 v[106:107], v121 offset:34592\n"
    "v_mfma_f32_16x16x32_bf16 v[196:199], v[84:87], v[52:55], v[196:199]\n"
    "s_add_u32 s88, s88, 28672\n"
    "ds_read_b64_tr_b16 v[108:109], v121 offset:38560\n"
    "buffer_load_dwordx4 a[4:7], v120, s[44:47], s28 offen offset:1024 sc1\n"
    "v_mfma_f32_16x16x32_bf16 a[192:195], v[80:83], v[40:43], a[192:195]\n"
    "ds_read_b64_tr_b16 v[110:111], v121 offset:38816\n"
    "v_mul_f32 v116, v9, v8\n"
    "v_mfma_f32_16x16x32_bf16 a[192:195], v[84:87], v[56:59], a[192:195]\n"
    "v_exp_f32 v14, v14\n"
    "v_mfma_f32_16x16x32_bf16 a[196:199], v[80:83], v[44:47], a[196:199]\n"
    "s_add_u32 m0, s89, 54784\n"
    "v_mul_f32 v118, v10, v8\n"
    "buffer_load_dwordx4 v125, s[56:59], s88 offen offset:128 lds\n"
    "v_mfma_f32_16x16x32_bf16 a[196:199], v[84:87], v[60:63], a[196:199]\n"
    "s_waitcnt lgkmcnt(8)\n"
    "v_mul_f32 v12, v113, v13\n"
    "v_fmac_f32 v12, v7, v14\n"
    "v_mfma_f32_16x16x32_bf16 v[200:203], v[88:91], v[32:35], v[200:203]\n"
    "s_waitcnt vmcnt(16)\n"
    "v_rcp_f32 v12, v12\n"
    "v_mfma_f32_16x16x32_bf16 v[200:203], v[92:95], v[48:51], v[200:203]\n"
    "s_barrier\n"
    "ds_read_b64_tr_b16 v[80:81], v121 offset:50688\n"
    "buffer_load_dwordx4 a[8:11], v120, s[44:47], s28 offen offset:2048 sc1\n"
    "v_mfma_f32_16x16x32_bf16 v[204:207], v[88:91], v[36:39], v[204:207]\n"
    "s_add_u32 s88, s88, 28672\n"
    "ds_read_b64_tr_b16 v[82:83], v121 offset:50944\n"
    "v_mul_f32 v117, v13, v12\n"
    "v_mfma_f32_16x16x32_bf16 v[204:207], v[92:95], v[52:55], v[204:207]\n"
    "s_add_u32 m0, s89, 59008\n"
    "v_mul_f32 v119, v14, v12\n"
    "buffer_load_dwordx4 v125, s[56:59], s88 offen offset:128 lds\n"
    "v_mfma_f32_16x16x32_bf16 a[200:203], v[88:91], v[40:43], a[200:203]\n"
    "s_add_u32 s88, s88, 28672\n"
    "ds_read_b64_tr_b16 v[84:85], v121 offset:54912\n"
    "ds_write_b128 v124, a[160:163] offset:16384\n"
    "v_mfma_f32_16x16x32_bf16 a[200:203], v[92:95], v[56:59], a[200:203]\n"
    "ds_read_b64_tr_b16 v[86:87], v121 offset:55168\n"
    "ds_write_b128 v124, a[164:167] offset:17408\n"
    "v_mfma_f32_16x16x32_bf16 a[204:207], v[88:91], v[44:47], a[204:207]\n"
    "buffer_load_dwordx4 a[12:15], v120, s[44:47], s28 offen offset:3072 sc1\n"
    "ds_write_b128 v124, a[168:171] offset:24576\n"
    "v_mfma_f32_16x16x32_bf16 a[204:207], v[92:95], v[60:63], a[204:207]\n"
    "s_waitcnt lgkmcnt(11)\n"
    "ds_write_b128 v124, a[172:175] offset:25600\n"
    "v_mfma_f32_16x16x32_bf16 v[208:211], v[96:99], v[32:35], v[208:211]\n"
    "s_add_u32 m0, s89, 63232\n"
    "ds_read_b64_tr_b16 v[88:89], v121 offset:50720\n"
    "buffer_load_dwordx4 v125, s[56:59], s88 offen offset:128 lds\n"
    "v_mfma_f32_16x16x32_bf16 v[208:211], v[100:103], v[48:51], v[208:211]\n"
    "ds_read_b64_tr_b16 v[90:91], v121 offset:50976\n"
    "v_mfma_f32_16x16x32_bf16 v[212:215], v[96:99], v[36:39], v[212:215]\n"
    "v_mfma_f32_16x16x32_bf16 v[212:215], v[100:103], v[52:55], v[212:215]\n"
    "ds_read_b64_tr_b16 v[92:93], v121 offset:54944\n"
    "v_mfma_f32_16x16x32_bf16 a[208:211], v[96:99], v[40:43], a[208:211]\n"
    "s_add_u32 s28, s28, 4096\n"
    "ds_read_b64_tr_b16 v[94:95], v121 offset:55200\n"
    "v_mfma_f32_16x16x32_bf16 a[208:211], v[100:103], v[56:59], a[208:211]\n"
    "v_mfma_f32_16x16x32_bf16 a[212:215], v[96:99], v[44:47], a[212:215]\n"
    "v_mfma_f32_16x16x32_bf16 a[212:215], v[100:103], v[60:63], a[212:215]\n"
    "s_waitcnt lgkmcnt(12)\n"
    "v_mfma_f32_16x16x32_bf16 v[216:219], v[104:107], v[32:35], v[216:219]\n"
    "ds_read_b64_tr_b16 v[96:97], v121 offset:51200\n"
    "v_mfma_f32_16x16x32_bf16 v[216:219], v[108:111], v[48:51], v[216:219]\n"
    "ds_read_b64_tr_b16 v[98:99], v121 offset:51456\n"
    "v_mfma_f32_16x16x32_bf16 v[220:223], v[104:107], v[36:39], v[220:223]\n"
    "buffer_load_dwordx4 a[16:19], v120, s[44:47], s28 offen offset:0 sc1\n"
    "v_mfma_f32_16x16x32_bf16 v[220:223], v[108:111], v[52:55], v[220:223]\n"
    "ds_read_b64_tr_b16 v[100:101], v121 offset:55424\n"
    "v_mfma_f32_16x16x32_bf16 a[216:219], v[104:107], v[40:43], a[216:219]\n"
    "ds_read_b64_tr_b16 v[102:103], v121 offset:55680\n"
    "v_mfma_f32_16x16x32_bf16 a[216:219], v[108:111], v[56:59], a[216:219]\n"
    "v_mfma_f32_16x16x32_bf16 a[220:223], v[104:107], v[44:47], a[220:223]\n"
    "v_mfma_f32_16x16x32_bf16 a[220:223], v[108:111], v[60:63], a[220:223]\n"
    "s_waitcnt lgkmcnt(11)\n"
    "buffer_load_dwordx4 a[20:23], v120, s[44:47], s28 offen offset:1024 sc1\n"
    "v_mfma_f32_16x16x32_bf16 v[224:227], v[80:83], v[32:35], v[224:227]\n"
    "ds_read_b64_tr_b16 v[104:105], v121 offset:51232\n"
    "v_mfma_f32_16x16x32_bf16 v[224:227], v[84:87], v[48:51], v[224:227]\n"
    "ds_read_b64_tr_b16 v[106:107], v121 offset:51488\n"
    "v_mfma_f32_16x16x32_bf16 v[228:231], v[80:83], v[36:39], v[228:231]\n"
    "v_mfma_f32_16x16x32_bf16 v[228:231], v[84:87], v[52:55], v[228:231]\n"
    "ds_read_b64_tr_b16 v[108:109], v121 offset:55456\n"
    "v_mfma_f32_16x16x32_bf16 a[224:227], v[80:83], v[40:43], a[224:227]\n"
    "ds_read_b64_tr_b16 v[110:111], v121 offset:55712\n"
    "v_mfma_f32_16x16x32_bf16 a[224:227], v[84:87], v[56:59], a[224:227]\n"
    "buffer_load_dwordx2 a[96:97], v120, s[44:47], s28 offen offset:2048 sc1\n"
    "v_mfma_f32_16x16x32_bf16 a[228:231], v[80:83], v[44:47], a[228:231]\n"
    "v_mfma_f32_16x16x32_bf16 a[228:231], v[84:87], v[60:63], a[228:231]\n"
    "s_waitcnt lgkmcnt(8)\n"
    "v_mfma_f32_16x16x32_bf16 v[232:235], v[88:91], v[32:35], v[232:235]\n"
    "v_mfma_f32_16x16x32_bf16 v[232:235], v[92:95], v[48:51], v[232:235]\n"
    "v_mfma_f32_16x16x32_bf16 v[236:239], v[88:91], v[36:39], v[236:239]\n"
    "v_mfma_f32_16x16x32_bf16 v[236:239], v[92:95], v[52:55], v[236:239]\n"
    "buffer_load_dwordx2 a[100:101], v120, s[44:47], s28 offen offset:2056 sc1\n"
    "v_mfma_f32_16x16x32_bf16 a[232:235], v[88:91], v[40:43], a[232:235]\n"
    "v_mfma_f32_16x16x32_bf16 a[232:235], v[92:95], v[56:59], a[232:235]\n"
    "v_mfma_f32_16x16x32_bf16 a[236:239], v[88:91], v[44:47], a[236:239]\n"
    "v_mfma_f32_16x16x32_bf16 a[236:239], v[92:95], v[60:63], a[236:239]\n"
    "s_waitcnt lgkmcnt(4)\n"
    "v_mfma_f32_16x16x32_bf16 v[240:243], v[96:99], v[32:35], v[240:243]\n"
    "s_add_u32 s28, s28, 139264\n"
    "v_mfma_f32_16x16x32_bf16 v[240:243], v[100:103], v[48:51], v[240:243]\n"
    "v_mfma_f32_16x16x32_bf16 v[244:247], v[96:99], v[36:39], v[244:247]\n"
    "v_mfma_f32_16x16x32_bf16 v[244:247], v[100:103], v[52:55], v[244:247]\n"
    "v_mfma_f32_16x16x32_bf16 a[240:243], v[96:99], v[40:43], a[240:243]\n"
    "v_mfma_f32_16x16x32_bf16 a[240:243], v[100:103], v[56:59], a[240:243]\n"
    "v_mfma_f32_16x16x32_bf16 a[244:247], v[96:99], v[44:47], a[244:247]\n"
    "buffer_load_dwordx4 a[24:27], v120, s[44:47], s28 offen offset:0 sc1\n"
    "v_mfma_f32_16x16x32_bf16 a[244:247], v[100:103], v[60:63], a[244:247]\n"
    "s_waitcnt lgkmcnt(0)\n"
    "v_mfma_f32_16x16x32_bf16 v[248:251], v[104:107], v[32:35], v[248:251]\n"
    "v_mfma_f32_16x16x32_bf16 v[248:251], v[108:111], v[48:51], v[248:251]\n"
    "v_mfma_f32_16x16x32_bf16 v[252:255], v[104:107], v[36:39], v[252:255]\n"
    "v_mfma_f32_16x16x32_bf16 v[252:255], v[108:111], v[52:55], v[252:255]\n"
    "v_mfma_f32_16x16x32_bf16 a[248:251], v[104:107], v[40:43], a[248:251]\n"
    "v_mfma_f32_16x16x32_bf16 a[248:251], v[108:111], v[56:59], a[248:251]\n"
    "v_mfma_f32_16x16x32_bf16 a[252:255], v[104:107], v[44:47], a[252:255]\n"
    "v_mfma_f32_16x16x32_bf16 a[252:255], v[108:111], v[60:63], a[252:255]\n"
    "s_xor_b32 s84, s81, 1\n"
    "s_lshl_b32 s84, s84, 11\n"
    "s_add_u32 s84, s84, 67584\n"
    "v_add_u32 v121, s84, v127\n"
    "v_add_u32 v122, 49152, v121\n"
    "v_mov_b32 v115, v16\n"
    "s_waitcnt lgkmcnt(0)\n"
    "s_barrier\n"
    "ds_read_b128 v[64:67], v122 offset:24576\n"
    "ds_read_b128 v[68:71], v122 offset:25600\n"
    "ds_read_b128 v[72:75], v122 offset:32768\n"
    "ds_read_b128 v[76:79], v122 offset:33792\n"
    "ds_read_b128 v[80:83], v121 offset:0\n"
    "ds_read_b128 v[84:87], v121 offset:1024\n"
    "ds_read_b128 v[88:91], v121 offset:8192\n"
    "ds_read_b128 v[92:95], v121 offset:9216\n"
    "ds_read_b128 v[96:99], v121 offset:16384\n"
    "ds_read_b128 v[100:103], v121 offset:17408\n"
    "ds_read_b128 v[104:107], v121 offset:24576\n"
    "ds_read_b128 v[108:111], v121 offset:25600\n"
    "ds_write_b128 v124, a[176:179] offset:32768\n"
    "ds_write_b128 v124, a[180:183] offset:33792\n"
    "s_waitcnt lgkmcnt(12)\n"
    "v_pk_mul_f32 v[128:129], v[128:129], v[116:117] op_sel_hi:[1,0]\n"
    "v_pk_mul_f32 v[130:131], v[130:131], v[116:117] op_sel_hi:[1,0]\n"
    "v_pk_fma_f32 v[128:129], v[64:65], v[118:119], v[128:129] op_sel_hi:[1,0,1]\n"
    "v_pk_fma_f32 v[130:131], v[66:67], v[118:119], v[130:131] op_sel_hi:[1,0,1]\n"
    "v_cvt_pk_bf16_f32 v0, v128, v129\n"
    "v_cvt_pk_bf16_f32 v1, v130, v131\n"
    "v_pk_mul_f32 v[132:133], v[132:133], v[116:117] op_sel:[0,1]\n"
    "v_pk_mul_f32 v[134:135], v[134:135], v[116:117] op_sel:[0,1]\n"
    "v_pk_fma_f32 v[132:133], v[68:69], v[118:119], v[132:133] op_sel:[0,1,0]\n"
    "v_pk_fma_f32 v[134:135], v[70:71], v[118:119], v[134:135] op_sel:[0,1,0]\n"
    "v_cvt_pk_bf16_f32 v4, v132, v133\n"
    "v_cvt_pk_bf16_f32 v5, v134, v135\n"
    "buffer_load_dwordx4 a[28:31], v120, s[44:47], s28 offen offset:1024 sc1\n"
    "buffer_load_dwordx4 a[32:35], v120, s[44:47], s28 offen offset:2048 sc1\n"
    "buffer_load_dwordx4 a[36:39], v120, s[44:47], s28 offen offset:3072 sc1\n"
    "ds_write_b128 v124, a[184:187] offset:40960\n"
    "ds_write_b128 v124, a[188:191] offset:41984\n"
    "s_waitcnt lgkmcnt(12)\n"
    "v_pk_mul_f32 v[136:137], v[136:137], v[116:117] op_sel_hi:[1,0]\n"
    "v_pk_mul_f32 v[138:139], v[138:139], v[116:117] op_sel_hi:[1,0]\n"
    "v_pk_fma_f32 v[136:137], v[72:73], v[118:119], v[136:137] op_sel_hi:[1,0,1]\n"
    "v_pk_fma_f32 v[138:139], v[74:75], v[118:119], v[138:139] op_sel_hi:[1,0,1]\n"
    "v_cvt_pk_bf16_f32 v2, v136, v137\n"
    "v_cvt_pk_bf16_f32 v3, v138, v139\n"
    "v_pk_mul_f32 v[140:141], v[140:141], v[116:117] op_sel:[0,1]\n"
    "v_pk_mul_f32 v[142:143], v[142:143], v[116:117] op_sel:[0,1]\n"
    "v_pk_fma_f32 v[140:141], v[76:77], v[118:119], v[140:141] op_sel:[0,1,0]\n"
    "v_pk_fma_f32 v[142:143], v[78:79], v[118:119], v[142:143] op_sel:[0,1,0]\n"
    "v_cvt_pk_bf16_f32 v6, v140, v141\n"
    "v_cvt_pk_bf16_f32 v7, v142, v143\n"
    "s_add_u32 s28, s28, 4096\n"
    "buffer_load_dwordx4 a[40:43], v120, s[44:47], s28 offen offset:0 sc1\n"
    "buffer_load_dwordx4 a[44:47], v120, s[44:47], s28 offen offset:1024 sc1\n"
    "ds_write_b128 v115, a[192:195] offset:0\n"
    "ds_write_b128 v115, a[196:199] offset:1024\n"
    "s_waitcnt lgkmcnt(12)\n"
    "v_pk_mul_f32 v[144:145], v[144:145], v[116:117] op_sel_hi:[1,0]\n"
    "v_pk_mul_f32 v[146:147], v[146:147], v[116:117] op_sel_hi:[1,0]\n"
    "v_pk_fma_f32 v[144:145], v[80:81], v[118:119], v[144:145] op_sel_hi:[1,0,1]\n"
    "v_pk_fma_f32 v[146:147], v[82:83], v[118:119], v[146:147] op_sel_hi:[1,0,1]\n"
    "v_cvt_pk_bf16_f32 v8, v144, v145\n"
    "v_cvt_pk_bf16_f32 v9, v146, v147\n"
    "v_pk_mul_f32 v[148:149], v[148:149], v[116:117] op_sel:[0,1]\n"
    "v_pk_mul_f32 v[150:151], v[150:151], v[116:117] op_sel:[0,1]\n"
    "v_pk_fma_f32 v[148:149], v[84:85], v[118:119], v[148:149] op_sel:[0,1,0]\n"
    "v_pk_fma_f32 v[150:151], v[86:87], v[118:119], v[150:151] op_sel:[0,1,0]\n"
    "v_cvt_pk_bf16_f32 v12, v148, v149\n"
    "v_cvt_pk_bf16_f32 v13, v150, v151\n"
    "buffer_load_dwordx2 a[98:99], v120, s[44:47], s28 offen offset:2048 sc1\n"
    "buffer_load_dwordx2 a[102:103], v120, s[44:47], s28 offen offset:2056 sc1\n"
    "s_add_u32 s28, s28, 139264\n"
    "ds_write_b128 v115, a[200:203] offset:8192\n"
    "ds_write_b128 v115, a[204:207] offset:9216\n"
    "s_waitcnt lgkmcnt(12)\n"
    "v_pk_mul_f32 v[152:153], v[152:153], v[116:117] op_sel_hi:[1,0]\n"
    "v_pk_mul_f32 v[154:155], v[154:155], v[116:117] op_sel_hi:[1,0]\n"
    "v_pk_fma_f32 v[152:153], v[88:89], v[118:119], v[152:153] op_sel_hi:[1,0,1]\n"
    "v_pk_fma_f32 v[154:155], v[90:91], v[118:119], v[154:155] op_sel_hi:[1,0,1]\n"
    "v_cvt_pk_bf16_f32 v10, v152, v153\n"
    "v_cvt_pk_bf16_f32 v11, v154, v155\n"
    "v_pk_mul_f32 v[156:157], v[156:157], v[116:117] op_sel:[0,1]\n"
    "v_pk_mul_f32 v[158:159], v[158:159], v[116:117] op_sel:[0,1]\n"
    "v_pk_fma_f32 v[156:157], v[92:93], v[118:119], v[156:157] op_sel:[0,1,0]\n"
    "v_pk_fma_f32 v[158:159], v[94:95], v[118:119], v[158:159] op_sel:[0,1,0]\n"
    "v_cvt_pk_bf16_f32 v14, v156, v157\n"
    "v_cvt_pk_bf16_f32 v15, v158, v159\n"
    "buffer_load_dwordx4 a[48:51], v120, s[44:47], s28 offen offset:0 sc1\n"
    "buffer_load_dwordx4 a[52:55], v120, s[44:47], s28 offen offset:1024 sc1\n"
    "buffer_load_dwordx4 a[56:59], v120, s[44:47], s28 offen offset:2048 sc1\n"
    "ds_write_b128 v115, a[208:211] offset:16384\n"
    "ds_write_b128 v115, a[212:215] offset:17408\n"
    "s_waitcnt lgkmcnt(12)\n"
    "v_pk_mul_f32 v[160:161], v[160:161], v[116:117] op_sel_hi:[1,0]\n"
    "v_pk_mul_f32 v[162:163], v[162:163], v[116:117] op_sel_hi:[1,0]\n"
    "v_pk_fma_f32 v[160:161], v[96:97], v[118:119], v[160:161] op_sel_hi:[1,0,1]\n"
    "v_pk_fma_f32 v[162:163], v[98:99], v[118:119], v[162:163] op_sel_hi:[1,0,1]\n"
    "v_cvt_pk_bf16_f32 v16, v160, v161\n"
    "v_cvt_pk_bf16_f32 v17, v162, v163\n"
    "v_pk_mul_f32 v[164:165], v[164:165], v[116:117] op_sel:[0,1]\n"
    "v_pk_mul_f32 v[166:167], v[166:167], v[116:117] op_sel:[0,1]\n"
    "v_pk_fma_f32 v[164:165], v[100:101], v[118:119], v[164:165] op_sel:[0,1,0]\n"
    "v_pk_fma_f32 v[166:167], v[102:103], v[118:119], v[166:167] op_sel:[0,1,0]\n"
    "v_cvt_pk_bf16_f32 v20, v164, v165\n"
    "v_cvt_pk_bf16_f32 v21, v166, v167\n"
    "buffer_load_dwordx4 a[60:63], v120, s[44:47], s28 offen offset:3072 sc1\n"
    "s_add_u32 s28, s28, 4096\n"
    "buffer_load_dwordx4 a[64:67], v120, s[44:47], s28 offen offset:0 sc1\n"
    "s_waitcnt lgkmcnt(10)\n"
    "v_pk_mul_f32 v[168:169], v[168:169], v[116:117] op_sel_hi:[1,0]\n"
    "v_pk_mul_f32 v[170:171], v[170:171], v[116:117] op_sel_hi:[1,0]\n"
    "v_pk_fma_f32 v[168:169], v[104:105], v[118:119], v[168:169] op_sel_hi:[1,0,1]\n"
    "v_pk_fma_f32 v[170:171], v[106:107], v[118:119], v[170:171] op_sel_hi:[1,0,1]\n"
    "v_cvt_pk_bf16_f32 v18, v168, v169\n"
    "v_cvt_pk_bf16_f32 v19, v170, v171\n"
    "v_pk_mul_f32 v[172:173], v[172:173], v[116:117] op_sel:[0,1]\n"
    "v_pk_mul_f32 v[174:175], v[174:175], v[116:117] op_sel:[0,1]\n"
    "v_pk_fma_f32 v[172:173], v[108:109], v[118:119], v[172:173] op_sel:[0,1,0]\n"
    "v_pk_fma_f32 v[174:175], v[110:111], v[118:119], v[174:175] op_sel:[0,1,0]\n"
    "v_cvt_pk_bf16_f32 v22, v172, v173\n"
    "v_cvt_pk_bf16_f32 v23, v174, v175\n"
    "buffer_load_dwordx4 a[68:71], v120, s[44:47], s28 offen offset:1024 sc1\n"
    "buffer_load_dwordx2 a[104:105], v120, s[44:47], s28 offen offset:2048 sc1\n"
    "buffer_load_dwordx2 a[108:109], v120, s[44:47], s28 offen offset:2056 sc1\n"
    "s_waitcnt lgkmcnt(0)\n"
    "s_barrier\n"
    "ds_read_b128 v[64:67], v121 offset:32768\n"
    "ds_read_b128 v[68:71], v121 offset:33792\n"
    "ds_read_b128 v[72:75], v121 offset:40960\n"
    "ds_read_b128 v[76:79], v121 offset:41984\n"
    "ds_read_b128 v[80:83], v122 offset:0\n"
    "ds_read_b128 v[84:87], v122 offset:1024\n"
    "ds_read_b128 v[88:91], v122 offset:8192\n"
    "ds_read_b128 v[92:95], v122 offset:9216\n"
    "ds_read_b128 v[96:99], v122 offset:16384\n"
    "ds_read_b128 v[100:103], v122 offset:17408\n"
    "ds_write_b128 v115, a[216:219] offset:24576\n"
    "ds_write_b128 v115, a[220:223] offset:25600\n"
    "s_waitcnt lgkmcnt(10)\n"
    "v_pk_mul_f32 v[176:177], v[176:177], v[116:117] op_sel_hi:[1,0]\n"
    "v_pk_mul_f32 v[178:179], v[178:179], v[116:117] op_sel_hi:[1,0]\n"
    "v_pk_fma_f32 v[176:177], v[64:65], v[118:119], v[176:177] op_sel_hi:[1,0,1]\n"
    "v_pk_fma_f32 v[178:179], v[66:67], v[118:119], v[178:179] op_sel_hi:[1,0,1]\n"
    "v_cvt_pk_bf16_f32 v24, v176, v177\n"
    "v_cvt_pk_bf16_f32 v25, v178, v179\n"
    "v_pk_mul_f32 v[180:181], v[180:181], v[116:117] op_sel:[0,1]\n"
    "v_pk_mul_f32 v[182:183], v[182:183], v[116:117] op_sel:[0,1]\n"
    "v_pk_fma_f32 v[180:181], v[68:69], v[118:119], v[180:181] op_sel:[0,1,0]\n"
    "v_pk_fma_f32 v[182:183], v[70:71], v[118:119], v[182:183] op_sel:[0,1,0]\n"
    "v_cvt_pk_bf16_f32 v28, v180, v181\n"
    "v_cvt_pk_bf16_f32 v29, v182, v183\n"
    "ds_write_b128 v115, a[224:227] offset:32768\n"
    "ds_write_b128 v115, a[228:231] offset:33792\n"
    "s_waitcnt lgkmcnt(10)\n"
    "v_pk_mul_f32 v[184:185], v[184:185], v[116:117] op_sel_hi:[1,0]\n"
    "v_pk_mul_f32 v[186:187], v[186:187], v[116:117] op_sel_hi:[1,0]\n"
    "v_pk_fma_f32 v[184:185], v[72:73], v[118:119], v[184:185] op_sel_hi:[1,0,1]\n"
    "v_pk_fma_f32 v[186:187], v[74:75], v[118:119], v[186:187] op_sel_hi:[1,0,1]\n"
    "v_cvt_pk_bf16_f32 v26, v184, v185\n"
    "v_cvt_pk_bf16_f32 v27, v186, v187\n"
    "v_pk_mul_f32 v[188:189], v[188:189], v[116:117] op_sel:[0,1]\n"
    "v_pk_mul_f32 v[190:191], v[190:191], v[116:117] op_sel:[0,1]\n"
    "v_pk_fma_f32 v[188:189], v[76:77], v[118:119], v[188:189] op_sel:[0,1,0]\n"
    "v_pk_fma_f32 v[190:191], v[78:79], v[118:119], v[190:191] op_sel:[0,1,0]\n"
    "v_cvt_pk_bf16_f32 v30, v188, v189\n"
    "v_cvt_pk_bf16_f32 v31, v190, v191\n"
    "ds_write_b128 v124, a[232:235] offset:0\n"
    "ds_write_b128 v124, a[236:239] offset:1024\n"
    "ds_write_b128 v124, a[240:243] offset:8192\n"
    "ds_write_b128 v124, a[244:247] offset:9216\n"
    "ds_write_b128 v124, a[248:251] offset:16384\n"
    "ds_write_b128 v124, a[252:255] offset:17408\n"
    "v_and_b32 v112, 3, v126\n"
    "v_bfe_u32 v113, v126, 2, 2\n"
    "v_lshrrev_b32 v114, 4, v126\n"
    "v_mul_u32_u24 v124, 1056, v114\n"
    "v_lshl_add_u32 v124, v113, 6, v124\n"
    "v_lshl_add_u32 v124, v112, 3, v124\n"
    "v_mov_b32 v112, v121\n"
    "v_mov_b32 v113, v122\n"
    "s_mul_i32 s86, s81, 28672\n"
    "v_add_u32 v122, s86, v127\n"
    "s_waitcnt lgkmcnt(0)\n"
    "s_waitcnt vmcnt(36)\n"
    "s_barrier\n"
    "v_add_u32 v121, 0, v124\n"
    "ds_read_b64_tr_b16 a[192:193], v121 offset:0\n"
    "ds_read_b64_tr_b16 a[194:195], v121 offset:256\n"
    "ds_read_b64_tr_b16 a[196:197], v121 offset:4224\n"
    "ds_read_b64_tr_b16 a[198:199], v121 offset:4480\n"
    "s_waitcnt lgkmcnt(0)\n"
    "v_mfma_f32_16x16x32_bf16 v[128:131], a[192:195], v[0:3], 0\n"
    "s_mov_b32 s88, 0\n"
    "s_add_u32 m0, s89, 100992\n"
    "ds_read_b64_tr_b16 a[200:201], v121 offset:8448\n"
    "ds_read_b64_tr_b16 a[202:203], v121 offset:8704\n"
    "v_mfma_f32_16x16x32_bf16 v[128:131], a[196:199], v[8:11], v[128:131]\n"
    "ds_read_b64_tr_b16 a[204:205], v121 offset:12672\n"
    "ds_read_b64_tr_b16 a[206:207], v121 offset:12928\n"
    "v_mfma_f32_16x16x32_bf16 v[132:135], a[192:195], v[4:7], 0\n"
    "ds_read_b64_tr_b16 a[208:209], v121 offset:32\n"
    "ds_read_b64_tr_b16 a[210:211], v121 offset:288\n"
    "v_mfma_f32_16x16x32_bf16 v[132:135], a[196:199], v[12:15], v[132:135]\n"
    "s_waitcnt lgkmcnt(2)\n"
    "ds_read_b64_tr_b16 a[212:213], v121 offset:4256\n"
    "ds_read_b64_tr_b16 a[214:215], v121 offset:4512\n"
    "v_mfma_f32_16x16x32_bf16 v[128:131], a[200:203], v[16:19], v[128:131]\n"
    "ds_read_b64_tr_b16 a[216:217], v121 offset:8480\n"
    "ds_read_b64_tr_b16 a[218:219], v121 offset:8736\n"
    "v_mfma_f32_16x16x32_bf16 v[128:131], a[204:207], v[24:27], v[128:131]\n"
    "buffer_load_dwordx4 v125, s[56:59], s88 offen offset:384 lds\n"
    "ds_read_b64_tr_b16 a[220:221], v121 offset:12704\n"
    "v_mfma_f32_16x16x32_bf16 v[132:135], a[200:203], v[20:23], v[132:135]\n"
    "s_add_u32 s88, s88, 28672\n"
    "ds_read_b64_tr_b16 a[222:223], v121 offset:12960\n"
    "ds_read_b64_tr_b16 a[224:225], v121 offset:512\n"
    "v_mfma_f32_16x16x32_bf16 v[132:135], a[204:207], v[28:31], v[132:135]\n"
    "s_waitcnt lgkmcnt(5)\n"
    "ds_read_b64_tr_b16 a[226:227], v121 offset:768\n"
    "ds_read_b64_tr_b16 a[228:229], v121 offset:4736\n"
    "v_mfma_f32_16x16x32_bf16 v[136:139], a[208:211], v[0:3], 0\n"
    "s_add_u32 m0, s89, 105216\n"
    "ds_read_b64_tr_b16 a[230:231], v121 offset:4992\n"
    "buffer_load_dwordx4 v125, s[56:59], s88 offen offset:384 lds\n"
    "v_mfma_f32_16x16x32_bf16 v[136:139], a[212:215], v[8:11], v[136:139]\n"
    "s_add_u32 s88, s88, 28672\n"
    "ds_read_b64_tr_b16 a[232:233], v121 offset:8960\n"
    "ds_read_b64_tr_b16 a[234:235], v121 offset:9216\n"
    "v_mfma_f32_16x16x32_bf16 v[140:143], a[208:211], v[4:7], 0\n"
    "s_add_u32 m0, s89, 109440\n"
    "ds_read_b64_tr_b16 a[236:237], v121 offset:13184\n"
    "ds_read_b64_tr_b16 a[238:239], v121 offset:13440\n"
    "v_mfma_f32_16x16x32_bf16 v[140:143], a[212:215], v[12:15], v[140:143]\n"
    "s_waitcnt lgkmcnt(8)\n"
    "buffer_load_dwordx4 v125, s[56:59], s88 offen offset:384 lds\n"
    "ds_read_b64_tr_b16 a[240:241], v121 offset:544\n"
    "v_mfma_f32_16x16x32_bf16 v[136:139], a[216:219], v[16:19], v[136:139]\n"
    "s_add_u32 s88, s88, 28672\n"
    "ds_read_b64_tr_b16 a[242:243], v121 offset:800\n"
    "ds_read_b64_tr_b16 a[244:245], v121 offset:4768\n"
    "v_mfma_f32_16x16x32_bf16 v[136:139], a[220:223], v[24:27], v[136:139]\n"
    "ds_read_b64_tr_b16 a[246:247], v121 offset:5024\n"
    "ds_read_b128 v[64:67], v113 offset:24576\n"
    "v_mfma_f32_16x16x32_bf16 v[140:143], a[216:219], v[20:23], v[140:143]\n"
    "s_add_u32 m0, s89, 113664\n"
    "ds_read_b128 v[68:71], v113 offset:25600\n"
    "ds_read_b64_tr_b16 a[248:249], v121 offset:8992\n"
    "v_mfma_f32_16x16x32_bf16 v[140:143], a[220:223], v[28:31], v[140:143]\n"
    "s_waitcnt lgkmcnt(11)\n"
    "buffer_load_dwordx4 v125, s[56:59], s88 offen offset:384 lds\n"
    "ds_read_b64_tr_b16 a[250:251], v121 offset:9248\n"
    "v_mfma_f32_16x16x32_bf16 v[144:147], a[224:227], v[0:3], 0\n"
    "s_waitcnt lgkmcnt(2)\n"
    "ds_read_b64_tr_b16 a[252:253], v121 offset:13216\n"
    "v_mul_f32 v192, v192, v116\n"
    "v_mfma_f32_16x16x32_bf16 v[144:147], a[228:231], v[8:11], v[144:147]\n"
    "ds_read_b64_tr_b16 a[254:255], v121 offset:13472\n"
    "v_fmac_f32 v192, v80, v118\n"
    "v_mfma_f32_16x16x32_bf16 v[148:151], a[224:227], v[4:7], 0\n"
    "s_waitcnt vmcnt(36)\n"
    "v_mul_f32 v193, v193, v116\n"
    "v_fmac_f32 v193, v81, v118\n"
    "v_mfma_f32_16x16x32_bf16 v[148:151], a[228:231], v[12:15], v[148:151]\n"
    "s_waitcnt lgkmcnt(10)\n"
    "v_mul_f32 v194, v194, v116\n"
    "v_fmac_f32 v194, v82, v118\n"
    "v_mfma_f32_16x16x32_bf16 v[144:147], a[232:235], v[16:19], v[144:147]\n"
    "s_mov_b32 s88, 114688\n"
    "s_add_u32 m0, s89, 117888\n"
    "v_mul_f32 v195, v195, v116\n"
    "v_fmac_f32 v195, v83, v118\n"
    "v_mfma_f32_16x16x32_bf16 v[144:147], a[236:239], v[24:27], v[144:147]\n"
    "v_mul_f32 v196, v196, v117\n"
    "v_fmac_f32 v196, v84, v119\n"
    "v_mfma_f32_16x16x32_bf16 v[148:151], a[232:235], v[20:23], v[148:151]\n"
    "v_mul_f32 v197, v197, v117\n"
    "v_fmac_f32 v197, v85, v119\n"
    "v_mfma_f32_16x16x32_bf16 v[148:151], a[236:239], v[28:31], v[148:151]\n"
    "s_waitcnt lgkmcnt(6)\n"
    "v_mul_f32 v198, v198, v117\n"
    "v_fmac_f32 v198, v86, v119\n"
    "v_mul_f32 v199, v199, v117\n"
    "v_fmac_f32 v199, v87, v119\n"
    "v_mul_f32 v200, v200, v116\n"
    "v_fmac_f32 v200, v88, v118\n"
    "v_mul_f32 v201, v201, v116\n"
    "v_fmac_f32 v201, v89, v118\n"
    "v_mul_f32 v202, v202, v116\n"
    "v_fmac_f32 v202, v90, v118\n"
    "v_mul_f32 v203, v203, v116\n"
    "v_fmac_f32 v203, v91, v118\n"
    "v_mul_f32 v204, v204, v117\n"
    "v_fmac_f32 v204, v92, v119\n"
    "v_mul_f32 v205, v205, v117\n"
    "v_fmac_f32 v205, v93, v119\n"
    "v_mul_f32 v206, v206, v117\n"
    "v_fmac_f32 v206, v94, v119\n"
    "v_mul_f32 v207, v207, v117\n"
    "v_fmac_f32 v207, v95, v119\n"
    "ds_read_b128 v[72:75], v113 offset:32768\n"
    "ds_read_b128 v[76:79], v113 offset:33792\n"
    "ds_read_b128 v[104:107], v112 offset:0\n"
    "ds_read_b128 v[108:111], v112 offset:1024\n"
    "ds_read_b128 v[80:83], v112 offset:8192\n"
    "ds_read_b128 v[84:87], v112 offset:9216\n"
    "ds_read_b128 v[88:91], v112 offset:16384\n"
    "ds_read_b128 v[92:95], v112 offset:17408\n"
    "v_mfma_f32_16x16x32_bf16 v[152:155], a[240:243], v[0:3], 0\n"
    "s_waitcnt lgkmcnt(6)\n"
    "buffer_load_dwordx4 v125, s[56:59], s88 offen offset:384 lds\n"
    "v_cvt_pk_bf16_f32 v32, v192, v193\n"
    "s_add_u32 s88, s88, 28672\n"
    "s_add_u32 m0, s89, 122112\n"
    "v_cvt_pk_bf16_f32 v33, v194, v195\n"
    "v_cvt_pk_bf16_f32 v36, v196, v197\n"
    "v_cvt_pk_bf16_f32 v37, v198, v199\n"
    "v_cvt_pk_bf16_f32 v34, v200, v201\n"
    "v_cvt_pk_bf16_f32 v35, v202, v203\n"
    "v_cvt_pk_bf16_f32 v38, v204, v205\n"
    "v_cvt_pk_bf16_f32 v39, v206, v207\n"
    "v_mul_f32 v208, v208, v116\n"
    "v_fmac_f32 v208, v96, v118\n"
    "v_mul_f32 v209, v209, v116\n"
    "v_fmac_f32 v209, v97, v118\n"
    "v_mul_f32 v210, v210, v116\n"
    "v_fmac_f32 v210, v98, v118\n"
    "v_mul_f32 v211, v211, v116\n"
    "v_fmac_f32 v211, v99, v118\n"
    "v_cvt_pk_bf16_f32 v40, v208, v209\n"
    "v_cvt_pk_bf16_f32 v41, v210, v211\n"
    "v_mul_f32 v212, v212, v117\n"
    "v_fmac_f32 v212, v100, v119\n"
    "v_mul_f32 v213, v213, v117\n"
    "v_fmac_f32 v213, v101, v119\n"
    "v_mul_f32 v214, v214, v117\n"
    "v_fmac_f32 v214, v102, v119\n"
    "v_mul_f32 v215, v215, v117\n"
    "v_fmac_f32 v215, v103, v119\n"
    "v_cvt_pk_bf16_f32 v44, v212, v213\n"
    "v_cvt_pk_bf16_f32 v45, v214, v215\n"
    "v_mul_f32 v216, v216, v116\n"
    "v_fmac_f32 v216, v64, v118\n"
    "v_mul_f32 v217, v217, v116\n"
    "v_fmac_f32 v217, v65, v118\n"
    "v_mul_f32 v218, v218, v116\n"
    "v_fmac_f32 v218, v66, v118\n"
    "v_mul_f32 v219, v219, v116\n"
    "v_fmac_f32 v219, v67, v118\n"
    "v_cvt_pk_bf16_f32 v42, v216, v217\n"
    "v_cvt_pk_bf16_f32 v43, v218, v219\n"
    "v_mul_f32 v220, v220, v117\n"
    "v_fmac_f32 v220, v68, v119\n"
    "v_mul_f32 v221, v221, v117\n"
    "v_fmac_f32 v221, v69, v119\n"
    "v_mul_f32 v222, v222, v117\n"
    "v_fmac_f32 v222, v70, v119\n"
    "v_mul_f32 v223, v223, v117\n"
    "v_fmac_f32 v223, v71, v119\n"
    "v_cvt_pk_bf16_f32 v46, v220, v221\n"
    "v_cvt_pk_bf16_f32 v47, v222, v223\n"
    "s_waitcnt lgkmcnt(4)\n"
    "s_waitcnt lgkmcnt(2)\n"
    "s_waitcnt lgkmcnt(0)\n"
    "s_barrier\n"
    "v_add_u32 v121, 16896, v124\n"
    "ds_read_b64_tr_b16 a[192:193], v121 offset:0\n"
    "v_mfma_f32_16x16x32_bf16 v[152:155], a[244:247], v[8:11], v[152:155]\n"
    "ds_read_b64_tr_b16 a[194:195], v121 offset:256\n"
    "ds_read_b64_tr_b16 a[196:197], v121 offset:4224\n"
    "v_mfma_f32_16x16x32_bf16 v[156:159], a[240:243], v[4:7], 0\n"
    "ds_read_b64_tr_b16 a[198:199], v121 offset:4480\n"
    "ds_read_b64_tr_b16 a[200:201], v121 offset:8448\n"
    "v_mfma_f32_16x16x32_bf16 v[156:159], a[244:247], v[12:15], v[156:159]\n"
    "s_waitcnt lgkmcnt(13)\n"
    "ds_read_b64_tr_b16 a[202:203], v121 offset:8704\n"
    "ds_read_b64_tr_b16 a[204:205], v121 offset:12672\n"
    "v_mfma_f32_16x16x32_bf16 v[152:155], a[248:251], v[16:19], v[152:155]\n"
    "buffer_load_dwordx4 v125, s[56:59], s88 offen offset:384 lds\n"
    "ds_read_b64_tr_b16 a[206:207], v121 offset:12928\n"
    "v_mfma_f32_16x16x32_bf16 v[152:155], a[252:255], v[24:27], v[152:155]\n"
    "s_add_u32 s88, s88, 28672\n"
    "ds_read_b64_tr_b16 a[208:209], v121 offset:32\n"
    "ds_read_b64_tr_b16 a[210:211], v121 offset:288\n"
    "v_mfma_f32_16x16x32_bf16 v[156:159], a[248:251], v[20:23], v[156:159]\n"
    "s_add_u32 m0, s89, 126336\n"
    "v_mul_f32 v224, v224, v116\n"
    "v_fmac_f32 v224, v72, v118\n"
    "v_mfma_f32_16x16x32_bf16 v[156:159], a[252:255], v[28:31], v[156:159]\n"
    "s_waitcnt lgkmcnt(6)\n"
    "v_mul_f32 v225, v225, v116\n"
    "v_fmac_f32 v225, v73, v118\n"
    "v_mfma_f32_16x16x32_bf16 v[128:131], a[192:195], v[32:35], v[128:131]\n"
    "v_mul_f32 v226, v226, v116\n"
    "v_fmac_f32 v226, v74, v118\n"
    "v_mfma_f32_16x16x32_bf16 v[128:131], a[196:199], v[40:43], v[128:131]\n"
    "v_mul_f32 v227, v227, v116\n"
    "v_fmac_f32 v227, v75, v118\n"
    "v_cvt_pk_bf16_f32 v48, v224, v225\n"
    "v_cvt_pk_bf16_f32 v49, v226, v227\n"
    "v_mul_f32 v228, v228, v117\n"
    "v_fmac_f32 v228, v76, v119\n"
    "v_mul_f32 v229, v229, v117\n"
    "v_fmac_f32 v229, v77, v119\n"
    "v_mul_f32 v230, v230, v117\n"
    "v_fmac_f32 v230, v78, v119\n"
    "v_mul_f32 v231, v231, v117\n"
    "v_fmac_f32 v231, v79, v119\n"
    "v_cvt_pk_bf16_f32 v52, v228, v229\n"
    "v_cvt_pk_bf16_f32 v53, v230, v231\n"
    "v_mul_f32 v232, v232, v116\n"
    "v_fmac_f32 v232, v104, v118\n"
    "v_mul_f32 v233, v233, v116\n"
    "v_fmac_f32 v233, v105, v118\n"
    "v_mul_f32 v234, v234, v116\n"
    "v_fmac_f32 v234, v106, v118\n"
    "v_mul_f32 v235, v235, v116\n"
    "v_fmac_f32 v235, v107, v118\n"
    "v_cvt_pk_bf16_f32 v50, v232, v233\n"
    "v_cvt_pk_bf16_f32 v51, v234, v235\n"
    "v_mul_f32 v236, v236, v117\n"
    "v_fmac_f32 v236, v108, v119\n"
    "v_mul_f32 v237, v237, v117\n"
    "v_fmac_f32 v237, v109, v119\n"
    "v_mul_f32 v238, v238, v117\n"
    "v_fmac_f32 v238, v110, v119\n"
    "v_mul_f32 v239, v239, v117\n"
    "v_fmac_f32 v239, v111, v119\n"
    "v_cvt_pk_bf16_f32 v54, v236, v237\n"
    "v_cvt_pk_bf16_f32 v55, v238, v239\n"
    "v_mul_f32 v240, v240, v116\n"
    "v_fmac_f32 v240, v80, v118\n"
    "v_mul_f32 v241, v241, v116\n"
    "v_fmac_f32 v241, v81, v118\n"
    "v_mul_f32 v242, v242, v116\n"
    "v_fmac_f32 v242, v82, v118\n"
    "v_mul_f32 v243, v243, v116\n"
    "v_fmac_f32 v243, v83, v118\n"
    "v_cvt_pk_bf16_f32 v56, v240, v241\n"
    "v_cvt_pk_bf16_f32 v57, v242, v243\n"
    "v_mul_f32 v244, v244, v117\n"
    "v_fmac_f32 v244, v84, v119\n"
    "v_mul_f32 v245, v245, v117\n"
    "v_fmac_f32 v245, v85, v119\n"
    "v_mul_f32 v246, v246, v117\n"
    "v_fmac_f32 v246, v86, v119\n"
    "v_mul_f32 v247, v247, v117\n"
    "v_fmac_f32 v247, v87, v119\n"
    "v_cvt_pk_bf16_f32 v60, v244, v245\n"
    "v_cvt_pk_bf16_f32 v61, v246, v247\n"
    "v_mul_f32 v248, v248, v116\n"
    "v_fmac_f32 v248, v88, v118\n"
    "v_mul_f32 v249, v249, v116\n"
    "v_fmac_f32 v249, v89, v118\n"
    "v_mul_f32 v250, v250, v116\n"
    "v_fmac_f32 v250, v90, v118\n"
    "v_mul_f32 v251, v251, v116\n"
    "v_fmac_f32 v251, v91, v118\n"
    "v_cvt_pk_bf16_f32 v58, v248, v249\n"
    "v_cvt_pk_bf16_f32 v59, v250, v251\n"
    "v_mul_f32 v252, v252, v117\n"
    "v_fmac_f32 v252, v92, v119\n"
    "v_mul_f32 v253, v253, v117\n"
    "v_fmac_f32 v253, v93, v119\n"
    "v_mul_f32 v254, v254, v117\n"
    "v_fmac_f32 v254, v94, v119\n"
    "v_mul_f32 v255, v255, v117\n"
    "v_fmac_f32 v255, v95, v119\n"
    "v_cvt_pk_bf16_f32 v62, v252, v253\n"
    "v_cvt_pk_bf16_f32 v63, v254, v255\n"
    "v_mfma_f32_16x16x32_bf16 v[132:135], a[192:195], v[36:39], v[132:135]\n"
    "ds_read_b64_tr_b16 a[212:213], v121 offset:4256\n"
    "ds_read_b64_tr_b16 a[214:215], v121 offset:4512\n"
    "v_mfma_f32_16x16x32_bf16 v[132:135], a[196:199], v[44:47], v[132:135]\n"
    "s_waitcnt lgkmcnt(4)\n"
    "buffer_load_dwordx4 v125, s[56:59], s88 offen offset:384 lds\n"
    "ds_read_b64_tr_b16 a[216:217], v121 offset:8480\n"
    "v_mfma_f32_16x16x32_bf16 v[128:131], a[200:203], v[48:51], v[128:131]\n"
    "s_add_u32 s88, s88, 28672\n"
    "ds_read_b64_tr_b16 a[218:219], v121 offset:8736\n"
    "ds_read_b64_tr_b16 a[220:221], v121 offset:12704\n"
    "v_mfma_f32_16x16x32_bf16 v[128:131], a[204:207], v[56:59], v[128:131]\n"
    "s_add_u32 m0, s89, 130560\n"
    "ds_read_b64_tr_b16 a[222:223], v121 offset:12960\n"
    "ds_read_b64_tr_b16 a[224:225], v121 offset:512\n"
    "v_mfma_f32_16x16x32_bf16 v[132:135], a[200:203], v[52:55], v[132:135]\n"
    "s_cmp_eq_u32 s79, 3\n"
    "s_cselect_b32 s87, 0x80000000, 0\n"
    "buffer_load_dwordx4 v125, s[56:59], s88 offen offset:384 lds\n"
    "ds_read_b64_tr_b16 a[226:227], v121 offset:768\n"
    "v_mfma_f32_16x16x32_bf16 v[132:135], a[204:207], v[60:63], v[132:135]\n"
    "s_waitcnt lgkmcnt(6)\n"
    "ds_read_b64_tr_b16 a[228:229], v121 offset:4736\n"
    "ds_read_b64_tr_b16 a[230:231], v121 offset:4992\n"
    "v_mfma_f32_16x16x32_bf16 v[136:139], a[208:211], v[32:35], v[136:139]\n"
    "s_mov_b32 s88, 0\n"
    "s_add_u32 m0, s89, 67328\n"
    "ds_read_b64_tr_b16 a[232:233], v121 offset:8960\n"
    "ds_read_b64_tr_b16 a[234:235], v121 offset:9216\n"
    "v_mfma_f32_16x16x32_bf16 v[136:139], a[212:215], v[40:43], v[136:139]\n"
    "s_add_u32 s87, s87, 128\n"
    "buffer_load_dwordx4 v125, s[56:59], s88 offen offset:256 lds\n"
    "ds_read_b64_tr_b16 a[236:237], v121 offset:13184\n"
    "v_mfma_f32_16x16x32_bf16 v[140:143], a[208:211], v[36:39], v[140:143]\n"
    "s_add_u32 s88, s88, 28672\n"
    "ds_read_b64_tr_b16 a[238:239], v121 offset:13440\n"
    "ds_read_b64_tr_b16 a[240:241], v121 offset:544\n"
    "v_mfma_f32_16x16x32_bf16 v[140:143], a[212:215], v[44:47], v[140:143]\n"
    "s_waitcnt lgkmcnt(9)\n"
    "ds_read_b64_tr_b16 a[242:243], v121 offset:800\n"
    "v_cvt_pk_bf16_f32 v192, v128, v129\n"
    "v_mfma_f32_16x16x32_bf16 v[136:139], a[216:219], v[48:51], v[136:139]\n"
    "s_add_u32 m0, s89, 71552\n"
    "ds_read_b64_tr_b16 a[244:245], v121 offset:4768\n"
    "v_cvt_pk_bf16_f32 v193, v130, v131\n"
    "v_mfma_f32_16x16x32_bf16 v[136:139], a[220:223], v[56:59], v[136:139]\n"
    "s_mul_i32 s84, s81, 397824\n"
    "v_cvt_pk_bf16_f32 v194, v132, v133\n"
    "v_cvt_pk_bf16_f32 v195, v134, v135\n"
    "v_mfma_f32_16x16x32_bf16 v[140:143], a[216:219], v[52:55], v[140:143]\n"
    "s_bitcmp1_b32 s78, 0\n"
    "s_cselect_b32 s99, 0x80000000, s92\n"
    "s_cbranch_scc0 Lo0%=\n"
    "ds_write_b128 v123, v[192:195] offset:0\n"
    "Lo0%=:\n"
    "buffer_load_dwordx4 v125, s[56:59], s88 offen offset:256 lds\n"
    "ds_read_b64_tr_b16 a[246:247], v121 offset:5024\n"
    "v_mfma_f32_16x16x32_bf16 v[140:143], a[220:223], v[60:63], v[140:143]\n"
    "s_waitcnt lgkmcnt(8)\n"
    "ds_read_b64_tr_b16 a[248:249], v121 offset:8992\n"
    "buffer_store_dwordx4 v[192:195], v122, s[60:63], s99 offen offset:0 sc1\n"
    "v_mfma_f32_16x16x32_bf16 v[144:147], a[224:227], v[32:35], v[144:147]\n"
    "s_add_u32 s88, s88, 28672\n"
    "ds_read_b64_tr_b16 a[250:251], v121 offset:9248\n"
    "ds_read_b64_tr_b16 a[252:253], v121 offset:13216\n"
    "v_mfma_f32_16x16x32_bf16 v[144:147], a[228:231], v[40:43], v[144:147]\n"
    "s_add_u32 m0, s89, 75776\n"
    "ds_read_b64_tr_b16 a[254:255], v121 offset:13472\n"
    "buffer_load_dwordx4 v125, s[56:59], s88 offen offset:256 lds\n"
    "v_mfma_f32_16x16x32_bf16 v[148:151], a[224:227], v[36:39], v[148:151]\n"
    "s_waitcnt vmcnt(40)\n"
    "v_cvt_pk_bf16_f32 v196, v136, v137\n"
    "v_cvt_pk_bf16_f32 v197, v138, v139\n"
    "v_mfma_f32_16x16x32_bf16 v[148:151], a[228:231], v[44:47], v[148:151]\n"
    "s_waitcnt lgkmcnt(8)\n"
    "v_cvt_pk_bf16_f32 v198, v140, v141\n"
    "v_cvt_pk_bf16_f32 v199, v142, v143\n"
    "v_mfma_f32_16x16x32_bf16 v[144:147], a[232:235], v[48:51], v[144:147]\n"
    "s_barrier\n"
    "v_add_u32 v121, 33792, v124\n"
    "ds_read_b64_tr_b16 a[192:193], v121 offset:0\n"
    "v_mfma_f32_16x16x32_bf16 v[144:147], a[236:239], v[56:59], v[144:147]\n"
    "s_add_u32 s88, s88, 28672\n"
    "ds_read_b64_tr_b16 a[194:195], v121 offset:256\n"
    "ds_read_b64_tr_b16 a[196:197], v121 offset:4224\n"
    "v_mfma_f32_16x16x32_bf16 v[148:151], a[232:235], v[52:55], v[148:151]\n"
    "s_add_u32 m0, s89, 80000\n"
    "ds_read_b64_tr_b16 a[198:199], v121 offset:4480\n"
    "ds_read_b64_tr_b16 a[200:201], v121 offset:8448\n"
    "v_mfma_f32_16x16x32_bf16 v[148:151], a[236:239], v[60:63], v[148:151]\n"
    "s_waitcnt lgkmcnt(9)\n"
    "buffer_load_dwordx4 v125, s[56:59], s88 offen offset:256 lds\n"
    "ds_read_b64_tr_b16 a[202:203], v121 offset:8704\n"
    "v_mfma_f32_16x16x32_bf16 v[152:155], a[240:243], v[32:35], v[152:155]\n"
    "s_bitcmp1_b32 s78, 1\n"
    "s_cselect_b32 s99, 0x80000000, s92\n"
    "s_cbranch_scc0 Lo1%=\n"
    "ds_write_b128 v123, v[196:199] offset:1024\n"
    "Lo1%=:\n"
    "ds_read_b64_tr_b16 a[204:205], v121 offset:12672\n"
    "buffer_store_dwordx4 v[196:199], v122, s[60:63], s99 offen offset:1024 sc1\n"
    "v_mfma_f32_16x16x32_bf16 v[152:155], a[244:247], v[40:43], v[152:155]\n"
    "ds_read_b64_tr_b16 a[206:207], v121 offset:12928\n"
    "v_add_u32 v212, s84, v125\n"
    "v_mfma_f32_16x16x32_bf16 v[156:159], a[240:243], v[36:39], v[156:159]\n"
    "ds_read_b64_tr_b16 a[208:209], v121 offset:32\n"
    "v_add_u32 v213, s87, v212\n"
    "v_mfma_f32_16x16x32_bf16 v[156:159], a[244:247], v[44:47], v[156:159]\n"
    "s_waitcnt lgkmcnt(9)\n"
    "ds_read_b64_tr_b16 a[210:211], v121 offset:288\n"
    "v_bfe_u32 v208, v126, 4, 1\n"
    "v_mfma_f32_16x16x32_bf16 v[152:155], a[248:251], v[48:51], v[152:155]\n"
    "s_mov_b32 s88, 114688\n"
    "s_add_u32 m0, s89, 84224\n"
    "ds_read_b64_tr_b16 a[212:213], v121 offset:4256\n"
    "buffer_load_dwordx4 v125, s[56:59], s88 offen offset:256 lds\n"
    "v_mfma_f32_16x16x32_bf16 v[152:155], a[252:255], v[56:59], v[152:155]\n"
    "s_add_u32 s88, s88, 28672\n"
    "ds_read_b64_tr_b16 a[214:215], v121 offset:4512\n"
    "v_mul_u32_u24 v208, 86016, v208\n"
    "v_mfma_f32_16x16x32_bf16 v[156:159], a[248:251], v[52:55], v[156:159]\n"
    "ds_read_b64_tr_b16 a[216:217], v121 offset:8480\n"
    "v_add_u32 v214, v208, v212\n"
    "v_mfma_f32_16x16x32_bf16 v[156:159], a[252:255], v[60:63], v[156:159]\n"
    "s_waitcnt lgkmcnt(9)\n"
    "ds_read_b64_tr_b16 a[218:219], v121 offset:8736\n"
    "v_add_u32 v215, v208, v213\n"
    "v_mfma_f32_16x16x32_bf16 v[160:163], a[192:195], v[0:3], 0\n"
    "s_add_u32 m0, s89, 88448\n"
    "ds_read_b64_tr_b16 a[220:221], v121 offset:12704\n"
    "buffer_load_dwordx4 v125, s[56:59], s88 offen offset:256 lds\n"
    "v_mfma_f32_16x16x32_bf16 v[160:163], a[196:199], v[8:11], v[160:163]\n"
    "s_add_u32 s88, s88, 28672\n"
    "ds_read_b64_tr_b16 a[222:223], v121 offset:12960\n"
    "v_and_b32 v64, 15, v126\n"
    "v_mfma_f32_16x16x32_bf16 v[164:167], a[192:195], v[4:7], 0\n"
    "ds_read_b64_tr_b16 a[224:225], v121 offset:512\n"
    "v_cvt_pk_bf16_f32 v200, v144, v145\n"
    "v_mfma_f32_16x16x32_bf16 v[164:167], a[196:199], v[12:15], v[164:167]\n"
    "s_waitcnt lgkmcnt(9)\n"
    "ds_read_b64_tr_b16 a[226:227], v121 offset:768\n"
    "v_cvt_pk_bf16_f32 v201, v146, v147\n"
    "v_mfma_f32_16x16x32_bf16 v[160:163], a[200:203], v[16:19], v[160:163]\n"
    "ds_read_b64_tr_b16 a[228:229], v121 offset:4736\n"
    "v_cvt_pk_bf16_f32 v202, v148, v149\n"
    "v_mfma_f32_16x16x32_bf16 v[160:163], a[204:207], v[24:27], v[160:163]\n"
    "s_add_u32 m0, s89, 92672\n"
    "ds_read_b64_tr_b16 a[230:231], v121 offset:4992\n"
    "buffer_load_dwordx4 v125, s[56:59], s88 offen offset:256 lds\n"
    "v_mfma_f32_16x16x32_bf16 v[164:167], a[200:203], v[20:23], v[164:167]\n"
    "s_add_u32 s88, s88, 28672\n"
    "ds_read_b64_tr_b16 a[232:233], v121 offset:8960\n"
    "v_cvt_pk_bf16_f32 v203, v150, v151\n"
    "v_mfma_f32_16x16x32_bf16 v[164:167], a[204:207], v[28:31], v[164:167]\n"
    "s_waitcnt lgkmcnt(9)\n"
    "ds_read_b64_tr_b16 a[234:235], v121 offset:9216\n"
    "v_lshrrev_b32 v65, 4, v126\n"
    "v_mfma_f32_16x16x32_bf16 v[168:171], a[208:211], v[0:3], 0\n"
    "s_bitcmp1_b32 s78, 2\n"
    "s_cselect_b32 s99, 0x80000000, s92\n"
    "s_cbranch_scc0 Lo2%=\n"
    "ds_write_b128 v123, v[200:203] offset:2048\n"
    "Lo2%=:\n"
    "ds_read_b64_tr_b16 a[236:237], v121 offset:13184\n"
    "buffer_store_dwordx4 v[200:203], v122, s[60:63], s99 offen offset:2048 sc1\n"
    "v_mfma_f32_16x16x32_bf16 v[168:171], a[212:215], v[8:11], v[168:171]\n"
    "ds_read_b64_tr_b16 a[238:239], v121 offset:13440\n"
    "v_lshrrev_b32 v66, 2, v64\n"
    "v_mfma_f32_16x16x32_bf16 v[172:175], a[208:211], v[4:7], 0\n"
    "s_add_u32 m0, s89, 96896\n"
    "ds_read_b64_tr_b16 a[240:241], v121 offset:544\n"
    "buffer_load_dwordx4 v125, s[56:59], s88 offen offset:256 lds\n"
    "v_mfma_f32_16x16x32_bf16 v[172:175], a[212:215], v[12:15], v[172:175]\n"
    "s_waitcnt lgkmcnt(9)\n"
    "ds_read_b64_tr_b16 a[242:243], v121 offset:800\n"
    "v_and_b32 v67, 3, v64\n"
    "v_mfma_f32_16x16x32_bf16 v[168:171], a[216:219], v[16:19], v[168:171]\n"
    "ds_read_b64_tr_b16 a[244:245], v121 offset:4768\n"
    "v_cmp_eq_u32 vcc, v65, v66\n"
    "v_mfma_f32_16x16x32_bf16 v[168:171], a[220:223], v[24:27], v[168:171]\n"
    "ds_read_b64_tr_b16 a[246:247], v121 offset:5024\n"
    "v_mov_b32 v68, -1\n"
    "v_mfma_f32_16x16x32_bf16 v[172:175], a[216:219], v[20:23], v[172:175]\n"
    "ds_read_b64_tr_b16 a[248:249], v121 offset:8992\n"
    "v_cndmask_b32 v67, v68, v67, vcc\n"
    "v_mfma_f32_16x16x32_bf16 v[172:175], a[220:223], v[28:31], v[172:175]\n"
    "s_waitcnt lgkmcnt(9)\n"
    "ds_read_b64_tr_b16 a[250:251], v121 offset:9248\n"
    "v_mov_b32 v68, 0x3f80\n"
    "v_mfma_f32_16x16x32_bf16 v[176:179], a[224:227], v[0:3], 0\n"
    "s_mov_b32 s88, 0\n"
    "s_add_u32 m0, s89, 134656\n"
    "ds_read_b64_tr_b16 a[252:253], v121 offset:13216\n"
    "buffer_load_dwordx4 v125, s[56:59], s88 offen offset:512 lds\n"
    "v_mfma_f32_16x16x32_bf16 v[176:179], a[228:231], v[8:11], v[176:179]\n"
    "s_add_u32 s88, s88, 28672\n"
    "ds_read_b64_tr_b16 a[254:255], v121 offset:13472\n"
    "v_mov_b32 v69, 0x3f800000\n"
    "v_mfma_f32_16x16x32_bf16 v[180:183], a[224:227], v[4:7], 0\n"
    "s_waitcnt vmcnt(40)\n"
    "v_cvt_pk_bf16_f32 v204, v152, v153\n"
    "v_cvt_pk_bf16_f32 v205, v154, v155\n"
    "v_mfma_f32_16x16x32_bf16 v[180:183], a[228:231], v[12:15], v[180:183]\n"
    "s_waitcnt lgkmcnt(8)\n"
    "v_cvt_pk_bf16_f32 v206, v156, v157\n"
    "v_cvt_pk_bf16_f32 v207, v158, v159\n"
    "v_mfma_f32_16x16x32_bf16 v[176:179], a[232:235], v[16:19], v[176:179]\n"
    "s_barrier\n"
    "v_add_u32 v121, 50688, v124\n"
    "ds_read_b64_tr_b16 a[192:193], v121 offset:0\n"
    "v_mfma_f32_16x16x32_bf16 v[176:179], a[236:239], v[24:27], v[176:179]\n"
    "s_add_u32 m0, s89, 138880\n"
    "ds_read_b64_tr_b16 a[194:195], v121 offset:256\n"
    "ds_read_b64_tr_b16 a[196:197], v121 offset:4224\n"
    "v_mfma_f32_16x16x32_bf16 v[180:183], a[232:235], v[20:23], v[180:183]\n"
    "buffer_load_dwordx4 v125, s[56:59], s88 offen offset:512 lds\n"
    "ds_read_b64_tr_b16 a[198:199], v121 offset:4480\n"
    "v_mfma_f32_16x16x32_bf16 v[180:183], a[236:239], v[28:31], v[180:183]\n"
    "s_waitcnt lgkmcnt(8)\n"
    "ds_read_b64_tr_b16 a[200:201], v121 offset:8448\n"
    "ds_read_b64_tr_b16 a[202:203], v121 offset:8704\n"
    "v_mfma_f32_16x16x32_bf16 v[184:187], a[240:243], v[0:3], 0\n"
    "s_add_u32 s88, s88, 28672\n"
    "ds_read_b64_tr_b16 a[204:205], v121 offset:12672\n"
    "v_mov_b32 v71, 0\n"
    "v_mfma_f32_16x16x32_bf16 v[184:187], a[244:247], v[8:11], v[184:187]\n"
    "s_add_u32 m0, s89, 143104\n"
    "ds_read_b64_tr_b16 a[206:207], v121 offset:12928\n"
    "buffer_load_dwordx4 v125, s[56:59], s88 offen offset:512 lds\n"
    "v_mfma_f32_16x16x32_bf16 v[188:191], a[240:243], v[4:7], 0\n"
    "s_add_u32 s88, s88, 28672\n"
    "ds_read_b64_tr_b16 a[208:209], v121 offset:32\n"
    "v_cmp_eq_u32 vcc, 0, v67\n"
    "v_mfma_f32_16x16x32_bf16 v[188:191], a[244:247], v[12:15], v[188:191]\n"
    "s_waitcnt lgkmcnt(9)\n"
    "ds_read_b64_tr_b16 a[210:211], v121 offset:288\n"
    "v_cndmask_b32 v70, 0, v68, vcc\n"
    "v_mfma_f32_16x16x32_bf16 v[184:187], a[248:251], v[16:19], v[184:187]\n"
    "s_bitcmp1_b32 s78, 3\n"
    "s_cselect_b32 s99, 0x80000000, s92\n"
    "s_cbranch_scc0 Lo3%=\n"
    "ds_write_b128 v123, v[204:207] offset:3072\n"
    "Lo3%=:\n"
    "ds_read_b64_tr_b16 a[212:213], v121 offset:4256\n"
    "buffer_store_dwordx4 v[204:207], v122, s[60:63], s99 offen offset:3072 sc1\n"
    "v_mfma_f32_16x16x32_bf16 v[184:187], a[252:255], v[24:27], v[184:187]\n"
    "ds_read_b64_tr_b16 a[214:215], v121 offset:4512\n"
    "v_cmp_eq_u32 vcc, 1, v67\n"
    "v_mfma_f32_16x16x32_bf16 v[188:191], a[248:251], v[20:23], v[188:191]\n"
    "s_add_u32 m0, s89, 147328\n"
    "ds_read_b64_tr_b16 a[216:217], v121 offset:8480\n"
    "buffer_load_dwordx4 v125, s[56:59], s88 offen offset:512 lds\n"
    "v_mfma_f32_16x16x32_bf16 v[188:191], a[252:255], v[28:31], v[188:191]\n"
    "s_waitcnt lgkmcnt(9)\n"
    "ds_read_b64_tr_b16 a[218:219], v121 offset:8736\n"
    "v_cndmask_b32 v70, v70, v69, vcc\n"
    "v_mfma_f32_16x16x32_bf16 v[160:163], a[192:195], v[32:35], v[160:163]\n"
    "ds_read_b64_tr_b16 a[220:221], v121 offset:12704\n"
    "v_accvgpr_write_b32 a112, v70\n"
    "v_mfma_f32_16x16x32_bf16 v[160:163], a[196:199], v[40:43], v[160:163]\n"
    "ds_read_b64_tr_b16 a[222:223], v121 offset:12960\n"
    "v_accvgpr_write_b32 a118, v70\n"
    "v_mfma_f32_16x16x32_bf16 v[164:167], a[192:195], v[36:39], v[164:167]\n"
    "ds_read_b64_tr_b16 a[224:225], v121 offset:512\n"
    "v_accvgpr_write_b32 a114, v71\n"
    "v_mfma_f32_16x16x32_bf16 v[164:167], a[196:199], v[44:47], v[164:167]\n"
    "s_waitcnt lgkmcnt(9)\n"
    "ds_read_b64_tr_b16 a[226:227], v121 offset:768\n"
    "v_accvgpr_write_b32 a116, v71\n"
    "v_mfma_f32_16x16x32_bf16 v[160:163], a[200:203], v[48:51], v[160:163]\n"
    "s_mov_b32 s88, 115200\n"
    "s_add_u32 m0, s89, 0\n"
    "ds_read_b64_tr_b16 a[228:229], v121 offset:4736\n"
    "buffer_load_dwordx4 v125, s[56:59], s88 offen offset:0 lds\n"
    "v_mfma_f32_16x16x32_bf16 v[160:163], a[204:207], v[56:59], v[160:163]\n"
    "s_add_u32 s88, s88, 28672\n"
    "ds_read_b64_tr_b16 a[230:231], v121 offset:4992\n"
    "v_cmp_eq_u32 vcc, 2, v67\n"
    "v_mfma_f32_16x16x32_bf16 v[164:167], a[200:203], v[52:55], v[164:167]\n"
    "ds_read_b64_tr_b16 a[232:233], v121 offset:8960\n"
    "v_cndmask_b32 v70, 0, v68, vcc\n"
    "v_mfma_f32_16x16x32_bf16 v[164:167], a[204:207], v[60:63], v[164:167]\n"
    "s_waitcnt lgkmcnt(9)\n"
    "ds_read_b64_tr_b16 a[234:235], v121 offset:9216\n"
    "v_cvt_pk_bf16_f32 v192, v160, v161\n"
    "v_mfma_f32_16x16x32_bf16 v[168:171], a[208:211], v[32:35], v[168:171]\n"
    "ds_read_b64_tr_b16 a[236:237], v121 offset:13184\n"
    "v_cvt_pk_bf16_f32 v193, v162, v163\n"
    "v_mfma_f32_16x16x32_bf16 v[168:171], a[212:215], v[40:43], v[168:171]\n"
    "s_add_u32 m0, s89, 4224\n"
    "ds_read_b64_tr_b16 a[238:239], v121 offset:13440\n"
    "buffer_load_dwordx4 v125, s[56:59], s88 offen offset:0 lds\n"
    "v_mfma_f32_16x16x32_bf16 v[172:175], a[208:211], v[36:39], v[172:175]\n"
    "s_add_u32 s88, s88, 28672\n"
    "ds_read_b64_tr_b16 a[240:241], v121 offset:544\n"
    "v_cvt_pk_bf16_f32 v194, v164, v165\n"
    "v_mfma_f32_16x16x32_bf16 v[172:175], a[212:215], v[44:47], v[172:175]\n"
    "s_waitcnt lgkmcnt(9)\n"
    "ds_read_b64_tr_b16 a[242:243], v121 offset:800\n"
    "v_cvt_pk_bf16_f32 v195, v166, v167\n"
    "v_mfma_f32_16x16x32_bf16 v[168:171], a[216:219], v[48:51], v[168:171]\n"
    "s_bitcmp1_b32 s78, 4\n"
    "s_cselect_b32 s99, 0x80000000, s93\n"
    "s_cbranch_scc0 Lo4%=\n"
    "ds_write_b128 v123, v[192:195] offset:4096\n"
    "Lo4%=:\n"
    "ds_read_b64_tr_b16 a[244:245], v121 offset:4768\n"
    "buffer_store_dwordx4 v[192:195], v122, s[60:63], s99 offen offset:0 sc1\n"
    "v_mfma_f32_16x16x32_bf16 v[168:171], a[220:223], v[56:59], v[168:171]\n"
    "ds_read_b64_tr_b16 a[246:247], v121 offset:5024\n"
    "v_cmp_eq_u32 vcc, 3, v67\n"
    "v_mfma_f32_16x16x32_bf16 v[172:175], a[216:219], v[52:55], v[172:175]\n"
    "s_add_u32 m0, s89, 8448\n"
    "ds_read_b64_tr_b16 a[248:249], v121 offset:8992\n"
    "buffer_load_dwordx4 v125, s[56:59], s88 offen offset:0 lds\n"
    "v_mfma_f32_16x16x32_bf16 v[172:175], a[220:223], v[60:63], v[172:175]\n"
    "s_waitcnt lgkmcnt(9)\n"
    "ds_read_b64_tr_b16 a[250:251], v121 offset:9248\n"
    "v_cndmask_b32 v70, v70, v69, vcc\n"
    "v_mfma_f32_16x16x32_bf16 v[176:179], a[224:227], v[32:35], v[176:179]\n"
    "s_add_u32 s88, s88, 28672\n"
    "ds_read_b64_tr_b16 a[252:253], v121 offset:13216\n"
    "v_accvgpr_write_b32 a113, v70\n"
    "v_mfma_f32_16x16x32_bf16 v[176:179], a[228:231], v[40:43], v[176:179]\n"
    "ds_read_b64_tr_b16 a[254:255], v121 offset:13472\n"
    "v_accvgpr_write_b32 a119, v70\n"
    "v_mfma_f32_16x16x32_bf16 v[180:183], a[224:227], v[36:39], v[180:183]\n"
    "s_waitcnt vmcnt(15)\n"
    "v_cvt_pk_bf16_f32 v196, v168, v169\n"
    "v_cvt_pk_bf16_f32 v197, v170, v171\n"
    "v_mfma_f32_16x16x32_bf16 v[180:183], a[228:231], v[44:47], v[180:183]\n"
    "s_waitcnt lgkmcnt(8)\n"
    "v_cvt_pk_bf16_f32 v198, v172, v173\n"
    "v_cvt_pk_bf16_f32 v199, v174, v175\n"
    "v_mfma_f32_16x16x32_bf16 v[176:179], a[232:235], v[48:51], v[176:179]\n"
    "s_add_u32 m0, s89, 12672\n"
    "v_accvgpr_write_b32 a115, v71\n"
    "buffer_load_dwordx4 v125, s[56:59], s88 offen offset:0 lds\n"
    "v_mfma_f32_16x16x32_bf16 v[176:179], a[236:239], v[56:59], v[176:179]\n"
    "s_barrier\n"
    "v_add_u32 v121, 67584, v124\n"
    "ds_read_b64_tr_b16 a[192:193], v121 offset:0\n"
    "v_mfma_f32_16x16x32_bf16 v[180:183], a[232:235], v[52:55], v[180:183]\n"
    "ds_read_b64_tr_b16 a[194:195], v121 offset:256\n"
    "ds_read_b64_tr_b16 a[196:197], v121 offset:4224\n"
    "v_mfma_f32_16x16x32_bf16 v[180:183], a[236:239], v[60:63], v[180:183]\n"
    "s_waitcnt lgkmcnt(7)\n"
    "ds_read_b64_tr_b16 a[198:199], v121 offset:4480\n"
    "ds_read_b64_tr_b16 a[200:201], v121 offset:8448\n"
    "v_mfma_f32_16x16x32_bf16 v[184:187], a[240:243], v[32:35], v[184:187]\n"
    "s_bitcmp1_b32 s78, 5\n"
    "s_cselect_b32 s99, 0x80000000, s93\n"
    "s_cbranch_scc0 Lo5%=\n"
    "ds_write_b128 v123, v[196:199] offset:5120\n"
    "Lo5%=:\n"
    "ds_read_b64_tr_b16 a[202:203], v121 offset:8704\n"
    "ds_read_b64_tr_b16 a[204:205], v121 offset:12672\n"
    "v_mfma_f32_16x16x32_bf16 v[184:187], a[244:247], v[40:43], v[184:187]\n"
    "ds_read_b64_tr_b16 a[206:207], v121 offset:12928\n"
    "buffer_store_dwordx4 v[196:199], v122, s[60:63], s99 offen offset:1024 sc1\n"
    "v_mfma_f32_16x16x32_bf16 v[188:191], a[240:243], v[36:39], v[188:191]\n"
    "s_mov_b32 s88, 0\n"
    "s_add_u32 m0, s89, 16256\n"
    "ds_read_b64_tr_b16 a[208:209], v121 offset:32\n"
    "buffer_load_dwordx4 v125, s[56:59], s88 offen offset:640 lds\n"
    "v_mfma_f32_16x16x32_bf16 v[188:191], a[244:247], v[44:47], v[188:191]\n"
    "s_waitcnt lgkmcnt(9)\n"
    "ds_read_b64_tr_b16 a[210:211], v121 offset:288\n"
    "v_accvgpr_write_b32 a117, v71\n"
    "v_mfma_f32_16x16x32_bf16 v[184:187], a[248:251], v[48:51], v[184:187]\n"
    "s_add_u32 s88, s88, 28672\n"
    "ds_read_b64_tr_b16 a[212:213], v121 offset:4256\n"
    "v_mfma_f32_16x16x32_bf16 v[184:187], a[252:255], v[56:59], v[184:187]\n"
    "s_mov_b32 s85, s77\n"
    "ds_read_b64_tr_b16 a[214:215], v121 offset:4512\n"
    "v_mfma_f32_16x16x32_bf16 v[188:191], a[248:251], v[52:55], v[188:191]\n"
    "s_mov_b32 s84, s79\n"
    "ds_read_b64_tr_b16 a[216:217], v121 offset:8480\n"
    "v_mfma_f32_16x16x32_bf16 v[188:191], a[252:255], v[60:63], v[188:191]\n"
    "s_waitcnt lgkmcnt(9)\n"
    "ds_read_b64_tr_b16 a[218:219], v121 offset:8736\n"
    "v_mfma_f32_16x16x32_bf16 v[128:131], a[192:195], v[0:3], 0\n"
    "s_add_u32 m0, s89, 20480\n"
    "ds_read_b64_tr_b16 a[220:221], v121 offset:12704\n"
    "buffer_load_dwordx4 v125, s[56:59], s88 offen offset:640 lds\n"
    "v_mfma_f32_16x16x32_bf16 v[128:131], a[196:199], v[8:11], v[128:131]\n"
    "s_add_u32 s88, s88, 28672\n"
    "ds_read_b64_tr_b16 a[222:223], v121 offset:12960\n"
    "v_mfma_f32_16x16x32_bf16 v[132:135], a[192:195], v[4:7], 0\n"
    "s_lshl_b32 s84, s84, 3\n"
    "ds_read_b64_tr_b16 a[224:225], v121 offset:512\n"
    "v_cvt_pk_bf16_f32 v200, v176, v177\n"
    "v_mfma_f32_16x16x32_bf16 v[132:135], a[196:199], v[12:15], v[132:135]\n"
    "s_waitcnt lgkmcnt(9)\n"
    "ds_read_b64_tr_b16 a[226:227], v121 offset:768\n"
    "v_cvt_pk_bf16_f32 v201, v178, v179\n"
    "v_mfma_f32_16x16x32_bf16 v[128:131], a[200:203], v[16:19], v[128:131]\n"
    "s_add_u32 m0, s89, 24704\n"
    "ds_read_b64_tr_b16 a[228:229], v121 offset:4736\n"
    "buffer_load_dwordx4 v125, s[56:59], s88 offen offset:640 lds\n"
    "v_mfma_f32_16x16x32_bf16 v[128:131], a[204:207], v[24:27], v[128:131]\n"
    "s_add_u32 s88, s88, 28672\n"
    "ds_read_b64_tr_b16 a[230:231], v121 offset:4992\n"
    "v_cvt_pk_bf16_f32 v202, v180, v181\n"
    "v_mfma_f32_16x16x32_bf16 v[132:135], a[200:203], v[20:23], v[132:135]\n"
    "s_lshl_b32 s86, s81, 1\n"
    "ds_read_b64_tr_b16 a[232:233], v121 offset:8960\n"
    "v_cvt_pk_bf16_f32 v203, v182, v183\n"
    "v_mfma_f32_16x16x32_bf16 v[132:135], a[204:207], v[28:31], v[132:135]\n"
    "s_waitcnt lgkmcnt(9)\n"
    "ds_read_b64_tr_b16 a[234:235], v121 offset:9216\n"
    "v_mfma_f32_16x16x32_bf16 v[136:139], a[208:211], v[0:3], 0\n"
    "s_bitcmp1_b32 s78, 6\n"
    "s_cselect_b32 s99, 0x80000000, s93\n"
    "s_cbranch_scc0 Lo6%=\n"
    "ds_write_b128 v123, v[200:203] offset:6144\n"
    "Lo6%=:\n"
    "ds_read_b64_tr_b16 a[236:237], v121 offset:13184\n"
    "buffer_store_dwordx4 v[200:203], v122, s[60:63], s99 offen offset:2048 sc1\n"
    "v_mfma_f32_16x16x32_bf16 v[136:139], a[212:215], v[8:11], v[136:139]\n"
    "s_add_u32 m0, s89, 28928\n"
    "ds_read_b64_tr_b16 a[238:239], v121 offset:13440\n"
    "buffer_load_dwordx4 v125, s[56:59], s88 offen offset:640 lds\n"
    "v_mfma_f32_16x16x32_bf16 v[140:143], a[208:211], v[4:7], 0\n"
    "s_add_u32 s84, s84, s86\n"
    "ds_read_b64_tr_b16 a[240:241], v121 offset:544\n"
    "v_mfma_f32_16x16x32_bf16 v[140:143], a[212:215], v[12:15], v[140:143]\n"
    "s_waitcnt lgkmcnt(9)\n"
    "ds_read_b64_tr_b16 a[242:243], v121 offset:800\n"
    "v_mfma_f32_16x16x32_bf16 v[136:139], a[216:219], v[16:19], v[136:139]\n"
    "s_cmp_ge_u32 s84, 28\n"
    "s_cselect_b32 s29, 0x80000000, 0\n"
    "ds_read_b64_tr_b16 a[244:245], v121 offset:4768\n"
    "v_mfma_f32_16x16x32_bf16 v[136:139], a[220:223], v[24:27], v[136:139]\n"
    "s_mul_i32 s86, s85, 28\n"
    "ds_read_b64_tr_b16 a[246:247], v121 offset:5024\n"
    "v_mfma_f32_16x16x32_bf16 v[140:143], a[216:219], v[20:23], v[140:143]\n"
    "s_add_u32 s86, s86, s84\n"
    "ds_read_b64_tr_b16 a[248:249], v121 offset:8992\n"
    "v_mfma_f32_16x16x32_bf16 v[140:143], a[220:223], v[28:31], v[140:143]\n"
    "s_waitcnt lgkmcnt(9)\n"
    "ds_read_b64_tr_b16 a[250:251], v121 offset:9248\n"
    "v_mfma_f32_16x16x32_bf16 v[144:147], a[224:227], v[0:3], 0\n"
    "s_mov_b32 s88, 114688\n"
    "s_add_u32 m0, s89, 33152\n"
    "ds_read_b64_tr_b16 a[252:253], v121 offset:13216\n"
    "buffer_load_dwordx4 v125, s[56:59], s88 offen offset:640 lds\n"
    "v_mfma_f32_16x16x32_bf16 v[144:147], a[228:231], v[8:11], v[144:147]\n"
    "s_add_u32 s88, s88, 28672\n"
    "ds_read_b64_tr_b16 a[254:255], v121 offset:13472\n"
    "v_mfma_f32_16x16x32_bf16 v[148:151], a[224:227], v[4:7], 0\n"
    "s_waitcnt vmcnt(17)\n"
    "v_cvt_pk_bf16_f32 v204, v184, v185\n"
    "v_cvt_pk_bf16_f32 v205, v186, v187\n"
    "v_mfma_f32_16x16x32_bf16 v[148:151], a[228:231], v[12:15], v[148:151]\n"
    "s_waitcnt lgkmcnt(8)\n"
    "v_cvt_pk_bf16_f32 v206, v188, v189\n"
    "v_cvt_pk_bf16_f32 v207, v190, v191\n"
    "v_mfma_f32_16x16x32_bf16 v[144:147], a[232:235], v[16:19], v[144:147]\n"
    "s_barrier\n"
    "v_add_u32 v121, 84480, v124\n"
    "ds_read_b64_tr_b16 a[192:193], v121 offset:0\n"
    "v_mfma_f32_16x16x32_bf16 v[144:147], a[236:239], v[24:27], v[144:147]\n"
    "s_add_u32 m0, s89, 37376\n"
    "ds_read_b64_tr_b16 a[194:195], v121 offset:256\n"
    "ds_read_b64_tr_b16 a[196:197], v121 offset:4224\n"
    "v_mfma_f32_16x16x32_bf16 v[148:151], a[232:235], v[20:23], v[148:151]\n"
    "s_lshl_b32 s86, s86, 10\n"
    "ds_read_b64_tr_b16 a[198:199], v121 offset:4480\n"
    "buffer_load_dwordx4 v125, s[56:59], s88 offen offset:640 lds\n"
    "v_mfma_f32_16x16x32_bf16 v[148:151], a[236:239], v[28:31], v[148:151]\n"
    "s_waitcnt lgkmcnt(8)\n"
    "ds_read_b64_tr_b16 a[200:201], v121 offset:8448\n"
    "ds_read_b64_tr_b16 a[202:203], v121 offset:8704\n"
    "v_mfma_f32_16x16x32_bf16 v[152:155], a[240:243], v[0:3], 0\n"
    "s_add_u32 s88, s88, 28672\n"
    "ds_read_b64_tr_b16 a[204:205], v121 offset:12672\n"
    "v_mfma_f32_16x16x32_bf16 v[152:155], a[244:247], v[8:11], v[152:155]\n"
    "s_bitcmp1_b32 s78, 7\n"
    "s_cselect_b32 s99, 0x80000000, s93\n"
    "s_cbranch_scc0 Lo7%=\n"
    "ds_write_b128 v123, v[204:207] offset:7168\n"
    "Lo7%=:\n"
    "ds_read_b64_tr_b16 a[206:207], v121 offset:12928\n"
    "buffer_store_dwordx4 v[204:207], v122, s[60:63], s99 offen offset:3072 sc1\n"
    "v_mfma_f32_16x16x32_bf16 v[156:159], a[240:243], v[4:7], 0\n"
    "s_add_u32 m0, s89, 41600\n"
    "ds_read_b64_tr_b16 a[208:209], v121 offset:32\n"
    "buffer_load_dwordx4 v125, s[56:59], s88 offen offset:640 lds\n"
    "v_mfma_f32_16x16x32_bf16 v[156:159], a[244:247], v[12:15], v[156:159]\n"
    "s_waitcnt lgkmcnt(9)\n"
    "ds_read_b64_tr_b16 a[210:211], v121 offset:288\n"
    "v_mfma_f32_16x16x32_bf16 v[152:155], a[248:251], v[16:19], v[152:155]\n"
    "s_add_u32 s88, s88, 28672\n"
    "ds_read_b64_tr_b16 a[212:213], v121 offset:4256\n"
    "v_mfma_f32_16x16x32_bf16 v[152:155], a[252:255], v[24:27], v[152:155]\n"
    "s_add_u32 s86, s86, s29\n"
    "ds_read_b64_tr_b16 a[214:215], v121 offset:4512\n"
    "v_add_u32 v218, s86, v127\n"
    "v_mfma_f32_16x16x32_bf16 v[156:159], a[248:251], v[20:23], v[156:159]\n"
    "s_mul_i32 s86, s85, 28672\n"
    "ds_read_b64_tr_b16 a[216:217], v121 offset:8480\n"
    "v_mfma_f32_16x16x32_bf16 v[156:159], a[252:255], v[28:31], v[156:159]\n"
    "s_waitcnt lgkmcnt(9)\n"
    "ds_read_b64_tr_b16 a[218:219], v121 offset:8736\n"
    "v_mfma_f32_16x16x32_bf16 v[128:131], a[192:195], v[32:35], v[128:131]\n"
    "s_add_u32 m0, s89, 45824\n"
    "ds_read_b64_tr_b16 a[220:221], v121 offset:12704\n"
    "buffer_load_dwordx4 v125, s[56:59], s88 offen offset:640 lds\n"
    "v_mfma_f32_16x16x32_bf16 v[128:131], a[196:199], v[40:43], v[128:131]\n"
    "s_lshl_b32 s84, s84, 5\n"
    "ds_read_b64_tr_b16 a[222:223], v121 offset:12960\n"
    "v_mfma_f32_16x16x32_bf16 v[132:135], a[192:195], v[36:39], v[132:135]\n"
    "s_add_u32 s86, s86, s84\n"
    "ds_read_b64_tr_b16 a[224:225], v121 offset:512\n"
    "v_mfma_f32_16x16x32_bf16 v[132:135], a[196:199], v[44:47], v[132:135]\n"
    "s_waitcnt lgkmcnt(9)\n"
    "ds_read_b64_tr_b16 a[226:227], v121 offset:768\n"
    "v_mfma_f32_16x16x32_bf16 v[128:131], a[200:203], v[48:51], v[128:131]\n"
    "s_add_u32 s86, s86, s29\n"
    "ds_read_b64_tr_b16 a[228:229], v121 offset:4736\n"
    "v_mul_u32_u24 v208, 896, v64\n"
    "v_mfma_f32_16x16x32_bf16 v[128:131], a[204:207], v[56:59], v[128:131]\n"
    "ds_read_b64_tr_b16 a[230:231], v121 offset:4992\n"
    "v_lshl_add_u32 v208, v65, 3, v208\n"
    "v_mfma_f32_16x16x32_bf16 v[132:135], a[200:203], v[52:55], v[132:135]\n"
    "s_mov_b32 s88, 0\n"
    "s_add_u32 m0, s89, 49920\n"
    "ds_read_b64_tr_b16 a[232:233], v121 offset:8960\n"
    "buffer_load_dwordx4 v125, s[56:59], s88 offen offset:768 lds\n"
    "v_mfma_f32_16x16x32_bf16 v[132:135], a[204:207], v[60:63], v[132:135]\n"
    "s_waitcnt lgkmcnt(9)\n"
    "ds_read_b64_tr_b16 a[234:235], v121 offset:9216\n"
    "v_cvt_pk_bf16_f32 v192, v128, v129\n"
    "v_mfma_f32_16x16x32_bf16 v[136:139], a[208:211], v[32:35], v[136:139]\n"
    "s_add_u32 s88, s88, 28672\n"
    "ds_read_b64_tr_b16 a[236:237], v121 offset:13184\n"
    "v_cvt_pk_bf16_f32 v193, v130, v131\n"
    "v_mfma_f32_16x16x32_bf16 v[136:139], a[212:215], v[40:43], v[136:139]\n"
    "ds_read_b64_tr_b16 a[238:239], v121 offset:13440\n"
    "v_cvt_pk_bf16_f32 v194, v132, v133\n"
    "v_mfma_f32_16x16x32_bf16 v[140:143], a[208:211], v[36:39], v[140:143]\n"
    "ds_read_b64_tr_b16 a[240:241], v121 offset:544\n"
    "v_cvt_pk_bf16_f32 v195, v134, v135\n"
    "v_mfma_f32_16x16x32_bf16 v[140:143], a[212:215], v[44:47], v[140:143]\n"
    "s_waitcnt lgkmcnt(9)\n"
    "ds_read_b64_tr_b16 a[242:243], v121 offset:800\n"
    "v_add_u32 v220, s86, v208\n"
    "v_mfma_f32_16x16x32_bf16 v[136:139], a[216:219], v[48:51], v[136:139]\n"
    "s_add_u32 m0, s89, 54144\n"
    "ds_read_b64_tr_b16 a[244:245], v121 offset:4768\n"
    "buffer_load_dwordx4 v125, s[56:59], s88 offen offset:768 lds\n"
    "v_mfma_f32_16x16x32_bf16 v[136:139], a[220:223], v[56:59], v[136:139]\n"
    "s_bitcmp1_b32 s78, 8\n"
    "s_cselect_b32 s99, 0x80000000, s94\n"
    "s_cbranch_scc0 Lo8%=\n"
    "ds_write_b128 v123, v[192:195] offset:0\n"
    "Lo8%=:\n"
    "ds_read_b64_tr_b16 a[246:247], v121 offset:5024\n"
    "buffer_store_dwordx4 v[192:195], v122, s[60:63], s99 offen offset:0 sc1\n"
    "v_mfma_f32_16x16x32_bf16 v[140:143], a[216:219], v[52:55], v[140:143]\n"
    "s_add_u32 s88, s88, 28672\n"
    "ds_read_b64_tr_b16 a[248:249], v121 offset:8992\n"
    "v_mfma_f32_16x16x32_bf16 v[140:143], a[220:223], v[60:63], v[140:143]\n"
    "s_waitcnt lgkmcnt(9)\n"
    "ds_read_b64_tr_b16 a[250:251], v121 offset:9248\n"
    "v_mfma_f32_16x16x32_bf16 v[144:147], a[224:227], v[32:35], v[144:147]\n"
    "s_add_u32 m0, s89, 58368\n"
    "ds_read_b64_tr_b16 a[252:253], v121 offset:13216\n"
    "buffer_load_dwordx4 v125, s[56:59], s88 offen offset:768 lds\n"
    "v_mfma_f32_16x16x32_bf16 v[144:147], a[228:231], v[40:43], v[144:147]\n"
    "s_add_u32 s88, s88, 28672\n"
    "ds_read_b64_tr_b16 a[254:255], v121 offset:13472\n"
    "v_mfma_f32_16x16x32_bf16 v[148:151], a[224:227], v[36:39], v[148:151]\n"
    "s_waitcnt vmcnt(40)\n"
    "v_cvt_pk_bf16_f32 v196, v136, v137\n"
    "v_cvt_pk_bf16_f32 v197, v138, v139\n"
    "v_mfma_f32_16x16x32_bf16 v[148:151], a[228:231], v[44:47], v[148:151]\n"
    "s_waitcnt lgkmcnt(8)\n"
    "v_cvt_pk_bf16_f32 v198, v140, v141\n"
    "v_cvt_pk_bf16_f32 v199, v142, v143\n"
    "v_mfma_f32_16x16x32_bf16 v[144:147], a[232:235], v[48:51], v[144:147]\n"
    "s_barrier\n"
    "v_add_u32 v121, 101376, v124\n"
    "ds_read_b64_tr_b16 a[192:193], v121 offset:0\n"
    "v_mfma_f32_16x16x32_bf16 v[144:147], a[236:239], v[56:59], v[144:147]\n"
    "s_add_u32 m0, s89, 62592\n"
    "ds_read_b64_tr_b16 a[194:195], v121 offset:256\n"
    "ds_read_b64_tr_b16 a[196:197], v121 offset:4224\n"
    "v_mfma_f32_16x16x32_bf16 v[148:151], a[232:235], v[52:55], v[148:151]\n"
    "s_lshl_b32 s85, s85, 7\n"
    "ds_read_b64_tr_b16 a[198:199], v121 offset:4480\n"
    "buffer_load_dwordx4 v125, s[56:59], s88 offen offset:768 lds\n"
    "v_mfma_f32_16x16x32_bf16 v[148:151], a[236:239], v[60:63], v[148:151]\n"
    "s_waitcnt lgkmcnt(8)\n"
    "ds_read_b64_tr_b16 a[200:201], v121 offset:8448\n"
    "ds_read_b64_tr_b16 a[202:203], v121 offset:8704\n"
    "v_mfma_f32_16x16x32_bf16 v[152:155], a[240:243], v[32:35], v[152:155]\n"
    "s_bitcmp1_b32 s78, 9\n"
    "s_cselect_b32 s99, 0x80000000, s94\n"
    "s_cbranch_scc0 Lo9%=\n"
    "ds_write_b128 v123, v[196:199] offset:1024\n"
    "Lo9%=:\n"
    "ds_read_b64_tr_b16 a[204:205], v121 offset:12672\n"
    "buffer_store_dwordx4 v[196:199], v122, s[60:63], s99 offen offset:1024 sc1\n"
    "v_mfma_f32_16x16x32_bf16 v[152:155], a[244:247], v[40:43], v[152:155]\n"
    "ds_read_b64_tr_b16 a[206:207], v121 offset:12928\n"
    "v_lshlrev_b32 v208, 2, v64\n"
    "v_mfma_f32_16x16x32_bf16 v[156:159], a[240:243], v[36:39], v[156:159]\n"
    "ds_read_b64_tr_b16 a[208:209], v121 offset:32\n"
    "v_add_u32 v208, s85, v208\n"
    "v_mfma_f32_16x16x32_bf16 v[156:159], a[244:247], v[44:47], v[156:159]\n"
    "s_waitcnt lgkmcnt(9)\n"
    "ds_read_b64_tr_b16 a[210:211], v121 offset:288\n"
    "v_add_u32 v208, 0x27800, v208\n"
    "v_mfma_f32_16x16x32_bf16 v[152:155], a[248:251], v[48:51], v[152:155]\n"
    "s_mov_b32 s88, 114688\n"
    "s_add_u32 m0, s89, 66816\n"
    "ds_read_b64_tr_b16 a[212:213], v121 offset:4256\n"
    "buffer_load_dwordx4 v125, s[56:59], s88 offen offset:768 lds\n"
    "v_mfma_f32_16x16x32_bf16 v[152:155], a[252:255], v[56:59], v[152:155]\n"
    "s_add_u32 s88, s88, 28672\n"
    "ds_read_b64_tr_b16 a[214:215], v121 offset:4512\n"
    "ds_read_b32 v230, v208\n"
    "v_mfma_f32_16x16x32_bf16 v[156:159], a[248:251], v[52:55], v[156:159]\n"
    "ds_read_b64_tr_b16 a[216:217], v121 offset:8480\n"
    "ds_read_b32 v231, v208 offset:64\n"
    "v_mfma_f32_16x16x32_bf16 v[156:159], a[252:255], v[60:63], v[156:159]\n"
    "s_waitcnt lgkmcnt(11)\n"
    "ds_read_b64_tr_b16 a[218:219], v121 offset:8736\n"
    "v_mfma_f32_16x16x32_bf16 v[160:163], a[192:195], v[0:3], 0\n"
    "s_movk_i32 s29, 14336\n"
    "ds_read_b64_tr_b16 a[220:221], v121 offset:12704\n"
    "buffer_load_dwordx2 v[232:233], v220, s[40:43], 0 offen offset:0\n"
    "v_mfma_f32_16x16x32_bf16 v[160:163], a[196:199], v[8:11], v[160:163]\n"
    "s_add_u32 m0, s89, 71040\n"
    "ds_read_b64_tr_b16 a[222:223], v121 offset:12960\n"
    "buffer_load_dwordx4 v125, s[56:59], s88 offen offset:768 lds\n"
    "v_mfma_f32_16x16x32_bf16 v[164:167], a[192:195], v[4:7], 0\n"
    "s_add_u32 s88, s88, 28672\n"
    "ds_read_b64_tr_b16 a[224:225], v121 offset:512\n"
    "buffer_load_dwordx2 v[234:235], v220, s[40:43], s29 offen offset:0\n"
    "v_mfma_f32_16x16x32_bf16 v[164:167], a[196:199], v[12:15], v[164:167]\n"
    "s_waitcnt lgkmcnt(11)\n"
    "buffer_load_dwordx2 v[236:237], v220, s[40:43], 0 offen offset:32\n"
    "buffer_load_dwordx2 v[238:239], v220, s[40:43], s29 offen offset:32\n"
    "v_mfma_f32_16x16x32_bf16 v[160:163], a[200:203], v[16:19], v[160:163]\n"
    "s_lshr_b32 s84, %1, 4\n"
    "ds_read_b64_tr_b16 a[226:227], v121 offset:768\n"
    "ds_read_b64_tr_b16 a[228:229], v121 offset:4736\n"
    "v_mfma_f32_16x16x32_bf16 v[160:163], a[204:207], v[24:27], v[160:163]\n"
    "s_mul_i32 s84, s84, 114688\n"
    "ds_read_b64_tr_b16 a[230:231], v121 offset:4992\n"
    "v_cvt_pk_bf16_f32 v200, v144, v145\n"
    "v_mfma_f32_16x16x32_bf16 v[164:167], a[200:203], v[20:23], v[164:167]\n"
    "s_add_u32 s52, s30, s84\n"
    "s_addc_u32 s53, s31, 0\n"
    "ds_read_b64_tr_b16 a[232:233], v121 offset:8960\n"
    "v_cvt_pk_bf16_f32 v201, v146, v147\n"
    "v_mfma_f32_16x16x32_bf16 v[164:167], a[204:207], v[28:31], v[164:167]\n"
    "s_waitcnt lgkmcnt(11)\n"
    "ds_read_b64_tr_b16 a[234:235], v121 offset:9216\n"
    "v_cvt_pk_bf16_f32 v202, v148, v149\n"
    "v_mfma_f32_16x16x32_bf16 v[168:171], a[208:211], v[0:3], 0\n"
    "s_mov_b32 s54, 114688\n"
    "s_mov_b32 s55, 0x20000\n"
    "s_and_b32 s84, %1, 7\n"
    "s_lshl_b32 s84, s84, 1\n"
    "s_bfe_u32 s85, %1, 0x10003\n"
    "s_add_u32 s84, s84, s85\n"
    "s_lshl_b32 s84, s84, 3\n"
    "s_add_u32 s84, s84, 256\n"
    "v_lshlrev_b32 v224, 12, v126\n"
    "v_add_u32 v224, s84, v224\n"
    "v_mov_b32 v226, s33\n"
    "v_mov_b32 v227, s34\n"
    "v_lshlrev_b32 v225, 3, v126\n"
    "s_and_b32 s84, %1, 15\n"
    "s_or_b32 s84, s84, s81\n"
    "s_cmp_lg_u32 s84, 0\n"
    "s_cselect_b32 s84, 0x80000000, 0\n"
    "v_lshlrev_b32 v222, 12, v126\n"
    "v_add_u32 v222, s84, v222\n"
    "v_mov_b32 v223, s35\n"
    "v_mfma_f32_16x16x32_bf16 v[168:171], a[212:215], v[8:11], v[168:171]\n"
    "s_add_u32 m0, s89, 75264\n"
    "ds_read_b64_tr_b16 a[236:237], v121 offset:13184\n"
    "buffer_load_dwordx4 v125, s[56:59], s88 offen offset:768 lds\n"
    "v_mfma_f32_16x16x32_bf16 v[172:175], a[208:211], v[4:7], 0\n"
    "s_add_u32 s88, s88, 28672\n"
    "ds_read_b64_tr_b16 a[238:239], v121 offset:13440\n"
    "ds_read_b64_tr_b16 a[240:241], v121 offset:544\n"
    "v_mfma_f32_16x16x32_bf16 v[172:175], a[212:215], v[12:15], v[172:175]\n"
    "s_waitcnt lgkmcnt(9)\n"
    "ds_read_b64_tr_b16 a[242:243], v121 offset:800\n"
    "v_cvt_pk_bf16_f32 v203, v150, v151\n"
    "v_mfma_f32_16x16x32_bf16 v[168:171], a[216:219], v[16:19], v[168:171]\n"
    "s_add_u32 m0, s89, 79488\n"
    "ds_read_b64_tr_b16 a[244:245], v121 offset:4768\n"
    "buffer_load_dwordx4 v125, s[56:59], s88 offen offset:768 lds\n"
    "v_mfma_f32_16x16x32_bf16 v[168:171], a[220:223], v[24:27], v[168:171]\n"
    "s_bitcmp1_b32 s78, 10\n"
    "s_cselect_b32 s99, 0x80000000, s94\n"
    "s_cbranch_scc0 Lo10%=\n"
    "ds_write_b128 v123, v[200:203] offset:2048\n"
    "Lo10%=:\n"
    "ds_read_b64_tr_b16 a[246:247], v121 offset:5024\n"
    "buffer_store_dwordx4 v[200:203], v122, s[60:63], s99 offen offset:2048 sc1\n"
    "v_mfma_f32_16x16x32_bf16 v[172:175], a[216:219], v[20:23], v[172:175]\n"
    "ds_read_b64_tr_b16 a[248:249], v121 offset:8992\n"
    "v_mfma_f32_16x16x32_bf16 v[172:175], a[220:223], v[28:31], v[172:175]\n"
    "s_waitcnt lgkmcnt(9)\n"
    "ds_read_b64_tr_b16 a[250:251], v121 offset:9248\n"
    "v_mfma_f32_16x16x32_bf16 v[176:179], a[224:227], v[0:3], 0\n"
    "ds_read_b64_tr_b16 a[252:253], v121 offset:13216\n"
    "v_mfma_f32_16x16x32_bf16 v[176:179], a[228:231], v[8:11], v[176:179]\n"
    "ds_read_b64_tr_b16 a[254:255], v121 offset:13472\n"
    "v_mfma_f32_16x16x32_bf16 v[180:183], a[224:227], v[4:7], 0\n"
    "s_waitcnt vmcnt(47)\n"
    "v_cvt_pk_bf16_f32 v204, v152, v153\n"
    "v_cvt_pk_bf16_f32 v205, v154, v155\n"
    "v_mfma_f32_16x16x32_bf16 v[180:183], a[228:231], v[12:15], v[180:183]\n"
    "s_waitcnt lgkmcnt(8)\n"
    "v_cvt_pk_bf16_f32 v206, v156, v157\n"
    "v_cvt_pk_bf16_f32 v207, v158, v159\n"
    "v_mfma_f32_16x16x32_bf16 v[176:179], a[232:235], v[16:19], v[176:179]\n"
    "s_barrier\n"
    "v_add_u32 v121, 118272, v124\n"
    "ds_read_b64_tr_b16 a[192:193], v121 offset:0\n"
    "v_mfma_f32_16x16x32_bf16 v[176:179], a[236:239], v[24:27], v[176:179]\n"
    "s_add_u32 s28, s28, 139264\n"
    "ds_read_b64_tr_b16 a[194:195], v121 offset:256\n"
    "ds_read_b64_tr_b16 a[196:197], v121 offset:4224\n"
    "v_mfma_f32_16x16x32_bf16 v[180:183], a[232:235], v[20:23], v[180:183]\n"
    "ds_read_b64_tr_b16 a[198:199], v121 offset:4480\n"
    "ds_read_b64_tr_b16 a[200:201], v121 offset:8448\n"
    "v_mfma_f32_16x16x32_bf16 v[180:183], a[236:239], v[28:31], v[180:183]\n"
    "s_waitcnt lgkmcnt(9)\n"
    "ds_read_b64_tr_b16 a[202:203], v121 offset:8704\n"
    "v_mfma_f32_16x16x32_bf16 v[184:187], a[240:243], v[0:3], 0\n"
    "s_bitcmp1_b32 s78, 11\n"
    "s_cselect_b32 s99, 0x80000000, s94\n"
    "s_cbranch_scc0 Lo11%=\n"
    "ds_write_b128 v123, v[204:207] offset:3072\n"
    "Lo11%=:\n"
    "ds_read_b64_tr_b16 a[204:205], v121 offset:12672\n"
    "buffer_store_dwordx4 v[204:207], v122, s[60:63], s99 offen offset:3072 sc1\n"
    "v_mfma_f32_16x16x32_bf16 v[184:187], a[244:247], v[8:11], v[184:187]\n"
    "ds_read_b64_tr_b16 a[206:207], v121 offset:12928\n"
    "v_mfma_f32_16x16x32_bf16 v[188:191], a[240:243], v[4:7], 0\n"
    "ds_read_b64_tr_b16 a[208:209], v121 offset:32\n"
    "v_mfma_f32_16x16x32_bf16 v[188:191], a[244:247], v[12:15], v[188:191]\n"
    "s_waitcnt lgkmcnt(9)\n"
    "ds_read_b64_tr_b16 a[210:211], v121 offset:288\n"
    "v_mfma_f32_16x16x32_bf16 v[184:187], a[248:251], v[16:19], v[184:187]\n"
    "ds_read_b64_tr_b16 a[212:213], v121 offset:4256\n"
    "buffer_load_dwordx4 a[72:75], v120, s[44:47], s28 offen offset:0 sc1\n"
    "v_mfma_f32_16x16x32_bf16 v[184:187], a[252:255], v[24:27], v[184:187]\n"
    "ds_read_b64_tr_b16 a[214:215], v121 offset:4512\n"
    "v_mfma_f32_16x16x32_bf16 v[188:191], a[248:251], v[20:23], v[188:191]\n"
    "ds_read_b64_tr_b16 a[216:217], v121 offset:8480\n"
    "v_mfma_f32_16x16x32_bf16 v[188:191], a[252:255], v[28:31], v[188:191]\n"
    "s_waitcnt lgkmcnt(9)\n"
    "ds_read_b64_tr_b16 a[218:219], v121 offset:8736\n"
    "v_mfma_f32_16x16x32_bf16 v[160:163], a[192:195], v[32:35], v[160:163]\n"
    "ds_read_b64_tr_b16 a[220:221], v121 offset:12704\n"
    "v_mfma_f32_16x16x32_bf16 v[160:163], a[196:199], v[40:43], v[160:163]\n"
    "ds_read_b64_tr_b16 a[222:223], v121 offset:12960\n"
    "v_mfma_f32_16x16x32_bf16 v[164:167], a[192:195], v[36:39], v[164:167]\n"
    "ds_read_b64_tr_b16 a[224:225], v121 offset:512\n"
    "v_mfma_f32_16x16x32_bf16 v[164:167], a[196:199], v[44:47], v[164:167]\n"
    "s_waitcnt lgkmcnt(9)\n"
    "ds_read_b64_tr_b16 a[226:227], v121 offset:768\n"
    "v_mfma_f32_16x16x32_bf16 v[160:163], a[200:203], v[48:51], v[160:163]\n"
    "ds_read_b64_tr_b16 a[228:229], v121 offset:4736\n"
    "v_mfma_f32_16x16x32_bf16 v[160:163], a[204:207], v[56:59], v[160:163]\n"
    "ds_read_b64_tr_b16 a[230:231], v121 offset:4992\n"
    "v_mfma_f32_16x16x32_bf16 v[164:167], a[200:203], v[52:55], v[164:167]\n"
    "ds_read_b64_tr_b16 a[232:233], v121 offset:8960\n"
    "buffer_load_dwordx4 a[76:79], v120, s[44:47], s28 offen offset:1024 sc1\n"
    "v_mfma_f32_16x16x32_bf16 v[164:167], a[204:207], v[60:63], v[164:167]\n"
    "s_waitcnt lgkmcnt(9)\n"
    "ds_read_b64_tr_b16 a[234:235], v121 offset:9216\n"
    "v_mfma_f32_16x16x32_bf16 v[168:171], a[208:211], v[32:35], v[168:171]\n"
    "ds_read_b64_tr_b16 a[236:237], v121 offset:13184\n"
    "v_cvt_pk_bf16_f32 v192, v160, v161\n"
    "v_mfma_f32_16x16x32_bf16 v[168:171], a[212:215], v[40:43], v[168:171]\n"
    "ds_read_b64_tr_b16 a[238:239], v121 offset:13440\n"
    "v_cvt_pk_bf16_f32 v193, v162, v163\n"
    "v_mfma_f32_16x16x32_bf16 v[172:175], a[208:211], v[36:39], v[172:175]\n"
    "ds_read_b64_tr_b16 a[240:241], v121 offset:544\n"
    "v_cvt_pk_bf16_f32 v194, v164, v165\n"
    "v_mfma_f32_16x16x32_bf16 v[172:175], a[212:215], v[44:47], v[172:175]\n"
    "s_waitcnt lgkmcnt(9)\n"
    "ds_read_b64_tr_b16 a[242:243], v121 offset:800\n"
    "v_cvt_pk_bf16_f32 v195, v166, v167\n"
    "v_mfma_f32_16x16x32_bf16 v[168:171], a[216:219], v[48:51], v[168:171]\n"
    "s_bitcmp1_b32 s78, 12\n"
    "s_cselect_b32 s99, 0x80000000, s95\n"
    "s_cbranch_scc0 Lo12%=\n"
    "ds_write_b128 v123, v[192:195] offset:4096\n"
    "Lo12%=:\n"
    "ds_read_b64_tr_b16 a[244:245], v121 offset:4768\n"
    "buffer_store_dwordx4 v[192:195], v122, s[60:63], s99 offen offset:0 sc1\n"
    "v_mfma_f32_16x16x32_bf16 v[168:171], a[220:223], v[56:59], v[168:171]\n"
    "ds_read_b64_tr_b16 a[246:247], v121 offset:5024\n"
    "v_mfma_f32_16x16x32_bf16 v[172:175], a[216:219], v[52:55], v[172:175]\n"
    "ds_read_b64_tr_b16 a[248:249], v121 offset:8992\n"
    "v_mfma_f32_16x16x32_bf16 v[172:175], a[220:223], v[60:63], v[172:175]\n"
    "s_waitcnt lgkmcnt(9)\n"
    "ds_read_b64_tr_b16 a[250:251], v121 offset:9248\n"
    "v_mfma_f32_16x16x32_bf16 v[176:179], a[224:227], v[32:35], v[176:179]\n"
    "ds_read_b64_tr_b16 a[252:253], v121 offset:13216\n"
    "buffer_load_dwordx4 a[80:83], v120, s[44:47], s28 offen offset:2048 sc1\n"
    "v_mfma_f32_16x16x32_bf16 v[176:179], a[228:231], v[40:43], v[176:179]\n"
    "ds_read_b64_tr_b16 a[254:255], v121 offset:13472\n"
    "v_mfma_f32_16x16x32_bf16 v[180:183], a[224:227], v[36:39], v[180:183]\n"
    "s_waitcnt vmcnt(36)\n"
    "v_cvt_pk_bf16_f32 v196, v168, v169\n"
    "v_cvt_pk_bf16_f32 v197, v170, v171\n"
    "v_mfma_f32_16x16x32_bf16 v[180:183], a[228:231], v[44:47], v[180:183]\n"
    "s_waitcnt lgkmcnt(8)\n"
    "v_cvt_pk_bf16_f32 v198, v172, v173\n"
    "v_cvt_pk_bf16_f32 v199, v174, v175\n"
    "v_mfma_f32_16x16x32_bf16 v[176:179], a[232:235], v[48:51], v[176:179]\n"
    "s_barrier\n"
    "v_add_u32 v121, 135168, v124\n"
    "ds_read_b64_tr_b16 a[192:193], v121 offset:0\n"
    "v_mfma_f32_16x16x32_bf16 v[176:179], a[236:239], v[56:59], v[176:179]\n"
    "ds_read_b64_tr_b16 a[194:195], v121 offset:256\n"
    "ds_read_b64_tr_b16 a[196:197], v121 offset:4224\n"
    "v_mfma_f32_16x16x32_bf16 v[180:183], a[232:235], v[52:55], v[180:183]\n"
    "ds_read_b64_tr_b16 a[198:199], v121 offset:4480\n"
    "ds_read_b64_tr_b16 a[200:201], v121 offset:8448\n"
    "v_mfma_f32_16x16x32_bf16 v[180:183], a[236:239], v[60:63], v[180:183]\n"
    "s_waitcnt lgkmcnt(9)\n"
    "ds_read_b64_tr_b16 a[202:203], v121 offset:8704\n"
    "v_mfma_f32_16x16x32_bf16 v[184:187], a[240:243], v[32:35], v[184:187]\n"
    "s_bitcmp1_b32 s78, 13\n"
    "s_cselect_b32 s99, 0x80000000, s95\n"
    "s_cbranch_scc0 Lo13%=\n"
    "ds_write_b128 v123, v[196:199] offset:5120\n"
    "Lo13%=:\n"
    "ds_read_b64_tr_b16 a[204:205], v121 offset:12672\n"
    "buffer_store_dwordx4 v[196:199], v122, s[60:63], s99 offen offset:1024 sc1\n"
    "v_mfma_f32_16x16x32_bf16 v[184:187], a[244:247], v[40:43], v[184:187]\n"
    "ds_read_b64_tr_b16 a[206:207], v121 offset:12928\n"
    "v_mfma_f32_16x16x32_bf16 v[188:191], a[240:243], v[36:39], v[188:191]\n"
    "ds_read_b64_tr_b16 a[208:209], v121 offset:32\n"
    "buffer_load_dwordx4 a[84:87], v120, s[44:47], s28 offen offset:3072 sc1\n"
    "v_mfma_f32_16x16x32_bf16 v[188:191], a[244:247], v[44:47], v[188:191]\n"
    "s_waitcnt lgkmcnt(9)\n"
    "ds_read_b64_tr_b16 a[210:211], v121 offset:288\n"
    "v_mfma_f32_16x16x32_bf16 v[184:187], a[248:251], v[48:51], v[184:187]\n"
    "ds_read_b64_tr_b16 a[212:213], v121 offset:4256\n"
    "v_mfma_f32_16x16x32_bf16 v[184:187], a[252:255], v[56:59], v[184:187]\n"
    "ds_read_b64_tr_b16 a[214:215], v121 offset:4512\n"
    "v_mfma_f32_16x16x32_bf16 v[188:191], a[248:251], v[52:55], v[188:191]\n"
    "ds_read_b64_tr_b16 a[216:217], v121 offset:8480\n"
    "v_mfma_f32_16x16x32_bf16 v[188:191], a[252:255], v[60:63], v[188:191]\n"
    "s_waitcnt lgkmcnt(9)\n"
    "ds_read_b64_tr_b16 a[218:219], v121 offset:8736\n"
    "v_mfma_f32_16x16x32_bf16 v[128:131], a[192:195], v[0:3], 0\n"
    "s_mul_i32 s73, s81, 16896\n"
    "ds_read_b64_tr_b16 a[220:221], v121 offset:12704\n"
    "v_mfma_f32_16x16x32_bf16 v[128:131], a[196:199], v[8:11], v[128:131]\n"
    "s_add_u32 s72, s73, 84480\n"
    "ds_read_b64_tr_b16 a[222:223], v121 offset:12960\n"
    "v_add_u32 v217, s72, v124\n"
    "v_mfma_f32_16x16x32_bf16 v[132:135], a[192:195], v[4:7], 0\n"
    "ds_read_b64_tr_b16 a[224:225], v121 offset:512\n"
    "v_cvt_pk_bf16_f32 v200, v176, v177\n"
    "v_mfma_f32_16x16x32_bf16 v[132:135], a[196:199], v[12:15], v[132:135]\n"
    "s_waitcnt lgkmcnt(9)\n"
    "ds_read_b64_tr_b16 a[226:227], v121 offset:768\n"
    "v_cvt_pk_bf16_f32 v201, v178, v179\n"
    "v_mfma_f32_16x16x32_bf16 v[128:131], a[200:203], v[16:19], v[128:131]\n"
    "s_add_u32 s28, s28, 4096\n"
    "ds_read_b64_tr_b16 a[228:229], v121 offset:4736\n"
    "v_cvt_pk_bf16_f32 v202, v180, v181\n"
    "v_mfma_f32_16x16x32_bf16 v[128:131], a[204:207], v[24:27], v[128:131]\n"
    "ds_read_b64_tr_b16 a[230:231], v121 offset:4992\n"
    "v_cvt_pk_bf16_f32 v203, v182, v183\n"
    "v_mfma_f32_16x16x32_bf16 v[132:135], a[200:203], v[20:23], v[132:135]\n"
    "ds_read_b64_tr_b16 a[232:233], v121 offset:8960\n"
    "v_mfma_f32_16x16x32_bf16 v[132:135], a[204:207], v[28:31], v[132:135]\n"
    "s_waitcnt lgkmcnt(9)\n"
    "ds_read_b64_tr_b16 a[234:235], v121 offset:9216\n"
    "v_mfma_f32_16x16x32_bf16 v[136:139], a[208:211], v[0:3], 0\n"
    "s_bitcmp1_b32 s78, 14\n"
    "s_cselect_b32 s99, 0x80000000, s95\n"
    "s_cbranch_scc0 Lo14%=\n"
    "ds_write_b128 v123, v[200:203] offset:6144\n"
    "Lo14%=:\n"
    "ds_read_b64_tr_b16 a[236:237], v121 offset:13184\n"
    "buffer_store_dwordx4 v[200:203], v122, s[60:63], s99 offen offset:2048 sc1\n"
    "v_mfma_f32_16x16x32_bf16 v[136:139], a[212:215], v[8:11], v[136:139]\n"
    "ds_read_b64_tr_b16 a[238:239], v121 offset:13440\n"
    "v_mfma_f32_16x16x32_bf16 v[140:143], a[208:211], v[4:7], 0\n"
    "ds_read_b64_tr_b16 a[240:241], v121 offset:544\n"
    "v_mfma_f32_16x16x32_bf16 v[140:143], a[212:215], v[12:15], v[140:143]\n"
    "s_waitcnt lgkmcnt(9)\n"
    "ds_read_b64_tr_b16 a[242:243], v121 offset:800\n"
    "v_mfma_f32_16x16x32_bf16 v[136:139], a[216:219], v[16:19], v[136:139]\n"
    "ds_read_b64_tr_b16 a[244:245], v121 offset:4768\n"
    "v_mfma_f32_16x16x32_bf16 v[136:139], a[220:223], v[24:27], v[136:139]\n"
    "ds_read_b64_tr_b16 a[246:247], v121 offset:5024\n"
    "v_mfma_f32_16x16x32_bf16 v[140:143], a[216:219], v[20:23], v[140:143]\n"
    "ds_read_b64_tr_b16 a[248:249], v121 offset:8992\n"
    "buffer_load_dwordx4 a[88:91], v120, s[44:47], s28 offen offset:0 sc1\n"
    "v_mfma_f32_16x16x32_bf16 v[140:143], a[220:223], v[28:31], v[140:143]\n"
    "s_waitcnt lgkmcnt(9)\n"
    "ds_read_b64_tr_b16 a[250:251], v121 offset:9248\n"
    "v_mfma_f32_16x16x32_bf16 v[144:147], a[224:227], v[0:3], 0\n"
    "ds_read_b64_tr_b16 a[252:253], v121 offset:13216\n"
    "v_mfma_f32_16x16x32_bf16 v[144:147], a[228:231], v[8:11], v[144:147]\n"
    "ds_read_b64_tr_b16 a[254:255], v121 offset:13472\n"
    "v_mfma_f32_16x16x32_bf16 v[148:151], a[224:227], v[4:7], 0\n"
    "s_waitcnt vmcnt(35)\n"
    "v_cvt_pk_bf16_f32 v204, v184, v185\n"
    "v_cvt_pk_bf16_f32 v205, v186, v187\n"
    "v_mfma_f32_16x16x32_bf16 v[148:151], a[228:231], v[12:15], v[148:151]\n"
    "s_waitcnt lgkmcnt(8)\n"
    "v_cvt_pk_bf16_f32 v206, v188, v189\n"
    "v_cvt_pk_bf16_f32 v207, v190, v191\n"
    "v_mfma_f32_16x16x32_bf16 v[144:147], a[232:235], v[16:19], v[144:147]\n"
    "s_barrier\n"
    "v_add_u32 v121, 0, v124\n"
    "ds_read_b64_tr_b16 a[192:193], v121 offset:0\n"
    "v_mfma_f32_16x16x32_bf16 v[144:147], a[236:239], v[24:27], v[144:147]\n"
    "ds_read_b64_tr_b16 a[194:195], v121 offset:256\n"
    "ds_read_b64_tr_b16 a[196:197], v121 offset:4224\n"
    "v_mfma_f32_16x16x32_bf16 v[148:151], a[232:235], v[20:23], v[148:151]\n"
    "ds_read_b64_tr_b16 a[198:199], v121 offset:4480\n"
    "ds_read_b64_tr_b16 a[200:201], v121 offset:8448\n"
    "v_mfma_f32_16x16x32_bf16 v[148:151], a[236:239], v[28:31], v[148:151]\n"
    "s_waitcnt lgkmcnt(9)\n"
    "ds_read_b64_tr_b16 a[202:203], v121 offset:8704\n"
    "v_mfma_f32_16x16x32_bf16 v[152:155], a[240:243], v[0:3], 0\n"
    "s_bitcmp1_b32 s78, 15\n"
    "s_cselect_b32 s99, 0x80000000, s95\n"
    "s_cbranch_scc0 Lo15%=\n"
    "ds_write_b128 v123, v[204:207] offset:7168\n"
    "Lo15%=:\n"
    "ds_read_b64_tr_b16 a[204:205], v121 offset:12672\n"
    "buffer_store_dwordx4 v[204:207], v122, s[60:63], s99 offen offset:3072 sc1\n"
    "v_mfma_f32_16x16x32_bf16 v[152:155], a[244:247], v[8:11], v[152:155]\n"
    "ds_read_b64_tr_b16 a[206:207], v121 offset:12928\n"
    "buffer_load_dwordx4 a[92:95], v120, s[44:47], s28 offen offset:1024 sc1\n"
    "v_mfma_f32_16x16x32_bf16 v[156:159], a[240:243], v[4:7], 0\n"
    "ds_read_b64_tr_b16 a[208:209], v121 offset:32\n"
    "v_mfma_f32_16x16x32_bf16 v[156:159], a[244:247], v[12:15], v[156:159]\n"
    "s_waitcnt lgkmcnt(9)\n"
    "ds_read_b64_tr_b16 a[210:211], v121 offset:288\n"
    "v_mfma_f32_16x16x32_bf16 v[152:155], a[248:251], v[16:19], v[152:155]\n"
    "ds_read_b64_tr_b16 a[212:213], v121 offset:4256\n"
    "v_mfma_f32_16x16x32_bf16 v[152:155], a[252:255], v[24:27], v[152:155]\n"
    "ds_read_b64_tr_b16 a[214:215], v121 offset:4512\n"
    "v_mfma_f32_16x16x32_bf16 v[156:159], a[248:251], v[20:23], v[156:159]\n"
    "ds_read_b64_tr_b16 a[216:217], v121 offset:8480\n"
    "v_mfma_f32_16x16x32_bf16 v[156:159], a[252:255], v[28:31], v[156:159]\n"
    "s_waitcnt lgkmcnt(9)\n"
    "ds_read_b64_tr_b16 a[218:219], v121 offset:8736\n"
    "v_mfma_f32_16x16x32_bf16 v[128:131], a[192:195], v[32:35], v[128:131]\n"
    "ds_read_b64_tr_b16 a[220:221], v121 offset:12704\n"
    "v_mfma_f32_16x16x32_bf16 v[128:131], a[196:199], v[40:43], v[128:131]\n"
    "ds_read_b64_tr_b16 a[222:223], v121 offset:12960\n"
    "v_mfma_f32_16x16x32_bf16 v[132:135], a[192:195], v[36:39], v[132:135]\n"
    "ds_read_b64_tr_b16 a[224:225], v121 offset:512\n"
    "buffer_load_dwordx2 a[106:107], v120, s[44:47], s28 offen offset:2048 sc1\n"
    "v_mfma_f32_16x16x32_bf16 v[132:135], a[196:199], v[44:47], v[132:135]\n"
    "s_waitcnt lgkmcnt(9)\n"
    "ds_read_b64_tr_b16 a[226:227], v121 offset:768\n"
    "v_mfma_f32_16x16x32_bf16 v[128:131], a[200:203], v[48:51], v[128:131]\n"
    "ds_read_b64_tr_b16 a[228:229], v121 offset:4736\n"
    "v_mfma_f32_16x16x32_bf16 v[128:131], a[204:207], v[56:59], v[128:131]\n"
    "ds_read_b64_tr_b16 a[230:231], v121 offset:4992\n"
    "v_mfma_f32_16x16x32_bf16 v[132:135], a[200:203], v[52:55], v[132:135]\n"
    "ds_read_b64_tr_b16 a[232:233], v121 offset:8960\n"
    "v_mfma_f32_16x16x32_bf16 v[132:135], a[204:207], v[60:63], v[132:135]\n"
    "s_waitcnt lgkmcnt(9)\n"
    "ds_read_b64_tr_b16 a[234:235], v121 offset:9216\n"
    "v_mfma_f32_16x16x32_bf16 v[136:139], a[208:211], v[32:35], v[136:139]\n"
    "ds_read_b64_tr_b16 a[236:237], v121 offset:13184\n"
    "v_cvt_pk_bf16_f32 v192, v128, v129\n"
    "v_mfma_f32_16x16x32_bf16 v[136:139], a[212:215], v[40:43], v[136:139]\n"
    "ds_read_b64_tr_b16 a[238:239], v121 offset:13440\n"
    "v_cvt_pk_bf16_f32 v193, v130, v131\n"
    "v_mfma_f32_16x16x32_bf16 v[140:143], a[208:211], v[36:39], v[140:143]\n"
    "ds_read_b64_tr_b16 a[240:241], v121 offset:544\n"
    "v_cvt_pk_bf16_f32 v194, v132, v133\n"
    "v_mfma_f32_16x16x32_bf16 v[140:143], a[212:215], v[44:47], v[140:143]\n"
    "s_waitcnt lgkmcnt(9)\n"
    "ds_read_b64_tr_b16 a[242:243], v121 offset:800\n"
    "v_cvt_pk_bf16_f32 v195, v134, v135\n"
    "v_mfma_f32_16x16x32_bf16 v[136:139], a[216:219], v[48:51], v[136:139]\n"
    "s_bitcmp1_b32 s78, 16\n"
    "s_cselect_b32 s99, 0x80000000, s96\n"
    "s_cbranch_scc0 Lo16%=\n"
    "ds_write_b128 v123, v[192:195] offset:0\n"
    "Lo16%=:\n"
    "ds_read_b64_tr_b16 a[244:245], v121 offset:4768\n"
    "buffer_store_dwordx4 v[192:195], v122, s[60:63], s99 offen offset:0 sc1\n"
    "v_mfma_f32_16x16x32_bf16 v[136:139], a[220:223], v[56:59], v[136:139]\n"
    "ds_read_b64_tr_b16 a[246:247], v121 offset:5024\n"
    "buffer_load_dwordx2 a[110:111], v120, s[44:47], s28 offen offset:2056 sc1\n"
    "v_mfma_f32_16x16x32_bf16 v[140:143], a[216:219], v[52:55], v[140:143]\n"
    "ds_read_b64_tr_b16 a[248:249], v121 offset:8992\n"
    "v_mfma_f32_16x16x32_bf16 v[140:143], a[220:223], v[60:63], v[140:143]\n"
    "s_waitcnt lgkmcnt(9)\n"
    "ds_read_b64_tr_b16 a[250:251], v121 offset:9248\n"
    "v_mfma_f32_16x16x32_bf16 v[144:147], a[224:227], v[32:35], v[144:147]\n"
    "ds_read_b64_tr_b16 a[252:253], v121 offset:13216\n"
    "v_mfma_f32_16x16x32_bf16 v[144:147], a[228:231], v[40:43], v[144:147]\n"
    "ds_read_b64_tr_b16 a[254:255], v121 offset:13472\n"
    "v_mfma_f32_16x16x32_bf16 v[148:151], a[224:227], v[36:39], v[148:151]\n"
    "s_waitcnt vmcnt(34)\n"
    "v_cvt_pk_bf16_f32 v196, v136, v137\n"
    "v_cvt_pk_bf16_f32 v197, v138, v139\n"
    "v_mfma_f32_16x16x32_bf16 v[148:151], a[228:231], v[44:47], v[148:151]\n"
    "s_waitcnt lgkmcnt(8)\n"
    "v_cvt_pk_bf16_f32 v198, v140, v141\n"
    "v_cvt_pk_bf16_f32 v199, v142, v143\n"
    "v_mfma_f32_16x16x32_bf16 v[144:147], a[232:235], v[48:51], v[144:147]\n"
    "s_barrier\n"
    "v_add_u32 v121, 16896, v124\n"
    "ds_read_b64_tr_b16 a[192:193], v121 offset:0\n"
    "v_mfma_f32_16x16x32_bf16 v[144:147], a[236:239], v[56:59], v[144:147]\n"
    "ds_read_b64_tr_b16 a[194:195], v121 offset:256\n"
    "ds_read_b64_tr_b16 a[196:197], v121 offset:4224\n"
    "v_mfma_f32_16x16x32_bf16 v[148:151], a[232:235], v[52:55], v[148:151]\n"
    "ds_read_b64_tr_b16 a[198:199], v121 offset:4480\n"
    "ds_read_b64_tr_b16 a[200:201], v121 offset:8448\n"
    "v_mfma_f32_16x16x32_bf16 v[148:151], a[236:239], v[60:63], v[148:151]\n"
    "s_waitcnt lgkmcnt(9)\n"
    "ds_read_b64_tr_b16 a[202:203], v121 offset:8704\n"
    "v_mfma_f32_16x16x32_bf16 v[152:155], a[240:243], v[32:35], v[152:155]\n"
    "s_add_u32 m0, s72, 0\n"
    "s_mov_b32 s74, 0\n"
    "ds_read_b64_tr_b16 a[204:205], v121 offset:12672\n"
    "buffer_load_dwordx4 v212, s[48:51], s74 offen lds\n"
    "v_mfma_f32_16x16x32_bf16 v[152:155], a[244:247], v[40:43], v[152:155]\n"
    "s_bitcmp1_b32 s78, 17\n"
    "s_cselect_b32 s99, 0x80000000, s96\n"
    "s_cbranch_scc0 Lo17%=\n"
    "ds_write_b128 v123, v[196:199] offset:1024\n"
    "Lo17%=:\n"
    "ds_read_b64_tr_b16 a[206:207], v121 offset:12928\n"
    "buffer_store_dwordx4 v[196:199], v122, s[60:63], s99 offen offset:1024 sc1\n"
    "v_mfma_f32_16x16x32_bf16 v[156:159], a[240:243], v[36:39], v[156:159]\n"
    "ds_read_b64_tr_b16 a[208:209], v121 offset:32\n"
    "v_mfma_f32_16x16x32_bf16 v[156:159], a[244:247], v[44:47], v[156:159]\n"
    "s_waitcnt lgkmcnt(9)\n"
    "ds_read_b64_tr_b16 a[210:211], v121 offset:288\n"
    "v_mfma_f32_16x16x32_bf16 v[152:155], a[248:251], v[48:51], v[152:155]\n"
    "ds_read_b64_tr_b16 a[212:213], v121 offset:4256\n"
    "v_mfma_f32_16x16x32_bf16 v[152:155], a[252:255], v[56:59], v[152:155]\n"
    "ds_read_b64_tr_b16 a[214:215], v121 offset:4512\n"
    "v_mfma_f32_16x16x32_bf16 v[156:159], a[248:251], v[52:55], v[156:159]\n"
    "s_add_u32 m0, s72, 1056\n"
    "s_mov_b32 s74, 3584\n"
    "ds_read_b64_tr_b16 a[216:217], v121 offset:8480\n"
    "buffer_load_dwordx4 v212, s[48:51], s74 offen lds\n"
    "v_mfma_f32_16x16x32_bf16 v[156:159], a[252:255], v[60:63], v[156:159]\n"
    "s_waitcnt lgkmcnt(9)\n"
    "ds_read_b64_tr_b16 a[218:219], v121 offset:8736\n"
    "v_mfma_f32_16x16x32_bf16 v[160:163], a[192:195], v[0:3], 0\n"
    "ds_read_b64_tr_b16 a[220:221], v121 offset:12704\n"
    "v_mfma_f32_16x16x32_bf16 v[160:163], a[196:199], v[8:11], v[160:163]\n"
    "ds_read_b64_tr_b16 a[222:223], v121 offset:12960\n"
    "v_mfma_f32_16x16x32_bf16 v[164:167], a[192:195], v[4:7], 0\n"
    "ds_read_b64_tr_b16 a[224:225], v121 offset:512\n"
    "v_cvt_pk_bf16_f32 v200, v144, v145\n"
    "v_mfma_f32_16x16x32_bf16 v[164:167], a[196:199], v[12:15], v[164:167]\n"
    "s_waitcnt lgkmcnt(9)\n"
    "ds_read_b64_tr_b16 a[226:227], v121 offset:768\n"
    "v_cvt_pk_bf16_f32 v201, v146, v147\n"
    "v_mfma_f32_16x16x32_bf16 v[160:163], a[200:203], v[16:19], v[160:163]\n"
    "ds_read_b64_tr_b16 a[228:229], v121 offset:4736\n"
    "v_cvt_pk_bf16_f32 v202, v148, v149\n"
    "v_mfma_f32_16x16x32_bf16 v[160:163], a[204:207], v[24:27], v[160:163]\n"
    "s_add_u32 m0, s72, 2112\n"
    "s_mov_b32 s74, 7168\n"
    "ds_read_b64_tr_b16 a[230:231], v121 offset:4992\n"
    "buffer_load_dwordx4 v212, s[48:51], s74 offen lds\n"
    "v_mfma_f32_16x16x32_bf16 v[164:167], a[200:203], v[20:23], v[164:167]\n"
    "ds_read_b64_tr_b16 a[232:233], v121 offset:8960\n"
    "v_cvt_pk_bf16_f32 v203, v150, v151\n"
    "v_mfma_f32_16x16x32_bf16 v[164:167], a[204:207], v[28:31], v[164:167]\n"
    "s_waitcnt lgkmcnt(9)\n"
    "ds_read_b64_tr_b16 a[234:235], v121 offset:9216\n"
    "v_mfma_f32_16x16x32_bf16 v[168:171], a[208:211], v[0:3], 0\n"
    "s_bitcmp1_b32 s78, 18\n"
    "s_cselect_b32 s99, 0x80000000, s96\n"
    "s_cbranch_scc0 Lo18%=\n"
    "ds_write_b128 v123, v[200:203] offset:2048\n"
    "Lo18%=:\n"
    "ds_read_b64_tr_b16 a[236:237], v121 offset:13184\n"
    "buffer_store_dwordx4 v[200:203], v122, s[60:63], s99 offen offset:2048 sc1\n"
    "v_mfma_f32_16x16x32_bf16 v[168:171], a[212:215], v[8:11], v[168:171]\n"
    "ds_read_b64_tr_b16 a[238:239], v121 offset:13440\n"
    "v_mfma_f32_16x16x32_bf16 v[172:175], a[208:211], v[4:7], 0\n"
    "ds_read_b64_tr_b16 a[240:241], v121 offset:544\n"
    "v_mfma_f32_16x16x32_bf16 v[172:175], a[212:215], v[12:15], v[172:175]\n"
    "s_waitcnt lgkmcnt(9)\n"
    "ds_read_b64_tr_b16 a[242:243], v121 offset:800\n"
    "v_mfma_f32_16x16x32_bf16 v[168:171], a[216:219], v[16:19], v[168:171]\n"
    "s_add_u32 m0, s72, 3168\n"
    "s_mov_b32 s74, 10752\n"
    "ds_read_b64_tr_b16 a[244:245], v121 offset:4768\n"
    "buffer_load_dwordx4 v212, s[48:51], s74 offen lds\n"
    "v_mfma_f32_16x16x32_bf16 v[168:171], a[220:223], v[24:27], v[168:171]\n"
    "ds_read_b64_tr_b16 a[246:247], v121 offset:5024\n"
    "v_mfma_f32_16x16x32_bf16 v[172:175], a[216:219], v[20:23], v[172:175]\n"
    "ds_read_b64_tr_b16 a[248:249], v121 offset:8992\n"
    "v_mfma_f32_16x16x32_bf16 v[172:175], a[220:223], v[28:31], v[172:175]\n"
    "s_waitcnt lgkmcnt(9)\n"
    "ds_read_b64_tr_b16 a[250:251], v121 offset:9248\n"
    "v_mfma_f32_16x16x32_bf16 v[176:179], a[224:227], v[0:3], 0\n"
    "ds_read_b64_tr_b16 a[252:253], v121 offset:13216\n"
    "v_mfma_f32_16x16x32_bf16 v[176:179], a[228:231], v[8:11], v[176:179]\n"
    "ds_read_b64_tr_b16 a[254:255], v121 offset:13472\n"
    "v_mfma_f32_16x16x32_bf16 v[180:183], a[224:227], v[4:7], 0\n"
    "s_add_u32 m0, s72, 4224\n"
    "s_mov_b32 s74, 0\n"
    "v_cvt_pk_bf16_f32 v204, v152, v153\n"
    "buffer_load_dwordx4 v213, s[48:51], s74 offen lds\n"
    "v_mfma_f32_16x16x32_bf16 v[180:183], a[228:231], v[12:15], v[180:183]\n"
    "s_waitcnt lgkmcnt(8)\n"
    "v_cvt_pk_bf16_f32 v205, v154, v155\n"
    "v_cvt_pk_bf16_f32 v206, v156, v157\n"
    "v_mfma_f32_16x16x32_bf16 v[176:179], a[232:235], v[16:19], v[176:179]\n"
    "s_waitcnt vmcnt(36)\n"
    "v_cvt_pk_bf16_f32 v207, v158, v159\n"
    "v_mfma_f32_16x16x32_bf16 v[176:179], a[236:239], v[24:27], v[176:179]\n"
    "s_barrier\n"
    "v_add_u32 v121, 33792, v124\n"
    "ds_read_b64_tr_b16 a[192:193], v121 offset:0\n"
    "v_mfma_f32_16x16x32_bf16 v[180:183], a[232:235], v[20:23], v[180:183]\n"
    "ds_read_b64_tr_b16 a[194:195], v121 offset:256\n"
    "ds_read_b64_tr_b16 a[196:197], v121 offset:4224\n"
    "v_mfma_f32_16x16x32_bf16 v[180:183], a[236:239], v[28:31], v[180:183]\n"
    "s_waitcnt lgkmcnt(7)\n"
    "ds_read_b64_tr_b16 a[198:199], v121 offset:4480\n"
    "ds_read_b64_tr_b16 a[200:201], v121 offset:8448\n"
    "v_mfma_f32_16x16x32_bf16 v[184:187], a[240:243], v[0:3], 0\n"
    "s_add_u32 m0, s72, 5280\n"
    "s_mov_b32 s74, 3584\n"
    "ds_read_b64_tr_b16 a[202:203], v121 offset:8704\n"
    "ds_read_b64_tr_b16 a[204:205], v121 offset:12672\n"
    "v_mfma_f32_16x16x32_bf16 v[184:187], a[244:247], v[8:11], v[184:187]\n"
    "s_bitcmp1_b32 s78, 19\n"
    "s_cselect_b32 s99, 0x80000000, s96\n"
    "s_cbranch_scc0 Lo19%=\n"
    "ds_write_b128 v123, v[204:207] offset:3072\n"
    "Lo19%=:\n"
    "buffer_load_dwordx4 v213, s[48:51], s74 offen lds\n"
    "ds_read_b64_tr_b16 a[206:207], v121 offset:12928\n"
    "v_mfma_f32_16x16x32_bf16 v[188:191], a[240:243], v[4:7], 0\n"
    "ds_read_b64_tr_b16 a[208:209], v121 offset:32\n"
    "buffer_store_dwordx4 v[204:207], v122, s[60:63], s99 offen offset:3072 sc1\n"
    "v_mfma_f32_16x16x32_bf16 v[188:191], a[244:247], v[12:15], v[188:191]\n"
    "s_waitcnt lgkmcnt(9)\n"
    "ds_read_b64_tr_b16 a[210:211], v121 offset:288\n"
    "v_mfma_f32_16x16x32_bf16 v[184:187], a[248:251], v[16:19], v[184:187]\n"
    "ds_read_b64_tr_b16 a[212:213], v121 offset:4256\n"
    "v_mfma_f32_16x16x32_bf16 v[184:187], a[252:255], v[24:27], v[184:187]\n"
    "ds_read_b64_tr_b16 a[214:215], v121 offset:4512\n"
    "v_mfma_f32_16x16x32_bf16 v[188:191], a[248:251], v[20:23], v[188:191]\n"
    "ds_read_b64_tr_b16 a[216:217], v121 offset:8480\n"
    "v_mfma_f32_16x16x32_bf16 v[188:191], a[252:255], v[28:31], v[188:191]\n"
    "s_waitcnt lgkmcnt(9)\n"
    "ds_read_b64_tr_b16 a[218:219], v121 offset:8736\n"
    "v_mfma_f32_16x16x32_bf16 v[160:163], a[192:195], v[32:35], v[160:163]\n"
    "s_add_u32 m0, s72, 6336\n"
    "s_mov_b32 s74, 7168\n"
    "ds_read_b64_tr_b16 a[220:221], v121 offset:12704\n"
    "buffer_load_dwordx4 v213, s[48:51], s74 offen lds\n"
    "v_mfma_f32_16x16x32_bf16 v[160:163], a[196:199], v[40:43], v[160:163]\n"
    "ds_read_b64_tr_b16 a[222:223], v121 offset:12960\n"
    "v_mfma_f32_16x16x32_bf16 v[164:167], a[192:195], v[36:39], v[164:167]\n"
    "ds_read_b64_tr_b16 a[224:225], v121 offset:512\n"
    "v_mfma_f32_16x16x32_bf16 v[164:167], a[196:199], v[44:47], v[164:167]\n"
    "s_waitcnt lgkmcnt(9)\n"
    "ds_read_b64_tr_b16 a[226:227], v121 offset:768\n"
    "v_mfma_f32_16x16x32_bf16 v[160:163], a[200:203], v[48:51], v[160:163]\n"
    "ds_read_b64_tr_b16 a[228:229], v121 offset:4736\n"
    "v_mfma_f32_16x16x32_bf16 v[160:163], a[204:207], v[56:59], v[160:163]\n"
    "s_add_u32 m0, s72, 7392\n"
    "s_mov_b32 s74, 10752\n"
    "ds_read_b64_tr_b16 a[230:231], v121 offset:4992\n"
    "buffer_load_dwordx4 v213, s[48:51], s74 offen lds\n"
    "v_mfma_f32_16x16x32_bf16 v[164:167], a[200:203], v[52:55], v[164:167]\n"
    "ds_read_b64_tr_b16 a[232:233], v121 offset:8960\n"
    "v_mfma_f32_16x16x32_bf16 v[164:167], a[204:207], v[60:63], v[164:167]\n"
    "s_waitcnt lgkmcnt(9)\n"
    "ds_read_b64_tr_b16 a[234:235], v121 offset:9216\n"
    "v_cvt_pk_bf16_f32 v192, v160, v161\n"
    "v_mfma_f32_16x16x32_bf16 v[168:171], a[208:211], v[32:35], v[168:171]\n"
    "ds_read_b64_tr_b16 a[236:237], v121 offset:13184\n"
    "v_cvt_pk_bf16_f32 v193, v162, v163\n"
    "v_mfma_f32_16x16x32_bf16 v[168:171], a[212:215], v[40:43], v[168:171]\n"
    "ds_read_b64_tr_b16 a[238:239], v121 offset:13440\n"
    "v_cvt_pk_bf16_f32 v194, v164, v165\n"
    "v_mfma_f32_16x16x32_bf16 v[172:175], a[208:211], v[36:39], v[172:175]\n"
    "ds_read_b64_tr_b16 a[240:241], v121 offset:544\n"
    "v_cvt_pk_bf16_f32 v195, v166, v167\n"
    "v_mfma_f32_16x16x32_bf16 v[172:175], a[212:215], v[44:47], v[172:175]\n"
    "s_waitcnt lgkmcnt(9)\n"
    "ds_read_b64_tr_b16 a[242:243], v121 offset:800\n"
    "v_mfma_f32_16x16x32_bf16 v[168:171], a[216:219], v[48:51], v[168:171]\n"
    "s_add_u32 m0, s72, 8448\n"
    "s_mov_b32 s74, 28672\n"
    "ds_read_b64_tr_b16 a[244:245], v121 offset:4768\n"
    "buffer_load_dwordx4 v212, s[48:51], s74 offen lds\n"
    "v_mfma_f32_16x16x32_bf16 v[168:171], a[220:223], v[56:59], v[168:171]\n"
    "s_bitcmp1_b32 s78, 20\n"
    "s_cselect_b32 s99, 0x80000000, s97\n"
    "s_cbranch_scc0 Lo20%=\n"
    "ds_write_b128 v123, v[192:195] offset:4096\n"
    "Lo20%=:\n"
    "ds_read_b64_tr_b16 a[246:247], v121 offset:5024\n"
    "buffer_store_dwordx4 v[192:195], v122, s[60:63], s99 offen offset:0 sc1\n"
    "v_mfma_f32_16x16x32_bf16 v[172:175], a[216:219], v[52:55], v[172:175]\n"
    "ds_read_b64_tr_b16 a[248:249], v121 offset:8992\n"
    "v_mfma_f32_16x16x32_bf16 v[172:175], a[220:223], v[60:63], v[172:175]\n"
    "s_waitcnt lgkmcnt(9)\n"
    "ds_read_b64_tr_b16 a[250:251], v121 offset:9248\n"
    "v_mfma_f32_16x16x32_bf16 v[176:179], a[224:227], v[32:35], v[176:179]\n"
    "ds_read_b64_tr_b16 a[252:253], v121 offset:13216\n"
    "v_mfma_f32_16x16x32_bf16 v[176:179], a[228:231], v[40:43], v[176:179]\n"
    "ds_read_b64_tr_b16 a[254:255], v121 offset:13472\n"
    "v_mfma_f32_16x16x32_bf16 v[180:183], a[224:227], v[36:39], v[180:183]\n"
    "s_add_u32 m0, s72, 9504\n"
    "s_mov_b32 s74, 32256\n"
    "v_cvt_pk_bf16_f32 v196, v168, v169\n"
    "buffer_load_dwordx4 v212, s[48:51], s74 offen lds\n"
    "v_mfma_f32_16x16x32_bf16 v[180:183], a[228:231], v[44:47], v[180:183]\n"
    "s_waitcnt lgkmcnt(8)\n"
    "v_cvt_pk_bf16_f32 v197, v170, v171\n"
    "v_cvt_pk_bf16_f32 v198, v172, v173\n"
    "v_mfma_f32_16x16x32_bf16 v[176:179], a[232:235], v[48:51], v[176:179]\n"
    "s_waitcnt vmcnt(38)\n"
    "v_cvt_pk_bf16_f32 v199, v174, v175\n"
    "v_mfma_f32_16x16x32_bf16 v[176:179], a[236:239], v[56:59], v[176:179]\n"
    "s_barrier\n"
    "v_add_u32 v121, 50688, v124\n"
    "ds_read_b64_tr_b16 a[192:193], v121 offset:0\n"
    "v_mfma_f32_16x16x32_bf16 v[180:183], a[232:235], v[52:55], v[180:183]\n"
    "ds_read_b64_tr_b16 a[194:195], v121 offset:256\n"
    "ds_read_b64_tr_b16 a[196:197], v121 offset:4224\n"
    "v_mfma_f32_16x16x32_bf16 v[180:183], a[236:239], v[60:63], v[180:183]\n"
    "s_waitcnt lgkmcnt(7)\n"
    "ds_read_b64_tr_b16 a[198:199], v121 offset:4480\n"
    "ds_read_b64_tr_b16 a[200:201], v121 offset:8448\n"
    "v_mfma_f32_16x16x32_bf16 v[184:187], a[240:243], v[32:35], v[184:187]\n"
    "s_bitcmp1_b32 s78, 21\n"
    "s_cselect_b32 s99, 0x80000000, s97\n"
    "s_cbranch_scc0 Lo21%=\n"
    "ds_write_b128 v123, v[196:199] offset:5120\n"
    "Lo21%=:\n"
    "ds_read_b64_tr_b16 a[202:203], v121 offset:8704\n"
    "ds_read_b64_tr_b16 a[204:205], v121 offset:12672\n"
    "v_mfma_f32_16x16x32_bf16 v[184:187], a[244:247], v[40:43], v[184:187]\n"
    "s_add_u32 m0, s72, 10560\n"
    "s_mov_b32 s74, 35840\n"
    "ds_read_b64_tr_b16 a[206:207], v121 offset:12928\n"
    "buffer_load_dwordx4 v212, s[48:51], s74 offen lds\n"
    "v_mfma_f32_16x16x32_bf16 v[188:191], a[240:243], v[36:39], v[188:191]\n"
    "ds_read_b64_tr_b16 a[208:209], v121 offset:32\n"
    "buffer_store_dwordx4 v[196:199], v122, s[60:63], s99 offen offset:1024 sc1\n"
    "v_mfma_f32_16x16x32_bf16 v[188:191], a[244:247], v[44:47], v[188:191]\n"
    "s_waitcnt lgkmcnt(9)\n"
    "ds_read_b64_tr_b16 a[210:211], v121 offset:288\n"
    "v_mfma_f32_16x16x32_bf16 v[184:187], a[248:251], v[48:51], v[184:187]\n"
    "ds_read_b64_tr_b16 a[212:213], v121 offset:4256\n"
    "v_mfma_f32_16x16x32_bf16 v[184:187], a[252:255], v[56:59], v[184:187]\n"
    "ds_read_b64_tr_b16 a[214:215], v121 offset:4512\n"
    "v_mfma_f32_16x16x32_bf16 v[188:191], a[248:251], v[52:55], v[188:191]\n"
    "ds_read_b64_tr_b16 a[216:217], v121 offset:8480\n"
    "v_mfma_f32_16x16x32_bf16 v[188:191], a[252:255], v[60:63], v[188:191]\n"
    "s_waitcnt lgkmcnt(9)\n"
    "ds_read_b64_tr_b16 a[218:219], v121 offset:8736\n"
    "v_mfma_f32_16x16x32_bf16 v[128:131], a[192:195], v[0:3], 0\n"
    "s_add_u32 m0, s72, 11616\n"
    "s_mov_b32 s74, 39424\n"
    "ds_read_b64_tr_b16 a[220:221], v121 offset:12704\n"
    "buffer_load_dwordx4 v212, s[48:51], s74 offen lds\n"
    "v_mfma_f32_16x16x32_bf16 v[128:131], a[196:199], v[8:11], v[128:131]\n"
    "ds_read_b64_tr_b16 a[222:223], v121 offset:12960\n"
    "v_mfma_f32_16x16x32_bf16 v[132:135], a[192:195], v[4:7], 0\n"
    "ds_read_b64_tr_b16 a[224:225], v121 offset:512\n"
    "v_cvt_pk_bf16_f32 v200, v176, v177\n"
    "v_mfma_f32_16x16x32_bf16 v[132:135], a[196:199], v[12:15], v[132:135]\n"
    "s_waitcnt lgkmcnt(9)\n"
    "ds_read_b64_tr_b16 a[226:227], v121 offset:768\n"
    "v_cvt_pk_bf16_f32 v201, v178, v179\n"
    "v_mfma_f32_16x16x32_bf16 v[128:131], a[200:203], v[16:19], v[128:131]\n"
    "ds_read_b64_tr_b16 a[228:229], v121 offset:4736\n"
    "v_cvt_pk_bf16_f32 v202, v180, v181\n"
    "v_mfma_f32_16x16x32_bf16 v[128:131], a[204:207], v[24:27], v[128:131]\n"
    "ds_read_b64_tr_b16 a[230:231], v121 offset:4992\n"
    "v_cvt_pk_bf16_f32 v203, v182, v183\n"
    "v_mfma_f32_16x16x32_bf16 v[132:135], a[200:203], v[20:23], v[132:135]\n"
    "s_add_u32 m0, s72, 12672\n"
    "s_mov_b32 s74, 28672\n"
    "ds_read_b64_tr_b16 a[232:233], v121 offset:8960\n"
    "buffer_load_dwordx4 v213, s[48:51], s74 offen lds\n"
    "v_mfma_f32_16x16x32_bf16 v[132:135], a[204:207], v[28:31], v[132:135]\n"
    "s_waitcnt lgkmcnt(9)\n"
    "ds_read_b64_tr_b16 a[234:235], v121 offset:9216\n"
    "v_mfma_f32_16x16x32_bf16 v[136:139], a[208:211], v[0:3], 0\n"
    "s_bitcmp1_b32 s78, 22\n"
    "s_cselect_b32 s99, 0x80000000, s97\n"
    "s_cbranch_scc0 Lo22%=\n"
    "ds_write_b128 v123, v[200:203] offset:6144\n"
    "Lo22%=:\n"
    "ds_read_b64_tr_b16 a[236:237], v121 offset:13184\n"
    "buffer_store_dwordx4 v[200:203], v122, s[60:63], s99 offen offset:2048 sc1\n"
    "v_mfma_f32_16x16x32_bf16 v[136:139], a[212:215], v[8:11], v[136:139]\n"
    "ds_read_b64_tr_b16 a[238:239], v121 offset:13440\n"
    "v_mfma_f32_16x16x32_bf16 v[140:143], a[208:211], v[4:7], 0\n"
    "ds_read_b64_tr_b16 a[240:241], v121 offset:544\n"
    "v_mfma_f32_16x16x32_bf16 v[140:143], a[212:215], v[12:15], v[140:143]\n"
    "s_waitcnt lgkmcnt(9)\n"
    "ds_read_b64_tr_b16 a[242:243], v121 offset:800\n"
    "v_mfma_f32_16x16x32_bf16 v[136:139], a[216:219], v[16:19], v[136:139]\n"
    "s_add_u32 m0, s72, 13728\n"
    "s_mov_b32 s74, 32256\n"
    "ds_read_b64_tr_b16 a[244:245], v121 offset:4768\n"
    "buffer_load_dwordx4 v213, s[48:51], s74 offen lds\n"
    "v_mfma_f32_16x16x32_bf16 v[136:139], a[220:223], v[24:27], v[136:139]\n"
    "ds_read_b64_tr_b16 a[246:247], v121 offset:5024\n"
    "v_mfma_f32_16x16x32_bf16 v[140:143], a[216:219], v[20:23], v[140:143]\n"
    "ds_read_b64_tr_b16 a[248:249], v121 offset:8992\n"
    "v_mfma_f32_16x16x32_bf16 v[140:143], a[220:223], v[28:31], v[140:143]\n"
    "s_waitcnt lgkmcnt(9)\n"
    "ds_read_b64_tr_b16 a[250:251], v121 offset:9248\n"
    "v_mfma_f32_16x16x32_bf16 v[144:147], a[224:227], v[0:3], 0\n"
    "ds_read_b64_tr_b16 a[252:253], v121 offset:13216\n"
    "v_mfma_f32_16x16x32_bf16 v[144:147], a[228:231], v[8:11], v[144:147]\n"
    "ds_read_b64_tr_b16 a[254:255], v121 offset:13472\n"
    "v_mfma_f32_16x16x32_bf16 v[148:151], a[224:227], v[4:7], 0\n"
    "s_waitcnt vmcnt(35)\n"
    "v_cvt_pk_bf16_f32 v204, v184, v185\n"
    "v_cvt_pk_bf16_f32 v205, v186, v187\n"
    "v_mfma_f32_16x16x32_bf16 v[148:151], a[228:231], v[12:15], v[148:151]\n"
    "s_waitcnt lgkmcnt(8)\n"
    "v_cvt_pk_bf16_f32 v206, v188, v189\n"
    "v_cvt_pk_bf16_f32 v207, v190, v191\n"
    "v_mfma_f32_16x16x32_bf16 v[144:147], a[232:235], v[16:19], v[144:147]\n"
    "s_add_u32 m0, s72, 14784\n"
    "s_mov_b32 s74, 35840\n"
    "v_mfma_f32_16x16x32_bf16 v[144:147], a[236:239], v[24:27], v[144:147]\n"
    "s_barrier\n"
    "v_add_u32 v121, 67584, v124\n"
    "ds_read_b64_tr_b16 a[192:193], v121 offset:0\n"
    "v_mfma_f32_16x16x32_bf16 v[148:151], a[232:235], v[20:23], v[148:151]\n"
    "ds_read_b64_tr_b16 a[194:195], v121 offset:256\n"
    "buffer_load_dwordx4 v213, s[48:51], s74 offen lds\n"
    "v_mfma_f32_16x16x32_bf16 v[148:151], a[236:239], v[28:31], v[148:151]\n"
    "s_waitcnt lgkmcnt(6)\n"
    "ds_read_b64_tr_b16 a[196:197], v121 offset:4224\n"
    "ds_read_b64_tr_b16 a[198:199], v121 offset:4480\n"
    "v_mfma_f32_16x16x32_bf16 v[152:155], a[240:243], v[0:3], 0\n"
    "s_bitcmp1_b32 s78, 23\n"
    "s_cselect_b32 s99, 0x80000000, s97\n"
    "s_cbranch_scc0 Lo23%=\n"
    "ds_write_b128 v123, v[204:207] offset:7168\n"
    "Lo23%=:\n"
    "ds_read_b64_tr_b16 a[200:201], v121 offset:8448\n"
    "ds_read_b64_tr_b16 a[202:203], v121 offset:8704\n"
    "v_mfma_f32_16x16x32_bf16 v[152:155], a[244:247], v[8:11], v[152:155]\n"
    "s_add_u32 m0, s72, 15840\n"
    "s_mov_b32 s74, 39424\n"
    "ds_read_b64_tr_b16 a[204:205], v121 offset:12672\n"
    "ds_read_b64_tr_b16 a[206:207], v121 offset:12928\n"
    "v_mfma_f32_16x16x32_bf16 v[156:159], a[240:243], v[4:7], 0\n"
    "buffer_load_dwordx4 v213, s[48:51], s74 offen lds\n"
    "ds_read_b64_tr_b16 a[208:209], v121 offset:32\n"
    "v_mfma_f32_16x16x32_bf16 v[156:159], a[244:247], v[12:15], v[156:159]\n"
    "s_waitcnt lgkmcnt(9)\n"
    "ds_read_b64_tr_b16 a[210:211], v121 offset:288\n"
    "buffer_store_dwordx4 v[204:207], v122, s[60:63], s99 offen offset:3072 sc1\n"
    "v_mfma_f32_16x16x32_bf16 v[152:155], a[248:251], v[16:19], v[152:155]\n"
    "ds_read_b64_tr_b16 a[212:213], v121 offset:4256\n"
    "v_mfma_f32_16x16x32_bf16 v[152:155], a[252:255], v[24:27], v[152:155]\n"
    "ds_read_b64_tr_b16 a[214:215], v121 offset:4512\n"
    "v_mfma_f32_16x16x32_bf16 v[156:159], a[248:251], v[20:23], v[156:159]\n"
    "ds_read_b64_tr_b16 a[216:217], v121 offset:8480\n"
    "v_mfma_f32_16x16x32_bf16 v[156:159], a[252:255], v[28:31], v[156:159]\n"
    "s_waitcnt lgkmcnt(9)\n"
    "ds_read_b64_tr_b16 a[218:219], v121 offset:8736\n"
    "v_mfma_f32_16x16x32_bf16 v[128:131], a[192:195], v[32:35], v[128:131]\n"
    "ds_read_b64_tr_b16 a[220:221], v121 offset:12704\n"
    "v_mfma_f32_16x16x32_bf16 v[128:131], a[196:199], v[40:43], v[128:131]\n"
    "ds_read_b64_tr_b16 a[222:223], v121 offset:12960\n"
    "v_mfma_f32_16x16x32_bf16 v[132:135], a[192:195], v[36:39], v[132:135]\n"
    "ds_read_b64_tr_b16 a[224:225], v121 offset:512\n"
    "v_mfma_f32_16x16x32_bf16 v[132:135], a[196:199], v[44:47], v[132:135]\n"
    "s_waitcnt lgkmcnt(9)\n"
    "ds_read_b64_tr_b16 a[226:227], v121 offset:768\n"
    "v_mfma_f32_16x16x32_bf16 v[128:131], a[200:203], v[48:51], v[128:131]\n"
    "ds_read_b64_tr_b16 a[228:229], v121 offset:4736\n"
    "v_mfma_f32_16x16x32_bf16 v[128:131], a[204:207], v[56:59], v[128:131]\n"
    "ds_read_b64_tr_b16 a[230:231], v121 offset:4992\n"
    "v_mfma_f32_16x16x32_bf16 v[132:135], a[200:203], v[52:55], v[132:135]\n"
    "ds_read_b64_tr_b16 a[232:233], v121 offset:8960\n"
    "v_mfma_f32_16x16x32_bf16 v[132:135], a[204:207], v[60:63], v[132:135]\n"
    "s_waitcnt lgkmcnt(9)\n"
    "ds_read_b64_tr_b16 a[234:235], v121 offset:9216\n"
    "v_mfma_f32_16x16x32_bf16 v[136:139], a[208:211], v[32:35], v[136:139]\n"
    "ds_read_b64_tr_b16 a[236:237], v121 offset:13184\n"
    "v_cvt_pk_bf16_f32 v192, v128, v129\n"
    "v_mfma_f32_16x16x32_bf16 v[136:139], a[212:215], v[40:43], v[136:139]\n"
    "ds_read_b64_tr_b16 a[238:239], v121 offset:13440\n"
    "v_cvt_pk_bf16_f32 v193, v130, v131\n"
    "v_mfma_f32_16x16x32_bf16 v[140:143], a[208:211], v[36:39], v[140:143]\n"
    "ds_read_b64_tr_b16 a[240:241], v121 offset:544\n"
    "v_cvt_pk_bf16_f32 v194, v132, v133\n"
    "v_mfma_f32_16x16x32_bf16 v[140:143], a[212:215], v[44:47], v[140:143]\n"
    "s_waitcnt lgkmcnt(9)\n"
    "ds_read_b64_tr_b16 a[242:243], v121 offset:800\n"
    "v_cvt_pk_bf16_f32 v195, v134, v135\n"
    "v_mfma_f32_16x16x32_bf16 v[136:139], a[216:219], v[48:51], v[136:139]\n"
    "s_bitcmp1_b32 s78, 24\n"
    "s_cselect_b32 s99, 0x80000000, s98\n"
    "s_cbranch_scc0 Lo24%=\n"
    "ds_write_b128 v123, v[192:195] offset:0\n"
    "Lo24%=:\n"
    "ds_read_b64_tr_b16 a[244:245], v121 offset:4768\n"
    "buffer_store_dwordx4 v[192:195], v122, s[60:63], s99 offen offset:0 sc1\n"
    "v_mfma_f32_16x16x32_bf16 v[136:139], a[220:223], v[56:59], v[136:139]\n"
    "ds_read_b64_tr_b16 a[246:247], v121 offset:5024\n"
    "v_mfma_f32_16x16x32_bf16 v[140:143], a[216:219], v[52:55], v[140:143]\n"
    "ds_read_b64_tr_b16 a[248:249], v121 offset:8992\n"
    "v_mfma_f32_16x16x32_bf16 v[140:143], a[220:223], v[60:63], v[140:143]\n"
    "s_waitcnt lgkmcnt(9)\n"
    "ds_read_b64_tr_b16 a[250:251], v121 offset:9248\n"
    "v_mfma_f32_16x16x32_bf16 v[144:147], a[224:227], v[32:35], v[144:147]\n"
    "ds_read_b64_tr_b16 a[252:253], v121 offset:13216\n"
    "v_cvt_pk_bf16_f32 v196, v136, v137\n"
    "v_mfma_f32_16x16x32_bf16 v[144:147], a[228:231], v[40:43], v[144:147]\n"
    "ds_read_b64_tr_b16 a[254:255], v121 offset:13472\n"
    "v_cvt_pk_bf16_f32 v197, v138, v139\n"
    "v_mfma_f32_16x16x32_bf16 v[148:151], a[224:227], v[36:39], v[148:151]\n"
    "s_waitcnt vmcnt(13)\n"
    "ds_read_b64_tr_b16 a[192:193], v217 offset:0\n"
    "v_cvt_pk_bf16_f32 v198, v140, v141\n"
    "v_mfma_f32_16x16x32_bf16 v[148:151], a[228:231], v[44:47], v[148:151]\n"
    "s_waitcnt lgkmcnt(9)\n"
    "ds_read_b64_tr_b16 a[194:195], v217 offset:256\n"
    "v_cvt_pk_bf16_f32 v199, v142, v143\n"
    "v_mfma_f32_16x16x32_bf16 v[144:147], a[232:235], v[48:51], v[144:147]\n"
    "s_bitcmp1_b32 s78, 25\n"
    "s_cselect_b32 s99, 0x80000000, s98\n"
    "s_cbranch_scc0 Lo25%=\n"
    "ds_write_b128 v123, v[196:199] offset:1024\n"
    "Lo25%=:\n"
    "ds_read_b64_tr_b16 a[196:197], v217 offset:32\n"
    "buffer_store_dwordx4 v[196:199], v122, s[60:63], s99 offen offset:1024 sc1\n"
    "v_mfma_f32_16x16x32_bf16 v[144:147], a[236:239], v[56:59], v[144:147]\n"
    "ds_read_b64_tr_b16 a[198:199], v217 offset:288\n"
    "v_mfma_f32_16x16x32_bf16 v[148:151], a[232:235], v[52:55], v[148:151]\n"
    "ds_read_b64_tr_b16 a[200:201], v217 offset:512\n"
    "v_mfma_f32_16x16x32_bf16 v[148:151], a[236:239], v[60:63], v[148:151]\n"
    "s_waitcnt lgkmcnt(9)\n"
    "ds_read_b64_tr_b16 a[202:203], v217 offset:768\n"
    "v_mfma_f32_16x16x32_bf16 v[152:155], a[240:243], v[32:35], v[152:155]\n"
    "ds_read_b64_tr_b16 a[204:205], v217 offset:544\n"
    "v_cvt_pk_bf16_f32 v200, v144, v145\n"
    "v_mfma_f32_16x16x32_bf16 v[152:155], a[244:247], v[40:43], v[152:155]\n"
    "ds_read_b64_tr_b16 a[206:207], v217 offset:800\n"
    "v_cvt_pk_bf16_f32 v201, v146, v147\n"
    "v_mfma_f32_16x16x32_bf16 v[156:159], a[240:243], v[36:39], v[156:159]\n"
    "ds_read_b64_tr_b16 a[208:209], v217 offset:4224\n"
    "v_cvt_pk_bf16_f32 v202, v148, v149\n"
    "v_mfma_f32_16x16x32_bf16 v[156:159], a[244:247], v[44:47], v[156:159]\n"
    "s_waitcnt lgkmcnt(9)\n"
    "ds_read_b64_tr_b16 a[210:211], v217 offset:4480\n"
    "v_cvt_pk_bf16_f32 v203, v150, v151\n"
    "v_mfma_f32_16x16x32_bf16 v[152:155], a[248:251], v[48:51], v[152:155]\n"
    "s_bitcmp1_b32 s78, 26\n"
    "s_cselect_b32 s99, 0x80000000, s98\n"
    "s_cbranch_scc0 Lo26%=\n"
    "ds_write_b128 v123, v[200:203] offset:2048\n"
    "Lo26%=:\n"
    "ds_read_b64_tr_b16 a[212:213], v217 offset:4256\n"
    "buffer_store_dwordx4 v[200:203], v122, s[60:63], s99 offen offset:2048 sc1\n"
    "v_mfma_f32_16x16x32_bf16 v[152:155], a[252:255], v[56:59], v[152:155]\n"
    "ds_read_b64_tr_b16 a[214:215], v217 offset:4512\n"
    "v_mfma_f32_16x16x32_bf16 v[156:159], a[248:251], v[52:55], v[156:159]\n"
    "ds_read_b64_tr_b16 a[216:217], v217 offset:4736\n"
    "v_mfma_f32_16x16x32_bf16 v[156:159], a[252:255], v[60:63], v[156:159]\n"
    "s_waitcnt lgkmcnt(9)\n"
    "ds_read_b64_tr_b16 a[218:219], v217 offset:4992\n"
    "v_mfma_f32_16x16x32_bf16 a[120:123], a[192:195], a[0:3], 0\n"
    "ds_read_b64_tr_b16 a[220:221], v217 offset:4768\n"
    "v_cvt_pk_bf16_f32 v204, v152, v153\n"
    "v_mfma_f32_16x16x32_bf16 a[124:127], a[192:195], a[4:7], 0\n"
    "ds_read_b64_tr_b16 a[222:223], v217 offset:5024\n"
    "v_cvt_pk_bf16_f32 v205, v154, v155\n"
    "v_mfma_f32_16x16x32_bf16 a[128:131], a[196:199], a[0:3], 0\n"
    "s_waitcnt vmcnt(4)\n"
    "ds_read_b64_tr_b16 a[224:225], v217 offset:8448\n"
    "v_cvt_pk_bf16_f32 v206, v156, v157\n"
    "v_mfma_f32_16x16x32_bf16 a[132:135], a[196:199], a[4:7], 0\n"
    "s_waitcnt lgkmcnt(9)\n"
    "ds_read_b64_tr_b16 a[226:227], v217 offset:8704\n"
    "v_cvt_pk_bf16_f32 v207, v158, v159\n"
    "v_mfma_f32_16x16x32_bf16 a[136:139], a[200:203], a[0:3], 0\n"
    "s_bitcmp1_b32 s78, 27\n"
    "s_cselect_b32 s99, 0x80000000, s98\n"
    "s_cbranch_scc0 Lo27%=\n"
    "ds_write_b128 v123, v[204:207] offset:3072\n"
    "Lo27%=:\n"
    "ds_read_b64_tr_b16 a[228:229], v217 offset:8480\n"
    "buffer_store_dwordx4 v[204:207], v122, s[60:63], s99 offen offset:3072 sc1\n"
    "v_mfma_f32_16x16x32_bf16 a[140:143], a[200:203], a[4:7], 0\n"
    "ds_read_b64_tr_b16 a[230:231], v217 offset:8736\n"
    "v_mfma_f32_16x16x32_bf16 a[144:147], a[204:207], a[0:3], 0\n"
    "ds_read_b64_tr_b16 a[232:233], v217 offset:8960\n"
    "v_mfma_f32_16x16x32_bf16 a[148:151], a[204:207], a[4:7], 0\n"
    "s_waitcnt lgkmcnt(9)\n"
    "ds_read_b64_tr_b16 a[234:235], v217 offset:9216\n"
    "v_mfma_f32_16x16x32_bf16 a[152:155], a[208:211], a[0:3], 0\n"
    "ds_read_b64_tr_b16 a[236:237], v217 offset:8992\n"
    "v_mfma_f32_16x16x32_bf16 a[156:159], a[208:211], a[4:7], 0\n"
    "ds_read_b64_tr_b16 a[238:239], v217 offset:9248\n"
    "v_mfma_f32_16x16x32_bf16 a[160:163], a[212:215], a[0:3], 0\n"
    "ds_read_b64_tr_b16 a[240:241], v217 offset:12672\n"
    "v_mfma_f32_16x16x32_bf16 a[164:167], a[212:215], a[4:7], 0\n"
    "s_waitcnt lgkmcnt(9)\n"
    "ds_read_b64_tr_b16 a[242:243], v217 offset:12928\n"
    "v_mfma_f32_16x16x32_bf16 a[168:171], a[216:219], a[0:3], 0\n"
    "ds_read_b64_tr_b16 a[244:245], v217 offset:12704\n"
    "v_mfma_f32_16x16x32_bf16 a[172:175], a[216:219], a[4:7], 0\n"
    "ds_read_b64_tr_b16 a[246:247], v217 offset:12960\n"
    "v_mfma_f32_16x16x32_bf16 a[176:179], a[220:223], a[0:3], 0\n"
    "ds_read_b64_tr_b16 a[248:249], v217 offset:13184\n"
    "v_mfma_f32_16x16x32_bf16 a[180:183], a[220:223], a[4:7], 0\n"
    "s_waitcnt lgkmcnt(9)\n"
    "ds_read_b64_tr_b16 a[250:251], v217 offset:13440\n"
    "v_mfma_f32_16x16x32_bf16 a[120:123], a[224:227], a[8:11], a[120:123]\n"
    "ds_read_b64_tr_b16 a[252:253], v217 offset:13216\n"
    "v_mfma_f32_16x16x32_bf16 a[124:127], a[224:227], a[12:15], a[124:127]\n"
    "ds_read_b64_tr_b16 a[254:255], v217 offset:13472\n"
    "v_mfma_f32_16x16x32_bf16 a[128:131], a[228:231], a[8:11], a[128:131]\n"
    "v_mfma_f32_16x16x32_bf16 a[132:135], a[228:231], a[12:15], a[132:135]\n"
    "s_waitcnt lgkmcnt(8)\n"
    "v_mfma_f32_16x16x32_bf16 a[136:139], a[232:235], a[8:11], a[136:139]\n"
    "v_mfma_f32_16x16x32_bf16 a[140:143], a[232:235], a[12:15], a[140:143]\n"
    "v_mfma_f32_16x16x32_bf16 a[144:147], a[236:239], a[8:11], a[144:147]\n"
    "v_mfma_f32_16x16x32_bf16 a[148:151], a[236:239], a[12:15], a[148:151]\n"
    "s_waitcnt lgkmcnt(4)\n"
    "v_mfma_f32_16x16x32_bf16 a[152:155], a[240:243], a[8:11], a[152:155]\n"
    "v_mfma_f32_16x16x32_bf16 a[156:159], a[240:243], a[12:15], a[156:159]\n"
    "v_mfma_f32_16x16x32_bf16 a[160:163], a[244:247], a[8:11], a[160:163]\n"
    "v_mfma_f32_16x16x32_bf16 a[164:167], a[244:247], a[12:15], a[164:167]\n"
    "s_waitcnt lgkmcnt(0)\n"
    "v_mfma_f32_16x16x32_bf16 a[168:171], a[248:251], a[8:11], a[168:171]\n"
    "v_mfma_f32_16x16x32_bf16 a[172:175], a[248:251], a[12:15], a[172:175]\n"
    "v_mfma_f32_16x16x32_bf16 a[176:179], a[252:255], a[8:11], a[176:179]\n"
    "v_mfma_f32_16x16x32_bf16 a[180:183], a[252:255], a[12:15], a[180:183]\n"
    "s_mov_b32 s85, 0x10000\n"
    "s_waitcnt vmcnt(0) lgkmcnt(0)\n"
    "s_barrier\n"
    "buffer_store_dwordx2 v[226:227], v224, s[64:67], 0 offen sc1\n"
    "s_lshl_b32 s84, s81, 11\n"
    "v_add_u32 v123, s84, v123\n"
    "ds_read_b128 v[0:3], v123 offset:0\n"
    "ds_read_b128 v[4:7], v123 offset:1024\n"
    "s_and_b32 s78, %1, 7\n"
    "s_lshl_b32 s78, s78, 1\n"
    "s_bfe_u32 s84, %1, 0x10003\n"
    "s_add_u32 s78, s78, s84\n"
    "s_lshl_b32 s78, 1, s78\n"
    "s_add_u32 m0, s73, 0\n"
    "s_mov_b32 s74, 57344\n"
    "buffer_load_dwordx4 v212, s[48:51], s74 offen lds\n"
    "s_add_u32 m0, s73, 1056\n"
    "s_mov_b32 s74, 60928\n"
    "buffer_load_dwordx4 v212, s[48:51], s74 offen lds\n"
    "s_add_u32 m0, s73, 2112\n"
    "s_mov_b32 s74, 64512\n"
    "buffer_load_dwordx4 v212, s[48:51], s74 offen lds\n"
    "s_add_u32 m0, s73, 3168\n"
    "s_mov_b32 s74, 68096\n"
    "buffer_load_dwordx4 v212, s[48:51], s74 offen lds\n"
    "s_add_u32 m0, s73, 4224\n"
    "s_mov_b32 s74, 57344\n"
    "buffer_load_dwordx4 v213, s[48:51], s74 offen lds\n"
    "s_add_u32 m0, s73, 5280\n"
    "s_mov_b32 s74, 60928\n"
    "buffer_load_dwordx4 v213, s[48:51], s74 offen lds\n"
    "s_add_u32 m0, s73, 6336\n"
    "s_mov_b32 s74, 64512\n"
    "buffer_load_dwordx4 v213, s[48:51], s74 offen lds\n"
    "s_add_u32 m0, s73, 7392\n"
    "s_mov_b32 s74, 68096\n"
    "buffer_load_dwordx4 v213, s[48:51], s74 offen lds\n"
    "s_add_u32 m0, s73, 8448\n"
    "s_mov_b32 s74, 100352\n"
    "buffer_load_dwordx4 v212, s[48:51], s74 offen lds\n"
    "s_add_u32 m0, s73, 9504\n"
    "s_mov_b32 s74, 103936\n"
    "buffer_load_dwordx4 v212, s[48:51], s74 offen lds\n"
    "s_add_u32 m0, s73, 10560\n"
    "s_mov_b32 s74, 107520\n"
    "buffer_load_dwordx4 v212, s[48:51], s74 offen lds\n"
    "s_add_u32 m0, s73, 11616\n"
    "s_mov_b32 s74, 111104\n"
    "buffer_load_dwordx4 v212, s[48:51], s74 offen lds\n"
    "s_add_u32 m0, s73, 12672\n"
    "s_mov_b32 s74, 100352\n"
    "buffer_load_dwordx4 v213, s[48:51], s74 offen lds\n"
    "s_add_u32 m0, s73, 13728\n"
    "s_mov_b32 s74, 103936\n"
    "buffer_load_dwordx4 v213, s[48:51], s74 offen lds\n"
    "s_add_u32 m0, s73, 14784\n"
    "s_mov_b32 s74, 107520\n"
    "buffer_load_dwordx4 v213, s[48:51], s74 offen lds\n"
    "s_add_u32 m0, s73, 15840\n"
    "s_mov_b32 s74, 111104\n"
    "buffer_load_dwordx4 v213, s[48:51], s74 offen lds\n"
    "s_add_u32 m0, s72, 0\n"
    "s_mov_b32 s74, 129024\n"
    "buffer_load_dwordx4 v212, s[48:51], s74 offen lds\n"
    "s_add_u32 m0, s72, 1056\n"
    "s_mov_b32 s74, 132608\n"
    "buffer_load_dwordx4 v212, s[48:51], s74 offen lds\n"
    "s_add_u32 m0, s72, 2112\n"
    "s_mov_b32 s74, 136192\n"
    "buffer_load_dwordx4 v212, s[48:51], s74 offen lds\n"
    "s_add_u32 m0, s72, 3168\n"
    "s_mov_b32 s74, 139776\n"
    "buffer_load_dwordx4 v212, s[48:51], s74 offen lds\n"
    "s_add_u32 m0, s72, 4224\n"
    "s_mov_b32 s74, 129024\n"
    "buffer_load_dwordx4 v213, s[48:51], s74 offen lds\n"
    "s_add_u32 m0, s72, 5280\n"
    "s_mov_b32 s74, 132608\n"
    "buffer_load_dwordx4 v213, s[48:51], s74 offen lds\n"
    "s_add_u32 m0, s72, 6336\n"
    "s_mov_b32 s74, 136192\n"
    "buffer_load_dwordx4 v213, s[48:51], s74 offen lds\n"
    "s_add_u32 m0, s72, 7392\n"
    "s_mov_b32 s74, 139776\n"
    "buffer_load_dwordx4 v213, s[48:51], s74 offen lds\n"
    "s_add_u32 m0, s72, 8448\n"
    "s_mov_b32 s74, 157696\n"
    "buffer_load_dwordx4 v212, s[48:51], s74 offen lds\n"
    "s_add_u32 m0, s72, 9504\n"
    "s_mov_b32 s74, 161280\n"
    "buffer_load_dwordx4 v212, s[48:51], s74 offen lds\n"
    "s_add_u32 m0, s72, 10560\n"
    "s_mov_b32 s74, 164864\n"
    "buffer_load_dwordx4 v212, s[48:51], s74 offen lds\n"
    "s_add_u32 m0, s72, 11616\n"
    "s_mov_b32 s74, 168448\n"
    "buffer_load_dwordx4 v212, s[48:51], s74 offen lds\n"
    "s_add_u32 m0, s72, 12672\n"
    "s_mov_b32 s74, 157696\n"
    "buffer_load_dwordx4 v213, s[48:51], s74 offen lds\n"
    "s_add_u32 m0, s72, 13728\n"
    "s_mov_b32 s74, 161280\n"
    "buffer_load_dwordx4 v213, s[48:51], s74 offen lds\n"
    "s_add_u32 m0, s72, 14784\n"
    "s_mov_b32 s74, 164864\n"
    "buffer_load_dwordx4 v213, s[48:51], s74 offen lds\n"
    "s_add_u32 m0, s72, 15840\n"
    "s_mov_b32 s74, 168448\n"
    "buffer_load_dwordx4 v213, s[48:51], s74 offen lds\n"
    "s_waitcnt lgkmcnt(0)\n"
    "v_mfma_f32_16x16x32_bf16 v[240:243], a[112:115], v[0:3], 0\n"
    "v_add_u32 v217, s73, v124\n"
    "v_lshlrev_b32 v200, 16, v232\n"
    "v_mfma_f32_16x16x32_bf16 v[244:247], a[116:119], v[0:3], 0\n"
    "s_waitcnt vmcnt(24)\n"
    "ds_read_b64_tr_b16 a[192:193], v217 offset:0\n"
    "ds_read_b64_tr_b16 a[194:195], v217 offset:256\n"
    "v_mfma_f32_16x16x32_bf16 v[248:251], a[112:115], v[4:7], 0\n"
    "ds_read_b64_tr_b16 a[196:197], v217 offset:32\n"
    "ds_read_b64_tr_b16 a[198:199], v217 offset:288\n"
    "v_mfma_f32_16x16x32_bf16 v[252:255], a[116:119], v[4:7], 0\n"
    "s_waitcnt lgkmcnt(0)\n"
    "ds_read_b64_tr_b16 a[200:201], v217 offset:512\n"
    "ds_read_b64_tr_b16 a[202:203], v217 offset:768\n"
    "v_mfma_f32_16x16x32_bf16 a[120:123], a[192:195], a[16:19], a[120:123]\n"
    "ds_read_b64_tr_b16 a[204:205], v217 offset:544\n"
    "ds_read_b64_tr_b16 a[206:207], v217 offset:800\n"
    "v_mfma_f32_16x16x32_bf16 a[124:127], a[192:195], a[20:23], a[124:127]\n"
    "ds_read_b64_tr_b16 a[208:209], v217 offset:4224\n"
    "ds_read_b64_tr_b16 a[210:211], v217 offset:4480\n"
    "v_mfma_f32_16x16x32_bf16 a[128:131], a[196:199], a[16:19], a[128:131]\n"
    "ds_read_b64_tr_b16 a[212:213], v217 offset:4256\n"
    "ds_read_b64_tr_b16 a[214:215], v217 offset:4512\n"
    "v_mfma_f32_16x16x32_bf16 a[132:135], a[196:199], a[20:23], a[132:135]\n"
    "s_waitcnt lgkmcnt(4)\n"
    "ds_read_b64_tr_b16 a[216:217], v217 offset:4736\n"
    "ds_read_b64_tr_b16 a[218:219], v217 offset:4992\n"
    "v_mfma_f32_16x16x32_bf16 a[136:139], a[200:203], a[16:19], a[136:139]\n"
    "ds_read_b64_tr_b16 a[220:221], v217 offset:4768\n"
    "v_and_b32 v201, 0xffff0000, v232\n"
    "v_mfma_f32_16x16x32_bf16 a[140:143], a[200:203], a[20:23], a[140:143]\n"
    "ds_read_b64_tr_b16 a[222:223], v217 offset:5024\n"
    "v_lshlrev_b32 v202, 16, v233\n"
    "v_mfma_f32_16x16x32_bf16 a[144:147], a[204:207], a[16:19], a[144:147]\n"
    "v_add_u32 v217, s73, v124\n"
    "v_and_b32 v203, 0xffff0000, v233\n"
    "v_mfma_f32_16x16x32_bf16 a[148:151], a[204:207], a[20:23], a[148:151]\n"
    "s_waitcnt lgkmcnt(4)\n"
    "v_lshlrev_b32 v204, 16, v234\n"
    "v_and_b32 v205, 0xffff0000, v234\n"
    "v_mfma_f32_16x16x32_bf16 a[152:155], a[208:211], a[16:19], a[152:155]\n"
    "s_waitcnt vmcnt(16)\n"
    "ds_read_b64_tr_b16 a[224:225], v217 offset:8448\n"
    "ds_read_b64_tr_b16 a[226:227], v217 offset:8704\n"
    "v_mfma_f32_16x16x32_bf16 a[156:159], a[208:211], a[20:23], a[156:159]\n"
    "ds_read_b64_tr_b16 a[228:229], v217 offset:8480\n"
    "ds_read_b64_tr_b16 a[230:231], v217 offset:8736\n"
    "v_mfma_f32_16x16x32_bf16 a[160:163], a[212:215], a[16:19], a[160:163]\n"
    "ds_read_b64_tr_b16 a[232:233], v217 offset:8960\n"
    "v_lshlrev_b32 v206, 16, v235\n"
    "v_mfma_f32_16x16x32_bf16 a[164:167], a[212:215], a[20:23], a[164:167]\n"
    "s_waitcnt lgkmcnt(5)\n"
    "ds_read_b64_tr_b16 a[234:235], v217 offset:9216\n"
    "v_and_b32 v207, 0xffff0000, v235\n"
    "v_mfma_f32_16x16x32_bf16 a[168:171], a[216:219], a[16:19], a[168:171]\n"
    "s_add_u32 m0, s73, 0\n"
    "s_mov_b32 s74, 86016\n"
    "buffer_load_dwordx4 v214, s[48:51], s74 offen lds\n"
    "ds_read_b64_tr_b16 a[236:237], v217 offset:8992\n"
    "v_mfma_f32_16x16x32_bf16 a[172:175], a[216:219], a[20:23], a[172:175]\n"
    "s_add_u32 m0, s73, 1056\n"
    "s_mov_b32 s74, 89600\n"
    "buffer_load_dwordx4 v214, s[48:51], s74 offen lds\n"
    "ds_read_b64_tr_b16 a[238:239], v217 offset:9248\n"
    "v_mfma_f32_16x16x32_bf16 a[176:179], a[220:223], a[16:19], a[176:179]\n"
    "s_add_u32 m0, s73, 2112\n"
    "s_mov_b32 s74, 93184\n"
    "buffer_load_dwordx4 v214, s[48:51], s74 offen lds\n"
    "ds_read_b64_tr_b16 a[240:241], v217 offset:12672\n"
    "v_mfma_f32_16x16x32_bf16 a[180:183], a[220:223], a[20:23], a[180:183]\n"
    "s_waitcnt lgkmcnt(5)\n"
    "buffer_load_dwordx2 v[228:229], v225, s[64:67], 0 offen offset:256 sc1\n"
    "ds_read_b64_tr_b16 a[242:243], v217 offset:12928\n"
    "v_mfma_f32_16x16x32_bf16 a[120:123], a[224:227], a[24:27], a[120:123]\n"
    "s_add_u32 m0, s73, 3168\n"
    "s_mov_b32 s74, 96768\n"
    "buffer_load_dwordx4 v214, s[48:51], s74 offen lds\n"
    "ds_read_b64_tr_b16 a[244:245], v217 offset:12704\n"
    "v_mfma_f32_16x16x32_bf16 a[124:127], a[224:227], a[28:31], a[124:127]\n"
    "s_add_u32 m0, s73, 4224\n"
    "s_mov_b32 s74, 86016\n"
    "buffer_load_dwordx4 v215, s[48:51], s74 offen lds\n"
    "ds_read_b64_tr_b16 a[246:247], v217 offset:12960\n"
    "v_mfma_f32_16x16x32_bf16 a[128:131], a[228:231], a[24:27], a[128:131]\n"
    "s_add_u32 m0, s73, 5280\n"
    "s_mov_b32 s74, 89600\n"
    "buffer_load_dwordx4 v215, s[48:51], s74 offen lds\n"
    "ds_read_b64_tr_b16 a[248:249], v217 offset:13184\n"
    "v_mfma_f32_16x16x32_bf16 a[132:135], a[228:231], a[28:31], a[132:135]\n"
    "s_add_u32 m0, s73, 6336\n"
    "s_mov_b32 s74, 93184\n"
    "s_waitcnt lgkmcnt(5)\n"
    "buffer_load_dwordx4 v215, s[48:51], s74 offen lds\n"
    "v_mfma_f32_16x16x32_bf16 a[136:139], a[232:235], a[24:27], a[136:139]\n"
    "s_add_u32 m0, s73, 7392\n"
    "s_mov_b32 s74, 96768\n"
    "buffer_load_dwordx4 v215, s[48:51], s74 offen lds\n"
    "ds_read_b64_tr_b16 a[250:251], v217 offset:13440\n"
    "v_mfma_f32_16x16x32_bf16 a[140:143], a[232:235], a[28:31], a[140:143]\n"
    "ds_read_b64_tr_b16 a[252:253], v217 offset:13216\n"
    "ds_read_b64_tr_b16 a[254:255], v217 offset:13472\n"
    "v_mfma_f32_16x16x32_bf16 a[144:147], a[236:239], a[24:27], a[144:147]\n"
    "v_add_u32 v217, s72, v124\n"
    "v_mfma_f32_16x16x32_bf16 a[148:151], a[236:239], a[28:31], a[148:151]\n"
    "s_waitcnt lgkmcnt(4)\n"
    "v_mfma_f32_16x16x32_bf16 a[152:155], a[240:243], a[24:27], a[152:155]\n"
    "s_waitcnt vmcnt(17)\n"
    "ds_read_b64_tr_b16 a[192:193], v217 offset:0\n"
    "ds_read_b64_tr_b16 a[194:195], v217 offset:256\n"
    "v_mfma_f32_16x16x32_bf16 a[156:159], a[240:243], a[28:31], a[156:159]\n"
    "ds_read_b64_tr_b16 a[196:197], v217 offset:32\n"
    "ds_read_b64_tr_b16 a[198:199], v217 offset:288\n"
    "v_mfma_f32_16x16x32_bf16 a[160:163], a[244:247], a[24:27], a[160:163]\n"
    "ds_read_b64_tr_b16 a[200:201], v217 offset:512\n"
    "v_mfma_f32_16x16x32_bf16 a[164:167], a[244:247], a[28:31], a[164:167]\n"
    "s_waitcnt lgkmcnt(5)\n"
    "ds_read_b64_tr_b16 a[202:203], v217 offset:768\n"
    "v_mfma_f32_16x16x32_bf16 a[168:171], a[248:251], a[24:27], a[168:171]\n"
    "s_add_u32 m0, s73, 8448\n"
    "s_mov_b32 s74, 200704\n"
    "buffer_load_dwordx4 v212, s[48:51], s74 offen lds\n"
    "ds_read_b64_tr_b16 a[204:205], v217 offset:544\n"
    "v_mfma_f32_16x16x32_bf16 a[172:175], a[248:251], a[28:31], a[172:175]\n"
    "s_add_u32 m0, s73, 9504\n"
    "s_mov_b32 s74, 204288\n"
    "buffer_load_dwordx4 v212, s[48:51], s74 offen lds\n"
    "ds_read_b64_tr_b16 a[206:207], v217 offset:800\n"
    "v_mfma_f32_16x16x32_bf16 a[176:179], a[252:255], a[24:27], a[176:179]\n"
    "s_add_u32 m0, s73, 10560\n"
    "s_mov_b32 s74, 207872\n"
    "buffer_load_dwordx4 v212, s[48:51], s74 offen lds\n"
    "ds_read_b64_tr_b16 a[208:209], v217 offset:4224\n"
    "v_mfma_f32_16x16x32_bf16 a[180:183], a[252:255], a[28:31], a[180:183]\n"
    "s_waitcnt lgkmcnt(5)\n"
    "ds_read_b64_tr_b16 a[210:211], v217 offset:4480\n"
    "v_mfma_f32_16x16x32_bf16 a[120:123], a[192:195], a[32:35], a[120:123]\n"
    "s_add_u32 m0, s73, 11616\n"
    "s_mov_b32 s74, 211456\n"
    "buffer_load_dwordx4 v212, s[48:51], s74 offen lds\n"
    "ds_read_b64_tr_b16 a[212:213], v217 offset:4256\n"
    "v_mfma_f32_16x16x32_bf16 a[124:127], a[192:195], a[36:39], a[124:127]\n"
    "s_add_u32 m0, s73, 12672\n"
    "s_mov_b32 s74, 200704\n"
    "buffer_load_dwordx4 v213, s[48:51], s74 offen lds\n"
    "ds_read_b64_tr_b16 a[214:215], v217 offset:4512\n"
    "v_mfma_f32_16x16x32_bf16 a[128:131], a[196:199], a[32:35], a[128:131]\n"
    "s_add_u32 m0, s73, 13728\n"
    "s_mov_b32 s74, 204288\n"
    "buffer_load_dwordx4 v213, s[48:51], s74 offen lds\n"
    "ds_read_b64_tr_b16 a[216:217], v217 offset:4736\n"
    "v_mfma_f32_16x16x32_bf16 a[132:135], a[196:199], a[36:39], a[132:135]\n"
    "s_add_u32 m0, s73, 14784\n"
    "s_mov_b32 s74, 207872\n"
    "s_waitcnt lgkmcnt(5)\n"
    "buffer_load_dwordx4 v213, s[48:51], s74 offen lds\n"
    "v_mfma_f32_16x16x32_bf16 a[136:139], a[200:203], a[32:35], a[136:139]\n"
    "s_add_u32 m0, s73, 15840\n"
    "s_mov_b32 s74, 211456\n"
    "buffer_load_dwordx4 v213, s[48:51], s74 offen lds\n"
    "ds_read_b64_tr_b16 a[218:219], v217 offset:4992\n"
    "v_mfma_f32_16x16x32_bf16 a[140:143], a[200:203], a[36:39], a[140:143]\n"
    "ds_read_b64_tr_b16 a[220:221], v217 offset:4768\n"
    "ds_read_b64_tr_b16 a[222:223], v217 offset:5024\n"
    "v_mfma_f32_16x16x32_bf16 a[144:147], a[204:207], a[32:35], a[144:147]\n"
    "v_add_u32 v217, s72, v124\n"
    "v_mfma_f32_16x16x32_bf16 a[148:151], a[204:207], a[36:39], a[148:151]\n"
    "s_waitcnt lgkmcnt(4)\n"
    "v_mfma_f32_16x16x32_bf16 a[152:155], a[208:211], a[32:35], a[152:155]\n"
    "s_waitcnt vmcnt(17)\n"
    "ds_read_b64_tr_b16 a[224:225], v217 offset:8448\n"
    "ds_read_b64_tr_b16 a[226:227], v217 offset:8704\n"
    "v_mfma_f32_16x16x32_bf16 a[156:159], a[208:211], a[36:39], a[156:159]\n"
    "ds_read_b64_tr_b16 a[228:229], v217 offset:8480\n"
    "ds_read_b64_tr_b16 a[230:231], v217 offset:8736\n"
    "v_mfma_f32_16x16x32_bf16 a[160:163], a[212:215], a[32:35], a[160:163]\n"
    "ds_read_b64_tr_b16 a[232:233], v217 offset:8960\n"
    "v_mfma_f32_16x16x32_bf16 a[164:167], a[212:215], a[36:39], a[164:167]\n"
    "s_waitcnt lgkmcnt(5)\n"
    "ds_read_b64_tr_b16 a[234:235], v217 offset:9216\n"
    "v_mfma_f32_16x16x32_bf16 a[168:171], a[216:219], a[32:35], a[168:171]\n"
    "s_add_u32 m0, s72, 0\n"
    "s_mov_b32 s74, 229376\n"
    "buffer_load_dwordx4 v212, s[48:51], s74 offen lds\n"
    "ds_read_b64_tr_b16 a[236:237], v217 offset:8992\n"
    "v_mfma_f32_16x16x32_bf16 a[172:175], a[216:219], a[36:39], a[172:175]\n"
    "s_add_u32 m0, s72, 1056\n"
    "s_mov_b32 s74, 232960\n"
    "buffer_load_dwordx4 v212, s[48:51], s74 offen lds\n"
    "ds_read_b64_tr_b16 a[238:239], v217 offset:9248\n"
    "v_mfma_f32_16x16x32_bf16 a[176:179], a[220:223], a[32:35], a[176:179]\n"
    "s_add_u32 m0, s72, 2112\n"
    "s_mov_b32 s74, 236544\n"
    "buffer_load_dwordx4 v212, s[48:51], s74 offen lds\n"
    "ds_read_b64_tr_b16 a[240:241], v217 offset:12672\n"
    "v_mfma_f32_16x16x32_bf16 a[180:183], a[220:223], a[36:39], a[180:183]\n"
    "s_waitcnt lgkmcnt(5)\n"
    "ds_read_b64_tr_b16 a[242:243], v217 offset:12928\n"
    "v_mfma_f32_16x16x32_bf16 a[120:123], a[224:227], a[40:43], a[120:123]\n"
    "s_add_u32 m0, s72, 3168\n"
    "s_mov_b32 s74, 240128\n"
    "buffer_load_dwordx4 v212, s[48:51], s74 offen lds\n"
    "ds_read_b64_tr_b16 a[244:245], v217 offset:12704\n"
    "v_mfma_f32_16x16x32_bf16 a[124:127], a[224:227], a[44:47], a[124:127]\n"
    "s_add_u32 m0, s72, 4224\n"
    "s_mov_b32 s74, 229376\n"
    "buffer_load_dwordx4 v213, s[48:51], s74 offen lds\n"
    "ds_read_b64_tr_b16 a[246:247], v217 offset:12960\n"
    "v_mfma_f32_16x16x32_bf16 a[128:131], a[228:231], a[40:43], a[128:131]\n"
    "s_add_u32 m0, s72, 5280\n"
    "s_mov_b32 s74, 232960\n"
    "buffer_load_dwordx4 v213, s[48:51], s74 offen lds\n"
    "ds_read_b64_tr_b16 a[248:249], v217 offset:13184\n"
    "v_mfma_f32_16x16x32_bf16 a[132:135], a[228:231], a[44:47], a[132:135]\n"
    "s_add_u32 m0, s72, 6336\n"
    "s_mov_b32 s74, 236544\n"
    "s_waitcnt lgkmcnt(5)\n"
    "buffer_load_dwordx4 v213, s[48:51], s74 offen lds\n"
    "v_mfma_f32_16x16x32_bf16 a[136:139], a[232:235], a[40:43], a[136:139]\n"
    "s_add_u32 m0, s72, 7392\n"
    "s_mov_b32 s74, 240128\n"
    "s_waitcnt vmcnt(20)\n"
    "v_cmp_eq_u32 vcc, s33, v228\n"
    "v_cmp_eq_u32 s[86:87], s34, v229\n"
    "s_nop 0\n"
    "s_and_b32 s75, vcc_lo, s86\n"
    "s_and_b32 s75, s75, 0xffff\n"
    "s_andn2_b32 s75, s75, s78\n"
    "buffer_load_dwordx4 v213, s[48:51], s74 offen lds\n"
    "v_mfma_f32_16x16x32_bf16 a[140:143], a[232:235], a[44:47], a[140:143]\n"
    "s_mov_b32 s28, 0\n"
    "s_bitcmp1_b32 s75, 0\n"
    "s_cselect_b32 s28, s28, 0x80000000\n"
    "ds_read_b64_tr_b16 a[250:251], v217 offset:13440\n"
    "buffer_load_dwordx4 v[64:67], v218, s[44:47], s28 offen offset:0 sc1\n"
    "v_mfma_f32_16x16x32_bf16 a[144:147], a[236:239], a[40:43], a[144:147]\n"
    "buffer_load_dwordx4 v[68:71], v218, s[44:47], s28 offen offset:1024 sc1\n"
    "ds_read_b64_tr_b16 a[252:253], v217 offset:13216\n"
    "v_mfma_f32_16x16x32_bf16 a[148:151], a[236:239], a[44:47], a[148:151]\n"
    "s_waitcnt lgkmcnt(3)\n"
    "ds_read_b64_tr_b16 a[254:255], v217 offset:13472\n"
    "v_add_u32 v217, s73, v124\n"
    "v_mfma_f32_16x16x32_bf16 a[152:155], a[240:243], a[40:43], a[152:155]\n"
    "s_waitcnt vmcnt(18)\n"
    "ds_read_b64_tr_b16 a[192:193], v217 offset:0\n"
    "ds_read_b64_tr_b16 a[194:195], v217 offset:256\n"
    "v_mfma_f32_16x16x32_bf16 a[156:159], a[240:243], a[44:47], a[156:159]\n"
    "s_mov_b32 s28, 143360\n"
    "s_bitcmp1_b32 s75, 1\n"
    "s_cselect_b32 s28, s28, 0x80000000\n"
    "ds_read_b64_tr_b16 a[196:197], v217 offset:32\n"
    "ds_read_b64_tr_b16 a[198:199], v217 offset:288\n"
    "v_mfma_f32_16x16x32_bf16 a[160:163], a[244:247], a[40:43], a[160:163]\n"
    "buffer_load_dwordx4 v[72:75], v218, s[44:47], s28 offen offset:0 sc1\n"
    "buffer_load_dwordx4 v[76:79], v218, s[44:47], s28 offen offset:1024 sc1\n"
    "v_mfma_f32_16x16x32_bf16 a[164:167], a[244:247], a[44:47], a[164:167]\n"
    "s_waitcnt lgkmcnt(4)\n"
    "ds_read_b64_tr_b16 a[200:201], v217 offset:512\n"
    "ds_read_b64_tr_b16 a[202:203], v217 offset:768\n"
    "v_mfma_f32_16x16x32_bf16 a[168:171], a[248:251], a[40:43], a[168:171]\n"
    "s_add_u32 m0, s72, 8448\n"
    "s_mov_b32 s74, 258048\n"
    "buffer_load_dwordx4 v212, s[48:51], s74 offen lds\n"
    "ds_read_b64_tr_b16 a[204:205], v217 offset:544\n"
    "v_mfma_f32_16x16x32_bf16 a[172:175], a[248:251], a[44:47], a[172:175]\n"
    "s_add_u32 m0, s72, 9504\n"
    "s_mov_b32 s74, 261632\n"
    "buffer_load_dwordx4 v212, s[48:51], s74 offen lds\n"
    "ds_read_b64_tr_b16 a[206:207], v217 offset:800\n"
    "v_mfma_f32_16x16x32_bf16 a[176:179], a[252:255], a[40:43], a[176:179]\n"
    "s_add_u32 m0, s72, 10560\n"
    "s_mov_b32 s74, 265216\n"
    "buffer_load_dwordx4 v212, s[48:51], s74 offen lds\n"
    "ds_read_b64_tr_b16 a[208:209], v217 offset:4224\n"
    "v_mfma_f32_16x16x32_bf16 a[180:183], a[252:255], a[44:47], a[180:183]\n"
    "s_waitcnt lgkmcnt(5)\n"
    "ds_read_b64_tr_b16 a[210:211], v217 offset:4480\n"
    "v_mfma_f32_16x16x32_bf16 a[120:123], a[192:195], a[96:99], a[120:123]\n"
    "s_add_u32 m0, s72, 11616\n"
    "s_mov_b32 s74, 268800\n"
    "buffer_load_dwordx4 v212, s[48:51], s74 offen lds\n"
    "ds_read_b64_tr_b16 a[212:213], v217 offset:4256\n"
    "v_mfma_f32_16x16x32_bf16 a[124:127], a[192:195], a[100:103], a[124:127]\n"
    "s_add_u32 m0, s72, 12672\n"
    "s_mov_b32 s74, 258048\n"
    "buffer_load_dwordx4 v213, s[48:51], s74 offen lds\n"
    "ds_read_b64_tr_b16 a[214:215], v217 offset:4512\n"
    "v_mfma_f32_16x16x32_bf16 a[128:131], a[196:199], a[96:99], a[128:131]\n"
    "s_add_u32 m0, s72, 13728\n"
    "s_mov_b32 s74, 261632\n"
    "buffer_load_dwordx4 v213, s[48:51], s74 offen lds\n"
    "ds_read_b64_tr_b16 a[216:217], v217 offset:4736\n"
    "v_mfma_f32_16x16x32_bf16 a[132:135], a[196:199], a[100:103], a[132:135]\n"
    "s_add_u32 m0, s72, 14784\n"
    "s_mov_b32 s74, 265216\n"
    "s_waitcnt lgkmcnt(5)\n"
    "buffer_load_dwordx4 v213, s[48:51], s74 offen lds\n"
    "v_mfma_f32_16x16x32_bf16 a[136:139], a[200:203], a[96:99], a[136:139]\n"
    "s_add_u32 m0, s72, 15840\n"
    "s_mov_b32 s74, 268800\n"
    "buffer_load_dwordx4 v213, s[48:51], s74 offen lds\n"
    "ds_read_b64_tr_b16 a[218:219], v217 offset:4992\n"
    "v_mfma_f32_16x16x32_bf16 a[140:143], a[200:203], a[100:103], a[140:143]\n"
    "s_mov_b32 s28, 286720\n"
    "s_bitcmp1_b32 s75, 2\n"
    "s_cselect_b32 s28, s28, 0x80000000\n"
    "buffer_load_dwordx4 v[80:83], v218, s[44:47], s28 offen offset:0 sc1\n"
    "buffer_load_dwordx4 v[84:87], v218, s[44:47], s28 offen offset:1024 sc1\n"
    "v_mfma_f32_16x16x32_bf16 a[144:147], a[204:207], a[96:99], a[144:147]\n"
    "ds_read_b64_tr_b16 a[220:221], v217 offset:4768\n"
    "ds_read_b64_tr_b16 a[222:223], v217 offset:5024\n"
    "v_mfma_f32_16x16x32_bf16 a[148:151], a[204:207], a[100:103], a[148:151]\n"
    "s_waitcnt lgkmcnt(4)\n"
    "v_add_u32 v217, s73, v124\n"
    "v_mfma_f32_16x16x32_bf16 a[152:155], a[208:211], a[96:99], a[152:155]\n"
    "s_waitcnt vmcnt(22)\n"
    "ds_read_b64_tr_b16 a[224:225], v217 offset:8448\n"
    "ds_read_b64_tr_b16 a[226:227], v217 offset:8704\n"
    "v_mfma_f32_16x16x32_bf16 a[156:159], a[208:211], a[100:103], a[156:159]\n"
    "s_mov_b32 s28, 430080\n"
    "s_bitcmp1_b32 s75, 3\n"
    "s_cselect_b32 s28, s28, 0x80000000\n"
    "ds_read_b64_tr_b16 a[228:229], v217 offset:8480\n"
    "ds_read_b64_tr_b16 a[230:231], v217 offset:8736\n"
    "v_mfma_f32_16x16x32_bf16 a[160:163], a[212:215], a[96:99], a[160:163]\n"
    "buffer_load_dwordx4 v[88:91], v218, s[44:47], s28 offen offset:0 sc1\n"
    "buffer_load_dwordx4 v[92:95], v218, s[44:47], s28 offen offset:1024 sc1\n"
    "v_mfma_f32_16x16x32_bf16 a[164:167], a[212:215], a[100:103], a[164:167]\n"
    "s_waitcnt lgkmcnt(4)\n"
    "ds_read_b64_tr_b16 a[232:233], v217 offset:8960\n"
    "ds_read_b64_tr_b16 a[234:235], v217 offset:9216\n"
    "v_mfma_f32_16x16x32_bf16 a[168:171], a[216:219], a[96:99], a[168:171]\n"
    "s_add_u32 m0, s73, 0\n"
    "s_mov_b32 s74, 301056\n"
    "buffer_load_dwordx4 v212, s[48:51], s74 offen lds\n"
    "ds_read_b64_tr_b16 a[236:237], v217 offset:8992\n"
    "v_mfma_f32_16x16x32_bf16 a[172:175], a[216:219], a[100:103], a[172:175]\n"
    "s_add_u32 m0, s73, 1056\n"
    "s_mov_b32 s74, 304640\n"
    "buffer_load_dwordx4 v212, s[48:51], s74 offen lds\n"
    "ds_read_b64_tr_b16 a[238:239], v217 offset:9248\n"
    "v_mfma_f32_16x16x32_bf16 a[176:179], a[220:223], a[96:99], a[176:179]\n"
    "s_add_u32 m0, s73, 2112\n"
    "s_mov_b32 s74, 308224\n"
    "buffer_load_dwordx4 v212, s[48:51], s74 offen lds\n"
    "ds_read_b64_tr_b16 a[240:241], v217 offset:12672\n"
    "v_mfma_f32_16x16x32_bf16 a[180:183], a[220:223], a[100:103], a[180:183]\n"
    "s_waitcnt lgkmcnt(5)\n"
    "ds_read_b64_tr_b16 a[242:243], v217 offset:12928\n"
    "v_mfma_f32_16x16x32_bf16 a[120:123], a[224:227], a[48:51], a[120:123]\n"
    "s_add_u32 m0, s73, 3168\n"
    "s_mov_b32 s74, 311808\n"
    "buffer_load_dwordx4 v212, s[48:51], s74 offen lds\n"
    "ds_read_b64_tr_b16 a[244:245], v217 offset:12704\n"
    "v_mfma_f32_16x16x32_bf16 a[124:127], a[224:227], a[52:55], a[124:127]\n"
    "s_add_u32 m0, s73, 4224\n"
    "s_mov_b32 s74, 301056\n"
    "buffer_load_dwordx4 v213, s[48:51], s74 offen lds\n"
    "ds_read_b64_tr_b16 a[246:247], v217 offset:12960\n"
    "v_mfma_f32_16x16x32_bf16 a[128:131], a[228:231], a[48:51], a[128:131]\n"
    "s_add_u32 m0, s73, 5280\n"
    "s_mov_b32 s74, 304640\n"
    "buffer_load_dwordx4 v213, s[48:51], s74 offen lds\n"
    "ds_read_b64_tr_b16 a[248:249], v217 offset:13184\n"
    "v_mfma_f32_16x16x32_bf16 a[132:135], a[228:231], a[52:55], a[132:135]\n"
    "s_add_u32 m0, s73, 6336\n"
    "s_mov_b32 s74, 308224\n"
    "s_waitcnt lgkmcnt(5)\n"
    "buffer_load_dwordx4 v213, s[48:51], s74 offen lds\n"
    "v_mfma_f32_16x16x32_bf16 a[136:139], a[232:235], a[48:51], a[136:139]\n"
    "s_add_u32 m0, s73, 7392\n"
    "s_mov_b32 s74, 311808\n"
    "buffer_load_dwordx4 v213, s[48:51], s74 offen lds\n"
    "ds_read_b64_tr_b16 a[250:251], v217 offset:13440\n"
    "v_mfma_f32_16x16x32_bf16 a[140:143], a[232:235], a[52:55], a[140:143]\n"
    "s_mov_b32 s28, 573440\n"
    "s_bitcmp1_b32 s75, 4\n"
    "s_cselect_b32 s28, s28, 0x80000000\n"
    "buffer_load_dwordx4 v[96:99], v218, s[44:47], s28 offen offset:0 sc1\n"
    "buffer_load_dwordx4 v[100:103], v218, s[44:47], s28 offen offset:1024 sc1\n"
    "v_mfma_f32_16x16x32_bf16 a[144:147], a[236:239], a[48:51], a[144:147]\n"
    "ds_read_b64_tr_b16 a[252:253], v217 offset:13216\n"
    "ds_read_b64_tr_b16 a[254:255], v217 offset:13472\n"
    "v_mfma_f32_16x16x32_bf16 a[148:151], a[236:239], a[52:55], a[148:151]\n"
    "s_waitcnt lgkmcnt(4)\n"
    "v_mfma_f32_16x16x32_bf16 a[152:155], a[240:243], a[48:51], a[152:155]\n"
    "v_mfma_f32_16x16x32_bf16 a[156:159], a[240:243], a[52:55], a[156:159]\n"
    "s_mov_b32 s28, 716800\n"
    "s_bitcmp1_b32 s75, 5\n"
    "s_cselect_b32 s28, s28, 0x80000000\n"
    "buffer_load_dwordx4 v[104:107], v218, s[44:47], s28 offen offset:0 sc1\n"
    "buffer_load_dwordx4 v[108:111], v218, s[44:47], s28 offen offset:1024 sc1\n"
    "v_mfma_f32_16x16x32_bf16 a[160:163], a[244:247], a[48:51], a[160:163]\n"
    "v_mfma_f32_16x16x32_bf16 a[164:167], a[244:247], a[52:55], a[164:167]\n"
    "s_waitcnt lgkmcnt(0)\n"
    "v_mfma_f32_16x16x32_bf16 a[168:171], a[248:251], a[48:51], a[168:171]\n"
    "s_add_u32 m0, s73, 8448\n"
    "s_mov_b32 s74, 329728\n"
    "buffer_load_dwordx4 v212, s[48:51], s74 offen lds\n"
    "v_mfma_f32_16x16x32_bf16 a[172:175], a[248:251], a[52:55], a[172:175]\n"
    "s_add_u32 m0, s73, 9504\n"
    "s_mov_b32 s74, 333312\n"
    "buffer_load_dwordx4 v212, s[48:51], s74 offen lds\n"
    "v_mfma_f32_16x16x32_bf16 a[176:179], a[252:255], a[48:51], a[176:179]\n"
    "s_add_u32 m0, s73, 10560\n"
    "s_mov_b32 s74, 336896\n"
    "buffer_load_dwordx4 v212, s[48:51], s74 offen lds\n"
    "v_add_u32 v217, s72, v124\n"
    "v_mfma_f32_16x16x32_bf16 a[180:183], a[252:255], a[52:55], a[180:183]\n"
    "s_waitcnt vmcnt(29)\n"
    "v_mfma_f32_16x16x32_bf16 v[240:243], a[112:115], v[64:67], v[240:243]\n"
    "s_add_u32 m0, s73, 11616\n"
    "s_mov_b32 s74, 340480\n"
    "buffer_load_dwordx4 v212, s[48:51], s74 offen lds\n"
    "v_mfma_f32_16x16x32_bf16 v[244:247], a[116:119], v[64:67], v[244:247]\n"
    "s_add_u32 m0, s73, 12672\n"
    "s_mov_b32 s74, 329728\n"
    "buffer_load_dwordx4 v213, s[48:51], s74 offen lds\n"
    "v_mfma_f32_16x16x32_bf16 v[248:251], a[112:115], v[68:71], v[248:251]\n"
    "s_add_u32 m0, s73, 13728\n"
    "s_mov_b32 s74, 333312\n"
    "buffer_load_dwordx4 v213, s[48:51], s74 offen lds\n"
    "v_mfma_f32_16x16x32_bf16 v[252:255], a[116:119], v[68:71], v[252:255]\n"
    "s_add_u32 m0, s73, 14784\n"
    "s_mov_b32 s74, 336896\n"
    "s_waitcnt vmcnt(30)\n"
    "buffer_load_dwordx4 v213, s[48:51], s74 offen lds\n"
    "v_mfma_f32_16x16x32_bf16 v[240:243], a[112:115], v[72:75], v[240:243]\n"
    "s_add_u32 m0, s73, 15840\n"
    "s_mov_b32 s74, 340480\n"
    "s_waitcnt vmcnt(35)\n"
    "ds_read_b64_tr_b16 a[192:193], v217 offset:0\n"
    "v_mfma_f32_16x16x32_bf16 v[244:247], a[116:119], v[72:75], v[244:247]\n"
    "buffer_load_dwordx4 v213, s[48:51], s74 offen lds\n"
    "ds_read_b64_tr_b16 a[194:195], v217 offset:256\n"
    "v_mfma_f32_16x16x32_bf16 v[248:251], a[112:115], v[76:79], v[248:251]\n"
    "ds_read_b64_tr_b16 a[196:197], v217 offset:32\n"
    "ds_read_b64_tr_b16 a[198:199], v217 offset:288\n"
    "v_mfma_f32_16x16x32_bf16 v[252:255], a[116:119], v[76:79], v[252:255]\n"
    "s_waitcnt lgkmcnt(0)\n"
    "ds_read_b64_tr_b16 a[200:201], v217 offset:512\n"
    "ds_read_b64_tr_b16 a[202:203], v217 offset:768\n"
    "v_mfma_f32_16x16x32_bf16 a[120:123], a[192:195], a[56:59], a[120:123]\n"
    "ds_read_b64_tr_b16 a[204:205], v217 offset:544\n"
    "ds_read_b64_tr_b16 a[206:207], v217 offset:800\n"
    "v_mfma_f32_16x16x32_bf16 a[124:127], a[192:195], a[60:63], a[124:127]\n"
    "ds_read_b64_tr_b16 a[208:209], v217 offset:4224\n"
    "ds_read_b64_tr_b16 a[210:211], v217 offset:4480\n"
    "v_mfma_f32_16x16x32_bf16 a[128:131], a[196:199], a[56:59], a[128:131]\n"
    "ds_read_b64_tr_b16 a[212:213], v217 offset:4256\n"
    "ds_read_b64_tr_b16 a[214:215], v217 offset:4512\n"
    "v_mfma_f32_16x16x32_bf16 a[132:135], a[196:199], a[60:63], a[132:135]\n"
    "s_waitcnt lgkmcnt(4)\n"
    "ds_read_b64_tr_b16 a[216:217], v217 offset:4736\n"
    "ds_read_b64_tr_b16 a[218:219], v217 offset:4992\n"
    "v_mfma_f32_16x16x32_bf16 a[136:139], a[200:203], a[56:59], a[136:139]\n"
    "s_mov_b32 s28, 860160\n"
    "s_bitcmp1_b32 s75, 6\n"
    "s_cselect_b32 s28, s28, 0x80000000\n"
    "buffer_load_dwordx4 v[112:115], v218, s[44:47], s28 offen offset:0 sc1\n"
    "buffer_load_dwordx4 v[116:119], v218, s[44:47], s28 offen offset:1024 sc1\n"
    "v_mfma_f32_16x16x32_bf16 a[140:143], a[200:203], a[60:63], a[140:143]\n"
    "ds_read_b64_tr_b16 a[220:221], v217 offset:4768\n"
    "ds_read_b64_tr_b16 a[222:223], v217 offset:5024\n"
    "v_mfma_f32_16x16x32_bf16 a[144:147], a[204:207], a[56:59], a[144:147]\n"
    "v_mfma_f32_16x16x32_bf16 a[148:151], a[204:207], a[60:63], a[148:151]\n"
    "s_waitcnt lgkmcnt(4)\n"
    "v_mfma_f32_16x16x32_bf16 a[152:155], a[208:211], a[56:59], a[152:155]\n"
    "v_mfma_f32_16x16x32_bf16 a[156:159], a[208:211], a[60:63], a[156:159]\n"
    "s_mov_b32 s28, 1003520\n"
    "s_bitcmp1_b32 s75, 7\n"
    "s_cselect_b32 s28, s28, 0x80000000\n"
    "buffer_load_dwordx4 v[128:131], v218, s[44:47], s28 offen offset:0 sc1\n"
    "buffer_load_dwordx4 v[132:135], v218, s[44:47], s28 offen offset:1024 sc1\n"
    "v_mfma_f32_16x16x32_bf16 a[160:163], a[212:215], a[56:59], a[160:163]\n"
    "v_mfma_f32_16x16x32_bf16 a[164:167], a[212:215], a[60:63], a[164:167]\n"
    "s_waitcnt lgkmcnt(0)\n"
    "v_mfma_f32_16x16x32_bf16 a[168:171], a[216:219], a[56:59], a[168:171]\n"
    "s_add_u32 m0, s72, 0\n"
    "s_mov_b32 s74, 358400\n"
    "buffer_load_dwordx4 v212, s[48:51], s74 offen lds\n"
    "v_mfma_f32_16x16x32_bf16 a[172:175], a[216:219], a[60:63], a[172:175]\n"
    "s_add_u32 m0, s72, 1056\n"
    "s_mov_b32 s74, 361984\n"
    "buffer_load_dwordx4 v212, s[48:51], s74 offen lds\n"
    "v_mfma_f32_16x16x32_bf16 a[176:179], a[220:223], a[56:59], a[176:179]\n"
    "s_add_u32 m0, s72, 2112\n"
    "s_mov_b32 s74, 365568\n"
    "buffer_load_dwordx4 v212, s[48:51], s74 offen lds\n"
    "v_add_u32 v217, s72, v124\n"
    "v_mfma_f32_16x16x32_bf16 a[180:183], a[220:223], a[60:63], a[180:183]\n"
    "s_waitcnt vmcnt(29)\n"
    "v_mfma_f32_16x16x32_bf16 v[240:243], a[112:115], v[80:83], v[240:243]\n"
    "s_add_u32 m0, s72, 3168\n"
    "s_mov_b32 s74, 369152\n"
    "buffer_load_dwordx4 v212, s[48:51], s74 offen lds\n"
    "v_mfma_f32_16x16x32_bf16 v[244:247], a[116:119], v[80:83], v[244:247]\n"
    "s_add_u32 m0, s72, 4224\n"
    "s_mov_b32 s74, 358400\n"
    "buffer_load_dwordx4 v213, s[48:51], s74 offen lds\n"
    "v_mfma_f32_16x16x32_bf16 v[248:251], a[112:115], v[84:87], v[248:251]\n"
    "s_add_u32 m0, s72, 5280\n"
    "s_mov_b32 s74, 361984\n"
    "buffer_load_dwordx4 v213, s[48:51], s74 offen lds\n"
    "v_mfma_f32_16x16x32_bf16 v[252:255], a[116:119], v[84:87], v[252:255]\n"
    "s_add_u32 m0, s72, 6336\n"
    "s_mov_b32 s74, 365568\n"
    "s_waitcnt vmcnt(30)\n"
    "buffer_load_dwordx4 v213, s[48:51], s74 offen lds\n"
    "v_mfma_f32_16x16x32_bf16 v[240:243], a[112:115], v[88:91], v[240:243]\n"
    "s_add_u32 m0, s72, 7392\n"
    "s_mov_b32 s74, 369152\n"
    "s_waitcnt vmcnt(35)\n"
    "ds_read_b64_tr_b16 a[224:225], v217 offset:8448\n"
    "v_mfma_f32_16x16x32_bf16 v[244:247], a[116:119], v[88:91], v[244:247]\n"
    "buffer_load_dwordx4 v213, s[48:51], s74 offen lds\n"
    "ds_read_b64_tr_b16 a[226:227], v217 offset:8704\n"
    "v_mfma_f32_16x16x32_bf16 v[248:251], a[112:115], v[92:95], v[248:251]\n"
    "ds_read_b64_tr_b16 a[228:229], v217 offset:8480\n"
    "ds_read_b64_tr_b16 a[230:231], v217 offset:8736\n"
    "v_mfma_f32_16x16x32_bf16 v[252:255], a[116:119], v[92:95], v[252:255]\n"
    "s_waitcnt lgkmcnt(0)\n"
    "ds_read_b64_tr_b16 a[232:233], v217 offset:8960\n"
    "ds_read_b64_tr_b16 a[234:235], v217 offset:9216\n"
    "v_mfma_f32_16x16x32_bf16 a[120:123], a[224:227], a[64:67], a[120:123]\n"
    "ds_read_b64_tr_b16 a[236:237], v217 offset:8992\n"
    "ds_read_b64_tr_b16 a[238:239], v217 offset:9248\n"
    "v_mfma_f32_16x16x32_bf16 a[124:127], a[224:227], a[68:71], a[124:127]\n"
    "ds_read_b64_tr_b16 a[240:241], v217 offset:12672\n"
    "ds_read_b64_tr_b16 a[242:243], v217 offset:12928\n"
    "v_mfma_f32_16x16x32_bf16 a[128:131], a[228:231], a[64:67], a[128:131]\n"
    "ds_read_b64_tr_b16 a[244:245], v217 offset:12704\n"
    "ds_read_b64_tr_b16 a[246:247], v217 offset:12960\n"
    "v_mfma_f32_16x16x32_bf16 a[132:135], a[228:231], a[68:71], a[132:135]\n"
    "s_waitcnt lgkmcnt(4)\n"
    "ds_read_b64_tr_b16 a[248:249], v217 offset:13184\n"
    "ds_read_b64_tr_b16 a[250:251], v217 offset:13440\n"
    "v_mfma_f32_16x16x32_bf16 a[136:139], a[232:235], a[64:67], a[136:139]\n"
    "s_mov_b32 s28, 1146880\n"
    "s_bitcmp1_b32 s75, 8\n"
    "s_cselect_b32 s28, s28, 0x80000000\n"
    "buffer_load_dwordx4 v[136:139], v218, s[44:47], s28 offen offset:0 sc1\n"
    "buffer_load_dwordx4 v[140:143], v218, s[44:47], s28 offen offset:1024 sc1\n"
    "v_mfma_f32_16x16x32_bf16 a[140:143], a[232:235], a[68:71], a[140:143]\n"
    "ds_read_b64_tr_b16 a[252:253], v217 offset:13216\n"
    "ds_read_b64_tr_b16 a[254:255], v217 offset:13472\n"
    "v_mfma_f32_16x16x32_bf16 a[144:147], a[236:239], a[64:67], a[144:147]\n"
    "v_mfma_f32_16x16x32_bf16 a[148:151], a[236:239], a[68:71], a[148:151]\n"
    "s_waitcnt lgkmcnt(4)\n"
    "v_mfma_f32_16x16x32_bf16 a[152:155], a[240:243], a[64:67], a[152:155]\n"
    "v_mfma_f32_16x16x32_bf16 a[156:159], a[240:243], a[68:71], a[156:159]\n"
    "s_mov_b32 s28, 1290240\n"
    "s_bitcmp1_b32 s75, 9\n"
    "s_cselect_b32 s28, s28, 0x80000000\n"
    "buffer_load_dwordx4 v[144:147], v218, s[44:47], s28 offen offset:0 sc1\n"
    "buffer_load_dwordx4 v[148:151], v218, s[44:47], s28 offen offset:1024 sc1\n"
    "v_mfma_f32_16x16x32_bf16 a[160:163], a[244:247], a[64:67], a[160:163]\n"
    "v_mfma_f32_16x16x32_bf16 a[164:167], a[244:247], a[68:71], a[164:167]\n"
    "s_waitcnt lgkmcnt(0)\n"
    "v_mfma_f32_16x16x32_bf16 a[168:171], a[248:251], a[64:67], a[168:171]\n"
    "s_add_u32 m0, s72, 8448\n"
    "s_mov_b32 s74, 286720\n"
    "buffer_load_dwordx4 v214, s[48:51], s74 offen lds\n"
    "v_mfma_f32_16x16x32_bf16 a[172:175], a[248:251], a[68:71], a[172:175]\n"
    "s_add_u32 m0, s72, 9504\n"
    "s_mov_b32 s74, 290304\n"
    "buffer_load_dwordx4 v214, s[48:51], s74 offen lds\n"
    "v_mfma_f32_16x16x32_bf16 a[176:179], a[252:255], a[64:67], a[176:179]\n"
    "s_add_u32 m0, s72, 10560\n"
    "s_mov_b32 s74, 293888\n"
    "buffer_load_dwordx4 v214, s[48:51], s74 offen lds\n"
    "v_add_u32 v217, s73, v124\n"
    "v_mfma_f32_16x16x32_bf16 a[180:183], a[252:255], a[68:71], a[180:183]\n"
    "s_waitcnt vmcnt(29)\n"
    "v_mfma_f32_16x16x32_bf16 v[240:243], a[112:115], v[96:99], v[240:243]\n"
    "s_add_u32 m0, s72, 11616\n"
    "s_mov_b32 s74, 297472\n"
    "buffer_load_dwordx4 v214, s[48:51], s74 offen lds\n"
    "v_mfma_f32_16x16x32_bf16 v[244:247], a[116:119], v[96:99], v[244:247]\n"
    "s_add_u32 m0, s72, 12672\n"
    "s_mov_b32 s74, 286720\n"
    "buffer_load_dwordx4 v215, s[48:51], s74 offen lds\n"
    "v_mfma_f32_16x16x32_bf16 v[248:251], a[112:115], v[100:103], v[248:251]\n"
    "s_add_u32 m0, s72, 13728\n"
    "s_mov_b32 s74, 290304\n"
    "buffer_load_dwordx4 v215, s[48:51], s74 offen lds\n"
    "v_mfma_f32_16x16x32_bf16 v[252:255], a[116:119], v[100:103], v[252:255]\n"
    "s_add_u32 m0, s72, 14784\n"
    "s_mov_b32 s74, 293888\n"
    "s_waitcnt vmcnt(30)\n"
    "buffer_load_dwordx4 v215, s[48:51], s74 offen lds\n"
    "v_mfma_f32_16x16x32_bf16 v[240:243], a[112:115], v[104:107], v[240:243]\n"
    "s_add_u32 m0, s72, 15840\n"
    "s_mov_b32 s74, 297472\n"
    "s_waitcnt vmcnt(35)\n"
    "ds_read_b64_tr_b16 a[192:193], v217 offset:0\n"
    "v_mfma_f32_16x16x32_bf16 v[244:247], a[116:119], v[104:107], v[244:247]\n"
    "buffer_load_dwordx4 v215, s[48:51], s74 offen lds\n"
    "ds_read_b64_tr_b16 a[194:195], v217 offset:256\n"
    "v_mfma_f32_16x16x32_bf16 v[248:251], a[112:115], v[108:111], v[248:251]\n"
    "ds_read_b64_tr_b16 a[196:197], v217 offset:32\n"
    "ds_read_b64_tr_b16 a[198:199], v217 offset:288\n"
    "v_mfma_f32_16x16x32_bf16 v[252:255], a[116:119], v[108:111], v[252:255]\n"
    "s_waitcnt lgkmcnt(0)\n"
    "ds_read_b64_tr_b16 a[200:201], v217 offset:512\n"
    "ds_read_b64_tr_b16 a[202:203], v217 offset:768\n"
    "v_mfma_f32_16x16x32_bf16 a[120:123], a[192:195], a[72:75], a[120:123]\n"
    "ds_read_b64_tr_b16 a[204:205], v217 offset:544\n"
    "ds_read_b64_tr_b16 a[206:207], v217 offset:800\n"
    "v_mfma_f32_16x16x32_bf16 a[124:127], a[192:195], a[76:79], a[124:127]\n"
    "ds_read_b64_tr_b16 a[208:209], v217 offset:4224\n"
    "ds_read_b64_tr_b16 a[210:211], v217 offset:4480\n"
    "v_mfma_f32_16x16x32_bf16 a[128:131], a[196:199], a[72:75], a[128:131]\n"
    "ds_read_b64_tr_b16 a[212:213], v217 offset:4256\n"
    "ds_read_b64_tr_b16 a[214:215], v217 offset:4512\n"
    "v_mfma_f32_16x16x32_bf16 a[132:135], a[196:199], a[76:79], a[132:135]\n"
    "s_waitcnt lgkmcnt(4)\n"
    "ds_read_b64_tr_b16 a[216:217], v217 offset:4736\n"
    "ds_read_b64_tr_b16 a[218:219], v217 offset:4992\n"
    "v_mfma_f32_16x16x32_bf16 a[136:139], a[200:203], a[72:75], a[136:139]\n"
    "s_mov_b32 s28, 1433600\n"
    "s_bitcmp1_b32 s75, 10\n"
    "s_cselect_b32 s28, s28, 0x80000000\n"
    "buffer_load_dwordx4 v[152:155], v218, s[44:47], s28 offen offset:0 sc1\n"
    "buffer_load_dwordx4 v[156:159], v218, s[44:47], s28 offen offset:1024 sc1\n"
    "v_mfma_f32_16x16x32_bf16 a[140:143], a[200:203], a[76:79], a[140:143]\n"
    "ds_read_b64_tr_b16 a[220:221], v217 offset:4768\n"
    "ds_read_b64_tr_b16 a[222:223], v217 offset:5024\n"
    "v_mfma_f32_16x16x32_bf16 a[144:147], a[204:207], a[72:75], a[144:147]\n"
    "v_add_u32 v208, s73, v127\n"
    "v_mfma_f32_16x16x32_bf16 a[148:151], a[204:207], a[76:79], a[148:151]\n"
    "s_waitcnt lgkmcnt(4)\n"
    "v_mfma_f32_16x16x32_bf16 a[152:155], a[208:211], a[72:75], a[152:155]\n"
    "s_lshl_b32 s84, s81, 12\n"
    "v_add_u32 v209, s84, v127\n"
    "v_mfma_f32_16x16x32_bf16 a[156:159], a[208:211], a[76:79], a[156:159]\n"
    "s_mov_b32 s28, 1576960\n"
    "s_bitcmp1_b32 s75, 11\n"
    "s_cselect_b32 s28, s28, 0x80000000\n"
    "buffer_load_dwordx4 v[160:163], v218, s[44:47], s28 offen offset:0 sc1\n"
    "buffer_load_dwordx4 v[164:167], v218, s[44:47], s28 offen offset:1024 sc1\n"
    "v_mfma_f32_16x16x32_bf16 a[160:163], a[212:215], a[72:75], a[160:163]\n"
    "v_mfma_f32_16x16x32_bf16 a[164:167], a[212:215], a[76:79], a[164:167]\n"
    "s_waitcnt lgkmcnt(0)\n"
    "v_mfma_f32_16x16x32_bf16 a[168:171], a[216:219], a[72:75], a[168:171]\n"
    "v_mfma_f32_16x16x32_bf16 a[172:175], a[216:219], a[76:79], a[172:175]\n"
    "v_mfma_f32_16x16x32_bf16 a[176:179], a[220:223], a[72:75], a[176:179]\n"
    "v_add_u32 v217, s73, v124\n"
    "v_mfma_f32_16x16x32_bf16 a[180:183], a[220:223], a[76:79], a[180:183]\n"
    "s_waitcnt vmcnt(26)\n"
    "v_mfma_f32_16x16x32_bf16 v[240:243], a[112:115], v[112:115], v[240:243]\n"
    "s_waitcnt vmcnt(28)\n"
    "ds_read_b64_tr_b16 a[224:225], v217 offset:8448\n"
    "ds_read_b64_tr_b16 a[226:227], v217 offset:8704\n"
    "v_mfma_f32_16x16x32_bf16 v[244:247], a[116:119], v[112:115], v[244:247]\n"
    "ds_read_b64_tr_b16 a[228:229], v217 offset:8480\n"
    "ds_read_b64_tr_b16 a[230:231], v217 offset:8736\n"
    "v_mfma_f32_16x16x32_bf16 v[248:251], a[112:115], v[116:119], v[248:251]\n"
    "ds_read_b64_tr_b16 a[232:233], v217 offset:8960\n"
    "v_mfma_f32_16x16x32_bf16 v[252:255], a[116:119], v[116:119], v[252:255]\n"
    "s_waitcnt vmcnt(24)\n"
    "ds_read_b64_tr_b16 a[234:235], v217 offset:9216\n"
    "v_mfma_f32_16x16x32_bf16 v[240:243], a[112:115], v[128:131], v[240:243]\n"
    "ds_read_b64_tr_b16 a[236:237], v217 offset:8992\n"
    "v_mfma_f32_16x16x32_bf16 v[244:247], a[116:119], v[128:131], v[244:247]\n"
    "ds_read_b64_tr_b16 a[238:239], v217 offset:9248\n"
    "v_mfma_f32_16x16x32_bf16 v[248:251], a[112:115], v[132:135], v[248:251]\n"
    "ds_read_b64_tr_b16 a[240:241], v217 offset:12672\n"
    "v_mfma_f32_16x16x32_bf16 v[252:255], a[116:119], v[132:135], v[252:255]\n"
    "s_waitcnt lgkmcnt(5)\n"
    "ds_read_b64_tr_b16 a[242:243], v217 offset:12928\n"
    "v_mfma_f32_16x16x32_bf16 a[120:123], a[224:227], a[80:83], a[120:123]\n"
    "ds_read_b64_tr_b16 a[244:245], v217 offset:12704\n"
    "v_mfma_f32_16x16x32_bf16 a[124:127], a[224:227], a[84:87], a[124:127]\n"
    "ds_read_b64_tr_b16 a[246:247], v217 offset:12960\n"
    "v_mfma_f32_16x16x32_bf16 a[128:131], a[228:231], a[80:83], a[128:131]\n"
    "ds_read_b64_tr_b16 a[248:249], v217 offset:13184\n"
    "v_mfma_f32_16x16x32_bf16 a[132:135], a[228:231], a[84:87], a[132:135]\n"
    "s_waitcnt lgkmcnt(5)\n"
    "ds_read_b64_tr_b16 a[250:251], v217 offset:13440\n"
    "v_mfma_f32_16x16x32_bf16 a[136:139], a[232:235], a[80:83], a[136:139]\n"
    "s_mov_b32 s28, 1720320\n"
    "s_bitcmp1_b32 s75, 12\n"
    "s_cselect_b32 s28, s28, 0x80000000\n"
    "buffer_load_dwordx4 v[168:171], v218, s[44:47], s28 offen offset:0 sc1\n"
    "buffer_load_dwordx4 v[172:175], v218, s[44:47], s28 offen offset:1024 sc1\n"
    "v_mfma_f32_16x16x32_bf16 a[140:143], a[232:235], a[84:87], a[140:143]\n"
    "ds_read_b64_tr_b16 a[252:253], v217 offset:13216\n"
    "ds_read_b64_tr_b16 a[254:255], v217 offset:13472\n"
    "v_mfma_f32_16x16x32_bf16 a[144:147], a[236:239], a[80:83], a[144:147]\n"
    "v_mfma_f32_16x16x32_bf16 a[148:151], a[236:239], a[84:87], a[148:151]\n"
    "s_waitcnt lgkmcnt(4)\n"
    "v_mfma_f32_16x16x32_bf16 a[152:155], a[240:243], a[80:83], a[152:155]\n"
    "v_mfma_f32_16x16x32_bf16 a[156:159], a[240:243], a[84:87], a[156:159]\n"
    "s_mov_b32 s28, 1863680\n"
    "s_bitcmp1_b32 s75, 13\n"
    "s_cselect_b32 s28, s28, 0x80000000\n"
    "buffer_load_dwordx4 v[176:179], v218, s[44:47], s28 offen offset:0 sc1\n"
    "buffer_load_dwordx4 v[180:183], v218, s[44:47], s28 offen offset:1024 sc1\n"
    "v_mfma_f32_16x16x32_bf16 a[160:163], a[244:247], a[80:83], a[160:163]\n"
    "v_mfma_f32_16x16x32_bf16 a[164:167], a[244:247], a[84:87], a[164:167]\n"
    "s_waitcnt lgkmcnt(0)\n"
    "v_mfma_f32_16x16x32_bf16 a[168:171], a[248:251], a[80:83], a[168:171]\n"
    "v_mfma_f32_16x16x32_bf16 a[172:175], a[248:251], a[84:87], a[172:175]\n"
    "v_mfma_f32_16x16x32_bf16 a[176:179], a[252:255], a[80:83], a[176:179]\n"
    "v_add_u32 v217, s72, v124\n"
    "v_mfma_f32_16x16x32_bf16 a[180:183], a[252:255], a[84:87], a[180:183]\n"
    "s_waitcnt vmcnt(18)\n"
    "v_mfma_f32_16x16x32_bf16 v[240:243], a[112:115], v[136:139], v[240:243]\n"
    "s_waitcnt vmcnt(20)\n"
    "ds_read_b64_tr_b16 a[192:193], v217 offset:0\n"
    "ds_read_b64_tr_b16 a[194:195], v217 offset:256\n"
    "v_mfma_f32_16x16x32_bf16 v[244:247], a[116:119], v[136:139], v[244:247]\n"
    "ds_read_b64_tr_b16 a[196:197], v217 offset:32\n"
    "ds_read_b64_tr_b16 a[198:199], v217 offset:288\n"
    "v_mfma_f32_16x16x32_bf16 v[248:251], a[112:115], v[140:143], v[248:251]\n"
    "ds_read_b64_tr_b16 a[200:201], v217 offset:512\n"
    "v_mfma_f32_16x16x32_bf16 v[252:255], a[116:119], v[140:143], v[252:255]\n"
    "s_waitcnt vmcnt(16)\n"
    "ds_read_b64_tr_b16 a[202:203], v217 offset:768\n"
    "v_mfma_f32_16x16x32_bf16 v[240:243], a[112:115], v[144:147], v[240:243]\n"
    "ds_read_b64_tr_b16 a[204:205], v217 offset:544\n"
    "v_mfma_f32_16x16x32_bf16 v[244:247], a[116:119], v[144:147], v[244:247]\n"
    "ds_read_b64_tr_b16 a[206:207], v217 offset:800\n"
    "v_mfma_f32_16x16x32_bf16 v[248:251], a[112:115], v[148:151], v[248:251]\n"
    "ds_read_b64_tr_b16 a[208:209], v217 offset:4224\n"
    "v_mfma_f32_16x16x32_bf16 v[252:255], a[116:119], v[148:151], v[252:255]\n"
    "s_waitcnt lgkmcnt(5)\n"
    "ds_read_b64_tr_b16 a[210:211], v217 offset:4480\n"
    "v_mfma_f32_16x16x32_bf16 a[120:123], a[192:195], a[88:91], a[120:123]\n"
    "ds_read_b64_tr_b16 a[212:213], v217 offset:4256\n"
    "v_mfma_f32_16x16x32_bf16 a[124:127], a[192:195], a[92:95], a[124:127]\n"
    "ds_read_b64_tr_b16 a[214:215], v217 offset:4512\n"
    "v_mfma_f32_16x16x32_bf16 a[128:131], a[196:199], a[88:91], a[128:131]\n"
    "ds_read_b64_tr_b16 a[216:217], v217 offset:4736\n"
    "v_mfma_f32_16x16x32_bf16 a[132:135], a[196:199], a[92:95], a[132:135]\n"
    "s_waitcnt lgkmcnt(5)\n"
    "ds_read_b64_tr_b16 a[218:219], v217 offset:4992\n"
    "v_mfma_f32_16x16x32_bf16 a[136:139], a[200:203], a[88:91], a[136:139]\n"
    "s_mov_b32 s28, 2007040\n"
    "s_bitcmp1_b32 s75, 14\n"
    "s_cselect_b32 s28, s28, 0x80000000\n"
    "buffer_load_dwordx4 v[184:187], v218, s[44:47], s28 offen offset:0 sc1\n"
    "buffer_load_dwordx4 v[188:191], v218, s[44:47], s28 offen offset:1024 sc1\n"
    "v_mfma_f32_16x16x32_bf16 a[140:143], a[200:203], a[92:95], a[140:143]\n"
    "ds_read_b64_tr_b16 a[220:221], v217 offset:4768\n"
    "ds_read_b64_tr_b16 a[222:223], v217 offset:5024\n"
    "v_mfma_f32_16x16x32_bf16 a[144:147], a[204:207], a[88:91], a[144:147]\n"
    "v_mfma_f32_16x16x32_bf16 a[148:151], a[204:207], a[92:95], a[148:151]\n"
    "s_waitcnt lgkmcnt(4)\n"
    "v_mfma_f32_16x16x32_bf16 a[152:155], a[208:211], a[88:91], a[152:155]\n"
    "v_mfma_f32_16x16x32_bf16 a[156:159], a[208:211], a[92:95], a[156:159]\n"
    "s_mov_b32 s28, 2150400\n"
    "s_bitcmp1_b32 s75, 15\n"
    "s_cselect_b32 s28, s28, 0x80000000\n"
    "buffer_load_dwordx4 v[192:195], v218, s[44:47], s28 offen offset:0 sc1\n"
    "buffer_load_dwordx4 v[196:199], v218, s[44:47], s28 offen offset:1024 sc1\n"
    "v_mfma_f32_16x16x32_bf16 a[160:163], a[212:215], a[88:91], a[160:163]\n"
    "v_mfma_f32_16x16x32_bf16 a[164:167], a[212:215], a[92:95], a[164:167]\n"
    "s_waitcnt lgkmcnt(0)\n"
    "v_mfma_f32_16x16x32_bf16 a[168:171], a[216:219], a[88:91], a[168:171]\n"
    "v_mfma_f32_16x16x32_bf16 a[172:175], a[216:219], a[92:95], a[172:175]\n"
    "v_mfma_f32_16x16x32_bf16 a[176:179], a[220:223], a[88:91], a[176:179]\n"
    "v_add_u32 v217, s72, v124\n"
    "v_mfma_f32_16x16x32_bf16 a[180:183], a[220:223], a[92:95], a[180:183]\n"
    "s_waitcnt vmcnt(10)\n"
    "v_mfma_f32_16x16x32_bf16 v[240:243], a[112:115], v[152:155], v[240:243]\n"
    "s_waitcnt vmcnt(12)\n"
    "ds_read_b64_tr_b16 a[224:225], v217 offset:8448\n"
    "ds_read_b64_tr_b16 a[226:227], v217 offset:8704\n"
    "v_mfma_f32_16x16x32_bf16 v[244:247], a[116:119], v[152:155], v[244:247]\n"
    "ds_read_b64_tr_b16 a[228:229], v217 offset:8480\n"
    "ds_read_b64_tr_b16 a[230:231], v217 offset:8736\n"
    "v_mfma_f32_16x16x32_bf16 v[248:251], a[112:115], v[156:159], v[248:251]\n"
    "ds_read_b64_tr_b16 a[232:233], v217 offset:8960\n"
    "v_mfma_f32_16x16x32_bf16 v[252:255], a[116:119], v[156:159], v[252:255]\n"
    "s_waitcnt vmcnt(8)\n"
    "ds_read_b64_tr_b16 a[234:235], v217 offset:9216\n"
    "v_mfma_f32_16x16x32_bf16 v[240:243], a[112:115], v[160:163], v[240:243]\n"
    "ds_read_b64_tr_b16 a[236:237], v217 offset:8992\n"
    "v_mfma_f32_16x16x32_bf16 v[244:247], a[116:119], v[160:163], v[244:247]\n"
    "ds_read_b64_tr_b16 a[238:239], v217 offset:9248\n"
    "v_mfma_f32_16x16x32_bf16 v[248:251], a[112:115], v[164:167], v[248:251]\n"
    "ds_read_b64_tr_b16 a[240:241], v217 offset:12672\n"
    "v_mfma_f32_16x16x32_bf16 v[252:255], a[116:119], v[164:167], v[252:255]\n"
    "s_waitcnt lgkmcnt(5)\n"
    "ds_read_b64_tr_b16 a[242:243], v217 offset:12928\n"
    "v_mfma_f32_16x16x32_bf16 a[120:123], a[224:227], a[104:107], a[120:123]\n"
    "ds_read_b64_tr_b16 a[244:245], v217 offset:12704\n"
    "v_mfma_f32_16x16x32_bf16 a[124:127], a[224:227], a[108:111], a[124:127]\n"
    "ds_read_b64_tr_b16 a[246:247], v217 offset:12960\n"
    "v_mfma_f32_16x16x32_bf16 a[128:131], a[228:231], a[104:107], a[128:131]\n"
    "ds_read_b64_tr_b16 a[248:249], v217 offset:13184\n"
    "v_mfma_f32_16x16x32_bf16 a[132:135], a[228:231], a[108:111], a[132:135]\n"
    "s_waitcnt lgkmcnt(5)\n"
    "ds_read_b64_tr_b16 a[250:251], v217 offset:13440\n"
    "ds_write_b128 v208, a[120:123] offset:0\n"
    "v_mfma_f32_16x16x32_bf16 a[136:139], a[232:235], a[104:107], a[136:139]\n"
    "ds_read_b64_tr_b16 a[252:253], v217 offset:13216\n"
    "ds_write_b128 v208, a[124:127] offset:1024\n"
    "v_mfma_f32_16x16x32_bf16 a[140:143], a[232:235], a[108:111], a[140:143]\n"
    "ds_read_b64_tr_b16 a[254:255], v217 offset:13472\n"
    "ds_write_b128 v208, a[128:131] offset:2048\n"
    "v_mfma_f32_16x16x32_bf16 a[144:147], a[236:239], a[104:107], a[144:147]\n"
    "ds_write_b128 v208, a[132:135] offset:3072\n"
    "v_mfma_f32_16x16x32_bf16 a[148:151], a[236:239], a[108:111], a[148:151]\n"
    "s_waitcnt lgkmcnt(8)\n"
    "ds_write_b128 v208, a[136:139] offset:4096\n"
    "v_mfma_f32_16x16x32_bf16 a[152:155], a[240:243], a[104:107], a[152:155]\n"
    "s_or_b32 s75, s75, s78\n"
    "ds_write_b128 v208, a[140:143] offset:5120\n"
    "v_mfma_f32_16x16x32_bf16 a[156:159], a[240:243], a[108:111], a[156:159]\n"
    "ds_write_b128 v208, a[144:147] offset:6144\n"
    "v_mfma_f32_16x16x32_bf16 a[160:163], a[244:247], a[104:107], a[160:163]\n"
    "s_cmp_eq_u32 s75, 0xffff\n"
    "s_cbranch_scc1 Lnq%=\n"
    "buffer_load_dwordx2 v[228:229], v225, s[64:67], 0 offen offset:256 sc1\n"
    "Lnq%=:\n"
    "ds_write_b128 v208, a[148:151] offset:7168\n"
    "v_mfma_f32_16x16x32_bf16 a[164:167], a[244:247], a[108:111], a[164:167]\n"
    "s_waitcnt lgkmcnt(6)\n"
    "ds_write_b128 v208, a[152:155] offset:8192\n"
    "v_mfma_f32_16x16x32_bf16 a[168:171], a[248:251], a[104:107], a[168:171]\n"
    "ds_write_b128 v208, a[156:159] offset:9216\n"
    "v_mfma_f32_16x16x32_bf16 a[172:175], a[248:251], a[108:111], a[172:175]\n"
    "ds_write_b128 v208, a[160:163] offset:10240\n"
    "v_mfma_f32_16x16x32_bf16 a[176:179], a[252:255], a[104:107], a[176:179]\n"
    "v_mfma_f32_16x16x32_bf16 a[180:183], a[252:255], a[108:111], a[180:183]\n"
    "s_waitcnt vmcnt(6)\n"
    "ds_write_b128 v208, a[164:167] offset:11264\n"
    "v_mfma_f32_16x16x32_bf16 v[240:243], a[112:115], v[168:171], v[240:243]\n"
    "ds_write_b128 v208, a[168:171] offset:12288\n"
    "v_mfma_f32_16x16x32_bf16 v[244:247], a[116:119], v[168:171], v[244:247]\n"
    "ds_write_b128 v208, a[172:175] offset:13312\n"
    "v_mfma_f32_16x16x32_bf16 v[248:251], a[112:115], v[172:175], v[248:251]\n"
    "ds_write_b128 v208, a[176:179] offset:14336\n"
    "ds_write_b128 v208, a[180:183] offset:15360\n"
    "v_mfma_f32_16x16x32_bf16 v[252:255], a[116:119], v[172:175], v[252:255]\n"
    "s_waitcnt vmcnt(4)\n"
    "v_mfma_f32_16x16x32_bf16 v[240:243], a[112:115], v[176:179], v[240:243]\n"
    "v_mfma_f32_16x16x32_bf16 v[244:247], a[116:119], v[176:179], v[244:247]\n"
    "v_mfma_f32_16x16x32_bf16 v[248:251], a[112:115], v[180:183], v[248:251]\n"
    "v_mfma_f32_16x16x32_bf16 v[252:255], a[116:119], v[180:183], v[252:255]\n"
    "s_waitcnt vmcnt(2)\n"
    "v_mfma_f32_16x16x32_bf16 v[240:243], a[112:115], v[184:187], v[240:243]\n"
    "v_mfma_f32_16x16x32_bf16 v[244:247], a[116:119], v[184:187], v[244:247]\n"
    "v_mfma_f32_16x16x32_bf16 v[248:251], a[112:115], v[188:191], v[248:251]\n"
    "v_mfma_f32_16x16x32_bf16 v[252:255], a[116:119], v[188:191], v[252:255]\n"
    "s_waitcnt vmcnt(0)\n"
    "v_mfma_f32_16x16x32_bf16 v[240:243], a[112:115], v[192:195], v[240:243]\n"
    "v_mfma_f32_16x16x32_bf16 v[244:247], a[116:119], v[192:195], v[244:247]\n"
    "v_mfma_f32_16x16x32_bf16 v[248:251], a[112:115], v[196:199], v[248:251]\n"
    "v_mfma_f32_16x16x32_bf16 v[252:255], a[116:119], v[196:199], v[252:255]\n"
    "s_waitcnt lgkmcnt(0)\n"
    "s_barrier\n"
    "ds_read_b128 v[0:3], v209 offset:0\n"
    "ds_read_b128 v[4:7], v209 offset:1024\n"
    "ds_read_b128 v[8:11], v209 offset:2048\n"
    "ds_read_b128 v[12:15], v209 offset:3072\n"
    "ds_read_b128 v[16:19], v209 offset:16896\n"
    "ds_read_b128 v[20:23], v209 offset:17920\n"
    "ds_read_b128 v[24:27], v209 offset:18944\n"
    "ds_read_b128 v[28:31], v209 offset:19968\n"
    "ds_read_b128 v[32:35], v209 offset:33792\n"
    "ds_read_b128 v[36:39], v209 offset:34816\n"
    "ds_read_b128 v[40:43], v209 offset:35840\n"
    "ds_read_b128 v[44:47], v209 offset:36864\n"
    "ds_read_b128 v[48:51], v209 offset:50688\n"
    "ds_read_b128 v[52:55], v209 offset:51712\n"
    "ds_read_b128 v[56:59], v209 offset:52736\n"
    "ds_read_b128 v[60:63], v209 offset:53760\n"
    "v_lshlrev_b32 v210, 16, v236\n"
    "v_and_b32 v211, 0xffff0000, v236\n"
    "v_lshlrev_b32 v212, 16, v237\n"
    "v_and_b32 v213, 0xffff0000, v237\n"
    "v_lshlrev_b32 v214, 16, v238\n"
    "v_and_b32 v215, 0xffff0000, v238\n"
    "v_lshlrev_b32 v216, 16, v239\n"
    "v_and_b32 v217, 0xffff0000, v239\n"
    "s_mov_b32 s76, 0\n"
    "s_cmp_eq_u32 s75, 0xffff\n"
    "s_cbranch_scc1 Lall%=\n"
    "s_waitcnt vmcnt(0)\n"
    "v_cmp_eq_u32 vcc, s33, v228\n"
    "v_cmp_eq_u32 s[86:87], s34, v229\n"
    "s_nop 0\n"
    "s_and_b32 s76, vcc_lo, s86\n"
    "s_and_b32 s76, s76, 0xffff\n"
    "s_andn2_b32 s76, s76, s75\n"
    "s_bitcmp1_b32 s76, 0\n"
    "s_cbranch_scc0 Lla0%=\n"
    "s_mov_b32 s28, 0\n"
    "buffer_load_dwordx4 v[64:67], v218, s[44:47], s28 offen offset:0 sc1\n"
    "buffer_load_dwordx4 v[68:71], v218, s[44:47], s28 offen offset:1024 sc1\n"
    "Lla0%=:\n"
    "s_bitcmp1_b32 s76, 1\n"
    "s_cbranch_scc0 Lla1%=\n"
    "s_mov_b32 s28, 143360\n"
    "buffer_load_dwordx4 v[72:75], v218, s[44:47], s28 offen offset:0 sc1\n"
    "buffer_load_dwordx4 v[76:79], v218, s[44:47], s28 offen offset:1024 sc1\n"
    "Lla1%=:\n"
    "s_bitcmp1_b32 s76, 2\n"
    "s_cbranch_scc0 Lla2%=\n"
    "s_mov_b32 s28, 286720\n"
    "buffer_load_dwordx4 v[80:83], v218, s[44:47], s28 offen offset:0 sc1\n"
    "buffer_load_dwordx4 v[84:87], v218, s[44:47], s28 offen offset:1024 sc1\n"
    "Lla2%=:\n"
    "s_bitcmp1_b32 s76, 3\n"
    "s_cbranch_scc0 Lla3%=\n"
    "s_mov_b32 s28, 430080\n"
    "buffer_load_dwordx4 v[88:91], v218, s[44:47], s28 offen offset:0 sc1\n"
    "buffer_load_dwordx4 v[92:95], v218, s[44:47], s28 offen offset:1024 sc1\n"
    "Lla3%=:\n"
    "s_bitcmp1_b32 s76, 4\n"
    "s_cbranch_scc0 Lla4%=\n"
    "s_mov_b32 s28, 573440\n"
    "buffer_load_dwordx4 v[96:99], v218, s[44:47], s28 offen offset:0 sc1\n"
    "buffer_load_dwordx4 v[100:103], v218, s[44:47], s28 offen offset:1024 sc1\n"
    "Lla4%=:\n"
    "s_bitcmp1_b32 s76, 5\n"
    "s_cbranch_scc0 Lla5%=\n"
    "s_mov_b32 s28, 716800\n"
    "buffer_load_dwordx4 v[104:107], v218, s[44:47], s28 offen offset:0 sc1\n"
    "buffer_load_dwordx4 v[108:111], v218, s[44:47], s28 offen offset:1024 sc1\n"
    "Lla5%=:\n"
    "s_bitcmp1_b32 s76, 6\n"
    "s_cbranch_scc0 Lla6%=\n"
    "s_mov_b32 s28, 860160\n"
    "buffer_load_dwordx4 v[112:115], v218, s[44:47], s28 offen offset:0 sc1\n"
    "buffer_load_dwordx4 v[116:119], v218, s[44:47], s28 offen offset:1024 sc1\n"
    "Lla6%=:\n"
    "s_bitcmp1_b32 s76, 7\n"
    "s_cbranch_scc0 Lla7%=\n"
    "s_mov_b32 s28, 1003520\n"
    "buffer_load_dwordx4 v[128:131], v218, s[44:47], s28 offen offset:0 sc1\n"
    "buffer_load_dwordx4 v[132:135], v218, s[44:47], s28 offen offset:1024 sc1\n"
    "Lla7%=:\n"
    "s_bitcmp1_b32 s76, 8\n"
    "s_cbranch_scc0 Lla8%=\n"
    "s_mov_b32 s28, 1146880\n"
    "buffer_load_dwordx4 v[136:139], v218, s[44:47], s28 offen offset:0 sc1\n"
    "buffer_load_dwordx4 v[140:143], v218, s[44:47], s28 offen offset:1024 sc1\n"
    "Lla8%=:\n"
    "s_bitcmp1_b32 s76, 9\n"
    "s_cbranch_scc0 Lla9%=\n"
    "s_mov_b32 s28, 1290240\n"
    "buffer_load_dwordx4 v[144:147], v218, s[44:47], s28 offen offset:0 sc1\n"
    "buffer_load_dwordx4 v[148:151], v218, s[44:47], s28 offen offset:1024 sc1\n"
    "Lla9%=:\n"
    "s_bitcmp1_b32 s76, 10\n"
    "s_cbranch_scc0 Lla10%=\n"
    "s_mov_b32 s28, 1433600\n"
    "buffer_load_dwordx4 v[152:155], v218, s[44:47], s28 offen offset:0 sc1\n"
    "buffer_load_dwordx4 v[156:159], v218, s[44:47], s28 offen offset:1024 sc1\n"
    "Lla10%=:\n"
    "s_bitcmp1_b32 s76, 11\n"
    "s_cbranch_scc0 Lla11%=\n"
    "s_mov_b32 s28, 1576960\n"
    "buffer_load_dwordx4 v[160:163], v218, s[44:47], s28 offen offset:0 sc1\n"
    "buffer_load_dwordx4 v[164:167], v218, s[44:47], s28 offen offset:1024 sc1\n"
    "Lla11%=:\n"
    "s_bitcmp1_b32 s76, 12\n"
    "s_cbranch_scc0 Lla12%=\n"
    "s_mov_b32 s28, 1720320\n"
    "buffer_load_dwordx4 v[168:171], v218, s[44:47], s28 offen offset:0 sc1\n"
    "buffer_load_dwordx4 v[172:175], v218, s[44:47], s28 offen offset:1024 sc1\n"
    "Lla12%=:\n"
    "s_bitcmp1_b32 s76, 13\n"
    "s_cbranch_scc0 Lla13%=\n"
    "s_mov_b32 s28, 1863680\n"
    "buffer_load_dwordx4 v[176:179], v218, s[44:47], s28 offen offset:0 sc1\n"
    "buffer_load_dwordx4 v[180:183], v218, s[44:47], s28 offen offset:1024 sc1\n"
    "Lla13%=:\n"
    "s_bitcmp1_b32 s76, 14\n"
    "s_cbranch_scc0 Lla14%=\n"
    "s_mov_b32 s28, 2007040\n"
    "buffer_load_dwordx4 v[184:187], v218, s[44:47], s28 offen offset:0 sc1\n"
    "buffer_load_dwordx4 v[188:191], v218, s[44:47], s28 offen offset:1024 sc1\n"
    "Lla14%=:\n"
    "s_bitcmp1_b32 s76, 15\n"
    "s_cbranch_scc0 Lla15%=\n"
    "s_mov_b32 s28, 2150400\n"
    "buffer_load_dwordx4 v[192:195], v218, s[44:47], s28 offen offset:0 sc1\n"
    "buffer_load_dwordx4 v[196:199], v218, s[44:47], s28 offen offset:1024 sc1\n"
    "Lla15%=:\n"
    "Lall%=:\n"
    "s_waitcnt lgkmcnt(8)\n"
    "v_pk_add_f32 v[0:1], v[0:1], v[16:17]\n"
    "v_pk_add_f32 v[2:3], v[2:3], v[18:19]\n"
    "v_pk_add_f32 v[4:5], v[4:5], v[20:21]\n"
    "v_pk_add_f32 v[6:7], v[6:7], v[22:23]\n"
    "v_pk_add_f32 v[8:9], v[8:9], v[24:25]\n"
    "v_pk_add_f32 v[10:11], v[10:11], v[26:27]\n"
    "v_pk_add_f32 v[12:13], v[12:13], v[28:29]\n"
    "v_pk_add_f32 v[14:15], v[14:15], v[30:31]\n"
    "s_waitcnt lgkmcnt(4)\n"
    "v_pk_add_f32 v[0:1], v[0:1], v[32:33]\n"
    "v_pk_add_f32 v[2:3], v[2:3], v[34:35]\n"
    "v_pk_add_f32 v[4:5], v[4:5], v[36:37]\n"
    "v_pk_add_f32 v[6:7], v[6:7], v[38:39]\n"
    "v_pk_add_f32 v[8:9], v[8:9], v[40:41]\n"
    "v_pk_add_f32 v[10:11], v[10:11], v[42:43]\n"
    "v_pk_add_f32 v[12:13], v[12:13], v[44:45]\n"
    "v_pk_add_f32 v[14:15], v[14:15], v[46:47]\n"
    "s_waitcnt lgkmcnt(0)\n"
    "v_pk_add_f32 v[0:1], v[0:1], v[48:49]\n"
    "v_pk_add_f32 v[2:3], v[2:3], v[50:51]\n"
    "v_pk_add_f32 v[4:5], v[4:5], v[52:53]\n"
    "v_pk_add_f32 v[6:7], v[6:7], v[54:55]\n"
    "v_pk_add_f32 v[8:9], v[8:9], v[56:57]\n"
    "v_pk_add_f32 v[10:11], v[10:11], v[58:59]\n"
    "v_pk_add_f32 v[12:13], v[12:13], v[60:61]\n"
    "v_pk_add_f32 v[14:15], v[14:15], v[62:63]\n"
    "v_pk_mul_f32 v[0:1], v[0:1], v[230:231] op_sel_hi:[1,0]\n"
    "v_pk_mul_f32 v[2:3], v[2:3], v[230:231] op_sel_hi:[1,0]\n"
    "v_pk_mul_f32 v[4:5], v[4:5], v[230:231] op_sel:[0,1]\n"
    "v_pk_mul_f32 v[6:7], v[6:7], v[230:231] op_sel:[0,1]\n"
    "v_pk_mul_f32 v[8:9], v[8:9], v[230:231] op_sel_hi:[1,0]\n"
    "v_pk_mul_f32 v[10:11], v[10:11], v[230:231] op_sel_hi:[1,0]\n"
    "v_pk_mul_f32 v[12:13], v[12:13], v[230:231] op_sel:[0,1]\n"
    "v_pk_mul_f32 v[14:15], v[14:15], v[230:231] op_sel:[0,1]\n"
    "v_mov_b32 v32, 0xbfb8aa3b\n"
    "v_pk_mul_f32 v[16:17], v[0:1], v[32:33] op_sel_hi:[1,0]\n"
    "v_pk_mul_f32 v[18:19], v[2:3], v[32:33] op_sel_hi:[1,0]\n"
    "v_pk_mul_f32 v[20:21], v[4:5], v[32:33] op_sel_hi:[1,0]\n"
    "v_pk_mul_f32 v[22:23], v[6:7], v[32:33] op_sel_hi:[1,0]\n"
    "v_pk_mul_f32 v[24:25], v[8:9], v[32:33] op_sel_hi:[1,0]\n"
    "v_pk_mul_f32 v[26:27], v[10:11], v[32:33] op_sel_hi:[1,0]\n"
    "v_pk_mul_f32 v[28:29], v[12:13], v[32:33] op_sel_hi:[1,0]\n"
    "v_pk_mul_f32 v[30:31], v[14:15], v[32:33] op_sel_hi:[1,0]\n"
    "v_exp_f32 v16, v16\n"
    "v_exp_f32 v17, v17\n"
    "v_exp_f32 v18, v18\n"
    "v_exp_f32 v19, v19\n"
    "v_exp_f32 v20, v20\n"
    "v_exp_f32 v21, v21\n"
    "v_exp_f32 v22, v22\n"
    "v_exp_f32 v23, v23\n"
    "v_exp_f32 v24, v24\n"
    "v_exp_f32 v25, v25\n"
    "v_exp_f32 v26, v26\n"
    "v_exp_f32 v27, v27\n"
    "v_exp_f32 v28, v28\n"
    "v_exp_f32 v29, v29\n"
    "v_exp_f32 v30, v30\n"
    "v_exp_f32 v31, v31\n"
    "v_pk_add_f32 v[16:17], v[16:17], 1.0 op_sel_hi:[1,0]\n"
    "v_pk_add_f32 v[18:19], v[18:19], 1.0 op_sel_hi:[1,0]\n"
    "v_pk_add_f32 v[20:21], v[20:21], 1.0 op_sel_hi:[1,0]\n"
    "v_pk_add_f32 v[22:23], v[22:23], 1.0 op_sel_hi:[1,0]\n"
    "v_pk_add_f32 v[24:25], v[24:25], 1.0 op_sel_hi:[1,0]\n"
    "v_pk_add_f32 v[26:27], v[26:27], 1.0 op_sel_hi:[1,0]\n"
    "v_pk_add_f32 v[28:29], v[28:29], 1.0 op_sel_hi:[1,0]\n"
    "v_pk_add_f32 v[30:31], v[30:31], 1.0 op_sel_hi:[1,0]\n"
    "v_rcp_f32 v16, v16\n"
    "v_rcp_f32 v17, v17\n"
    "v_rcp_f32 v18, v18\n"
    "v_rcp_f32 v19, v19\n"
    "v_rcp_f32 v20, v20\n"
    "v_rcp_f32 v21, v21\n"
    "v_rcp_f32 v22, v22\n"
    "v_rcp_f32 v23, v23\n"
    "v_rcp_f32 v24, v24\n"
    "v_rcp_f32 v25, v25\n"
    "v_rcp_f32 v26, v26\n"
    "v_rcp_f32 v27, v27\n"
    "v_rcp_f32 v28, v28\n"
    "v_rcp_f32 v29, v29\n"
    "v_rcp_f32 v30, v30\n"
    "v_rcp_f32 v31, v31\n"
    "v_pk_mul_f32 v[0:1], v[0:1], v[16:17]\n"
    "v_pk_mul_f32 v[2:3], v[2:3], v[18:19]\n"
    "v_pk_mul_f32 v[4:5], v[4:5], v[20:21]\n"
    "v_pk_mul_f32 v[6:7], v[6:7], v[22:23]\n"
    "v_pk_mul_f32 v[8:9], v[8:9], v[24:25]\n"
    "v_pk_mul_f32 v[10:11], v[10:11], v[26:27]\n"
    "v_pk_mul_f32 v[12:13], v[12:13], v[28:29]\n"
    "v_pk_mul_f32 v[14:15], v[14:15], v[30:31]\n"
    "s_cmp_eq_u32 s75, 0xffff\n"
    "s_cbranch_scc1 Lfin%=\n"
    "Lpass%=:\n"
    "s_waitcnt vmcnt(0)\n"
    "s_or_b32 s75, s75, s76\n"
    "s_cmp_eq_u32 s75, 0xffff\n"
    "s_cbranch_scc1 Lnp%=\n"
    "buffer_load_dwordx2 v[228:229], v225, s[64:67], 0 offen offset:256 sc1\n"
    "Lnp%=:\n"
    "s_bitcmp1_b32 s76, 0\n"
    "s_cbranch_scc0 Lmb0%=\n"
    "v_mfma_f32_16x16x32_bf16 v[240:243], a[112:115], v[64:67], v[240:243]\n"
    "v_mfma_f32_16x16x32_bf16 v[244:247], a[116:119], v[64:67], v[244:247]\n"
    "v_mfma_f32_16x16x32_bf16 v[248:251], a[112:115], v[68:71], v[248:251]\n"
    "v_mfma_f32_16x16x32_bf16 v[252:255], a[116:119], v[68:71], v[252:255]\n"
    "Lmb0%=:\n"
    "s_bitcmp1_b32 s76, 1\n"
    "s_cbranch_scc0 Lmb1%=\n"
    "v_mfma_f32_16x16x32_bf16 v[240:243], a[112:115], v[72:75], v[240:243]\n"
    "v_mfma_f32_16x16x32_bf16 v[244:247], a[116:119], v[72:75], v[244:247]\n"
    "v_mfma_f32_16x16x32_bf16 v[248:251], a[112:115], v[76:79], v[248:251]\n"
    "v_mfma_f32_16x16x32_bf16 v[252:255], a[116:119], v[76:79], v[252:255]\n"
    "Lmb1%=:\n"
    "s_bitcmp1_b32 s76, 2\n"
    "s_cbranch_scc0 Lmb2%=\n"
    "v_mfma_f32_16x16x32_bf16 v[240:243], a[112:115], v[80:83], v[240:243]\n"
    "v_mfma_f32_16x16x32_bf16 v[244:247], a[116:119], v[80:83], v[244:247]\n"
    "v_mfma_f32_16x16x32_bf16 v[248:251], a[112:115], v[84:87], v[248:251]\n"
    "v_mfma_f32_16x16x32_bf16 v[252:255], a[116:119], v[84:87], v[252:255]\n"
    "Lmb2%=:\n"
    "s_bitcmp1_b32 s76, 3\n"
    "s_cbranch_scc0 Lmb3%=\n"
    "v_mfma_f32_16x16x32_bf16 v[240:243], a[112:115], v[88:91], v[240:243]\n"
    "v_mfma_f32_16x16x32_bf16 v[244:247], a[116:119], v[88:91], v[244:247]\n"
    "v_mfma_f32_16x16x32_bf16 v[248:251], a[112:115], v[92:95], v[248:251]\n"
    "v_mfma_f32_16x16x32_bf16 v[252:255], a[116:119], v[92:95], v[252:255]\n"
    "Lmb3%=:\n"
    "s_bitcmp1_b32 s76, 4\n"
    "s_cbranch_scc0 Lmb4%=\n"
    "v_mfma_f32_16x16x32_bf16 v[240:243], a[112:115], v[96:99], v[240:243]\n"
    "v_mfma_f32_16x16x32_bf16 v[244:247], a[116:119], v[96:99], v[244:247]\n"
    "v_mfma_f32_16x16x32_bf16 v[248:251], a[112:115], v[100:103], v[248:251]\n"
    "v_mfma_f32_16x16x32_bf16 v[252:255], a[116:119], v[100:103], v[252:255]\n"
    "Lmb4%=:\n"
    "s_bitcmp1_b32 s76, 5\n"
    "s_cbranch_scc0 Lmb5%=\n"
    "v_mfma_f32_16x16x32_bf16 v[240:243], a[112:115], v[104:107], v[240:243]\n"
    "v_mfma_f32_16x16x32_bf16 v[244:247], a[116:119], v[104:107], v[244:247]\n"
    "v_mfma_f32_16x16x32_bf16 v[248:251], a[112:115], v[108:111], v[248:251]\n"
    "v_mfma_f32_16x16x32_bf16 v[252:255], a[116:119], v[108:111], v[252:255]\n"
    "Lmb5%=:\n"
    "s_bitcmp1_b32 s76, 6\n"
    "s_cbranch_scc0 Lmb6%=\n"
    "v_mfma_f32_16x16x32_bf16 v[240:243], a[112:115], v[112:115], v[240:243]\n"
    "v_mfma_f32_16x16x32_bf16 v[244:247], a[116:119], v[112:115], v[244:247]\n"
    "v_mfma_f32_16x16x32_bf16 v[248:251], a[112:115], v[116:119], v[248:251]\n"
    "v_mfma_f32_16x16x32_bf16 v[252:255], a[116:119], v[116:119], v[252:255]\n"
    "Lmb6%=:\n"
    "s_bitcmp1_b32 s76, 7\n"
    "s_cbranch_scc0 Lmb7%=\n"
    "v_mfma_f32_16x16x32_bf16 v[240:243], a[112:115], v[128:131], v[240:243]\n"
    "v_mfma_f32_16x16x32_bf16 v[244:247], a[116:119], v[128:131], v[244:247]\n"
    "v_mfma_f32_16x16x32_bf16 v[248:251], a[112:115], v[132:135], v[248:251]\n"
    "v_mfma_f32_16x16x32_bf16 v[252:255], a[116:119], v[132:135], v[252:255]\n"
    "Lmb7%=:\n"
    "s_bitcmp1_b32 s76, 8\n"
    "s_cbranch_scc0 Lmb8%=\n"
    "v_mfma_f32_16x16x32_bf16 v[240:243], a[112:115], v[136:139], v[240:243]\n"
    "v_mfma_f32_16x16x32_bf16 v[244:247], a[116:119], v[136:139], v[244:247]\n"
    "v_mfma_f32_16x16x32_bf16 v[248:251], a[112:115], v[140:143], v[248:251]\n"
    "v_mfma_f32_16x16x32_bf16 v[252:255], a[116:119], v[140:143], v[252:255]\n"
    "Lmb8%=:\n"
    "s_bitcmp1_b32 s76, 9\n"
    "s_cbranch_scc0 Lmb9%=\n"
    "v_mfma_f32_16x16x32_bf16 v[240:243], a[112:115], v[144:147], v[240:243]\n"
    "v_mfma_f32_16x16x32_bf16 v[244:247], a[116:119], v[144:147], v[244:247]\n"
    "v_mfma_f32_16x16x32_bf16 v[248:251], a[112:115], v[148:151], v[248:251]\n"
    "v_mfma_f32_16x16x32_bf16 v[252:255], a[116:119], v[148:151], v[252:255]\n"
    "Lmb9%=:\n"
    "s_bitcmp1_b32 s76, 10\n"
    "s_cbranch_scc0 Lmb10%=\n"
    "v_mfma_f32_16x16x32_bf16 v[240:243], a[112:115], v[152:155], v[240:243]\n"
    "v_mfma_f32_16x16x32_bf16 v[244:247], a[116:119], v[152:155], v[244:247]\n"
    "v_mfma_f32_16x16x32_bf16 v[248:251], a[112:115], v[156:159], v[248:251]\n"
    "v_mfma_f32_16x16x32_bf16 v[252:255], a[116:119], v[156:159], v[252:255]\n"
    "Lmb10%=:\n"
    "s_bitcmp1_b32 s76, 11\n"
    "s_cbranch_scc0 Lmb11%=\n"
    "v_mfma_f32_16x16x32_bf16 v[240:243], a[112:115], v[160:163], v[240:243]\n"
    "v_mfma_f32_16x16x32_bf16 v[244:247], a[116:119], v[160:163], v[244:247]\n"
    "v_mfma_f32_16x16x32_bf16 v[248:251], a[112:115], v[164:167], v[248:251]\n"
    "v_mfma_f32_16x16x32_bf16 v[252:255], a[116:119], v[164:167], v[252:255]\n"
    "Lmb11%=:\n"
    "s_bitcmp1_b32 s76, 12\n"
    "s_cbranch_scc0 Lmb12%=\n"
    "v_mfma_f32_16x16x32_bf16 v[240:243], a[112:115], v[168:171], v[240:243]\n"
    "v_mfma_f32_16x16x32_bf16 v[244:247], a[116:119], v[168:171], v[244:247]\n"
    "v_mfma_f32_16x16x32_bf16 v[248:251], a[112:115], v[172:175], v[248:251]\n"
    "v_mfma_f32_16x16x32_bf16 v[252:255], a[116:119], v[172:175], v[252:255]\n"
    "Lmb12%=:\n"
    "s_bitcmp1_b32 s76, 13\n"
    "s_cbranch_scc0 Lmb13%=\n"
    "v_mfma_f32_16x16x32_bf16 v[240:243], a[112:115], v[176:179], v[240:243]\n"
    "v_mfma_f32_16x16x32_bf16 v[244:247], a[116:119], v[176:179], v[244:247]\n"
    "v_mfma_f32_16x16x32_bf16 v[248:251], a[112:115], v[180:183], v[248:251]\n"
    "v_mfma_f32_16x16x32_bf16 v[252:255], a[116:119], v[180:183], v[252:255]\n"
    "Lmb13%=:\n"
    "s_bitcmp1_b32 s76, 14\n"
    "s_cbranch_scc0 Lmb14%=\n"
    "v_mfma_f32_16x16x32_bf16 v[240:243], a[112:115], v[184:187], v[240:243]\n"
    "v_mfma_f32_16x16x32_bf16 v[244:247], a[116:119], v[184:187], v[244:247]\n"
    "v_mfma_f32_16x16x32_bf16 v[248:251], a[112:115], v[188:191], v[248:251]\n"
    "v_mfma_f32_16x16x32_bf16 v[252:255], a[116:119], v[188:191], v[252:255]\n"
    "Lmb14%=:\n"
    "s_bitcmp1_b32 s76, 15\n"
    "s_cbranch_scc0 Lmb15%=\n"
    "v_mfma_f32_16x16x32_bf16 v[240:243], a[112:115], v[192:195], v[240:243]\n"
    "v_mfma_f32_16x16x32_bf16 v[244:247], a[116:119], v[192:195], v[244:247]\n"
    "v_mfma_f32_16x16x32_bf16 v[248:251], a[112:115], v[196:199], v[248:251]\n"
    "v_mfma_f32_16x16x32_bf16 v[252:255], a[116:119], v[196:199], v[252:255]\n"
    "Lmb15%=:\n"
    "s_cmp_eq_u32 s75, 0xffff\n"
    "s_cbranch_scc1 Lfin%=\n"
    "Lspin%=:\n"
    "s_waitcnt vmcnt(0)\n"
    "v_cmp_eq_u32 vcc, s33, v228\n"
    "v_cmp_eq_u32 s[86:87], s34, v229\n"
    "s_nop 0\n"
    "s_and_b32 s76, vcc_lo, s86\n"
    "s_and_b32 s76, s76, 0xffff\n"
    "s_andn2_b32 s76, s76, s75\n"
    "s_cbranch_scc1 Lnew%=\n"
    "buffer_load_dwordx2 v[228:229], v225, s[64:67], 0 offen offset:256 sc1\n"
    "s_sub_u32 s85, s85, 1\n"
    "s_cbranch_scc0 Lspin%=\n"
    "s_branch Lfin%=\n"
    "Lnew%=:\n"
    "s_bitcmp1_b32 s76, 0\n"
    "s_cbranch_scc0 Llb0%=\n"
    "s_mov_b32 s28, 0\n"
    "buffer_load_dwordx4 v[64:67], v218, s[44:47], s28 offen offset:0 sc1\n"
    "buffer_load_dwordx4 v[68:71], v218, s[44:47], s28 offen offset:1024 sc1\n"
    "Llb0%=:\n"
    "s_bitcmp1_b32 s76, 1\n"
    "s_cbranch_scc0 Llb1%=\n"
    "s_mov_b32 s28, 143360\n"
    "buffer_load_dwordx4 v[72:75], v218, s[44:47], s28 offen offset:0 sc1\n"
    "buffer_load_dwordx4 v[76:79], v218, s[44:47], s28 offen offset:1024 sc1\n"
    "Llb1%=:\n"
    "s_bitcmp1_b32 s76, 2\n"
    "s_cbranch_scc0 Llb2%=\n"
    "s_mov_b32 s28, 286720\n"
    "buffer_load_dwordx4 v[80:83], v218, s[44:47], s28 offen offset:0 sc1\n"
    "buffer_load_dwordx4 v[84:87], v218, s[44:47], s28 offen offset:1024 sc1\n"
    "Llb2%=:\n"
    "s_bitcmp1_b32 s76, 3\n"
    "s_cbranch_scc0 Llb3%=\n"
    "s_mov_b32 s28, 430080\n"
    "buffer_load_dwordx4 v[88:91], v218, s[44:47], s28 offen offset:0 sc1\n"
    "buffer_load_dwordx4 v[92:95], v218, s[44:47], s28 offen offset:1024 sc1\n"
    "Llb3%=:\n"
    "s_bitcmp1_b32 s76, 4\n"
    "s_cbranch_scc0 Llb4%=\n"
    "s_mov_b32 s28, 573440\n"
    "buffer_load_dwordx4 v[96:99], v218, s[44:47], s28 offen offset:0 sc1\n"
    "buffer_load_dwordx4 v[100:103], v218, s[44:47], s28 offen offset:1024 sc1\n"
    "Llb4%=:\n"
    "s_bitcmp1_b32 s76, 5\n"
    "s_cbranch_scc0 Llb5%=\n"
    "s_mov_b32 s28, 716800\n"
    "buffer_load_dwordx4 v[104:107], v218, s[44:47], s28 offen offset:0 sc1\n"
    "buffer_load_dwordx4 v[108:111], v218, s[44:47], s28 offen offset:1024 sc1\n"
    "Llb5%=:\n"
    "s_bitcmp1_b32 s76, 6\n"
    "s_cbranch_scc0 Llb6%=\n"
    "s_mov_b32 s28, 860160\n"
    "buffer_load_dwordx4 v[112:115], v218, s[44:47], s28 offen offset:0 sc1\n"
    "buffer_load_dwordx4 v[116:119], v218, s[44:47], s28 offen offset:1024 sc1\n"
    "Llb6%=:\n"
    "s_bitcmp1_b32 s76, 7\n"
    "s_cbranch_scc0 Llb7%=\n"
    "s_mov_b32 s28, 1003520\n"
    "buffer_load_dwordx4 v[128:131], v218, s[44:47], s28 offen offset:0 sc1\n"
    "buffer_load_dwordx4 v[132:135], v218, s[44:47], s28 offen offset:1024 sc1\n"
    "Llb7%=:\n"
    "s_bitcmp1_b32 s76, 8\n"
    "s_cbranch_scc0 Llb8%=\n"
    "s_mov_b32 s28, 1146880\n"
    "buffer_load_dwordx4 v[136:139], v218, s[44:47], s28 offen offset:0 sc1\n"
    "buffer_load_dwordx4 v[140:143], v218, s[44:47], s28 offen offset:1024 sc1\n"
    "Llb8%=:\n"
    "s_bitcmp1_b32 s76, 9\n"
    "s_cbranch_scc0 Llb9%=\n"
    "s_mov_b32 s28, 1290240\n"
    "buffer_load_dwordx4 v[144:147], v218, s[44:47], s28 offen offset:0 sc1\n"
    "buffer_load_dwordx4 v[148:151], v218, s[44:47], s28 offen offset:1024 sc1\n"
    "Llb9%=:\n"
    "s_bitcmp1_b32 s76, 10\n"
    "s_cbranch_scc0 Llb10%=\n"
    "s_mov_b32 s28, 1433600\n"
    "buffer_load_dwordx4 v[152:155], v218, s[44:47], s28 offen offset:0 sc1\n"
    "buffer_load_dwordx4 v[156:159], v218, s[44:47], s28 offen offset:1024 sc1\n"
    "Llb10%=:\n"
    "s_bitcmp1_b32 s76, 11\n"
    "s_cbranch_scc0 Llb11%=\n"
    "s_mov_b32 s28, 1576960\n"
    "buffer_load_dwordx4 v[160:163], v218, s[44:47], s28 offen offset:0 sc1\n"
    "buffer_load_dwordx4 v[164:167], v218, s[44:47], s28 offen offset:1024 sc1\n"
    "Llb11%=:\n"
    "s_bitcmp1_b32 s76, 12\n"
    "s_cbranch_scc0 Llb12%=\n"
    "s_mov_b32 s28, 1720320\n"
    "buffer_load_dwordx4 v[168:171], v218, s[44:47], s28 offen offset:0 sc1\n"
    "buffer_load_dwordx4 v[172:175], v218, s[44:47], s28 offen offset:1024 sc1\n"
    "Llb12%=:\n"
    "s_bitcmp1_b32 s76, 13\n"
    "s_cbranch_scc0 Llb13%=\n"
    "s_mov_b32 s28, 1863680\n"
    "buffer_load_dwordx4 v[176:179], v218, s[44:47], s28 offen offset:0 sc1\n"
    "buffer_load_dwordx4 v[180:183], v218, s[44:47], s28 offen offset:1024 sc1\n"
    "Llb13%=:\n"
    "s_bitcmp1_b32 s76, 14\n"
    "s_cbranch_scc0 Llb14%=\n"
    "s_mov_b32 s28, 2007040\n"
    "buffer_load_dwordx4 v[184:187], v218, s[44:47], s28 offen offset:0 sc1\n"
    "buffer_load_dwordx4 v[188:191], v218, s[44:47], s28 offen offset:1024 sc1\n"
    "Llb14%=:\n"
    "s_bitcmp1_b32 s76, 15\n"
    "s_cbranch_scc0 Llb15%=\n"
    "s_mov_b32 s28, 2150400\n"
    "buffer_load_dwordx4 v[192:195], v218, s[44:47], s28 offen offset:0 sc1\n"
    "buffer_load_dwordx4 v[196:199], v218, s[44:47], s28 offen offset:1024 sc1\n"
    "Llb15%=:\n"
    "s_branch Lpass%=\n"
    "Lfin%=:\n"
    "s_nop 7\n"
    "v_pk_fma_f32 v[200:201], v[240:241], v[0:1], v[200:201]\n"
    "v_pk_fma_f32 v[202:203], v[242:243], v[2:3], v[202:203]\n"
    "v_pk_fma_f32 v[204:205], v[244:245], v[4:5], v[204:205]\n"
    "v_pk_fma_f32 v[206:207], v[246:247], v[6:7], v[206:207]\n"
    "v_pk_fma_f32 v[210:211], v[248:249], v[8:9], v[210:211]\n"
    "v_pk_fma_f32 v[212:213], v[250:251], v[10:11], v[212:213]\n"
    "v_pk_fma_f32 v[214:215], v[252:253], v[12:13], v[214:215]\n"
    "v_pk_fma_f32 v[216:217], v[254:255], v[14:15], v[216:217]\n"
    "v_cvt_pk_bf16_f32 v232, v200, v201\n"
    "v_cvt_pk_bf16_f32 v233, v202, v203\n"
    "v_cvt_pk_bf16_f32 v234, v204, v205\n"
    "v_cvt_pk_bf16_f32 v235, v206, v207\n"
    "v_cvt_pk_bf16_f32 v236, v210, v211\n"
    "v_cvt_pk_bf16_f32 v237, v212, v213\n"
    "v_cvt_pk_bf16_f32 v238, v214, v215\n"
    "v_cvt_pk_bf16_f32 v239, v216, v217\n"
    "buffer_store_dwordx2 v[232:233], v220, s[52:55], 0 offen offset:0\n"
    "buffer_store_dwordx2 v[234:235], v220, s[52:55], s29 offen offset:0\n"
    "buffer_store_dwordx2 v[236:237], v220, s[52:55], 0 offen offset:32\n"
    "buffer_store_dwordx2 v[238:239], v220, s[52:55], s29 offen offset:32\n"
    "buffer_store_dword v223, v222, s[64:67], 0 offen sc1\n"
    "s_branch Lend%=\n"
    "Lside%=:\n"
    "s_cmp_eq_u32 s79, 3\n"
    "s_cbranch_scc0 Lside2%=\n"
    "v_mbcnt_lo_u32_b32 v125, -1, 0\n"
    "v_mbcnt_hi_u32_b32 v125, -1, v125\n"
    "v_lshlrev_b32 v125, 3, v125\n"
    "s_getpc_b64 s[52:53]\n"
    "Lpc2%=:\n"
    "s_sub_u32 s52, s52, Lpc2%=-LbackL%=\n"
    "s_subb_u32 s53, s53, 0\n"
    "s_mov_b32 s54, Lpc2%=-LbackL%=\n"
    "s_mov_b32 s55, 0x20000\n"
    "s_lshr_b32 s84, %1, 3\n"
    "s_lshl_b32 s84, s84, 11\n"
    "s_cmp_eq_u32 s81, 0\n"
    "s_cselect_b32 s84, s84, 0x80000000\n"
    "v_lshlrev_b32 v126, 4, v125\n"
    "v_cmp_gt_u32 vcc, 0x800, v126\n"
    "v_mov_b32 v127, 0x80000000\n"
    "s_nop 0\n"
    "v_cndmask_b32 v126, v127, v126, vcc\n"
    "buffer_load_dword v126, v126, s[52:55], s84 offen\n"
    "buffer_load_dwordx2 v[126:127], v125, s[64:67], 0 offen offset:128 sc1\n"
    "s_branch Lsret%=\n"
    "Lside2%=:\n"
    "s_cmp_eq_u32 s79, 2\n"
    "s_cbranch_scc0 Lside3%=\n"
    "s_waitcnt vmcnt(24)\n"
    "s_mov_b32 s85, 0x10000\n"
    "Lpa%=:\n"
    "v_cmp_eq_u32 vcc, s33, v126\n"
    "v_cmp_eq_u32 s[86:87], s34, v127\n"
    "s_nop 0\n"
    "s_and_b32 s84, vcc_lo, s86\n"
    "s_and_b32 s84, s84, 0xffff\n"
    "s_cmp_eq_u32 s84, 0xffff\n"
    "s_cbranch_scc1 Lqa%=\n"
    "s_sub_u32 s85, s85, 1\n"
    "s_cbranch_scc1 Lqa%=\n"
    "s_sleep 1\n"
    "buffer_load_dwordx2 v[126:127], v125, s[64:67], 0 offen offset:128 sc1\n"
    "s_waitcnt vmcnt(0)\n"
    "s_branch Lpa%=\n"
    "Lqa%=:\n"
    "s_lshl_b32 s84, %1, 16\n"
    "s_add_u32 s84, s84, 0x4000000\n"
    "s_sub_u32 s44, s68, s84\n"
    "s_subb_u32 s45, s69, 0\n"
    "s_lshr_b32 s85, %1, 4\n"
    "s_mul_i32 s84, s85, 2293760\n"
    "s_add_u32 s44, s44, s84\n"
    "s_addc_u32 s45, s45, 0\n"
    "s_mov_b32 s46, 2293760\n"
    "s_mov_b32 s47, 0x20000\n"
    "s_cmp_eq_u32 s81, 0\n"
    "s_cselect_b32 s84, 64, 0x80000000\n"
    "v_lshlrev_b32 v126, 9, v125\n"
    "v_add_u32 v126, s84, v126\n"
    "v_mov_b32 v125, 1\n"
    "buffer_atomic_add v125, v126, s[64:67], 0 offen sc0\n"
    "s_branch Lsret%=\n"
    "Lside3%=:\n"
    "s_cmp_eq_u32 s79, 1\n"
    "s_cbranch_scc0 Lsret%=\n"
    "s_waitcnt vmcnt(24)\n"
    "s_lshl_b32 s84, s81, 2\n"
    "s_add_u32 s84, s84, 162304\n"
    "v_readfirstlane_b32 s85, v125\n"
    "v_mov_b32 v125, s84\n"
    "s_nop 0\n"
    "v_mov_b32 v126, s85\n"
    "ds_write_b32 v125, v126\n"
    "s_branch Lsret%=\n"
    "Lslow%=:\n"
    "s_waitcnt vmcnt(0) lgkmcnt(0)\n"
    "s_nop 15\n"
    "v_mbcnt_lo_u32_b32 v126, -1, 0\n"
    "v_mbcnt_hi_u32_b32 v126, -1, v126\n"
    "s_lshl_b32 s84, s81, 14\n"
    "v_lshlrev_b32 v124, 4, v126\n"
    "v_add_u32 v124, s84, v124\n"
    "buffer_store_dwordx4 v[80:83], v124, s[68:71], s92 offen offset:0\n"
    "buffer_store_dwordx4 v[84:87], v124, s[68:71], s92 offen offset:1024\n"
    "buffer_store_dwordx4 v[88:91], v124, s[68:71], s92 offen offset:2048\n"
    "buffer_store_dwordx4 v[92:95], v124, s[68:71], s92 offen offset:3072\n"
    "buffer_store_dwordx4 v[96:99], v124, s[68:71], s93 offen offset:0\n"
    "buffer_store_dwordx4 v[100:103], v124, s[68:71], s93 offen offset:1024\n"
    "buffer_store_dwordx4 v[104:107], v124, s[68:71], s93 offen offset:2048\n"
    "buffer_store_dwordx4 v[108:111], v124, s[68:71], s93 offen offset:3072\n"
    "buffer_store_dwordx4 v[0:3], v124, s[68:71], s94 offen offset:0\n"
    "buffer_store_dwordx4 v[4:7], v124, s[68:71], s94 offen offset:1024\n"
    "buffer_store_dwordx4 v[8:11], v124, s[68:71], s94 offen offset:2048\n"
    "buffer_store_dwordx4 v[12:15], v124, s[68:71], s94 offen offset:3072\n"
    "buffer_store_dwordx4 v[16:19], v124, s[68:71], s95 offen offset:0\n"
    "buffer_store_dwordx4 v[20:23], v124, s[68:71], s95 offen offset:1024\n"
    "buffer_store_dwordx4 v[24:27], v124, s[68:71], s95 offen offset:2048\n"
    "buffer_store_dwordx4 v[28:31], v124, s[68:71], s95 offen offset:3072\n"
    "s_nop 1\n"
    "v_bfe_u32 v127, v126, 2, 2\n"
    "v_lshlrev_b32 v127, 3, v127\n"
    "v_and_b32 v116, 3, v126\n"
    "v_add_u32 v127, v127, v116\n"
    "v_lshlrev_b32 v127, 9, v127\n"
    "v_lshrrev_b32 v116, 4, v126\n"
    "v_lshl_add_u32 v127, v116, 4, v127\n"
    "s_lshl_b32 s84, s82, 19\n"
    "v_add_u32 v127, s84, v127\n"
    "s_waitcnt vmcnt(0)\n"
    "v_mov_b32 v116, 0\n"
    "v_mov_b32 v117, 0\n"
    "v_mov_b32 v118, 0\n"
    "v_mov_b32 v119, 0\n"
    "s_add_u32 s84, s28, 0\n"
    "buffer_load_dwordx4 v[80:83], v127, s[24:27], s84 offen offset:0\n"
    "buffer_load_dwordx4 v[84:87], v127, s[24:27], s84 offen offset:64\n"
    "buffer_load_dwordx4 v[88:91], v127, s[24:27], s84 offen offset:128\n"
    "buffer_load_dwordx4 v[92:95], v127, s[24:27], s84 offen offset:192\n"
    "buffer_load_dwordx4 v[96:99], v127, s[24:27], s84 offen offset:256\n"
    "buffer_load_dwordx4 v[100:103], v127, s[24:27], s84 offen offset:320\n"
    "buffer_load_dwordx4 v[104:107], v127, s[24:27], s84 offen offset:384\n"
    "buffer_load_dwordx4 v[108:111], v127, s[24:27], s84 offen offset:448\n"
    "s_waitcnt vmcnt(0)\n"
    "v_mfma_f32_16x16x32_bf16 v[0:3], v[80:83], a[0:3], v[64:67]\n"
    "v_mfma_f32_16x16x32_bf16 v[4:7], v[80:83], a[4:7], v[68:71]\n"
    "v_mfma_f32_16x16x32_bf16 v[8:11], v[80:83], a[8:11], v[72:75]\n"
    "v_mfma_f32_16x16x32_bf16 v[12:15], v[80:83], a[12:15], v[76:79]\n"
    "v_mfma_f32_16x16x32_bf16 v[0:3], v[84:87], a[16:19], v[0:3]\n"
    "v_mfma_f32_16x16x32_bf16 v[4:7], v[84:87], a[20:23], v[4:7]\n"
    "v_mfma_f32_16x16x32_bf16 v[8:11], v[84:87], a[24:27], v[8:11]\n"
    "v_mfma_f32_16x16x32_bf16 v[12:15], v[84:87], a[28:31], v[12:15]\n"
    "v_mfma_f32_16x16x32_bf16 v[0:3], v[88:91], a[32:35], v[0:3]\n"
    "v_mfma_f32_16x16x32_bf16 v[4:7], v[88:91], a[36:39], v[4:7]\n"
    "v_mfma_f32_16x16x32_bf16 v[8:11], v[88:91], a[40:43], v[8:11]\n"
    "v_mfma_f32_16x16x32_bf16 v[12:15], v[88:91], a[44:47], v[12:15]\n"
    "v_mfma_f32_16x16x32_bf16 v[0:3], v[92:95], a[48:51], v[0:3]\n"
    "v_mfma_f32_16x16x32_bf16 v[4:7], v[92:95], a[52:55], v[4:7]\n"
    "v_mfma_f32_16x16x32_bf16 v[8:11], v[92:95], a[56:59], v[8:11]\n"
    "v_mfma_f32_16x16x32_bf16 v[12:15], v[92:95], a[60:63], v[12:15]\n"
    "v_mfma_f32_16x16x32_bf16 v[0:3], v[96:99], a[64:67], v[0:3]\n"
    "v_mfma_f32_16x16x32_bf16 v[4:7], v[96:99], a[68:71], v[4:7]\n"
    "v_mfma_f32_16x16x32_bf16 v[8:11], v[96:99], a[72:75], v[8:11]\n"
    "v_mfma_f32_16x16x32_bf16 v[12:15], v[96:99], a[76:79], v[12:15]\n"
    "v_mfma_f32_16x16x32_bf16 v[0:3], v[100:103], a[80:83], v[0:3]\n"
    "v_mfma_f32_16x16x32_bf16 v[4:7], v[100:103], a[84:87], v[4:7]\n"
    "v_mfma_f32_16x16x32_bf16 v[8:11], v[100:103], a[88:91], v[8:11]\n"
    "v_mfma_f32_16x16x32_bf16 v[12:15], v[100:103], a[92:95], v[12:15]\n"
    "v_mfma_f32_16x16x32_bf16 v[0:3], v[104:107], a[96:99], v[0:3]\n"
    "v_mfma_f32_16x16x32_bf16 v[4:7], v[104:107], a[100:103], v[4:7]\n"
    "v_mfma_f32_16x16x32_bf16 v[8:11], v[104:107], a[104:107], v[8:11]\n"
    "v_mfma_f32_16x16x32_bf16 v[12:15], v[104:107], a[108:111], v[12:15]\n"
    "v_mfma_f32_16x16x32_bf16 v[0:3], v[108:111], a[112:115], v[0:3]\n"
    "v_mfma_f32_16x16x32_bf16 v[4:7], v[108:111], a[116:119], v[4:7]\n"
    "v_mfma_f32_16x16x32_bf16 v[8:11], v[108:111], a[120:123], v[8:11]\n"
    "v_mfma_f32_16x16x32_bf16 v[12:15], v[108:111], a[124:127], v[12:15]\n"
    "s_nop 7\n"
    "v_max3_f32 v116, v116, v0, v1\n"
    "v_max3_f32 v116, v116, v2, v3\n"
    "v_max3_f32 v117, v117, v4, v5\n"
    "v_max3_f32 v117, v117, v6, v7\n"
    "v_max3_f32 v118, v118, v8, v9\n"
    "v_max3_f32 v118, v118, v10, v11\n"
    "v_max3_f32 v119, v119, v12, v13\n"
    "v_max3_f32 v119, v119, v14, v15\n"
    "s_add_u32 s84, s28, 2048\n"
    "buffer_load_dwordx4 v[80:83], v127, s[24:27], s84 offen offset:0\n"
    "buffer_load_dwordx4 v[84:87], v127, s[24:27], s84 offen offset:64\n"
    "buffer_load_dwordx4 v[88:91], v127, s[24:27], s84 offen offset:128\n"
    "buffer_load_dwordx4 v[92:95], v127, s[24:27], s84 offen offset:192\n"
    "buffer_load_dwordx4 v[96:99], v127, s[24:27], s84 offen offset:256\n"
    "buffer_load_dwordx4 v[100:103], v127, s[24:27], s84 offen offset:320\n"
    "buffer_load_dwordx4 v[104:107], v127, s[24:27], s84 offen offset:384\n"
    "buffer_load_dwordx4 v[108:111], v127, s[24:27], s84 offen offset:448\n"
    "s_waitcnt vmcnt(0)\n"
    "v_mfma_f32_16x16x32_bf16 v[0:3], v[80:83], a[0:3], v[64:67]\n"
    "v_mfma_f32_16x16x32_bf16 v[4:7], v[80:83], a[4:7], v[68:71]\n"
    "v_mfma_f32_16x16x32_bf16 v[8:11], v[80:83], a[8:11], v[72:75]\n"
    "v_mfma_f32_16x16x32_bf16 v[12:15], v[80:83], a[12:15], v[76:79]\n"
    "v_mfma_f32_16x16x32_bf16 v[0:3], v[84:87], a[16:19], v[0:3]\n"
    "v_mfma_f32_16x16x32_bf16 v[4:7], v[84:87], a[20:23], v[4:7]\n"
    "v_mfma_f32_16x16x32_bf16 v[8:11], v[84:87], a[24:27], v[8:11]\n"
    "v_mfma_f32_16x16x32_bf16 v[12:15], v[84:87], a[28:31], v[12:15]\n"
    "v_mfma_f32_16x16x32_bf16 v[0:3], v[88:91], a[32:35], v[0:3]\n"
    "v_mfma_f32_16x16x32_bf16 v[4:7], v[88:91], a[36:39], v[4:7]\n"
    "v_mfma_f32_16x16x32_bf16 v[8:11], v[88:91], a[40:43], v[8:11]\n"
    "v_mfma_f32_16x16x32_bf16 v[12:15], v[88:91], a[44:47], v[12:15]\n"
    "v_mfma_f32_16x16x32_bf16 v[0:3], v[92:95], a[48:51], v[0:3]\n"
    "v_mfma_f32_16x16x32_bf16 v[4:7], v[92:95], a[52:55], v[4:7]\n"
    "v_mfma_f32_16x16x32_bf16 v[8:11], v[92:95], a[56:59], v[8:11]\n"
    "v_mfma_f32_16x16x32_bf16 v[12:15], v[92:95], a[60:63], v[12:15]\n"
    "v_mfma_f32_16x16x32_bf16 v[0:3], v[96:99], a[64:67], v[0:3]\n"
    "v_mfma_f32_16x16x32_bf16 v[4:7], v[96:99], a[68:71], v[4:7]\n"
    "v_mfma_f32_16x16x32_bf16 v[8:11], v[96:99], a[72:75], v[8:11]\n"
    "v_mfma_f32_16x16x32_bf16 v[12:15], v[96:99], a[76:79], v[12:15]\n"
    "v_mfma_f32_16x16x32_bf16 v[0:3], v[100:103], a[80:83], v[0:3]\n"
    "v_mfma_f32_16x16x32_bf16 v[4:7], v[100:103], a[84:87], v[4:7]\n"
    "v_mfma_f32_16x16x32_bf16 v[8:11], v[100:103], a[88:91], v[8:11]\n"
    "v_mfma_f32_16x16x32_bf16 v[12:15], v[100:103], a[92:95], v[12:15]\n"
    "v_mfma_f32_16x16x32_bf16 v[0:3], v[104:107], a[96:99], v[0:3]\n"
    "v_mfma_f32_16x16x32_bf16 v[4:7], v[104:107], a[100:103], v[4:7]\n"
    "v_mfma_f32_16x16x32_bf16 v[8:11], v[104:107], a[104:107], v[8:11]\n"
    "v_mfma_f32_16x16x32_bf16 v[12:15], v[104:107], a[108:111], v[12:15]\n"
    "v_mfma_f32_16x16x32_bf16 v[0:3], v[108:111], a[112:115], v[0:3]\n"
    "v_mfma_f32_16x16x32_bf16 v[4:7], v[108:111], a[116:119], v[4:7]\n"
    "v_mfma_f32_16x16x32_bf16 v[8:11], v[108:111], a[120:123], v[8:11]\n"
    "v_mfma_f32_16x16x32_bf16 v[12:15], v[108:111], a[124:127], v[12:15]\n"
    "s_nop 7\n"
    "v_max3_f32 v116, v116, v0, v1\n"
    "v_max3_f32 v116, v116, v2, v3\n"
    "v_max3_f32 v117, v117, v4, v5\n"
    "v_max3_f32 v117, v117, v6, v7\n"
    "v_max3_f32 v118, v118, v8, v9\n"
    "v_max3_f32 v118, v118, v10, v11\n"
    "v_max3_f32 v119, v119, v12, v13\n"
    "v_max3_f32 v119, v119, v14, v15\n"
    "s_add_u32 s84, s28, 16384\n"
    "buffer_load_dwordx4 v[80:83], v127, s[24:27], s84 offen offset:0\n"
    "buffer_load_dwordx4 v[84:87], v127, s[24:27], s84 offen offset:64\n"
    "buffer_load_dwordx4 v[88:91], v127, s[24:27], s84 offen offset:128\n"
    "buffer_load_dwordx4 v[92:95], v127, s[24:27], s84 offen offset:192\n"
    "buffer_load_dwordx4 v[96:99], v127, s[24:27], s84 offen offset:256\n"
    "buffer_load_dwordx4 v[100:103], v127, s[24:27], s84 offen offset:320\n"
    "buffer_load_dwordx4 v[104:107], v127, s[24:27], s84 offen offset:384\n"
    "buffer_load_dwordx4 v[108:111], v127, s[24:27], s84 offen offset:448\n"
    "s_waitcnt vmcnt(0)\n"
    "v_mfma_f32_16x16x32_bf16 v[0:3], v[80:83], a[0:3], v[64:67]\n"
    "v_mfma_f32_16x16x32_bf16 v[4:7], v[80:83], a[4:7], v[68:71]\n"
    "v_mfma_f32_16x16x32_bf16 v[8:11], v[80:83], a[8:11], v[72:75]\n"
    "v_mfma_f32_16x16x32_bf16 v[12:15], v[80:83], a[12:15], v[76:79]\n"
    "v_mfma_f32_16x16x32_bf16 v[0:3], v[84:87], a[16:19], v[0:3]\n"
    "v_mfma_f32_16x16x32_bf16 v[4:7], v[84:87], a[20:23], v[4:7]\n"
    "v_mfma_f32_16x16x32_bf16 v[8:11], v[84:87], a[24:27], v[8:11]\n"
    "v_mfma_f32_16x16x32_bf16 v[12:15], v[84:87], a[28:31], v[12:15]\n"
    "v_mfma_f32_16x16x32_bf16 v[0:3], v[88:91], a[32:35], v[0:3]\n"
    "v_mfma_f32_16x16x32_bf16 v[4:7], v[88:91], a[36:39], v[4:7]\n"
    "v_mfma_f32_16x16x32_bf16 v[8:11], v[88:91], a[40:43], v[8:11]\n"
    "v_mfma_f32_16x16x32_bf16 v[12:15], v[88:91], a[44:47], v[12:15]\n"
    "v_mfma_f32_16x16x32_bf16 v[0:3], v[92:95], a[48:51], v[0:3]\n"
    "v_mfma_f32_16x16x32_bf16 v[4:7], v[92:95], a[52:55], v[4:7]\n"
    "v_mfma_f32_16x16x32_bf16 v[8:11], v[92:95], a[56:59], v[8:11]\n"
    "v_mfma_f32_16x16x32_bf16 v[12:15], v[92:95], a[60:63], v[12:15]\n"
    "v_mfma_f32_16x16x32_bf16 v[0:3], v[96:99], a[64:67], v[0:3]\n"
    "v_mfma_f32_16x16x32_bf16 v[4:7], v[96:99], a[68:71], v[4:7]\n"
    "v_mfma_f32_16x16x32_bf16 v[8:11], v[96:99], a[72:75], v[8:11]\n"
    "v_mfma_f32_16x16x32_bf16 v[12:15], v[96:99], a[76:79], v[12:15]\n"
    "v_mfma_f32_16x16x32_bf16 v[0:3], v[100:103], a[80:83], v[0:3]\n"
    "v_mfma_f32_16x16x32_bf16 v[4:7], v[100:103], a[84:87], v[4:7]\n"
    "v_mfma_f32_16x16x32_bf16 v[8:11], v[100:103], a[88:91], v[8:11]\n"
    "v_mfma_f32_16x16x32_bf16 v[12:15], v[100:103], a[92:95], v[12:15]\n"
    "v_mfma_f32_16x16x32_bf16 v[0:3], v[104:107], a[96:99], v[0:3]\n"
    "v_mfma_f32_16x16x32_bf16 v[4:7], v[104:107], a[100:103], v[4:7]\n"
    "v_mfma_f32_16x16x32_bf16 v[8:11], v[104:107], a[104:107], v[8:11]\n"
    "v_mfma_f32_16x16x32_bf16 v[12:15], v[104:107], a[108:111], v[12:15]\n"
    "v_mfma_f32_16x16x32_bf16 v[0:3], v[108:111], a[112:115], v[0:3]\n"
    "v_mfma_f32_16x16x32_bf16 v[4:7], v[108:111], a[116:119], v[4:7]\n"
    "v_mfma_f32_16x16x32_bf16 v[8:11], v[108:111], a[120:123], v[8:11]\n"
    "v_mfma_f32_16x16x32_bf16 v[12:15], v[108:111], a[124:127], v[12:15]\n"
    "s_nop 7\n"
    "v_max3_f32 v116, v116, v0, v1\n"
    "v_max3_f32 v116, v116, v2, v3\n"
    "v_max3_f32 v117, v117, v4, v5\n"
    "v_max3_f32 v117, v117, v6, v7\n"
    "v_max3_f32 v118, v118, v8, v9\n"
    "v_max3_f32 v118, v118, v10, v11\n"
    "v_max3_f32 v119, v119, v12, v13\n"
    "v_max3_f32 v119, v119, v14, v15\n"
    "s_add_u32 s84, s28, 18432\n"
    "buffer_load_dwordx4 v[80:83], v127, s[24:27], s84 offen offset:0\n"
    "buffer_load_dwordx4 v[84:87], v127, s[24:27], s84 offen offset:64\n"
    "buffer_load_dwordx4 v[88:91], v127, s[24:27], s84 offen offset:128\n"
    "buffer_load_dwordx4 v[92:95], v127, s[24:27], s84 offen offset:192\n"
    "buffer_load_dwordx4 v[96:99], v127, s[24:27], s84 offen offset:256\n"
    "buffer_load_dwordx4 v[100:103], v127, s[24:27], s84 offen offset:320\n"
    "buffer_load_dwordx4 v[104:107], v127, s[24:27], s84 offen offset:384\n"
    "buffer_load_dwordx4 v[108:111], v127, s[24:27], s84 offen offset:448\n"
    "s_waitcnt vmcnt(0)\n"
    "v_mfma_f32_16x16x32_bf16 v[0:3], v[80:83], a[0:3], v[64:67]\n"
    "v_mfma_f32_16x16x32_bf16 v[4:7], v[80:83], a[4:7], v[68:71]\n"
    "v_mfma_f32_16x16x32_bf16 v[8:11], v[80:83], a[8:11], v[72:75]\n"
    "v_mfma_f32_16x16x32_bf16 v[12:15], v[80:83], a[12:15], v[76:79]\n"
    "v_mfma_f32_16x16x32_bf16 v[0:3], v[84:87], a[16:19], v[0:3]\n"
    "v_mfma_f32_16x16x32_bf16 v[4:7], v[84:87], a[20:23], v[4:7]\n"
    "v_mfma_f32_16x16x32_bf16 v[8:11], v[84:87], a[24:27], v[8:11]\n"
    "v_mfma_f32_16x16x32_bf16 v[12:15], v[84:87], a[28:31], v[12:15]\n"
    "v_mfma_f32_16x16x32_bf16 v[0:3], v[88:91], a[32:35], v[0:3]\n"
    "v_mfma_f32_16x16x32_bf16 v[4:7], v[88:91], a[36:39], v[4:7]\n"
    "v_mfma_f32_16x16x32_bf16 v[8:11], v[88:91], a[40:43], v[8:11]\n"
    "v_mfma_f32_16x16x32_bf16 v[12:15], v[88:91], a[44:47], v[12:15]\n"
    "v_mfma_f32_16x16x32_bf16 v[0:3], v[92:95], a[48:51], v[0:3]\n"
    "v_mfma_f32_16x16x32_bf16 v[4:7], v[92:95], a[52:55], v[4:7]\n"
    "v_mfma_f32_16x16x32_bf16 v[8:11], v[92:95], a[56:59], v[8:11]\n"
    "v_mfma_f32_16x16x32_bf16 v[12:15], v[92:95], a[60:63], v[12:15]\n"
    "v_mfma_f32_16x16x32_bf16 v[0:3], v[96:99], a[64:67], v[0:3]\n"
    "v_mfma_f32_16x16x32_bf16 v[4:7], v[96:99], a[68:71], v[4:7]\n"
    "v_mfma_f32_16x16x32_bf16 v[8:11], v[96:99], a[72:75], v[8:11]\n"
    "v_mfma_f32_16x16x32_bf16 v[12:15], v[96:99], a[76:79], v[12:15]\n"
    "v_mfma_f32_16x16x32_bf16 v[0:3], v[100:103], a[80:83], v[0:3]\n"
    "v_mfma_f32_16x16x32_bf16 v[4:7], v[100:103], a[84:87], v[4:7]\n"
    "v_mfma_f32_16x16x32_bf16 v[8:11], v[100:103], a[88:91], v[8:11]\n"
    "v_mfma_f32_16x16x32_bf16 v[12:15], v[100:103], a[92:95], v[12:15]\n"
    "v_mfma_f32_16x16x32_bf16 v[0:3], v[104:107], a[96:99], v[0:3]\n"
    "v_mfma_f32_16x16x32_bf16 v[4:7], v[104:107], a[100:103], v[4:7]\n"
    "v_mfma_f32_16x16x32_bf16 v[8:11], v[104:107], a[104:107], v[8:11]\n"
    "v_mfma_f32_16x16x32_bf16 v[12:15], v[104:107], a[108:111], v[12:15]\n"
    "v_mfma_f32_16x16x32_bf16 v[0:3], v[108:111], a[112:115], v[0:3]\n"
    "v_mfma_f32_16x16x32_bf16 v[4:7], v[108:111], a[116:119], v[4:7]\n"
    "v_mfma_f32_16x16x32_bf16 v[8:11], v[108:111], a[120:123], v[8:11]\n"
    "v_mfma_f32_16x16x32_bf16 v[12:15], v[108:111], a[124:127], v[12:15]\n"
    "s_nop 7\n"
    "v_max3_f32 v116, v116, v0, v1\n"
    "v_max3_f32 v116, v116, v2, v3\n"
    "v_max3_f32 v117, v117, v4, v5\n"
    "v_max3_f32 v117, v117, v6, v7\n"
    "v_max3_f32 v118, v118, v8, v9\n"
    "v_max3_f32 v118, v118, v10, v11\n"
    "v_max3_f32 v119, v119, v12, v13\n"
    "v_max3_f32 v119, v119, v14, v15\n"
    "v_mov_b32 v16, v116\n"
    "v_mov_b32 v17, v117\n"
    "v_mov_b32 v18, v118\n"
    "v_mov_b32 v19, v119\n"
    "s_nop 1\n"
    "v_permlane16_swap_b32 v116, v16\n"
    "v_permlane16_swap_b32 v117, v17\n"
    "v_permlane16_swap_b32 v118, v18\n"
    "v_permlane16_swap_b32 v119, v19\n"
    "v_max_f32 v116, v116, v16\n"
    "v_max_f32 v117, v117, v17\n"
    "v_max_f32 v118, v118, v18\n"
    "v_max_f32 v119, v119, v19\n"
    "v_mov_b32 v16, v116\n"
    "v_mov_b32 v17, v117\n"
    "v_mov_b32 v18, v118\n"
    "v_mov_b32 v19, v119\n"
    "s_nop 1\n"
    "v_permlane32_swap_b32 v116, v16\n"
    "v_permlane32_swap_b32 v117, v17\n"
    "v_permlane32_swap_b32 v118, v18\n"
    "v_permlane32_swap_b32 v119, v19\n"
    "v_max_f32 v116, v116, v16\n"
    "v_max_f32 v117, v117, v17\n"
    "v_max_f32 v118, v118, v18\n"
    "v_max_f32 v119, v119, v19\n"
    "v_mov_b32 v20, v116\n"
    "v_mov_b32 v21, v117\n"
    "v_mov_b32 v22, v118\n"
    "v_mov_b32 v23, v119\n"
    "v_exp_f32 v24, -v20\n"
    "v_exp_f32 v25, -v21\n"
    "v_exp_f32 v26, -v22\n"
    "v_exp_f32 v27, -v23\n"
    "v_sub_f32 v64, v64, v20\n"
    "v_sub_f32 v65, v65, v20\n"
    "v_sub_f32 v66, v66, v20\n"
    "v_sub_f32 v67, v67, v20\n"
    "v_sub_f32 v68, v68, v21\n"
    "v_sub_f32 v69, v69, v21\n"
    "v_sub_f32 v70, v70, v21\n"
    "v_sub_f32 v71, v71, v21\n"
    "v_sub_f32 v72, v72, v22\n"
    "v_sub_f32 v73, v73, v22\n"
    "v_sub_f32 v74, v74, v22\n"
    "v_sub_f32 v75, v75, v22\n"
    "v_sub_f32 v76, v76, v23\n"
    "v_sub_f32 v77, v77, v23\n"
    "v_sub_f32 v78, v78, v23\n"
    "v_sub_f32 v79, v79, v23\n"
    "v_mul_f32 v112, v112, v24\n"
    "v_mul_f32 v113, v113, v25\n"
    "v_mul_f32 v114, v114, v26\n"
    "v_mul_f32 v115, v115, v27\n"
    "v_mul_f32 v128, v128, v24\n"
    "v_mul_f32 v129, v129, v24\n"
    "v_mul_f32 v130, v130, v24\n"
    "v_mul_f32 v131, v131, v24\n"
    "v_mul_f32 v132, v132, v25\n"
    "v_mul_f32 v133, v133, v25\n"
    "v_mul_f32 v134, v134, v25\n"
    "v_mul_f32 v135, v135, v25\n"
    "v_accvgpr_read_b32 v0, a128\n"
    "v_accvgpr_read_b32 v1, a129\n"
    "v_accvgpr_read_b32 v2, a130\n"
    "v_accvgpr_read_b32 v3, a131\n"
    "v_mul_f32 v0, v0, v26\n"
    "v_mul_f32 v1, v1, v26\n"
    "v_mul_f32 v2, v2, v26\n"
    "v_mul_f32 v3, v3, v26\n"
    "v_accvgpr_write_b32 a128, v0\n"
    "v_accvgpr_write_b32 a129, v1\n"
    "v_accvgpr_write_b32 a130, v2\n"
    "v_accvgpr_write_b32 a131, v3\n"
    "v_accvgpr_read_b32 v0, a132\n"
    "v_accvgpr_read_b32 v1, a133\n"
    "v_accvgpr_read_b32 v2, a134\n"
    "v_accvgpr_read_b32 v3, a135\n"
    "v_mul_f32 v0, v0, v27\n"
    "v_mul_f32 v1, v1, v27\n"
    "v_mul_f32 v2, v2, v27\n"
    "v_mul_f32 v3, v3, v27\n"
    "v_accvgpr_write_b32 a132, v0\n"
    "v_accvgpr_write_b32 a133, v1\n"
    "v_accvgpr_write_b32 a134, v2\n"
    "v_accvgpr_write_b32 a135, v3\n"
    "v_mul_f32 v136, v136, v24\n"
    "v_mul_f32 v137, v137, v24\n"
    "v_mul_f32 v138, v138, v24\n"
    "v_mul_f32 v139, v139, v24\n"
    "v_mul_f32 v140, v140, v25\n"
    "v_mul_f32 v141, v141, v25\n"
    "v_mul_f32 v142, v142, v25\n"
    "v_mul_f32 v143, v143, v25\n"
    "v_accvgpr_read_b32 v0, a136\n"
    "v_accvgpr_read_b32 v1, a137\n"
    "v_accvgpr_read_b32 v2, a138\n"
    "v_accvgpr_read_b32 v3, a139\n"
    "v_mul_f32 v0, v0, v26\n"
    "v_mul_f32 v1, v1, v26\n"
    "v_mul_f32 v2, v2, v26\n"
    "v_mul_f32 v3, v3, v26\n"
    "v_accvgpr_write_b32 a136, v0\n"
    "v_accvgpr_write_b32 a137, v1\n"
    "v_accvgpr_write_b32 a138, v2\n"
    "v_accvgpr_write_b32 a139, v3\n"
    "v_accvgpr_read_b32 v0, a140\n"
    "v_accvgpr_read_b32 v1, a141\n"
    "v_accvgpr_read_b32 v2, a142\n"
    "v_accvgpr_read_b32 v3, a143\n"
    "v_mul_f32 v0, v0, v27\n"
    "v_mul_f32 v1, v1, v27\n"
    "v_mul_f32 v2, v2, v27\n"
    "v_mul_f32 v3, v3, v27\n"
    "v_accvgpr_write_b32 a140, v0\n"
    "v_accvgpr_write_b32 a141, v1\n"
    "v_accvgpr_write_b32 a142, v2\n"
    "v_accvgpr_write_b32 a143, v3\n"
    "v_mul_f32 v144, v144, v24\n"
    "v_mul_f32 v145, v145, v24\n"
    "v_mul_f32 v146, v146, v24\n"
    "v_mul_f32 v147, v147, v24\n"
    "v_mul_f32 v148, v148, v25\n"
    "v_mul_f32 v149, v149, v25\n"
    "v_mul_f32 v150, v150, v25\n"
    "v_mul_f32 v151, v151, v25\n"
    "v_accvgpr_read_b32 v0, a144\n"
    "v_accvgpr_read_b32 v1, a145\n"
    "v_accvgpr_read_b32 v2, a146\n"
    "v_accvgpr_read_b32 v3, a147\n"
    "v_mul_f32 v0, v0, v26\n"
    "v_mul_f32 v1, v1, v26\n"
    "v_mul_f32 v2, v2, v26\n"
    "v_mul_f32 v3, v3, v26\n"
    "v_accvgpr_write_b32 a144, v0\n"
    "v_accvgpr_write_b32 a145, v1\n"
    "v_accvgpr_write_b32 a146, v2\n"
    "v_accvgpr_write_b32 a147, v3\n"
    "v_accvgpr_read_b32 v0, a148\n"
    "v_accvgpr_read_b32 v1, a149\n"
    "v_accvgpr_read_b32 v2, a150\n"
    "v_accvgpr_read_b32 v3, a151\n"
    "v_mul_f32 v0, v0, v27\n"
    "v_mul_f32 v1, v1, v27\n"
    "v_mul_f32 v2, v2, v27\n"
    "v_mul_f32 v3, v3, v27\n"
    "v_accvgpr_write_b32 a148, v0\n"
    "v_accvgpr_write_b32 a149, v1\n"
    "v_accvgpr_write_b32 a150, v2\n"
    "v_accvgpr_write_b32 a151, v3\n"
    "v_mul_f32 v152, v152, v24\n"
    "v_mul_f32 v153, v153, v24\n"
    "v_mul_f32 v154, v154, v24\n"
    "v_mul_f32 v155, v155, v24\n"
    "v_mul_f32 v156, v156, v25\n"
    "v_mul_f32 v157, v157, v25\n"
    "v_mul_f32 v158, v158, v25\n"
    "v_mul_f32 v159, v159, v25\n"
    "v_accvgpr_read_b32 v0, a152\n"
    "v_accvgpr_read_b32 v1, a153\n"
    "v_accvgpr_read_b32 v2, a154\n"
    "v_accvgpr_read_b32 v3, a155\n"
    "v_mul_f32 v0, v0, v26\n"
    "v_mul_f32 v1, v1, v26\n"
    "v_mul_f32 v2, v2, v26\n"
    "v_mul_f32 v3, v3, v26\n"
    "v_accvgpr_write_b32 a152, v0\n"
    "v_accvgpr_write_b32 a153, v1\n"
    "v_accvgpr_write_b32 a154, v2\n"
    "v_accvgpr_write_b32 a155, v3\n"
    "v_accvgpr_read_b32 v0, a156\n"
    "v_accvgpr_read_b32 v1, a157\n"
    "v_accvgpr_read_b32 v2, a158\n"
    "v_accvgpr_read_b32 v3, a159\n"
    "v_mul_f32 v0, v0, v27\n"
    "v_mul_f32 v1, v1, v27\n"
    "v_mul_f32 v2, v2, v27\n"
    "v_mul_f32 v3, v3, v27\n"
    "v_accvgpr_write_b32 a156, v0\n"
    "v_accvgpr_write_b32 a157, v1\n"
    "v_accvgpr_write_b32 a158, v2\n"
    "v_accvgpr_write_b32 a159, v3\n"
    "v_mul_f32 v160, v160, v24\n"
    "v_mul_f32 v161, v161, v24\n"
    "v_mul_f32 v162, v162, v24\n"
    "v_mul_f32 v163, v163, v24\n"
    "v_mul_f32 v164, v164, v25\n"
    "v_mul_f32 v165, v165, v25\n"
    "v_mul_f32 v166, v166, v25\n"
    "v_mul_f32 v167, v167, v25\n"
    "v_accvgpr_read_b32 v0, a160\n"
    "v_accvgpr_read_b32 v1, a161\n"
    "v_accvgpr_read_b32 v2, a162\n"
    "v_accvgpr_read_b32 v3, a163\n"
    "v_mul_f32 v0, v0, v26\n"
    "v_mul_f32 v1, v1, v26\n"
    "v_mul_f32 v2, v2, v26\n"
    "v_mul_f32 v3, v3, v26\n"
    "v_accvgpr_write_b32 a160, v0\n"
    "v_accvgpr_write_b32 a161, v1\n"
    "v_accvgpr_write_b32 a162, v2\n"
    "v_accvgpr_write_b32 a163, v3\n"
    "v_accvgpr_read_b32 v0, a164\n"
    "v_accvgpr_read_b32 v1, a165\n"
    "v_accvgpr_read_b32 v2, a166\n"
    "v_accvgpr_read_b32 v3, a167\n"
    "v_mul_f32 v0, v0, v27\n"
    "v_mul_f32 v1, v1, v27\n"
    "v_mul_f32 v2, v2, v27\n"
    "v_mul_f32 v3, v3, v27\n"
    "v_accvgpr_write_b32 a164, v0\n"
    "v_accvgpr_write_b32 a165, v1\n"
    "v_accvgpr_write_b32 a166, v2\n"
    "v_accvgpr_write_b32 a167, v3\n"
    "v_mul_f32 v168, v168, v24\n"
    "v_mul_f32 v169, v169, v24\n"
    "v_mul_f32 v170, v170, v24\n"
    "v_mul_f32 v171, v171, v24\n"
    "v_mul_f32 v172, v172, v25\n"
    "v_mul_f32 v173, v173, v25\n"
    "v_mul_f32 v174, v174, v25\n"
    "v_mul_f32 v175, v175, v25\n"
    "v_accvgpr_read_b32 v0, a168\n"
    "v_accvgpr_read_b32 v1, a169\n"
    "v_accvgpr_read_b32 v2, a170\n"
    "v_accvgpr_read_b32 v3, a171\n"
    "v_mul_f32 v0, v0, v26\n"
    "v_mul_f32 v1, v1, v26\n"
    "v_mul_f32 v2, v2, v26\n"
    "v_mul_f32 v3, v3, v26\n"
    "v_accvgpr_write_b32 a168, v0\n"
    "v_accvgpr_write_b32 a169, v1\n"
    "v_accvgpr_write_b32 a170, v2\n"
    "v_accvgpr_write_b32 a171, v3\n"
    "v_accvgpr_read_b32 v0, a172\n"
    "v_accvgpr_read_b32 v1, a173\n"
    "v_accvgpr_read_b32 v2, a174\n"
    "v_accvgpr_read_b32 v3, a175\n"
    "v_mul_f32 v0, v0, v27\n"
    "v_mul_f32 v1, v1, v27\n"
    "v_mul_f32 v2, v2, v27\n"
    "v_mul_f32 v3, v3, v27\n"
    "v_accvgpr_write_b32 a172, v0\n"
    "v_accvgpr_write_b32 a173, v1\n"
    "v_accvgpr_write_b32 a174, v2\n"
    "v_accvgpr_write_b32 a175, v3\n"
    "v_mul_f32 v176, v176, v24\n"
    "v_mul_f32 v177, v177, v24\n"
    "v_mul_f32 v178, v178, v24\n"
    "v_mul_f32 v179, v179, v24\n"
    "v_mul_f32 v180, v180, v25\n"
    "v_mul_f32 v181, v181, v25\n"
    "v_mul_f32 v182, v182, v25\n"
    "v_mul_f32 v183, v183, v25\n"
    "v_accvgpr_read_b32 v0, a176\n"
    "v_accvgpr_read_b32 v1, a177\n"
    "v_accvgpr_read_b32 v2, a178\n"
    "v_accvgpr_read_b32 v3, a179\n"
    "v_mul_f32 v0, v0, v26\n"
    "v_mul_f32 v1, v1, v26\n"
    "v_mul_f32 v2, v2, v26\n"
    "v_mul_f32 v3, v3, v26\n"
    "v_accvgpr_write_b32 a176, v0\n"
    "v_accvgpr_write_b32 a177, v1\n"
    "v_accvgpr_write_b32 a178, v2\n"
    "v_accvgpr_write_b32 a179, v3\n"
    "v_accvgpr_read_b32 v0, a180\n"
    "v_accvgpr_read_b32 v1, a181\n"
    "v_accvgpr_read_b32 v2, a182\n"
    "v_accvgpr_read_b32 v3, a183\n"
    "v_mul_f32 v0, v0, v27\n"
    "v_mul_f32 v1, v1, v27\n"
    "v_mul_f32 v2, v2, v27\n"
    "v_mul_f32 v3, v3, v27\n"
    "v_accvgpr_write_b32 a180, v0\n"
    "v_accvgpr_write_b32 a181, v1\n"
    "v_accvgpr_write_b32 a182, v2\n"
    "v_accvgpr_write_b32 a183, v3\n"
    "v_mul_f32 v184, v184, v24\n"
    "v_mul_f32 v185, v185, v24\n"
    "v_mul_f32 v186, v186, v24\n"
    "v_mul_f32 v187, v187, v24\n"
    "v_mul_f32 v188, v188, v25\n"
    "v_mul_f32 v189, v189, v25\n"
    "v_mul_f32 v190, v190, v25\n"
    "v_mul_f32 v191, v191, v25\n"
    "v_accvgpr_read_b32 v0, a184\n"
    "v_accvgpr_read_b32 v1, a185\n"
    "v_accvgpr_read_b32 v2, a186\n"
    "v_accvgpr_read_b32 v3, a187\n"
    "v_mul_f32 v0, v0, v26\n"
    "v_mul_f32 v1, v1, v26\n"
    "v_mul_f32 v2, v2, v26\n"
    "v_mul_f32 v3, v3, v26\n"
    "v_accvgpr_write_b32 a184, v0\n"
    "v_accvgpr_write_b32 a185, v1\n"
    "v_accvgpr_write_b32 a186, v2\n"
    "v_accvgpr_write_b32 a187, v3\n"
    "v_accvgpr_read_b32 v0, a188\n"
    "v_accvgpr_read_b32 v1, a189\n"
    "v_accvgpr_read_b32 v2, a190\n"
    "v_accvgpr_read_b32 v3, a191\n"
    "v_mul_f32 v0, v0, v27\n"
    "v_mul_f32 v1, v1, v27\n"
    "v_mul_f32 v2, v2, v27\n"
    "v_mul_f32 v3, v3, v27\n"
    "v_accvgpr_write_b32 a188, v0\n"
    "v_accvgpr_write_b32 a189, v1\n"
    "v_accvgpr_write_b32 a190, v2\n"
    "v_accvgpr_write_b32 a191, v3\n"
    "v_mul_f32 v192, v192, v24\n"
    "v_mul_f32 v193, v193, v24\n"
    "v_mul_f32 v194, v194, v24\n"
    "v_mul_f32 v195, v195, v24\n"
    "v_mul_f32 v196, v196, v25\n"
    "v_mul_f32 v197, v197, v25\n"
    "v_mul_f32 v198, v198, v25\n"
    "v_mul_f32 v199, v199, v25\n"
    "v_accvgpr_read_b32 v0, a192\n"
    "v_accvgpr_read_b32 v1, a193\n"
    "v_accvgpr_read_b32 v2, a194\n"
    "v_accvgpr_read_b32 v3, a195\n"
    "v_mul_f32 v0, v0, v26\n"
    "v_mul_f32 v1, v1, v26\n"
    "v_mul_f32 v2, v2, v26\n"
    "v_mul_f32 v3, v3, v26\n"
    "v_accvgpr_write_b32 a192, v0\n"
    "v_accvgpr_write_b32 a193, v1\n"
    "v_accvgpr_write_b32 a194, v2\n"
    "v_accvgpr_write_b32 a195, v3\n"
    "v_accvgpr_read_b32 v0, a196\n"
    "v_accvgpr_read_b32 v1, a197\n"
    "v_accvgpr_read_b32 v2, a198\n"
    "v_accvgpr_read_b32 v3, a199\n"
    "v_mul_f32 v0, v0, v27\n"
    "v_mul_f32 v1, v1, v27\n"
    "v_mul_f32 v2, v2, v27\n"
    "v_mul_f32 v3, v3, v27\n"
    "v_accvgpr_write_b32 a196, v0\n"
    "v_accvgpr_write_b32 a197, v1\n"
    "v_accvgpr_write_b32 a198, v2\n"
    "v_accvgpr_write_b32 a199, v3\n"
    "v_mul_f32 v200, v200, v24\n"
    "v_mul_f32 v201, v201, v24\n"
    "v_mul_f32 v202, v202, v24\n"
    "v_mul_f32 v203, v203, v24\n"
    "v_mul_f32 v204, v204, v25\n"
    "v_mul_f32 v205, v205, v25\n"
    "v_mul_f32 v206, v206, v25\n"
    "v_mul_f32 v207, v207, v25\n"
    "v_accvgpr_read_b32 v0, a200\n"
    "v_accvgpr_read_b32 v1, a201\n"
    "v_accvgpr_read_b32 v2, a202\n"
    "v_accvgpr_read_b32 v3, a203\n"
    "v_mul_f32 v0, v0, v26\n"
    "v_mul_f32 v1, v1, v26\n"
    "v_mul_f32 v2, v2, v26\n"
    "v_mul_f32 v3, v3, v26\n"
    "v_accvgpr_write_b32 a200, v0\n"
    "v_accvgpr_write_b32 a201, v1\n"
    "v_accvgpr_write_b32 a202, v2\n"
    "v_accvgpr_write_b32 a203, v3\n"
    "v_accvgpr_read_b32 v0, a204\n"
    "v_accvgpr_read_b32 v1, a205\n"
    "v_accvgpr_read_b32 v2, a206\n"
    "v_accvgpr_read_b32 v3, a207\n"
    "v_mul_f32 v0, v0, v27\n"
    "v_mul_f32 v1, v1, v27\n"
    "v_mul_f32 v2, v2, v27\n"
    "v_mul_f32 v3, v3, v27\n"
    "v_accvgpr_write_b32 a204, v0\n"
    "v_accvgpr_write_b32 a205, v1\n"
    "v_accvgpr_write_b32 a206, v2\n"
    "v_accvgpr_write_b32 a207, v3\n"
    "v_mul_f32 v208, v208, v24\n"
    "v_mul_f32 v209, v209, v24\n"
    "v_mul_f32 v210, v210, v24\n"
    "v_mul_f32 v211, v211, v24\n"
    "v_mul_f32 v212, v212, v25\n"
    "v_mul_f32 v213, v213, v25\n"
    "v_mul_f32 v214, v214, v25\n"
    "v_mul_f32 v215, v215, v25\n"
    "v_accvgpr_read_b32 v0, a208\n"
    "v_accvgpr_read_b32 v1, a209\n"
    "v_accvgpr_read_b32 v2, a210\n"
    "v_accvgpr_read_b32 v3, a211\n"
    "v_mul_f32 v0, v0, v26\n"
    "v_mul_f32 v1, v1, v26\n"
    "v_mul_f32 v2, v2, v26\n"
    "v_mul_f32 v3, v3, v26\n"
    "v_accvgpr_write_b32 a208, v0\n"
    "v_accvgpr_write_b32 a209, v1\n"
    "v_accvgpr_write_b32 a210, v2\n"
    "v_accvgpr_write_b32 a211, v3\n"
    "v_accvgpr_read_b32 v0, a212\n"
    "v_accvgpr_read_b32 v1, a213\n"
    "v_accvgpr_read_b32 v2, a214\n"
    "v_accvgpr_read_b32 v3, a215\n"
    "v_mul_f32 v0, v0, v27\n"
    "v_mul_f32 v1, v1, v27\n"
    "v_mul_f32 v2, v2, v27\n"
    "v_mul_f32 v3, v3, v27\n"
    "v_accvgpr_write_b32 a212, v0\n"
    "v_accvgpr_write_b32 a213, v1\n"
    "v_accvgpr_write_b32 a214, v2\n"
    "v_accvgpr_write_b32 a215, v3\n"
    "v_mul_f32 v216, v216, v24\n"
    "v_mul_f32 v217, v217, v24\n"
    "v_mul_f32 v218, v218, v24\n"
    "v_mul_f32 v219, v219, v24\n"
    "v_mul_f32 v220, v220, v25\n"
    "v_mul_f32 v221, v221, v25\n"
    "v_mul_f32 v222, v222, v25\n"
    "v_mul_f32 v223, v223, v25\n"
    "v_accvgpr_read_b32 v0, a216\n"
    "v_accvgpr_read_b32 v1, a217\n"
    "v_accvgpr_read_b32 v2, a218\n"
    "v_accvgpr_read_b32 v3, a219\n"
    "v_mul_f32 v0, v0, v26\n"
    "v_mul_f32 v1, v1, v26\n"
    "v_mul_f32 v2, v2, v26\n"
    "v_mul_f32 v3, v3, v26\n"
    "v_accvgpr_write_b32 a216, v0\n"
    "v_accvgpr_write_b32 a217, v1\n"
    "v_accvgpr_write_b32 a218, v2\n"
    "v_accvgpr_write_b32 a219, v3\n"
    "v_accvgpr_read_b32 v0, a220\n"
    "v_accvgpr_read_b32 v1, a221\n"
    "v_accvgpr_read_b32 v2, a222\n"
    "v_accvgpr_read_b32 v3, a223\n"
    "v_mul_f32 v0, v0, v27\n"
    "v_mul_f32 v1, v1, v27\n"
    "v_mul_f32 v2, v2, v27\n"
    "v_mul_f32 v3, v3, v27\n"
    "v_accvgpr_write_b32 a220, v0\n"
    "v_accvgpr_write_b32 a221, v1\n"
    "v_accvgpr_write_b32 a222, v2\n"
    "v_accvgpr_write_b32 a223, v3\n"
    "v_mul_f32 v224, v224, v24\n"
    "v_mul_f32 v225, v225, v24\n"
    "v_mul_f32 v226, v226, v24\n"
    "v_mul_f32 v227, v227, v24\n"
    "v_mul_f32 v228, v228, v25\n"
    "v_mul_f32 v229, v229, v25\n"
    "v_mul_f32 v230, v230, v25\n"
    "v_mul_f32 v231, v231, v25\n"
    "v_accvgpr_read_b32 v0, a224\n"
    "v_accvgpr_read_b32 v1, a225\n"
    "v_accvgpr_read_b32 v2, a226\n"
    "v_accvgpr_read_b32 v3, a227\n"
    "v_mul_f32 v0, v0, v26\n"
    "v_mul_f32 v1, v1, v26\n"
    "v_mul_f32 v2, v2, v26\n"
    "v_mul_f32 v3, v3, v26\n"
    "v_accvgpr_write_b32 a224, v0\n"
    "v_accvgpr_write_b32 a225, v1\n"
    "v_accvgpr_write_b32 a226, v2\n"
    "v_accvgpr_write_b32 a227, v3\n"
    "v_accvgpr_read_b32 v0, a228\n"
    "v_accvgpr_read_b32 v1, a229\n"
    "v_accvgpr_read_b32 v2, a230\n"
    "v_accvgpr_read_b32 v3, a231\n"
    "v_mul_f32 v0, v0, v27\n"
    "v_mul_f32 v1, v1, v27\n"
    "v_mul_f32 v2, v2, v27\n"
    "v_mul_f32 v3, v3, v27\n"
    "v_accvgpr_write_b32 a228, v0\n"
    "v_accvgpr_write_b32 a229, v1\n"
    "v_accvgpr_write_b32 a230, v2\n"
    "v_accvgpr_write_b32 a231, v3\n"
    "v_mul_f32 v232, v232, v24\n"
    "v_mul_f32 v233, v233, v24\n"
    "v_mul_f32 v234, v234, v24\n"
    "v_mul_f32 v235, v235, v24\n"
    "v_mul_f32 v236, v236, v25\n"
    "v_mul_f32 v237, v237, v25\n"
    "v_mul_f32 v238, v238, v25\n"
    "v_mul_f32 v239, v239, v25\n"
    "v_accvgpr_read_b32 v0, a232\n"
    "v_accvgpr_read_b32 v1, a233\n"
    "v_accvgpr_read_b32 v2, a234\n"
    "v_accvgpr_read_b32 v3, a235\n"
    "v_mul_f32 v0, v0, v26\n"
    "v_mul_f32 v1, v1, v26\n"
    "v_mul_f32 v2, v2, v26\n"
    "v_mul_f32 v3, v3, v26\n"
    "v_accvgpr_write_b32 a232, v0\n"
    "v_accvgpr_write_b32 a233, v1\n"
    "v_accvgpr_write_b32 a234, v2\n"
    "v_accvgpr_write_b32 a235, v3\n"
    "v_accvgpr_read_b32 v0, a236\n"
    "v_accvgpr_read_b32 v1, a237\n"
    "v_accvgpr_read_b32 v2, a238\n"
    "v_accvgpr_read_b32 v3, a239\n"
    "v_mul_f32 v0, v0, v27\n"
    "v_mul_f32 v1, v1, v27\n"
    "v_mul_f32 v2, v2, v27\n"
    "v_mul_f32 v3, v3, v27\n"
    "v_accvgpr_write_b32 a236, v0\n"
    "v_accvgpr_write_b32 a237, v1\n"
    "v_accvgpr_write_b32 a238, v2\n"
    "v_accvgpr_write_b32 a239, v3\n"
    "v_mul_f32 v240, v240, v24\n"
    "v_mul_f32 v241, v241, v24\n"
    "v_mul_f32 v242, v242, v24\n"
    "v_mul_f32 v243, v243, v24\n"
    "v_mul_f32 v244, v244, v25\n"
    "v_mul_f32 v245, v245, v25\n"
    "v_mul_f32 v246, v246, v25\n"
    "v_mul_f32 v247, v247, v25\n"
    "v_accvgpr_read_b32 v0, a240\n"
    "v_accvgpr_read_b32 v1, a241\n"
    "v_accvgpr_read_b32 v2, a242\n"
    "v_accvgpr_read_b32 v3, a243\n"
    "v_mul_f32 v0, v0, v26\n"
    "v_mul_f32 v1, v1, v26\n"
    "v_mul_f32 v2, v2, v26\n"
    "v_mul_f32 v3, v3, v26\n"
    "v_accvgpr_write_b32 a240, v0\n"
    "v_accvgpr_write_b32 a241, v1\n"
    "v_accvgpr_write_b32 a242, v2\n"
    "v_accvgpr_write_b32 a243, v3\n"
    "v_accvgpr_read_b32 v0, a244\n"
    "v_accvgpr_read_b32 v1, a245\n"
    "v_accvgpr_read_b32 v2, a246\n"
    "v_accvgpr_read_b32 v3, a247\n"
    "v_mul_f32 v0, v0, v27\n"
    "v_mul_f32 v1, v1, v27\n"
    "v_mul_f32 v2, v2, v27\n"
    "v_mul_f32 v3, v3, v27\n"
    "v_accvgpr_write_b32 a244, v0\n"
    "v_accvgpr_write_b32 a245, v1\n"
    "v_accvgpr_write_b32 a246, v2\n"
    "v_accvgpr_write_b32 a247, v3\n"
    "v_mul_f32 v248, v248, v24\n"
    "v_mul_f32 v249, v249, v24\n"
    "v_mul_f32 v250, v250, v24\n"
    "v_mul_f32 v251, v251, v24\n"
    "v_mul_f32 v252, v252, v25\n"
    "v_mul_f32 v253, v253, v25\n"
    "v_mul_f32 v254, v254, v25\n"
    "v_mul_f32 v255, v255, v25\n"
    "v_accvgpr_read_b32 v0, a248\n"
    "v_accvgpr_read_b32 v1, a249\n"
    "v_accvgpr_read_b32 v2, a250\n"
    "v_accvgpr_read_b32 v3, a251\n"
    "v_mul_f32 v0, v0, v26\n"
    "v_mul_f32 v1, v1, v26\n"
    "v_mul_f32 v2, v2, v26\n"
    "v_mul_f32 v3, v3, v26\n"
    "v_accvgpr_write_b32 a248, v0\n"
    "v_accvgpr_write_b32 a249, v1\n"
    "v_accvgpr_write_b32 a250, v2\n"
    "v_accvgpr_write_b32 a251, v3\n"
    "v_accvgpr_read_b32 v0, a252\n"
    "v_accvgpr_read_b32 v1, a253\n"
    "v_accvgpr_read_b32 v2, a254\n"
    "v_accvgpr_read_b32 v3, a255\n"
    "v_mul_f32 v0, v0, v27\n"
    "v_mul_f32 v1, v1, v27\n"
    "v_mul_f32 v2, v2, v27\n"
    "v_mul_f32 v3, v3, v27\n"
    "v_accvgpr_write_b32 a252, v0\n"
    "v_accvgpr_write_b32 a253, v1\n"
    "v_accvgpr_write_b32 a254, v2\n"
    "v_accvgpr_write_b32 a255, v3\n"
    "v_mov_b32 v116, 0\n"
    "v_mov_b32 v117, 0\n"
    "v_mov_b32 v118, 0\n"
    "v_mov_b32 v119, 0\n"
    "s_add_u32 s84, s28, 0\n"
    "buffer_load_dwordx4 v[80:83], v127, s[24:27], s84 offen offset:0\n"
    "buffer_load_dwordx4 v[84:87], v127, s[24:27], s84 offen offset:64\n"
    "buffer_load_dwordx4 v[88:91], v127, s[24:27], s84 offen offset:128\n"
    "buffer_load_dwordx4 v[92:95], v127, s[24:27], s84 offen offset:192\n"
    "buffer_load_dwordx4 v[96:99], v127, s[24:27], s84 offen offset:256\n"
    "buffer_load_dwordx4 v[100:103], v127, s[24:27], s84 offen offset:320\n"
    "buffer_load_dwordx4 v[104:107], v127, s[24:27], s84 offen offset:384\n"
    "buffer_load_dwordx4 v[108:111], v127, s[24:27], s84 offen offset:448\n"
    "s_waitcnt vmcnt(0)\n"
    "v_mfma_f32_16x16x32_bf16 v[0:3], v[80:83], a[0:3], v[64:67]\n"
    "v_mfma_f32_16x16x32_bf16 v[4:7], v[80:83], a[4:7], v[68:71]\n"
    "v_mfma_f32_16x16x32_bf16 v[8:11], v[80:83], a[8:11], v[72:75]\n"
    "v_mfma_f32_16x16x32_bf16 v[12:15], v[80:83], a[12:15], v[76:79]\n"
    "v_mfma_f32_16x16x32_bf16 v[0:3], v[84:87], a[16:19], v[0:3]\n"
    "v_mfma_f32_16x16x32_bf16 v[4:7], v[84:87], a[20:23], v[4:7]\n"
    "v_mfma_f32_16x16x32_bf16 v[8:11], v[84:87], a[24:27], v[8:11]\n"
    "v_mfma_f32_16x16x32_bf16 v[12:15], v[84:87], a[28:31], v[12:15]\n"
    "v_mfma_f32_16x16x32_bf16 v[0:3], v[88:91], a[32:35], v[0:3]\n"
    "v_mfma_f32_16x16x32_bf16 v[4:7], v[88:91], a[36:39], v[4:7]\n"
    "v_mfma_f32_16x16x32_bf16 v[8:11], v[88:91], a[40:43], v[8:11]\n"
    "v_mfma_f32_16x16x32_bf16 v[12:15], v[88:91], a[44:47], v[12:15]\n"
    "v_mfma_f32_16x16x32_bf16 v[0:3], v[92:95], a[48:51], v[0:3]\n"
    "v_mfma_f32_16x16x32_bf16 v[4:7], v[92:95], a[52:55], v[4:7]\n"
    "v_mfma_f32_16x16x32_bf16 v[8:11], v[92:95], a[56:59], v[8:11]\n"
    "v_mfma_f32_16x16x32_bf16 v[12:15], v[92:95], a[60:63], v[12:15]\n"
    "v_mfma_f32_16x16x32_bf16 v[0:3], v[96:99], a[64:67], v[0:3]\n"
    "v_mfma_f32_16x16x32_bf16 v[4:7], v[96:99], a[68:71], v[4:7]\n"
    "v_mfma_f32_16x16x32_bf16 v[8:11], v[96:99], a[72:75], v[8:11]\n"
    "v_mfma_f32_16x16x32_bf16 v[12:15], v[96:99], a[76:79], v[12:15]\n"
    "v_mfma_f32_16x16x32_bf16 v[0:3], v[100:103], a[80:83], v[0:3]\n"
    "v_mfma_f32_16x16x32_bf16 v[4:7], v[100:103], a[84:87], v[4:7]\n"
    "v_mfma_f32_16x16x32_bf16 v[8:11], v[100:103], a[88:91], v[8:11]\n"
    "v_mfma_f32_16x16x32_bf16 v[12:15], v[100:103], a[92:95], v[12:15]\n"
    "v_mfma_f32_16x16x32_bf16 v[0:3], v[104:107], a[96:99], v[0:3]\n"
    "v_mfma_f32_16x16x32_bf16 v[4:7], v[104:107], a[100:103], v[4:7]\n"
    "v_mfma_f32_16x16x32_bf16 v[8:11], v[104:107], a[104:107], v[8:11]\n"
    "v_mfma_f32_16x16x32_bf16 v[12:15], v[104:107], a[108:111], v[12:15]\n"
    "v_mfma_f32_16x16x32_bf16 v[0:3], v[108:111], a[112:115], v[0:3]\n"
    "v_mfma_f32_16x16x32_bf16 v[4:7], v[108:111], a[116:119], v[4:7]\n"
    "v_mfma_f32_16x16x32_bf16 v[8:11], v[108:111], a[120:123], v[8:11]\n"
    "v_mfma_f32_16x16x32_bf16 v[12:15], v[108:111], a[124:127], v[12:15]\n"
    "s_nop 7\n"
    "v_exp_f32 v0, v0\n"
    "v_exp_f32 v1, v1\n"
    "v_exp_f32 v2, v2\n"
    "v_exp_f32 v3, v3\n"
    "v_exp_f32 v4, v4\n"
    "v_exp_f32 v5, v5\n"
    "v_exp_f32 v6, v6\n"
    "v_exp_f32 v7, v7\n"
    "v_exp_f32 v8, v8\n"
    "v_exp_f32 v9, v9\n"
    "v_exp_f32 v10, v10\n"
    "v_exp_f32 v11, v11\n"
    "v_exp_f32 v12, v12\n"
    "v_exp_f32 v13, v13\n"
    "v_exp_f32 v14, v14\n"
    "v_exp_f32 v15, v15\n"
    "s_nop 0\n"
    "v_add_f32 v116, v116, v0\n"
    "v_add_f32 v116, v116, v1\n"
    "v_add_f32 v116, v116, v2\n"
    "v_add_f32 v116, v116, v3\n"
    "v_cvt_pk_bf16_f32 v32, v0, v1\n"
    "v_cvt_pk_bf16_f32 v33, v2, v3\n"
    "v_add_f32 v117, v117, v4\n"
    "v_add_f32 v117, v117, v5\n"
    "v_add_f32 v117, v117, v6\n"
    "v_add_f32 v117, v117, v7\n"
    "v_cvt_pk_bf16_f32 v36, v4, v5\n"
    "v_cvt_pk_bf16_f32 v37, v6, v7\n"
    "v_add_f32 v118, v118, v8\n"
    "v_add_f32 v118, v118, v9\n"
    "v_add_f32 v118, v118, v10\n"
    "v_add_f32 v118, v118, v11\n"
    "v_cvt_pk_bf16_f32 v40, v8, v9\n"
    "v_cvt_pk_bf16_f32 v41, v10, v11\n"
    "v_add_f32 v119, v119, v12\n"
    "v_add_f32 v119, v119, v13\n"
    "v_add_f32 v119, v119, v14\n"
    "v_add_f32 v119, v119, v15\n"
    "v_cvt_pk_bf16_f32 v44, v12, v13\n"
    "v_cvt_pk_bf16_f32 v45, v14, v15\n"
    "s_add_u32 s84, s28, 2048\n"
    "buffer_load_dwordx4 v[80:83], v127, s[24:27], s84 offen offset:0\n"
    "buffer_load_dwordx4 v[84:87], v127, s[24:27], s84 offen offset:64\n"
    "buffer_load_dwordx4 v[88:91], v127, s[24:27], s84 offen offset:128\n"
    "buffer_load_dwordx4 v[92:95], v127, s[24:27], s84 offen offset:192\n"
    "buffer_load_dwordx4 v[96:99], v127, s[24:27], s84 offen offset:256\n"
    "buffer_load_dwordx4 v[100:103], v127, s[24:27], s84 offen offset:320\n"
    "buffer_load_dwordx4 v[104:107], v127, s[24:27], s84 offen offset:384\n"
    "buffer_load_dwordx4 v[108:111], v127, s[24:27], s84 offen offset:448\n"
    "s_waitcnt vmcnt(0)\n"
    "v_mfma_f32_16x16x32_bf16 v[0:3], v[80:83], a[0:3], v[64:67]\n"
    "v_mfma_f32_16x16x32_bf16 v[4:7], v[80:83], a[4:7], v[68:71]\n"
    "v_mfma_f32_16x16x32_bf16 v[8:11], v[80:83], a[8:11], v[72:75]\n"
    "v_mfma_f32_16x16x32_bf16 v[12:15], v[80:83], a[12:15], v[76:79]\n"
    "v_mfma_f32_16x16x32_bf16 v[0:3], v[84:87], a[16:19], v[0:3]\n"
    "v_mfma_f32_16x16x32_bf16 v[4:7], v[84:87], a[20:23], v[4:7]\n"
    "v_mfma_f32_16x16x32_bf16 v[8:11], v[84:87], a[24:27], v[8:11]\n"
    "v_mfma_f32_16x16x32_bf16 v[12:15], v[84:87], a[28:31], v[12:15]\n"
    "v_mfma_f32_16x16x32_bf16 v[0:3], v[88:91], a[32:35], v[0:3]\n"
    "v_mfma_f32_16x16x32_bf16 v[4:7], v[88:91], a[36:39], v[4:7]\n"
    "v_mfma_f32_16x16x32_bf16 v[8:11], v[88:91], a[40:43], v[8:11]\n"
    "v_mfma_f32_16x16x32_bf16 v[12:15], v[88:91], a[44:47], v[12:15]\n"
    "v_mfma_f32_16x16x32_bf16 v[0:3], v[92:95], a[48:51], v[0:3]\n"
    "v_mfma_f32_16x16x32_bf16 v[4:7], v[92:95], a[52:55], v[4:7]\n"
    "v_mfma_f32_16x16x32_bf16 v[8:11], v[92:95], a[56:59], v[8:11]\n"
    "v_mfma_f32_16x16x32_bf16 v[12:15], v[92:95], a[60:63], v[12:15]\n"
    "v_mfma_f32_16x16x32_bf16 v[0:3], v[96:99], a[64:67], v[0:3]\n"
    "v_mfma_f32_16x16x32_bf16 v[4:7], v[96:99], a[68:71], v[4:7]\n"
    "v_mfma_f32_16x16x32_bf16 v[8:11], v[96:99], a[72:75], v[8:11]\n"
    "v_mfma_f32_16x16x32_bf16 v[12:15], v[96:99], a[76:79], v[12:15]\n"
    "v_mfma_f32_16x16x32_bf16 v[0:3], v[100:103], a[80:83], v[0:3]\n"
    "v_mfma_f32_16x16x32_bf16 v[4:7], v[100:103], a[84:87], v[4:7]\n"
    "v_mfma_f32_16x16x32_bf16 v[8:11], v[100:103], a[88:91], v[8:11]\n"
    "v_mfma_f32_16x16x32_bf16 v[12:15], v[100:103], a[92:95], v[12:15]\n"
    "v_mfma_f32_16x16x32_bf16 v[0:3], v[104:107], a[96:99], v[0:3]\n"
    "v_mfma_f32_16x16x32_bf16 v[4:7], v[104:107], a[100:103], v[4:7]\n"
    "v_mfma_f32_16x16x32_bf16 v[8:11], v[104:107], a[104:107], v[8:11]\n"
    "v_mfma_f32_16x16x32_bf16 v[12:15], v[104:107], a[108:111], v[12:15]\n"
    "v_mfma_f32_16x16x32_bf16 v[0:3], v[108:111], a[112:115], v[0:3]\n"
    "v_mfma_f32_16x16x32_bf16 v[4:7], v[108:111], a[116:119], v[4:7]\n"
    "v_mfma_f32_16x16x32_bf16 v[8:11], v[108:111], a[120:123], v[8:11]\n"
    "v_mfma_f32_16x16x32_bf16 v[12:15], v[108:111], a[124:127], v[12:15]\n"
    "s_nop 7\n"
    "v_exp_f32 v0, v0\n"
    "v_exp_f32 v1, v1\n"
    "v_exp_f32 v2, v2\n"
    "v_exp_f32 v3, v3\n"
    "v_exp_f32 v4, v4\n"
    "v_exp_f32 v5, v5\n"
    "v_exp_f32 v6, v6\n"
    "v_exp_f32 v7, v7\n"
    "v_exp_f32 v8, v8\n"
    "v_exp_f32 v9, v9\n"
    "v_exp_f32 v10, v10\n"
    "v_exp_f32 v11, v11\n"
    "v_exp_f32 v12, v12\n"
    "v_exp_f32 v13, v13\n"
    "v_exp_f32 v14, v14\n"
    "v_exp_f32 v15, v15\n"
    "s_nop 0\n"
    "v_add_f32 v116, v116, v0\n"
    "v_add_f32 v116, v116, v1\n"
    "v_add_f32 v116, v116, v2\n"
    "v_add_f32 v116, v116, v3\n"
    "v_cvt_pk_bf16_f32 v34, v0, v1\n"
    "v_cvt_pk_bf16_f32 v35, v2, v3\n"
    "v_add_f32 v117, v117, v4\n"
    "v_add_f32 v117, v117, v5\n"
    "v_add_f32 v117, v117, v6\n"
    "v_add_f32 v117, v117, v7\n"
    "v_cvt_pk_bf16_f32 v38, v4, v5\n"
    "v_cvt_pk_bf16_f32 v39, v6, v7\n"
    "v_add_f32 v118, v118, v8\n"
    "v_add_f32 v118, v118, v9\n"
    "v_add_f32 v118, v118, v10\n"
    "v_add_f32 v118, v118, v11\n"
    "v_cvt_pk_bf16_f32 v42, v8, v9\n"
    "v_cvt_pk_bf16_f32 v43, v10, v11\n"
    "v_add_f32 v119, v119, v12\n"
    "v_add_f32 v119, v119, v13\n"
    "v_add_f32 v119, v119, v14\n"
    "v_add_f32 v119, v119, v15\n"
    "v_cvt_pk_bf16_f32 v46, v12, v13\n"
    "v_cvt_pk_bf16_f32 v47, v14, v15\n"
    "s_add_u32 s84, s28, 16384\n"
    "buffer_load_dwordx4 v[80:83], v127, s[24:27], s84 offen offset:0\n"
    "buffer_load_dwordx4 v[84:87], v127, s[24:27], s84 offen offset:64\n"
    "buffer_load_dwordx4 v[88:91], v127, s[24:27], s84 offen offset:128\n"
    "buffer_load_dwordx4 v[92:95], v127, s[24:27], s84 offen offset:192\n"
    "buffer_load_dwordx4 v[96:99], v127, s[24:27], s84 offen offset:256\n"
    "buffer_load_dwordx4 v[100:103], v127, s[24:27], s84 offen offset:320\n"
    "buffer_load_dwordx4 v[104:107], v127, s[24:27], s84 offen offset:384\n"
    "buffer_load_dwordx4 v[108:111], v127, s[24:27], s84 offen offset:448\n"
    "s_waitcnt vmcnt(0)\n"
    "v_mfma_f32_16x16x32_bf16 v[0:3], v[80:83], a[0:3], v[64:67]\n"
    "v_mfma_f32_16x16x32_bf16 v[4:7], v[80:83], a[4:7], v[68:71]\n"
    "v_mfma_f32_16x16x32_bf16 v[8:11], v[80:83], a[8:11], v[72:75]\n"
    "v_mfma_f32_16x16x32_bf16 v[12:15], v[80:83], a[12:15], v[76:79]\n"
    "v_mfma_f32_16x16x32_bf16 v[0:3], v[84:87], a[16:19], v[0:3]\n"
    "v_mfma_f32_16x16x32_bf16 v[4:7], v[84:87], a[20:23], v[4:7]\n"
    "v_mfma_f32_16x16x32_bf16 v[8:11], v[84:87], a[24:27], v[8:11]\n"
    "v_mfma_f32_16x16x32_bf16 v[12:15], v[84:87], a[28:31], v[12:15]\n"
    "v_mfma_f32_16x16x32_bf16 v[0:3], v[88:91], a[32:35], v[0:3]\n"
    "v_mfma_f32_16x16x32_bf16 v[4:7], v[88:91], a[36:39], v[4:7]\n"
    "v_mfma_f32_16x16x32_bf16 v[8:11], v[88:91], a[40:43], v[8:11]\n"
    "v_mfma_f32_16x16x32_bf16 v[12:15], v[88:91], a[44:47], v[12:15]\n"
    "v_mfma_f32_16x16x32_bf16 v[0:3], v[92:95], a[48:51], v[0:3]\n"
    "v_mfma_f32_16x16x32_bf16 v[4:7], v[92:95], a[52:55], v[4:7]\n"
    "v_mfma_f32_16x16x32_bf16 v[8:11], v[92:95], a[56:59], v[8:11]\n"
    "v_mfma_f32_16x16x32_bf16 v[12:15], v[92:95], a[60:63], v[12:15]\n"
    "v_mfma_f32_16x16x32_bf16 v[0:3], v[96:99], a[64:67], v[0:3]\n"
    "v_mfma_f32_16x16x32_bf16 v[4:7], v[96:99], a[68:71], v[4:7]\n"
    "v_mfma_f32_16x16x32_bf16 v[8:11], v[96:99], a[72:75], v[8:11]\n"
    "v_mfma_f32_16x16x32_bf16 v[12:15], v[96:99], a[76:79], v[12:15]\n"
    "v_mfma_f32_16x16x32_bf16 v[0:3], v[100:103], a[80:83], v[0:3]\n"
    "v_mfma_f32_16x16x32_bf16 v[4:7], v[100:103], a[84:87], v[4:7]\n"
    "v_mfma_f32_16x16x32_bf16 v[8:11], v[100:103], a[88:91], v[8:11]\n"
    "v_mfma_f32_16x16x32_bf16 v[12:15], v[100:103], a[92:95], v[12:15]\n"
    "v_mfma_f32_16x16x32_bf16 v[0:3], v[104:107], a[96:99], v[0:3]\n"
    "v_mfma_f32_16x16x32_bf16 v[4:7], v[104:107], a[100:103], v[4:7]\n"
    "v_mfma_f32_16x16x32_bf16 v[8:11], v[104:107], a[104:107], v[8:11]\n"
    "v_mfma_f32_16x16x32_bf16 v[12:15], v[104:107], a[108:111], v[12:15]\n"
    "v_mfma_f32_16x16x32_bf16 v[0:3], v[108:111], a[112:115], v[0:3]\n"
    "v_mfma_f32_16x16x32_bf16 v[4:7], v[108:111], a[116:119], v[4:7]\n"
    "v_mfma_f32_16x16x32_bf16 v[8:11], v[108:111], a[120:123], v[8:11]\n"
    "v_mfma_f32_16x16x32_bf16 v[12:15], v[108:111], a[124:127], v[12:15]\n"
    "s_nop 7\n"
    "v_exp_f32 v0, v0\n"
    "v_exp_f32 v1, v1\n"
    "v_exp_f32 v2, v2\n"
    "v_exp_f32 v3, v3\n"
    "v_exp_f32 v4, v4\n"
    "v_exp_f32 v5, v5\n"
    "v_exp_f32 v6, v6\n"
    "v_exp_f32 v7, v7\n"
    "v_exp_f32 v8, v8\n"
    "v_exp_f32 v9, v9\n"
    "v_exp_f32 v10, v10\n"
    "v_exp_f32 v11, v11\n"
    "v_exp_f32 v12, v12\n"
    "v_exp_f32 v13, v13\n"
    "v_exp_f32 v14, v14\n"
    "v_exp_f32 v15, v15\n"
    "s_nop 0\n"
    "v_add_f32 v116, v116, v0\n"
    "v_add_f32 v116, v116, v1\n"
    "v_add_f32 v116, v116, v2\n"
    "v_add_f32 v116, v116, v3\n"
    "v_cvt_pk_bf16_f32 v48, v0, v1\n"
    "v_cvt_pk_bf16_f32 v49, v2, v3\n"
    "v_add_f32 v117, v117, v4\n"
    "v_add_f32 v117, v117, v5\n"
    "v_add_f32 v117, v117, v6\n"
    "v_add_f32 v117, v117, v7\n"
    "v_cvt_pk_bf16_f32 v52, v4, v5\n"
    "v_cvt_pk_bf16_f32 v53, v6, v7\n"
    "v_add_f32 v118, v118, v8\n"
    "v_add_f32 v118, v118, v9\n"
    "v_add_f32 v118, v118, v10\n"
    "v_add_f32 v118, v118, v11\n"
    "v_cvt_pk_bf16_f32 v56, v8, v9\n"
    "v_cvt_pk_bf16_f32 v57, v10, v11\n"
    "v_add_f32 v119, v119, v12\n"
    "v_add_f32 v119, v119, v13\n"
    "v_add_f32 v119, v119, v14\n"
    "v_add_f32 v119, v119, v15\n"
    "v_cvt_pk_bf16_f32 v60, v12, v13\n"
    "v_cvt_pk_bf16_f32 v61, v14, v15\n"
    "s_add_u32 s84, s28, 18432\n"
    "buffer_load_dwordx4 v[80:83], v127, s[24:27], s84 offen offset:0\n"
    "buffer_load_dwordx4 v[84:87], v127, s[24:27], s84 offen offset:64\n"
    "buffer_load_dwordx4 v[88:91], v127, s[24:27], s84 offen offset:128\n"
    "buffer_load_dwordx4 v[92:95], v127, s[24:27], s84 offen offset:192\n"
    "buffer_load_dwordx4 v[96:99], v127, s[24:27], s84 offen offset:256\n"
    "buffer_load_dwordx4 v[100:103], v127, s[24:27], s84 offen offset:320\n"
    "buffer_load_dwordx4 v[104:107], v127, s[24:27], s84 offen offset:384\n"
    "buffer_load_dwordx4 v[108:111], v127, s[24:27], s84 offen offset:448\n"
    "s_waitcnt vmcnt(0)\n"
    "v_mfma_f32_16x16x32_bf16 v[0:3], v[80:83], a[0:3], v[64:67]\n"
    "v_mfma_f32_16x16x32_bf16 v[4:7], v[80:83], a[4:7], v[68:71]\n"
    "v_mfma_f32_16x16x32_bf16 v[8:11], v[80:83], a[8:11], v[72:75]\n"
    "v_mfma_f32_16x16x32_bf16 v[12:15], v[80:83], a[12:15], v[76:79]\n"
    "v_mfma_f32_16x16x32_bf16 v[0:3], v[84:87], a[16:19], v[0:3]\n"
    "v_mfma_f32_16x16x32_bf16 v[4:7], v[84:87], a[20:23], v[4:7]\n"
    "v_mfma_f32_16x16x32_bf16 v[8:11], v[84:87], a[24:27], v[8:11]\n"
    "v_mfma_f32_16x16x32_bf16 v[12:15], v[84:87], a[28:31], v[12:15]\n"
    "v_mfma_f32_16x16x32_bf16 v[0:3], v[88:91], a[32:35], v[0:3]\n"
    "v_mfma_f32_16x16x32_bf16 v[4:7], v[88:91], a[36:39], v[4:7]\n"
    "v_mfma_f32_16x16x32_bf16 v[8:11], v[88:91], a[40:43], v[8:11]\n"
    "v_mfma_f32_16x16x32_bf16 v[12:15], v[88:91], a[44:47], v[12:15]\n"
    "v_mfma_f32_16x16x32_bf16 v[0:3], v[92:95], a[48:51], v[0:3]\n"
    "v_mfma_f32_16x16x32_bf16 v[4:7], v[92:95], a[52:55], v[4:7]\n"
    "v_mfma_f32_16x16x32_bf16 v[8:11], v[92:95], a[56:59], v[8:11]\n"
    "v_mfma_f32_16x16x32_bf16 v[12:15], v[92:95], a[60:63], v[12:15]\n"
    "v_mfma_f32_16x16x32_bf16 v[0:3], v[96:99], a[64:67], v[0:3]\n"
    "v_mfma_f32_16x16x32_bf16 v[4:7], v[96:99], a[68:71], v[4:7]\n"
    "v_mfma_f32_16x16x32_bf16 v[8:11], v[96:99], a[72:75], v[8:11]\n"
    "v_mfma_f32_16x16x32_bf16 v[12:15], v[96:99], a[76:79], v[12:15]\n"
    "v_mfma_f32_16x16x32_bf16 v[0:3], v[100:103], a[80:83], v[0:3]\n"
    "v_mfma_f32_16x16x32_bf16 v[4:7], v[100:103], a[84:87], v[4:7]\n"
    "v_mfma_f32_16x16x32_bf16 v[8:11], v[100:103], a[88:91], v[8:11]\n"
    "v_mfma_f32_16x16x32_bf16 v[12:15], v[100:103], a[92:95], v[12:15]\n"
    "v_mfma_f32_16x16x32_bf16 v[0:3], v[104:107], a[96:99], v[0:3]\n"
    "v_mfma_f32_16x16x32_bf16 v[4:7], v[104:107], a[100:103], v[4:7]\n"
    "v_mfma_f32_16x16x32_bf16 v[8:11], v[104:107], a[104:107], v[8:11]\n"
    "v_mfma_f32_16x16x32_bf16 v[12:15], v[104:107], a[108:111], v[12:15]\n"
    "v_mfma_f32_16x16x32_bf16 v[0:3], v[108:111], a[112:115], v[0:3]\n"
    "v_mfma_f32_16x16x32_bf16 v[4:7], v[108:111], a[116:119], v[4:7]\n"
    "v_mfma_f32_16x16x32_bf16 v[8:11], v[108:111], a[120:123], v[8:11]\n"
    "v_mfma_f32_16x16x32_bf16 v[12:15], v[108:111], a[124:127], v[12:15]\n"
    "s_nop 7\n"
    "v_exp_f32 v0, v0\n"
    "v_exp_f32 v1, v1\n"
    "v_exp_f32 v2, v2\n"
    "v_exp_f32 v3, v3\n"
    "v_exp_f32 v4, v4\n"
    "v_exp_f32 v5, v5\n"
    "v_exp_f32 v6, v6\n"
    "v_exp_f32 v7, v7\n"
    "v_exp_f32 v8, v8\n"
    "v_exp_f32 v9, v9\n"
    "v_exp_f32 v10, v10\n"
    "v_exp_f32 v11, v11\n"
    "v_exp_f32 v12, v12\n"
    "v_exp_f32 v13, v13\n"
    "v_exp_f32 v14, v14\n"
    "v_exp_f32 v15, v15\n"
    "s_nop 0\n"
    "v_add_f32 v116, v116, v0\n"
    "v_add_f32 v116, v116, v1\n"
    "v_add_f32 v116, v116, v2\n"
    "v_add_f32 v116, v116, v3\n"
    "v_cvt_pk_bf16_f32 v50, v0, v1\n"
    "v_cvt_pk_bf16_f32 v51, v2, v3\n"
    "v_add_f32 v117, v117, v4\n"
    "v_add_f32 v117, v117, v5\n"
    "v_add_f32 v117, v117, v6\n"
    "v_add_f32 v117, v117, v7\n"
    "v_cvt_pk_bf16_f32 v54, v4, v5\n"
    "v_cvt_pk_bf16_f32 v55, v6, v7\n"
    "v_add_f32 v118, v118, v8\n"
    "v_add_f32 v118, v118, v9\n"
    "v_add_f32 v118, v118, v10\n"
    "v_add_f32 v118, v118, v11\n"
    "v_cvt_pk_bf16_f32 v58, v8, v9\n"
    "v_cvt_pk_bf16_f32 v59, v10, v11\n"
    "v_add_f32 v119, v119, v12\n"
    "v_add_f32 v119, v119, v13\n"
    "v_add_f32 v119, v119, v14\n"
    "v_add_f32 v119, v119, v15\n"
    "v_cvt_pk_bf16_f32 v62, v12, v13\n"
    "v_cvt_pk_bf16_f32 v63, v14, v15\n"
    "v_mov_b32 v80, v20\n"
    "v_mov_b32 v81, v21\n"
    "v_mov_b32 v82, v22\n"
    "v_mov_b32 v83, v23\n"
    "buffer_load_dwordx4 v[0:3], v124, s[68:71], s94 offen offset:0\n"
    "buffer_load_dwordx4 v[4:7], v124, s[68:71], s94 offen offset:1024\n"
    "buffer_load_dwordx4 v[8:11], v124, s[68:71], s94 offen offset:2048\n"
    "buffer_load_dwordx4 v[12:15], v124, s[68:71], s94 offen offset:3072\n"
    "buffer_load_dwordx4 v[16:19], v124, s[68:71], s95 offen offset:0\n"
    "buffer_load_dwordx4 v[20:23], v124, s[68:71], s95 offen offset:1024\n"
    "buffer_load_dwordx4 v[24:27], v124, s[68:71], s95 offen offset:2048\n"
    "buffer_load_dwordx4 v[28:31], v124, s[68:71], s95 offen offset:3072\n"
    "s_waitcnt vmcnt(0)\n"
    "v_sub_f32 v0, v0, v80\n"
    "v_sub_f32 v1, v1, v80\n"
    "v_sub_f32 v2, v2, v80\n"
    "v_sub_f32 v3, v3, v80\n"
    "v_sub_f32 v4, v4, v81\n"
    "v_sub_f32 v5, v5, v81\n"
    "v_sub_f32 v6, v6, v81\n"
    "v_sub_f32 v7, v7, v81\n"
    "v_sub_f32 v8, v8, v82\n"
    "v_sub_f32 v9, v9, v82\n"
    "v_sub_f32 v10, v10, v82\n"
    "v_sub_f32 v11, v11, v82\n"
    "v_sub_f32 v12, v12, v83\n"
    "v_sub_f32 v13, v13, v83\n"
    "v_sub_f32 v14, v14, v83\n"
    "v_sub_f32 v15, v15, v83\n"
    "v_sub_f32 v16, v16, v80\n"
    "v_sub_f32 v17, v17, v80\n"
    "v_sub_f32 v18, v18, v80\n"
    "v_sub_f32 v19, v19, v80\n"
    "v_sub_f32 v20, v20, v81\n"
    "v_sub_f32 v21, v21, v81\n"
    "v_sub_f32 v22, v22, v81\n"
    "v_sub_f32 v23, v23, v81\n"
    "v_sub_f32 v24, v24, v82\n"
    "v_sub_f32 v25, v25, v82\n"
    "v_sub_f32 v26, v26, v82\n"
    "v_sub_f32 v27, v27, v82\n"
    "v_sub_f32 v28, v28, v83\n"
    "v_sub_f32 v29, v29, v83\n"
    "v_sub_f32 v30, v30, v83\n"
    "v_sub_f32 v31, v31, v83\n"
    "buffer_load_dwordx4 v[80:83], v124, s[68:71], s92 offen offset:0\n"
    "buffer_load_dwordx4 v[84:87], v124, s[68:71], s92 offen offset:1024\n"
    "buffer_load_dwordx4 v[88:91], v124, s[68:71], s92 offen offset:2048\n"
    "buffer_load_dwordx4 v[92:95], v124, s[68:71], s92 offen offset:3072\n"
    "buffer_load_dwordx4 v[96:99], v124, s[68:71], s93 offen offset:0\n"
    "buffer_load_dwordx4 v[100:103], v124, s[68:71], s93 offen offset:1024\n"
    "buffer_load_dwordx4 v[104:107], v124, s[68:71], s93 offen offset:2048\n"
    "buffer_load_dwordx4 v[108:111], v124, s[68:71], s93 offen offset:3072\n"
    "s_waitcnt vmcnt(0)\n"
    "s_setpc_b64 s[90:91]\n"
    "Lend%=:\n"
    :
    : "s"(karg), "s"(bid)
    : "memory", "vcc", "scc", "v0","v1","v2","v3","v4","v5","v6","v7","v8","v9","v10","v11","v12","v13","v14","v15","v16","v17","v18","v19","v20","v21","v22","v23","v24","v25","v26","v27","v28","v29","v30","v31","v32","v33","v34","v35","v36","v37","v38","v39","v40","v41","v42","v43","v44","v45","v46","v47","v48","v49","v50","v51","v52","v53","v54","v55","v56","v57","v58","v59","v60","v61","v62","v63","v64","v65","v66","v67","v68","v69","v70","v71","v72","v73","v74","v75","v76","v77","v78","v79","v80","v81","v82","v83","v84","v85","v86","v87","v88","v89","v90","v91","v92","v93","v94","v95","v96","v97","v98","v99","v100","v101","v102","v103","v104","v105","v106","v107","v108","v109","v110","v111","v112","v113","v114","v115","v116","v117","v118","v119","v120","v121","v122","v123","v124","v125","v126","v127","v128","v129","v130","v131","v132","v133","v134","v135","v136","v137","v138","v139","v140","v141","v142","v143","v144","v145","v146","v147","v148","v149","v150","v151","v152","v153","v154","v155","v156","v157","v158","v159","v160","v161","v162","v163","v164","v165","v166","v167","v168","v169","v170","v171","v172","v173","v174","v175","v176","v177","v178","v179","v180","v181","v182","v183","v184","v185","v186","v187","v188","v189","v190","v191","v192","v193","v194","v195","v196","v197","v198","v199","v200","v201","v202","v203","v204","v205","v206","v207","v208","v209","v210","v211","v212","v213","v214","v215","v216","v217","v218","v219","v220","v221","v222","v223","v224","v225","v226","v227","v228","v229","v230","v231","v232","v233","v234","v235","v236","v237","v238","v239","v240","v241","v242","v243","v244","v245","v246","v247","v248","v249","v250","v251","v252","v253","v254","v255","a0","a1","a2","a3","a4","a5","a6","a7","a8","a9","a10","a11","a12","a13","a14","a15","a16","a17","a18","a19","a20","a21","a22","a23","a24","a25","a26","a27","a28","a29","a30","a31","a32","a33","a34","a35","a36","a37","a38","a39","a40","a41","a42","a43","a44","a45","a46","a47","a48","a49","a50","a51","a52","a53","a54","a55","a56","a57","a58","a59","a60","a61","a62","a63","a64","a65","a66","a67","a68","a69","a70","a71","a72","a73","a74","a75","a76","a77","a78","a79","a80","a81","a82","a83","a84","a85","a86","a87","a88","a89","a90","a91","a92","a93","a94","a95","a96","a97","a98","a99","a100","a101","a102","a103","a104","a105","a106","a107","a108","a109","a110","a111","a112","a113","a114","a115","a116","a117","a118","a119","a120","a121","a122","a123","a124","a125","a126","a127","a128","a129","a130","a131","a132","a133","a134","a135","a136","a137","a138","a139","a140","a141","a142","a143","a144","a145","a146","a147","a148","a149","a150","a151","a152","a153","a154","a155","a156","a157","a158","a159","a160","a161","a162","a163","a164","a165","a166","a167","a168","a169","a170","a171","a172","a173","a174","a175","a176","a177","a178","a179","a180","a181","a182","a183","a184","a185","a186","a187","a188","a189","a190","a191","a192","a193","a194","a195","a196","a197","a198","a199","a200","a201","a202","a203","a204","a205","a206","a207","a208","a209","a210","a211","a212","a213","a214","a215","a216","a217","a218","a219","a220","a221","a222","a223","a224","a225","a226","a227","a228","a229","a230","a231","a232","a233","a234","a235","a236","a237","a238","a239","a240","a241","a242","a243","a244","a245","a246","a247","a248","a249","a250","a251","a252","a253","a254","a255","s4","s5","s6","s7","s8","s9","s10","s11","s12","s13","s14","s15","s16","s17","s18","s19","s20","s21","s22","s23","s24","s25","s26","s27","s28","s29","s30","s31","s33","s34","s35","s36","s37","s38","s39","s40","s41","s42","s43","s44","s45","s46","s47","s48","s49","s50","s51","s52","s53","s54","s55","s56","s57","s58","s59","s60","s61","s62","s63","s64","s65","s66","s67","s68","s69","s70","s71","s72","s73","s74","s75","s76","s77","s78","s79","s80","s81","s82","s83","s84","s85","s86","s87","s88","s89","s90","s91","s92","s93","s94","s95","s96","s97","s98","s99");
}

extern "C" void kernel_launch(void* const* d_in, const int* in_sizes, int n_in, void* d_out, int out_size, void* d_ws, size_t ws_size,
                              hipStream_t stream) {
  (void)in_sizes; (void)n_in; (void)out_size; (void)ws_size;
  (void)hipFuncSetAttribute((const void*)fused_main, hipFuncAttributeMaxDynamicSharedMemorySize, 163840);
  fused_main<<<256, 256, 163840, stream>>>(d_in[0], d_in[1], d_in[2], d_in[3], d_in[4], d_in[6], d_in[7], d_in[8], d_ws, d_out);
}
```
